```python
import torch

def _softmax(x, axis=-1):
    return torch.softmax(x, dim=axis)
def _gelu(x):
    return torch.nn.functional.gelu(x, approximate="tanh")
def _silu(x):
    return torch.nn.functional.silu(x)
def _sigmoid(x):
    return torch.sigmoid(x)
def _layernorm(x, axis=-1, eps=1e-5):
    m = x.mean(dim=axis, keepdim=True)
    v = x.var(dim=axis, keepdim=True, unbiased=False)
    return (x - m) / torch.sqrt(v + eps)
def _scatter_add(tgt, idx, src, axis=0):
    return tgt.clone().index_add_(axis, idx, src)
def _segment_sum(src, seg, n):
    out = torch.zeros((n,) + src.shape[1:], dtype=src.dtype)
    return out.index_add_(0, seg, src)
def _pad(x, pw):
    flat = tuple(v for pair in reversed(pw) for v in pair)
    return torch.nn.functional.pad(x, flat)

kernel_name = 'concat_heads__ssm_conv_s4d__ssm_conv_h3__prompt'

def setup_inputs():

    _w = (lambda a: (lambda t: t.to(torch.bfloat16) if t.is_floating_point() else t)(torch.as_tensor(a)))
    def _A_setup():
        Bsz, L, D, N = 1, 2048, 1024, 128
        return {
            "u": _w((lambda *s: torch.randn(*s, dtype=torch.bfloat16) * 0.05)(Bsz, L, D)),
            "log_A_real": _w((lambda *s: torch.randn(*s, dtype=torch.bfloat16) * 3.2)(D, N) * 0.5),
            "A_imag": _w((lambda *s: torch.randn(*s, dtype=torch.bfloat16) * 0.05)(D, N) * 3.14),
            "C_real": _w((lambda *s: torch.randn(*s, dtype=torch.bfloat16) * 0.1)(D, N) / N ** 0.5),
            "C_imag": _w((lambda *s: torch.randn(*s, dtype=torch.bfloat16) * 0.1)(D, N) / N ** 0.5),
            "log_dt": _w((lambda *s: torch.randn(*s, dtype=torch.bfloat16) * 0.05)(D) * 0.5 - 4.0),
            "D_skip": _w((lambda *s: torch.randn(*s, dtype=torch.bfloat16) * 0.05)(D)),
        }
    _A_in = _A_setup()

    def _B_setup():
        B, S, D = 1, 2048, 1024
        ks = 4
        return {
            "q": _w((lambda *s: torch.randn(*s, dtype=torch.bfloat16) * 0.05)(B, S, D)),
            "k": _w((lambda *s: torch.randn(*s, dtype=torch.bfloat16) * 0.2)(B, S, D)),
            "v": _w((lambda *s: torch.randn(*s, dtype=torch.bfloat16) * 0.2)(B, S, D)),
            "shift_k": _w((lambda *s: torch.randn(*s, dtype=torch.bfloat16) * 0.2)(D, ks) * 0.5),
            "ssm_k": _w((lambda *s: torch.randn(*s, dtype=torch.bfloat16) * 0.8)(D, S) * (1.0 / S ** 0.5)),
            "ssm_D": _w((lambda *s: torch.randn(*s, dtype=torch.bfloat16) * 0.2)(D)),
        }
    _B_in = _B_setup()

    return {
        "A_u": _A_in["u"],
        "A_log_A_real": _A_in["log_A_real"],
        "A_A_imag": _A_in["A_imag"],
        "A_C_real": _A_in["C_real"],
        "A_C_imag": _A_in["C_imag"],
        "A_log_dt": _A_in["log_dt"],
        "A_D_skip": _A_in["D_skip"],
        "B_q": _B_in["q"],
        "B_k": _B_in["k"],
        "B_v": _B_in["v"],
        "B_shift_k": _B_in["shift_k"],
        "B_ssm_k": _B_in["ssm_k"],
        "B_ssm_D": _B_in["ssm_D"],
    }

def reference(A_u, A_log_A_real, A_A_imag, A_C_real, A_C_imag, A_log_dt, A_D_skip, B_q, B_k, B_v, B_shift_k, B_ssm_k, B_ssm_D):
    def _A_ref(u, log_A_real, A_imag, C_real, C_imag, log_dt, D_skip):
        L = u.shape[1]
        dt = torch.exp(log_dt)
        A = -torch.exp(log_A_real) + 1j * A_imag
        C = (C_real + 1j * C_imag) * dt[:, None]
        pos = (lambda x, dt: x.to(dt))(torch.arange(L), log_dt.dtype)
        P = torch.exp((A * dt[:, None])[:, :, None] * pos[None, None, :])
        K = 2.0 * torch.einsum("dn,dnl->dl", C, P).real
        L2 = 2 * L
        u_t = (lambda x, *d: x.permute(*d))(u, 0, 2, 1)
        Kf = (lambda x, *a, **k: torch.fft.rfft(x.float(), *a, **k))(K, L2)
        Uf = (lambda x, *a, **k: torch.fft.rfft(x.float(), *a, **k))(u_t, L2)
        y = (lambda x, *a, **k: torch.fft.irfft(x, *a, **k).to(torch.bfloat16))(Uf * Kf[None, :, :], L2)[:, :, :L]
        y = (lambda x, *d: x.permute(*d))(y, 0, 2, 1)
        return y + u * D_skip[None, None, :]

    def _B_ref(q, k, v, shift_k, ssm_k, ssm_D):
        B, S, D = q.shape
        ks = shift_k.shape[-1]
        k_pad = _pad(k, ((0, 0), (ks - 1, 0), (0, 0)))
        k_shift = torch.zeros_like(k)
        for t in range(ks):
            k_shift = k_shift + k_pad[:, t:t + S, :] * shift_k[:, ks - 1 - t][None, None, :]
        kv = k_shift * v
        L = 2 * S
        kv_pad = _pad(kv, ((0, 0), (0, S), (0, 0)))
        kf_pad = _pad(ssm_k, ((0, 0), (0, S)))
        kv_f = (lambda x, axes=(-2,-1): torch.fft.rfft2(x.float(), dim=axes))(kv_pad[:, None, :, :], axes=(1, 2))
        kf_f = (lambda x, axes=(-2,-1): torch.fft.rfft2(x.float(), dim=axes))(kf_pad[:, None, :], axes=(1, 2))
        kf_f = (lambda x, *d: x.permute(*d))(kf_f, 1, 2, 0)
        y_f = kv_f * kf_f[None, :, :, :]
        y = (lambda x, s, axes=(-2,-1): torch.fft.irfft2(x, s=s, dim=axes).to(torch.bfloat16))(y_f, (1, L), axes=(1, 2))[:, 0, :S, :]
        y = y + kv * ssm_D[None, None, :]
        return q * y

    a = _A_ref(A_u, A_log_A_real, A_A_imag, A_C_real, A_C_imag, A_log_dt, A_D_skip)
    b = _B_ref(B_q, B_k, B_v, B_shift_k, B_ssm_k, B_ssm_D)
    return torch.concatenate([a, b], axis=-1)
```

```cpp
#include <hip/hip_runtime.h>
#include <hip/hip_bf16.h>
#include <stdint.h>

typedef unsigned int u32;
typedef unsigned short u16;
typedef u32 u32x4 __attribute__((ext_vector_type(4)));
typedef u32 u32x2 __attribute__((ext_vector_type(2)));
typedef float f32x4 __attribute__((ext_vector_type(4)));
typedef __bf16 bf16x8 __attribute__((ext_vector_type(8)));
typedef _Float16 f16x8 __attribute__((ext_vector_type(8)));

#define L_SEQ 2048
#define D_CH 1024
#define N_MODE 128

__device__ __forceinline__ float bflo(u32 d) { return __uint_as_float(d << 16); }
__device__ __forceinline__ float bfhi(u32 d) { return __uint_as_float(d & 0xffff0000u); }
__device__ __forceinline__ float bf2f(u16 h) { return __uint_as_float(((u32)h) << 16); }
__device__ __forceinline__ u32 pk_bf16(float lo, float hi) {
  typedef __bf16 bf16x2 __attribute__((ext_vector_type(2)));
  typedef float f32x2 __attribute__((ext_vector_type(2)));
  f32x2 v = {lo, hi};
  bf16x2 r = __builtin_convertvector(v, bf16x2);
  return __builtin_bit_cast(u32, r);
}
__device__ __forceinline__ float rbf(float x) { return bflo(pk_bf16(x, 0.f)); }

__device__ __forceinline__ int xcd_group(int b) {
  int xcd = b & 7, idx = b >> 3;
  return ((xcd + 8 * (idx >> 3)) << 3) | (idx & 7);
}

struct cplx { float r, i; };
__device__ __forceinline__ cplx csq(cplx a) { cplx o; o.r = a.r * a.r - a.i * a.i; o.i = (a.r + a.r) * a.i; return o; }
__device__ __forceinline__ cplx cmul(cplx a, cplx b) { cplx o; o.r = a.r * b.r - a.i * b.i; o.i = a.r * b.i + a.i * b.r; return o; }
__device__ __forceinline__ cplx cexp_pos(float ar, float ai, float pos) {
  const float LOG2E = 1.44269504088896340736f;
  const float I2PI_HI = 0.15915494309189533577f;
  const float I2PI_LO = 6.4206383e-9f;
  float xr = ar * pos;
  float xi = ai * pos;
  float e = __builtin_amdgcn_exp2f(xr * LOG2E);
  float r = xi * I2PI_HI;
  float err = __builtin_fmaf(xi, I2PI_HI, -r);
  float f = __builtin_amdgcn_fractf(r);
  f += __builtin_fmaf(xi, I2PI_LO, err);
  cplx o;
  o.r = e * __builtin_amdgcn_cosf(f);
  o.i = e * __builtin_amdgcn_sinf(f);
  return o;
}
__device__ __forceinline__ void split_pk(float a, float b, u32& hi, u32& lo) {
  hi = pk_bf16(a, b);
  lo = pk_bf16(a - bflo(hi), b - bfhi(hi));
}

#define NB_T 128

__global__ __launch_bounds__(256) void prep_kernel(
    const u16* __restrict__ u, const u16* __restrict__ lar, const u16* __restrict__ aim,
    const u16* __restrict__ cre, const u16* __restrict__ cim, const u16* __restrict__ ldt,
    const u16* __restrict__ kin, const u16* __restrict__ vin, const u16* __restrict__ shk,
    u16* __restrict__ uT, u16* __restrict__ kvT, u16* __restrict__ h16, int* __restrict__ hexp) {
  __shared__ __attribute__((aligned(16))) float smem[4 * 672 + 4 * 128 + 16];
  const int b = blockIdx.x;
  const int t = threadIdx.x;
  const int lane = t & 63, wv = t >> 6;
  if (b < 2 * NB_T) {
    const int arr = b >> 7;
    const int bb = b & 127;
    const int xcd = bb & 7, rest = bb >> 3;
    const int cb = xcd + 8 * (rest & 1);
    const int rblk = rest >> 1;
    const int cg = lane & 7, rb = lane >> 3;
    const int l0 = rblk * 256 + wv * 64 + rb * 8;
    const int c0 = cb * 64 + cg * 8;
    if (arr == 1) {
      u32x4 r[8];
#pragma unroll
      for (int i = 0; i < 8; i++) r[i] = *(const u32x4*)(u + (size_t)(l0 + i) * D_CH + c0);
#pragma unroll
      for (int c = 0; c < 8; c++) {
        u32x4 o;
#pragma unroll
        for (int p = 0; p < 4; p++) {
          u32 a = r[2 * p][c >> 1], bq = r[2 * p + 1][c >> 1];
          o[p] = (c & 1) ? ((a >> 16) | (bq & 0xffff0000u)) : ((a & 0xffffu) | (bq << 16));
        }
        *(u32x4*)(uT + (size_t)(c0 + c) * L_SEQ + l0) = o;
      }
    } else {
      u32x4 kk[11], vv[8], sw[4];
#pragma unroll
      for (int i = 0; i < 11; i++) {
        int l = l0 + i - 3;
        u32x4 z = {0, 0, 0, 0};
        kk[i] = (l >= 0) ? *(const u32x4*)(kin + (size_t)l * D_CH + c0) : z;
      }
#pragma unroll
      for (int i = 0; i < 8; i++) vv[i] = *(const u32x4*)(vin + (size_t)(l0 + i) * D_CH + c0);
#pragma unroll
      for (int i = 0; i < 4; i++) sw[i] = *(const u32x4*)(shk + (size_t)c0 * 4 + i * 8);
#pragma unroll
      for (int c = 0; c < 8; c++) {
        u32 s01 = sw[c >> 1][(c & 1) * 2], s23 = sw[c >> 1][(c & 1) * 2 + 1];
        float w0 = bflo(s01), w1 = bfhi(s01), w2 = bflo(s23), w3 = bfhi(s23);
        float kf[11];
#pragma unroll
        for (int i = 0; i < 11; i++) kf[i] = (c & 1) ? bfhi(kk[i][c >> 1]) : bflo(kk[i][c >> 1]);
        float res[8];
#pragma unroll
        for (int i = 0; i < 8; i += 2) {
          u32 p3 = pk_bf16(kf[i] * w3, kf[i + 1] * w3);
          u32 p2 = pk_bf16(kf[i + 1] * w2, kf[i + 2] * w2);
          u32 p1 = pk_bf16(kf[i + 2] * w1, kf[i + 3] * w1);
          u32 p0 = pk_bf16(kf[i + 3] * w0, kf[i + 4] * w0);
          u32 a = pk_bf16(bflo(p3) + bflo(p2), bfhi(p3) + bfhi(p2));
          a = pk_bf16(bflo(a) + bflo(p1), bfhi(a) + bfhi(p1));
          a = pk_bf16(bflo(a) + bflo(p0), bfhi(a) + bfhi(p0));
          float v0 = (c & 1) ? bfhi(vv[i][c >> 1]) : bflo(vv[i][c >> 1]);
          float v1 = (c & 1) ? bfhi(vv[i + 1][c >> 1]) : bflo(vv[i + 1][c >> 1]);
          res[i] = bflo(a) * v0;
          res[i + 1] = bfhi(a) * v1;
        }
        u32x4 o;
#pragma unroll
        for (int p = 0; p < 4; p++) o[p] = pk_bf16(res[7 - 2 * p], res[6 - 2 * p]);
        *(u32x4*)(kvT + (size_t)(c0 + c) * L_SEQ + l0) = o;
      }
    }
    return;
  }
  const int gb = b - 2 * NB_T;
  const int d = ((gb & 7) + 8 * ((gb >> 3) & 1)) * 64 + (gb >> 4);
  float* red = smem;
  f32x4* prm = (f32x4*)(smem + 4 * 672);
  float* wmax = smem + 4 * 672 + 4 * 128;
  if (t < N_MODE) {
    float dt = rbf(expf(bf2f(ldt[d])));
    float Ar = -rbf(expf(bf2f(lar[d * N_MODE + t])));
    float Ai = bf2f(aim[d * N_MODE + t]);
    f32x4 p;
    p[0] = Ar * dt;
    p[1] = Ai * dt;
    p[2] = 2.0f * (bf2f(cre[d * N_MODE + t]) * dt);
    p[3] = 2.0f * (bf2f(cim[d * N_MODE + t]) * dt);
    prm[t] = p;
  }
  __syncthreads();
  const int c16 = lane & 15, q = lane >> 4;
  f32x4 acc[4];
#pragma unroll
  for (int s = 0; s < 4; s++) acc[s] = (f32x4){0.f, 0.f, 0.f, 0.f};
  const float posF = (float)c16, posG1 = (float)(16 * c16), posG2 = (float)(256 + 32 * c16);
#pragma unroll
  for (int st = 0; st < 2; st++) {
    u32x4 Ah[4], Al[4], Bh[4], Bl[4];
#pragma unroll
    for (int i = 0; i < 4; i++) {
      f32x4 p = prm[32 * wv + 16 * st + 4 * q + i];
      cplx C; C.r = p[2]; C.i = p[3];
      cplx F = cexp_pos(p[0], p[1], posF);
      cplx g1 = cexp_pos(p[0], p[1], posG1);
      cplx g2 = cexp_pos(p[0], p[1], posG2);
      cplx G1 = cmul(C, g1);
      cplx G2 = cmul(C, g2);
      cplx G3 = cmul(G2, g2);
      cplx g3 = csq(g2);
      cplx G4 = cmul(G3, g3);
      u32 hi, lo;
      split_pk(G1.r, -G1.i, hi, lo); Ah[0][i] = hi; Al[0][i] = lo;
      split_pk(G2.r, -G2.i, hi, lo); Ah[1][i] = hi; Al[1][i] = lo;
      split_pk(G3.r, -G3.i, hi, lo); Ah[2][i] = hi; Al[2][i] = lo;
      split_pk(G4.r, -G4.i, hi, lo); Ah[3][i] = hi; Al[3][i] = lo;
      split_pk(F.r, F.i, hi, lo); Bh[0][i] = hi; Bl[0][i] = lo;
      F = csq(F);
      split_pk(F.r, F.i, hi, lo); Bh[1][i] = hi; Bl[1][i] = lo;
      F = csq(F);
      split_pk(F.r, F.i, hi, lo); Bh[2][i] = hi; Bl[2][i] = lo;
      F = csq(F);
      split_pk(F.r, F.i, hi, lo); Bh[3][i] = hi; Bl[3][i] = lo;
    }
#pragma unroll
    for (int s = 0; s < 4; s++) {
      acc[s] = __builtin_amdgcn_mfma_f32_16x16x32_bf16(__builtin_bit_cast(bf16x8, Al[s]), __builtin_bit_cast(bf16x8, Bh[s]), acc[s], 0, 0, 0);
      acc[s] = __builtin_amdgcn_mfma_f32_16x16x32_bf16(__builtin_bit_cast(bf16x8, Ah[s]), __builtin_bit_cast(bf16x8, Bl[s]), acc[s], 0, 0, 0);
      acc[s] = __builtin_amdgcn_mfma_f32_16x16x32_bf16(__builtin_bit_cast(bf16x8, Ah[s]), __builtin_bit_cast(bf16x8, Bh[s]), acc[s], 0, 0, 0);
    }
  }
  {
    float* rw = red + wv * 672;
#pragma unroll
    for (int rg = 0; rg < 4; rg++) {
      int uu = 4 * q + rg;
      rw[16 * uu + c16] = acc[0][rg];
      if (uu < 8) {
        rw[256 + 16 * uu + c16] = acc[1][rg];
        rw[384 + 16 * uu + c16] = acc[2][rg];
      }
      if (uu < 9) rw[512 + 16 * uu + c16] = acc[3][rg];
    }
  }
  __syncthreads();
  float kv0, kv1, kv2 = 0.f;
  kv0 = red[t] + red[672 + t] + red[2 * 672 + t] + red[3 * 672 + t];
  kv1 = red[256 + t] + red[672 + 256 + t] + red[2 * 672 + 256 + t] + red[3 * 672 + 256 + t];
  if (t < 129) kv2 = red[512 + t] + red[672 + 512 + t] + red[2 * 672 + 512 + t] + red[3 * 672 + 512 + t];
  float m = fmaxf(fabsf(kv0), fmaxf(fabsf(kv1), fabsf(kv2)));
#pragma unroll
  for (int o = 32; o > 0; o >>= 1) m = fmaxf(m, __shfl_xor(m, o));
  if (lane == 0) wmax[wv] = m;
  __syncthreads();
  m = fmaxf(fmaxf(wmax[0], wmax[1]), fmaxf(wmax[2], wmax[3]));
  int e = 0;
  if (m > 0.f && m < 3.0e38f) e = __builtin_amdgcn_frexp_expf(m) - 15;
  u16* kc = (u16*)red;
  {
    _Float16 a0 = (_Float16)__builtin_ldexpf(kv0, -e);
    _Float16 a1 = (_Float16)__builtin_ldexpf(kv1, -e);
    _Float16 a2 = (_Float16)__builtin_ldexpf(kv2, -e);
    kc[t] = __builtin_bit_cast(u16, a0);
    kc[256 + t] = __builtin_bit_cast(u16, a1);
    if (t < 129) kc[512 + t] = __builtin_bit_cast(u16, a2);
  }
  if (t == 0) hexp[d] = e;
  __syncthreads();
  {
    u32x4 o;
#pragma unroll
    for (int p = 0; p < 4; p++) {
      u32 w2[2];
#pragma unroll
      for (int hh = 0; hh < 2; hh++) {
        int l = 8 * t + 2 * p + hh;
        int pp = (int)rbf((float)l);
        int idx = pp < 256 ? pp : (pp < 512 ? 256 + ((pp - 256) >> 1) : (pp < 1024 ? 384 + ((pp - 512) >> 2) : 512 + ((pp - 1024) >> 3)));
        w2[hh] = kc[idx];
      }
      o[p] = w2[0] | (w2[1] << 16);
    }
    *(u32x4*)(h16 + (size_t)d * L_SEQ + 8 * t) = o;
  }
}

#define XS_BYTES 4608
#define HC_BYTES 2368
#define HS_BYTES (4 * HC_BYTES)
#define LDS_BYTES (8 * XS_BYTES + 8 * HS_BYTES)

template <bool IS_A>
__device__ __forceinline__ f32x4 mma(u32x4 a, u32x4 b, f32x4 c) {
  if (IS_A) return __builtin_amdgcn_mfma_f32_16x16x32_f16(__builtin_bit_cast(f16x8, a), __builtin_bit_cast(f16x8, b), c, 0, 0, 0);
  else return __builtin_amdgcn_mfma_f32_16x16x32_bf16(__builtin_bit_cast(bf16x8, a), __builtin_bit_cast(bf16x8, b), c, 0, 0, 0);
}

template <bool IS_A>
__device__ __forceinline__ void conv_body(char* smem, int g, const u16* __restrict__ xT, const u16* __restrict__ hT,
                                          const int* __restrict__ hexp, const u16* __restrict__ dvec,
                                          const u16* __restrict__ qin, u16* __restrict__ out) {
  const int t = threadIdx.x, lane = t & 63;
  const int w = __builtin_amdgcn_readfirstlane(t >> 6);
  const int ch = (g & 127) * 8 + w;
  char* xs = smem + w * XS_BYTES;
  char* hs = smem + 8 * XS_BYTES + w * HS_BYTES;
  const u16* xrow = xT + (size_t)ch * L_SEQ;
  const u16* hrow = hT + (size_t)ch * L_SEQ;
  u32x4 xr[4];
#pragma unroll
  for (int j = 0; j < 4; j++) xr[j] = *(const u32x4*)(xrow + 8 * (64 * j + lane));
  u32x4 hd[6];
  u32x2 he[6];
  const int ha = lane / 6, hi_ = lane - 6 * ha;
  const bool hact = lane < 48;
#pragma unroll
  for (int s = 0; s < 6; s++) {
    int hc = 32 * ha + 16 * (s / 3) + 6 * (s % 3) + hi_ - 2;
    u32x4 z4 = {0, 0, 0, 0};
    u32x2 z2 = {0, 0};
    hd[s] = (hact && hc >= 0 && hc < 256) ? *(const u32x4*)(hrow + 8 * hc) : z4;
    he[s] = (hact && hc + 1 >= 0 && hc + 1 < 256) ? *(const u32x2*)(hrow + 8 * hc + 8) : z2;
  }
  int ex = 0;
  if (lane < 30) *(u32x4*)(xs + 16 * lane) = (u32x4){0, 0, 0, 0};
  if (IS_A) {
    u32 mx = 0;
#pragma unroll
    for (int j = 0; j < 4; j++)
#pragma unroll
      for (int p = 0; p < 4; p++) {
        u32 dd = xr[j][p];
        mx = max(mx, max(dd & 0x7fffu, (dd >> 16) & 0x7fffu));
      }
#pragma unroll
    for (int o = 32; o > 0; o >>= 1) mx = max(mx, (u32)__shfl_xor((int)mx, o));
    ex = (int)(mx >> 7) - 127 - 14;
#pragma unroll
    for (int j = 0; j < 4; j++) {
      u32x4 o;
#pragma unroll
      for (int p = 0; p < 4; p++) {
        u32 dd = xr[j][3 - p];
        float lo = __builtin_ldexpf(bfhi(dd), -ex), hi = __builtin_ldexpf(bflo(dd), -ex);
        o[p] = __builtin_bit_cast(u32, __builtin_amdgcn_cvt_pkrtz(lo, hi));
      }
      *(u32x4*)(xs + 480 + 16 * (64 * j + lane)) = o;
    }
  } else {
#pragma unroll
    for (int j = 0; j < 4; j++) *(u32x4*)(xs + 480 + 16 * (64 * j + lane)) = xr[j];
  }
  const int r = lane & 15, q = lane >> 4;
  f32x4 acc[8];
#pragma unroll
  for (int i = 0; i < 8; i++) acc[i] = (f32x4){0.f, 0.f, 0.f, 0.f};
  const int lam = 25 + r - 8 * q;
  const char* abase = hs + (lam & 3) * HC_BYTES + 2 * (lam & ~3);
  const char* bbase = xs + 448 + 32 * r + 16 * q;
  char* hw = hs + ha * 288 + hi_ * 16;
#pragma unroll
  for (int hf = 0; hf < 2; hf++) {
    if (hact) {
#pragma unroll
      for (int j = 0; j < 3; j++) {
        u32x4 d4 = hd[3 * hf + j];
        u32x2 e2 = he[3 * hf + j];
        u32 a01 = __builtin_amdgcn_alignbit(d4[1], d4[0], 16);
        u32 a12 = __builtin_amdgcn_alignbit(d4[2], d4[1], 16);
        u32 a23 = __builtin_amdgcn_alignbit(d4[3], d4[2], 16);
        u32 a3e = __builtin_amdgcn_alignbit(e2[0], d4[3], 16);
        u32 aee = __builtin_amdgcn_alignbit(e2[1], e2[0], 16);
        *(u32x4*)(hw + 96 * j) = d4;
        *(u32x4*)(hw + 96 * j + HC_BYTES) = (u32x4){a01, a12, a23, a3e};
        *(u32x4*)(hw + 96 * j + 2 * HC_BYTES) = (u32x4){d4[1], d4[2], d4[3], e2[0]};
        *(u32x4*)(hw + 96 * j + 3 * HC_BYTES) = (u32x4){a12, a23, a3e, aee};
      }
    }
    __builtin_amdgcn_wave_barrier();
#pragma unroll
    for (int mm = 0; mm < 4; mm++) {
      u32x4 A[8], B[8];
#pragma unroll
      for (int a = 0; a < 8; a++) {
        u32x2 lo = *(const u32x2*)(abase + 64 * mm + 288 * a);
        u32x2 hi = *(const u32x2*)(abase + 64 * mm + 288 * a + 8);
        A[a] = (u32x4){lo[0], lo[1], hi[0], hi[1]};
      }
#pragma unroll
      for (int e = 0; e < 8; e++) B[e] = *(const u32x4*)(bbase + 512 * e - 64 * (4 * hf + mm));
#pragma unroll
      for (int a = 0; a < 8; a++)
#pragma unroll
        for (int e = 0; e + a < 8; e++) acc[a + e] = mma<IS_A>(A[a], B[e], acc[a + e]);
    }
    __builtin_amdgcn_wave_barrier();
  }
  const float dsk = bf2f(dvec[ch]);
  int esc = 0;
  if (IS_A) esc = hexp[ch] + ex;
  char* ys = hs;
#pragma unroll
  for (int ct = 0; ct < 8; ct++) {
    const int l = 256 * ct + 16 * r + 4 * q;
    float x0, x1, x2, x3;
    if (IS_A) {
      u32x2 xv = *(const u32x2*)(xrow + l);
      x0 = bflo(xv[0]); x1 = bfhi(xv[0]); x2 = bflo(xv[1]); x3 = bfhi(xv[1]);
    } else {
      u32x2 xv = *(const u32x2*)(xs + 480 + 2 * ((l & ~7) + 4 - (l & 4)));
      x3 = bflo(xv[0]); x2 = bfhi(xv[0]); x1 = bflo(xv[1]); x0 = bfhi(xv[1]);
    }
    float y0 = acc[ct][0], y1 = acc[ct][1], y2 = acc[ct][2], y3 = acc[ct][3];
    if (IS_A) {
      y0 = __builtin_ldexpf(y0, esc); y1 = __builtin_ldexpf(y1, esc);
      y2 = __builtin_ldexpf(y2, esc); y3 = __builtin_ldexpf(y3, esc);
    }
    u32 yb01 = pk_bf16(y0, y1), yb23 = pk_bf16(y2, y3);
    u32 s01 = pk_bf16(x0 * dsk, x1 * dsk), s23 = pk_bf16(x2 * dsk, x3 * dsk);
    u32x2 o;
    o[0] = pk_bf16(bflo(yb01) + bflo(s01), bfhi(yb01) + bfhi(s01));
    o[1] = pk_bf16(bflo(yb23) + bflo(s23), bfhi(yb23) + bfhi(s23));
    *(u32x2*)(ys + 2 * l) = o;
  }
  __syncthreads();
  {
    u32x2 yv[8];
#pragma unroll
    for (int c = 0; c < 8; c++) yv[c] = *(const u32x2*)(smem + 8 * XS_BYTES + c * HS_BYTES + 8 * t);
#pragma unroll
    for (int i = 0; i < 4; i++) {
      const int l = 4 * t + i;
      u32x4 o;
#pragma unroll
      for (int p = 0; p < 4; p++) {
        u32 a = yv[2 * p][i >> 1], bq = yv[2 * p + 1][i >> 1];
        o[p] = (i & 1) ? ((a >> 16) | (bq & 0xffff0000u)) : ((a & 0xffffu) | (bq << 16));
      }
      if (!IS_A) {
        u32x4 qv = *(const u32x4*)(qin + (size_t)l * D_CH + (g & 127) * 8);
#pragma unroll
        for (int p = 0; p < 4; p++) o[p] = pk_bf16(bflo(o[p]) * bflo(qv[p]), bfhi(o[p]) * bfhi(qv[p]));
      }
      *(u32x4*)(out + (size_t)l * (2 * D_CH) + g * 8) = o;
    }
  }
}

__global__ __launch_bounds__(512) void conv_kernel(const u16* __restrict__ uT, const u16* __restrict__ kvT,
                                                   const u16* __restrict__ h16, const int* __restrict__ hexp,
                                                   const u16* __restrict__ dskip, const u16* __restrict__ ssmk,
                                                   const u16* __restrict__ ssmd, const u16* __restrict__ qin,
                                                   u16* __restrict__ out) {
  extern __shared__ __attribute__((aligned(16))) char smem[];
  const int g = xcd_group(blockIdx.x);
  if (g < 128) conv_body<true>(smem, g, uT, h16, hexp, dskip, qin, out);
  else conv_body<false>(smem, g, kvT, ssmk, hexp, ssmd, qin, out);
}

extern "C" void kernel_launch(void* const* d_in, const int* in_sizes, int n_in, void* d_out, int out_size,
                              void* d_ws, size_t ws_size, hipStream_t stream) {
  const u16* u = (const u16*)d_in[0];
  const u16* lar = (const u16*)d_in[1];
  const u16* aim = (const u16*)d_in[2];
  const u16* cre = (const u16*)d_in[3];
  const u16* cim = (const u16*)d_in[4];
  const u16* ldt = (const u16*)d_in[5];
  const u16* dsk = (const u16*)d_in[6];
  const u16* q = (const u16*)d_in[7];
  const u16* k = (const u16*)d_in[8];
  const u16* v = (const u16*)d_in[9];
  const u16* shk = (const u16*)d_in[10];
  const u16* ssmk = (const u16*)d_in[11];
  const u16* ssmd = (const u16*)d_in[12];
  char* ws = (char*)d_ws;
  u16* uT = (u16*)ws;
  u16* kvT = (u16*)(ws + (size_t)4 * 1024 * 1024);
  u16* h16 = (u16*)(ws + (size_t)8 * 1024 * 1024);
  int* hexp = (int*)(ws + (size_t)12 * 1024 * 1024);
  hipFuncSetAttribute((const void*)conv_kernel, hipFuncAttributeMaxDynamicSharedMemorySize, LDS_BYTES);
  prep_kernel<<<2 * NB_T + D_CH, 256, 0, stream>>>(u, lar, aim, cre, cim, ldt, k, v, shk, uT, kvT, h16, hexp);
  conv_kernel<<<256, 512, LDS_BYTES, stream>>>(uT, kvT, h16, hexp, dsk, ssmk, ssmd, q, (u16*)d_out);
}
```

```cpp
#include <hip/hip_runtime.h>
#include <hip/hip_bf16.h>
#include <stdint.h>

typedef unsigned int u32;
typedef unsigned short u16;
typedef u32 u32x4 __attribute__((ext_vector_type(4)));
typedef u32 u32x2 __attribute__((ext_vector_type(2)));
typedef float f32x4 __attribute__((ext_vector_type(4)));
typedef __bf16 bf16x8 __attribute__((ext_vector_type(8)));
typedef _Float16 f16x8 __attribute__((ext_vector_type(8)));

#define L_SEQ 2048
#define D_CH 1024
#define N_MODE 128

__device__ __forceinline__ float bflo(u32 d) { return __uint_as_float(d << 16); }
__device__ __forceinline__ float bfhi(u32 d) { return __uint_as_float(d & 0xffff0000u); }
__device__ __forceinline__ float bf2f(u16 h) { return __uint_as_float(((u32)h) << 16); }
__device__ __forceinline__ u32 pk_bf16(float lo, float hi) {
  typedef __bf16 bf16x2 __attribute__((ext_vector_type(2)));
  typedef float f32x2 __attribute__((ext_vector_type(2)));
  f32x2 v = {lo, hi};
  bf16x2 r = __builtin_convertvector(v, bf16x2);
  return __builtin_bit_cast(u32, r);
}
__device__ __forceinline__ float rbf(float x) { return bflo(pk_bf16(x, 0.f)); }

__device__ __forceinline__ int xcd_group(int b) {
  int xcd = b & 7, idx = b >> 3;
  return ((xcd + 8 * (idx >> 3)) << 3) | (idx & 7);
}

struct cplx { float r, i; };
__device__ __forceinline__ cplx csq(cplx a) { cplx o; o.r = a.r * a.r - a.i * a.i; o.i = (a.r + a.r) * a.i; return o; }
__device__ __forceinline__ cplx cmul(cplx a, cplx b) { cplx o; o.r = a.r * b.r - a.i * b.i; o.i = a.r * b.i + a.i * b.r; return o; }
__device__ __forceinline__ cplx cexp_pos(float ar, float ai, float pos) {
  const float LOG2E = 1.44269504088896340736f;
  const float I2PI_HI = 0.15915494309189533577f;
  const float I2PI_LO = 6.4206383e-9f;
  float xr = ar * pos;
  float xi = ai * pos;
  float e = __builtin_amdgcn_exp2f(xr * LOG2E);
  float r = xi * I2PI_HI;
  float err = __builtin_fmaf(xi, I2PI_HI, -r);
  float f = __builtin_amdgcn_fractf(r);
  f += __builtin_fmaf(xi, I2PI_LO, err);
  cplx o;
  o.r = e * __builtin_amdgcn_cosf(f);
  o.i = e * __builtin_amdgcn_sinf(f);
  return o;
}
__device__ __forceinline__ void split_h(float a, float b, u32& hi, u32& lo) {
  hi = __builtin_bit_cast(u32, __builtin_amdgcn_cvt_pkrtz(a, b));
  u32 l;
  asm("v_fma_mixlo_f16 %0, %1, -1.0, %2 op_sel_hi:[1,0,0]" : "=v"(l) : "v"(hi), "v"(a));
  asm("v_fma_mixhi_f16 %0, %1, -1.0, %2 op_sel:[1,0,0] op_sel_hi:[1,0,0]" : "+v"(l) : "v"(hi), "v"(b));
  lo = l;
}

#define GT_ROW 288
#define GT_TAB (16 * GT_ROW)
#define GW_BYTES (2 * GT_TAB)
#define RED_F 656

__global__ __launch_bounds__(256) void prep_kernel(
    const u16* __restrict__ u, const u16* __restrict__ lar, const u16* __restrict__ aim,
    const u16* __restrict__ cre, const u16* __restrict__ cim, const u16* __restrict__ ldt,
    const u16* __restrict__ kin, const u16* __restrict__ vin, const u16* __restrict__ shk,
    u16* __restrict__ uT, u16* __restrict__ kvT, u16* __restrict__ h16, int* __restrict__ hexp) {
  __shared__ __attribute__((aligned(16))) char smem[4 * GW_BYTES + 64];
  const int b = blockIdx.x;
  const int t = threadIdx.x;
  const int lane = t & 63;
  const int wv = __builtin_amdgcn_readfirstlane(t >> 6);
  const int xcd = b & 7, jj = b >> 3, rid = jj >> 1;
  if ((jj & 1) == 0) {
    const int arr = rid & 1;
    const int cb = xcd + 8 * ((rid >> 1) & 1);
    const int rblk = rid >> 2;
    const int cg = lane & 7, rb = lane >> 3;
    const int l0 = rblk * 128 + (wv >> 1) * 64 + rb * 8;
    const int c0 = cb * 64 + (wv & 1) * 32 + cg * 4;
    if (arr == 1) {
      u32x2 r[8];
#pragma unroll
      for (int i = 0; i < 8; i++) r[i] = *(const u32x2*)(u + (size_t)(l0 + i) * D_CH + c0);
#pragma unroll
      for (int c = 0; c < 4; c++) {
        u32x4 o;
#pragma unroll
        for (int p = 0; p < 4; p++)
          o[p] = __builtin_amdgcn_perm(r[2 * p + 1][c >> 1], r[2 * p][c >> 1], (c & 1) ? 0x07060302u : 0x05040100u);
        *(u32x4*)(uT + (size_t)(c0 + c) * L_SEQ + l0) = o;
      }
    } else {
      u32x2 kk[11], vv[8];
      u32x4 sw[2];
#pragma unroll
      for (int i = 0; i < 11; i++) {
        int l = l0 + i - 3;
        u32x2 z = {0, 0};
        kk[i] = (l >= 0) ? *(const u32x2*)(kin + (size_t)l * D_CH + c0) : z;
      }
#pragma unroll
      for (int i = 0; i < 8; i++) vv[i] = *(const u32x2*)(vin + (size_t)(l0 + i) * D_CH + c0);
#pragma unroll
      for (int i = 0; i < 2; i++) sw[i] = *(const u32x4*)(shk + (size_t)c0 * 4 + i * 8);
#pragma unroll
      for (int c = 0; c < 4; c++) {
        u32 s01 = sw[c >> 1][(c & 1) * 2], s23 = sw[c >> 1][(c & 1) * 2 + 1];
        float w0 = bflo(s01), w1 = bfhi(s01), w2 = bflo(s23), w3 = bfhi(s23);
        float kf[11];
#pragma unroll
        for (int i = 0; i < 11; i++) kf[i] = (c & 1) ? bfhi(kk[i][c >> 1]) : bflo(kk[i][c >> 1]);
        float res[8];
#pragma unroll
        for (int i = 0; i < 8; i += 2) {
          u32 p3 = pk_bf16(kf[i] * w3, kf[i + 1] * w3);
          u32 p2 = pk_bf16(kf[i + 1] * w2, kf[i + 2] * w2);
          u32 p1 = pk_bf16(kf[i + 2] * w1, kf[i + 3] * w1);
          u32 p0 = pk_bf16(kf[i + 3] * w0, kf[i + 4] * w0);
          u32 a = pk_bf16(bflo(p3) + bflo(p2), bfhi(p3) + bfhi(p2));
          a = pk_bf16(bflo(a) + bflo(p1), bfhi(a) + bfhi(p1));
          a = pk_bf16(bflo(a) + bflo(p0), bfhi(a) + bfhi(p0));
          float v0 = (c & 1) ? bfhi(vv[i][c >> 1]) : bflo(vv[i][c >> 1]);
          float v1 = (c & 1) ? bfhi(vv[i + 1][c >> 1]) : bflo(vv[i + 1][c >> 1]);
          res[i] = bflo(a) * v0;
          res[i + 1] = bfhi(a) * v1;
        }
        u32x4 o;
#pragma unroll
        for (int p = 0; p < 4; p++) o[p] = pk_bf16(res[7 - 2 * p], res[6 - 2 * p]);
        *(u32x4*)(kvT + (size_t)(c0 + c) * L_SEQ + l0) = o;
      }
    }
    return;
  }
  const int d = (xcd + 8 * (rid & 1)) * 64 + (rid >> 1) * 2 + (wv >> 1);
  const int n = (wv & 1) * 64 + lane;
  char* tab = smem + wv * GW_BYTES;
  float ar, ai;
  cplx C;
  int ec;
  {
    float dt = rbf(expf(bf2f(ldt[d])));
    float Ar = -rbf(expf(bf2f(lar[d * N_MODE + n])));
    float Ai = bf2f(aim[d * N_MODE + n]);
    ar = Ar * dt;
    ai = Ai * dt;
    C.r = 2.0f * (bf2f(cre[d * N_MODE + n]) * dt);
    C.i = 2.0f * (bf2f(cim[d * N_MODE + n]) * dt);
    float m = fmaxf(fabsf(C.r), fabsf(C.i));
#pragma unroll
    for (int o = 32; o > 0; o >>= 1) m = fmaxf(m, __shfl_xor(m, o));
    ec = (m > 0.f && m < 3.0e38f) ? __builtin_amdgcn_frexp_expf(m) - 14 : 0;
    C.r = __builtin_ldexpf(C.r, -ec);
    C.i = __builtin_ldexpf(C.i, -ec);
  }
  const cplx e1 = cexp_pos(ar, ai, 1.0f);
  const cplx f8 = cexp_pos(ar, ai, 8.0f);
  const cplx E = cexp_pos(ar, ai, 128.0f);
  char* wp = tab + 4 * lane;
  const int c16 = lane & 15, q = lane >> 4;
  const char* rp = tab + c16 * GT_ROW + 16 * q;
#define PUT_ROW(row, re, im) { u32 hi_, lo_; split_h((re), (im), hi_, lo_); \
    *(u32*)(wp + (row) * GT_ROW) = hi_; *(u32*)(wp + (row) * GT_ROW + GT_TAB) = lo_; }
  {
    cplx F; F.r = 16384.0f; F.i = 0.f;
#pragma unroll
    for (int w = 0; w < 16; w++) {
      PUT_ROW(w, F.r, F.i);
      if (w < 15) F = cmul(F, f8);
    }
  }
  __builtin_amdgcn_wave_barrier();
  u32x4 Fh[4], Fl[4];
#pragma unroll
  for (int ks = 0; ks < 4; ks++) {
    Fh[ks] = *(const u32x4*)(rp + 64 * ks);
    Fl[ks] = *(const u32x4*)(rp + 64 * ks + GT_TAB);
  }
  __builtin_amdgcn_wave_barrier();
  f32x4 acc[3];
#define TILE_MMA(ti) { __builtin_amdgcn_wave_barrier();                                              \
    f32x4 a_ = {0.f, 0.f, 0.f, 0.f};                                                               \
    _Pragma("unroll") for (int ks = 0; ks < 4; ks++) {                                             \
      u32x4 Gh = *(const u32x4*)(rp + 64 * ks), Gl = *(const u32x4*)(rp + 64 * ks + GT_TAB);       \
      a_ = __builtin_amdgcn_mfma_f32_16x16x32_f16(__builtin_bit_cast(f16x8, Gl), __builtin_bit_cast(f16x8, Fh[ks]), a_, 0, 0, 0); \
      a_ = __builtin_amdgcn_mfma_f32_16x16x32_f16(__builtin_bit_cast(f16x8, Gh), __builtin_bit_cast(f16x8, Fl[ks]), a_, 0, 0, 0); \
      a_ = __builtin_amdgcn_mfma_f32_16x16x32_f16(__builtin_bit_cast(f16x8, Gh), __builtin_bit_cast(f16x8, Fh[ks]), a_, 0, 0, 0); \
    }                                                                                              \
    acc[ti] = a_; __builtin_amdgcn_wave_barrier(); }
  cplx g[8];
  g[0] = C;
#pragma unroll
  for (int be = 1; be < 8; be++) g[be] = cmul(g[be - 1], e1);
#pragma unroll
  for (int be = 0; be < 8; be++) PUT_ROW(be, g[be].r, -g[be].i);
#pragma unroll
  for (int be = 0; be < 8; be++) { g[be] = cmul(g[be], E); PUT_ROW(8 + be, g[be].r, -g[be].i); }
  TILE_MMA(0);
#pragma unroll
  for (int al = 0; al < 2; al++)
#pragma unroll
    for (int be = 0; be < 8; be += 2) { g[be] = cmul(g[be], E); PUT_ROW(4 * al + (be >> 1), g[be].r, -g[be].i); }
#pragma unroll
  for (int al = 0; al < 4; al++)
#pragma unroll
    for (int be = 0; be < 8; be += 4) { g[be] = cmul(g[be], E); PUT_ROW(8 + 2 * al + (be >> 2), g[be].r, -g[be].i); }
  TILE_MMA(1);
#pragma unroll
  for (int al = 0; al < 9; al++) { g[0] = cmul(g[0], E); PUT_ROW(al, g[0].r, -g[0].i); }
  TILE_MMA(2);
  {
    float* rw = (float*)tab;
#pragma unroll
    for (int rg = 0; rg < 4; rg++) {
      const int row = 4 * q + rg;
      rw[128 * (row >> 3) + (row & 7) + 8 * c16] = __builtin_ldexpf(acc[0][rg], ec - 14);
      int i1 = (row < 8) ? 256 + 64 * (row >> 2) + (row & 3) + 4 * c16 : 384 + 32 * ((row - 8) >> 1) + (row & 1) + 2 * c16;
      rw[i1] = __builtin_ldexpf(acc[1][rg], ec - 14);
      if (row < 9) rw[512 + 16 * row + c16] = __builtin_ldexpf(acc[2][rg], ec - 14);
    }
  }
  __syncthreads();
  const int tc = t & 127;
  const float* r0 = (const float*)(smem + (wv & 2) * GW_BYTES);
  const float* r1 = (const float*)(smem + ((wv & 2) + 1) * GW_BYTES);
  float kvl[6];
  float m = 0.f;
#pragma unroll
  for (int i = 0; i < 5; i++) { kvl[i] = r0[tc + 128 * i] + r1[tc + 128 * i]; m = fmaxf(m, fabsf(kvl[i])); }
  kvl[5] = r0[640] + r1[640];
  m = fmaxf(m, fabsf(kvl[5]));
#pragma unroll
  for (int o = 32; o > 0; o >>= 1) m = fmaxf(m, __shfl_xor(m, o));
  float* wmax = (float*)(smem + 4 * GW_BYTES);
  if (lane == 0) wmax[wv] = m;
  __syncthreads();
  m = fmaxf(wmax[wv & 2], wmax[(wv & 2) + 1]);
  int e = 0;
  if (m > 0.f && m < 3.0e38f) e = __builtin_amdgcn_frexp_expf(m) - 15;
  u16* kc = (u16*)(smem + (wv & 2) * GW_BYTES);
#pragma unroll
  for (int i = 0; i < 5; i++) kc[tc + 128 * i] = __builtin_bit_cast(u16, (_Float16)__builtin_ldexpf(kvl[i], -e));
  if (tc == 0) { kc[640] = __builtin_bit_cast(u16, (_Float16)__builtin_ldexpf(kvl[5], -e)); hexp[d] = e; }
  __syncthreads();
#pragma unroll
  for (int hb = 0; hb < 2; hb++) {
    u32x4 o;
#pragma unroll
    for (int p = 0; p < 4; p++) {
      const int l = 1024 * hb + 8 * tc + 2 * p;
      u32 pb = pk_bf16((float)l, (float)(l + 1));
      int i0 = (l < 256) ? l : (int)(pb & 0xffffu) - 17024;
      int i1 = (l < 256) ? l + 1 : (int)(pb >> 16) - 17024;
      o[p] = (u32)kc[i0] | ((u32)kc[i1] << 16);
    }
    *(u32x4*)(h16 + (size_t)d * L_SEQ + 1024 * hb + 8 * tc) = o;
  }
}

#define XS_BYTES 4608
#define HC_BYTES 2368
#define HS_BYTES (4 * HC_BYTES)
#define LDS_BYTES (8 * XS_BYTES + 8 * HS_BYTES)

template <bool IS_A>
__device__ __forceinline__ f32x4 mma(u32x4 a, u32x4 b, f32x4 c) {
  if (IS_A) return __builtin_amdgcn_mfma_f32_16x16x32_f16(__builtin_bit_cast(f16x8, a), __builtin_bit_cast(f16x8, b), c, 0, 0, 0);
  else return __builtin_amdgcn_mfma_f32_16x16x32_bf16(__builtin_bit_cast(bf16x8, a), __builtin_bit_cast(bf16x8, b), c, 0, 0, 0);
}

__device__ __forceinline__ u32 lds_off(const char* p) {
  return (u32)(uintptr_t)(const __attribute__((address_space(3))) char*)p;
}
#define DSR64(dst, addr, off) asm volatile("ds_read_b64 %0, %1 offset:%2" : "=v"(dst) : "v"(addr), "i"(off) : "memory")
#define DSR128(dst, addr, off) asm volatile("ds_read_b128 %0, %1 offset:%2" : "=v"(dst) : "v"(addr), "i"(off) : "memory")
#define LOAD_FRAGS(it, A, B)                                                   \
  _Pragma("unroll") for (int a_ = 0; a_ < 8; a_++) {                           \
    u32x2 lo_, hi_2;                                                           \
    DSR64(lo_, abase, 64 * ((it) & 3) + 288 * a_);                             \
    DSR64(hi_2, abase, 64 * ((it) & 3) + 288 * a_ + 8);                        \
    A[a_] = (u32x4){lo_[0], lo_[1], hi_2[0], hi_2[1]};                         \
    DSR128(B[a_], bbase, 448 + 512 * a_ - 64 * (it));                          \
  }                                                                            \
  __builtin_amdgcn_sched_barrier(0);
#define WAIT_FRAGS(A, B)                                                                              \
  __builtin_amdgcn_sched_barrier(0);                                                                  \
  asm volatile("s_waitcnt lgkmcnt(0)"                                                                 \
               : "+v"(A[0]), "+v"(A[1]), "+v"(A[2]), "+v"(A[3]), "+v"(A[4]), "+v"(A[5]), "+v"(A[6]), "+v"(A[7]), \
                 "+v"(B[0]), "+v"(B[1]), "+v"(B[2]), "+v"(B[3]), "+v"(B[4]), "+v"(B[5]), "+v"(B[6]), "+v"(B[7])  \
               :: "memory")

template <bool IS_A>
__device__ __forceinline__ void conv_body(char* smem, int g, const u16* __restrict__ xT, const u16* __restrict__ hT,
                                          const int* __restrict__ hexp, const u16* __restrict__ dvec,
                                          const u16* __restrict__ qin, u16* __restrict__ out) {
  const int t = threadIdx.x, lane = t & 63;
  const int w = __builtin_amdgcn_readfirstlane(t >> 6);
  const int ch = (g & 127) * 8 + w;
  char* xs = smem + w * XS_BYTES;
  char* hs = smem + 8 * XS_BYTES + w * HS_BYTES;
  const u16* xrow = xT + (size_t)ch * L_SEQ;
  const u16* hrow = hT + (size_t)ch * L_SEQ;
  u32x4 xr[4];
#pragma unroll
  for (int j = 0; j < 4; j++) xr[j] = *(const u32x4*)(xrow + 8 * (64 * j + lane));
  u32x4 hd[6];
  u32x2 he[6];
  const int ha = lane / 6, hi_ = lane - 6 * ha;
  const bool hact = lane < 48;
#pragma unroll
  for (int s = 0; s < 6; s++) {
    int hc = 32 * ha + 16 * (s / 3) + 6 * (s % 3) + hi_ - 2;
    u32x4 z4 = {0, 0, 0, 0};
    u32x2 z2 = {0, 0};
    hd[s] = (hact && hc >= 0 && hc < 256) ? *(const u32x4*)(hrow + 8 * hc) : z4;
    he[s] = (hact && hc + 1 >= 0 && hc + 1 < 256) ? *(const u32x2*)(hrow + 8 * hc + 8) : z2;
  }
  int ex = 0;
  if (lane < 30) *(u32x4*)(xs + 16 * lane) = (u32x4){0, 0, 0, 0};
  if (IS_A) {
    u32 mx = 0;
#pragma unroll
    for (int j = 0; j < 4; j++)
#pragma unroll
      for (int p = 0; p < 4; p++) {
        u32 dd = xr[j][p];
        mx = max(mx, max(dd & 0x7fffu, (dd >> 16) & 0x7fffu));
      }
#pragma unroll
    for (int o = 32; o > 0; o >>= 1) mx = max(mx, (u32)__shfl_xor((int)mx, o));
    ex = (int)(mx >> 7) - 127 - 14;
#pragma unroll
    for (int j = 0; j < 4; j++) {
      u32x4 o;
#pragma unroll
      for (int p = 0; p < 4; p++) {
        u32 dd = xr[j][3 - p];
        float lo = __builtin_ldexpf(bfhi(dd), -ex), hi = __builtin_ldexpf(bflo(dd), -ex);
        o[p] = __builtin_bit_cast(u32, __builtin_amdgcn_cvt_pkrtz(lo, hi));
      }
      *(u32x4*)(xs + 480 + 16 * (64 * j + lane)) = o;
    }
  } else {
#pragma unroll
    for (int j = 0; j < 4; j++) *(u32x4*)(xs + 480 + 16 * (64 * j + lane)) = xr[j];
  }
  const int r = lane & 15, q = lane >> 4;
  f32x4 acc[8];
#pragma unroll
  for (int i = 0; i < 8; i++) acc[i] = (f32x4){0.f, 0.f, 0.f, 0.f};
  const int lam = 25 + r - 8 * q;
  const u32 abase = lds_off(hs) + (lam & 3) * HC_BYTES + 2 * (lam & ~3);
  const u32 bbase = lds_off(xs) + 32 * r + 16 * q;
  char* hw = hs + ha * 288 + hi_ * 16;
  auto stage_h = [&](int hf) {
    if (hact) {
#pragma unroll
      for (int j = 0; j < 3; j++) {
        u32x4 d4 = hd[3 * hf + j];
        u32x2 e2 = he[3 * hf + j];
        u32 a01 = __builtin_amdgcn_alignbit(d4[1], d4[0], 16);
        u32 a12 = __builtin_amdgcn_alignbit(d4[2], d4[1], 16);
        u32 a23 = __builtin_amdgcn_alignbit(d4[3], d4[2], 16);
        u32 a3e = __builtin_amdgcn_alignbit(e2[0], d4[3], 16);
        u32 aee = __builtin_amdgcn_alignbit(e2[1], e2[0], 16);
        *(u32x4*)(hw + 96 * j) = d4;
        *(u32x4*)(hw + 96 * j + HC_BYTES) = (u32x4){a01, a12, a23, a3e};
        *(u32x4*)(hw + 96 * j + 2 * HC_BYTES) = (u32x4){d4[1], d4[2], d4[3], e2[0]};
        *(u32x4*)(hw + 96 * j + 3 * HC_BYTES) = (u32x4){a12, a23, a3e, aee};
      }
    }
  };
  u32x4 A0[8], B0[8], A1[8], B1[8];
  stage_h(0);
  LOAD_FRAGS(0, A0, B0);
  u32x4 qv[4];
  u32x2 uv[8];
  if (IS_A) {
#pragma unroll
    for (int ct = 0; ct < 8; ct++) uv[ct] = *(const u32x2*)(xrow + 256 * ct + 16 * r + 4 * q);
  } else {
#pragma unroll
    for (int i = 0; i < 4; i++) qv[i] = *(const u32x4*)(qin + (size_t)(4 * t + i) * D_CH + (g & 127) * 8);
  }
#define MMA_ALL(A, B)                                                                   \
  _Pragma("unroll") for (int a = 0; a < 8; a++)                                         \
  _Pragma("unroll") for (int e = 0; e + a < 8; e++) acc[a + e] = mma<IS_A>(A[a], B[e], acc[a + e]); \
  asm volatile("" : "+v"(acc[0]), "+v"(acc[1]), "+v"(acc[2]), "+v"(acc[3]), "+v"(acc[4]), "+v"(acc[5]), "+v"(acc[6]), "+v"(acc[7]));
  WAIT_FRAGS(A0, B0); LOAD_FRAGS(1, A1, B1); MMA_ALL(A0, B0);
  WAIT_FRAGS(A1, B1); LOAD_FRAGS(2, A0, B0); MMA_ALL(A1, B1);
  WAIT_FRAGS(A0, B0); LOAD_FRAGS(3, A1, B1);
  stage_h(1);
  MMA_ALL(A0, B0);
  WAIT_FRAGS(A1, B1); LOAD_FRAGS(4, A0, B0); MMA_ALL(A1, B1);
  WAIT_FRAGS(A0, B0); LOAD_FRAGS(5, A1, B1); MMA_ALL(A0, B0);
  WAIT_FRAGS(A1, B1); LOAD_FRAGS(6, A0, B0); MMA_ALL(A1, B1);
  WAIT_FRAGS(A0, B0); LOAD_FRAGS(7, A1, B1); MMA_ALL(A0, B0);
  WAIT_FRAGS(A1, B1); MMA_ALL(A1, B1);
  const float dsk = bf2f(dvec[ch]);
  int esc = 0;
  if (IS_A) esc = hexp[ch] + ex;
  char* ys = hs;
#pragma unroll
  for (int ct = 0; ct < 8; ct++) {
    const int l = 256 * ct + 16 * r + 4 * q;
    float x0, x1, x2, x3;
    if (IS_A) {
      u32x2 xv = uv[ct];
      x0 = bflo(xv[0]); x1 = bfhi(xv[0]); x2 = bflo(xv[1]); x3 = bfhi(xv[1]);
    } else {
      u32x2 xv = *(const u32x2*)(xs + 480 + 2 * ((l & ~7) + 4 - (l & 4)));
      x3 = bflo(xv[0]); x2 = bfhi(xv[0]); x1 = bflo(xv[1]); x0 = bfhi(xv[1]);
    }
    float y0 = acc[ct][0], y1 = acc[ct][1], y2 = acc[ct][2], y3 = acc[ct][3];
    if (IS_A) {
      y0 = __builtin_ldexpf(y0, esc); y1 = __builtin_ldexpf(y1, esc);
      y2 = __builtin_ldexpf(y2, esc); y3 = __builtin_ldexpf(y3, esc);
    }
    u32 yb01 = pk_bf16(y0, y1), yb23 = pk_bf16(y2, y3);
    u32 s01 = pk_bf16(x0 * dsk, x1 * dsk), s23 = pk_bf16(x2 * dsk, x3 * dsk);
    u32x2 o;
    o[0] = pk_bf16(bflo(yb01) + bflo(s01), bfhi(yb01) + bfhi(s01));
    o[1] = pk_bf16(bflo(yb23) + bflo(s23), bfhi(yb23) + bfhi(s23));
    *(u32x2*)(ys + 2 * l) = o;
  }
  __syncthreads();
  {
    u32x2 yv[8];
#pragma unroll
    for (int c = 0; c < 8; c++) yv[c] = *(const u32x2*)(smem + 8 * XS_BYTES + c * HS_BYTES + 8 * t);
#pragma unroll
    for (int i = 0; i < 4; i++) {
      const int l = 4 * t + i;
      u32x4 o;
#pragma unroll
      for (int p = 0; p < 4; p++) {
        u32 a = yv[2 * p][i >> 1], bq = yv[2 * p + 1][i >> 1];
        o[p] = (i & 1) ? ((a >> 16) | (bq & 0xffff0000u)) : ((a & 0xffffu) | (bq << 16));
      }
      if (!IS_A) {
#pragma unroll
        for (int p = 0; p < 4; p++) o[p] = pk_bf16(bflo(o[p]) * bflo(qv[i][p]), bfhi(o[p]) * bfhi(qv[i][p]));
      }
      *(u32x4*)(out + (size_t)l * (2 * D_CH) + g * 8) = o;
    }
  }
}

__global__ __launch_bounds__(512) void conv_kernel(const u16* __restrict__ uT, const u16* __restrict__ kvT,
                                                   const u16* __restrict__ h16, const int* __restrict__ hexp,
                                                   const u16* __restrict__ dskip, const u16* __restrict__ ssmk,
                                                   const u16* __restrict__ ssmd, const u16* __restrict__ qin,
                                                   u16* __restrict__ out) {
  extern __shared__ __attribute__((aligned(16))) char smem[];
  const int g = xcd_group(blockIdx.x);
  if (g < 128) conv_body<true>(smem, g, uT, h16, hexp, dskip, qin, out);
  else conv_body<false>(smem, g, kvT, ssmk, hexp, ssmd, qin, out);
}

extern "C" void kernel_launch(void* const* d_in, const int* in_sizes, int n_in, void* d_out, int out_size,
                              void* d_ws, size_t ws_size, hipStream_t stream) {
  const u16* u = (const u16*)d_in[0];
  const u16* lar = (const u16*)d_in[1];
  const u16* aim = (const u16*)d_in[2];
  const u16* cre = (const u16*)d_in[3];
  const u16* cim = (const u16*)d_in[4];
  const u16* ldt = (const u16*)d_in[5];
  const u16* dsk = (const u16*)d_in[6];
  const u16* q = (const u16*)d_in[7];
  const u16* k = (const u16*)d_in[8];
  const u16* v = (const u16*)d_in[9];
  const u16* shk = (const u16*)d_in[10];
  const u16* ssmk = (const u16*)d_in[11];
  const u16* ssmd = (const u16*)d_in[12];
  char* ws = (char*)d_ws;
  u16* uT = (u16*)ws;
  u16* kvT = (u16*)(ws + (size_t)4 * 1024 * 1024);
  u16* h16 = (u16*)(ws + (size_t)8 * 1024 * 1024);
  int* hexp = (int*)(ws + (size_t)12 * 1024 * 1024);
  hipFuncSetAttribute((const void*)conv_kernel, hipFuncAttributeMaxDynamicSharedMemorySize, LDS_BYTES);
  prep_kernel<<<1024, 256, 0, stream>>>(u, lar, aim, cre, cim, ldt, k, v, shk, uT, kvT, h16, hexp);
  conv_kernel<<<256, 512, LDS_BYTES, stream>>>(uT, kvT, h16, hexp, dsk, ssmk, ssmd, q, (u16*)d_out);
}
```

```cpp
#include <hip/hip_runtime.h>
#include <hip/hip_bf16.h>
#include <stdint.h>

typedef unsigned int u32;
typedef unsigned short u16;
typedef u32 u32x4 __attribute__((ext_vector_type(4)));
typedef u32 u32x2 __attribute__((ext_vector_type(2)));
typedef float f32x4 __attribute__((ext_vector_type(4)));
typedef __bf16 bf16x8 __attribute__((ext_vector_type(8)));
typedef _Float16 f16x8 __attribute__((ext_vector_type(8)));

#define L_SEQ 2048
#define D_CH 1024
#define N_MODE 128

__device__ __forceinline__ float bflo(u32 d) { return __uint_as_float(d << 16); }
__device__ __forceinline__ float bfhi(u32 d) { return __uint_as_float(d & 0xffff0000u); }
__device__ __forceinline__ float bf2f(u16 h) { return __uint_as_float(((u32)h) << 16); }
__device__ __forceinline__ u32 pk_bf16(float lo, float hi) {
  typedef __bf16 bf16x2 __attribute__((ext_vector_type(2)));
  typedef float f32x2 __attribute__((ext_vector_type(2)));
  f32x2 v = {lo, hi};
  bf16x2 r = __builtin_convertvector(v, bf16x2);
  return __builtin_bit_cast(u32, r);
}
__device__ __forceinline__ float rbf(float x) { return bflo(pk_bf16(x, 0.f)); }

__device__ __forceinline__ int xcd_group(int b) {
  int xcd = b & 7, idx = b >> 3;
  return ((xcd + 8 * (idx >> 3)) << 3) | (idx & 7);
}

struct cplx { float r, i; };
__device__ __forceinline__ cplx csq(cplx a) { cplx o; o.r = a.r * a.r - a.i * a.i; o.i = (a.r + a.r) * a.i; return o; }
__device__ __forceinline__ cplx cmul(cplx a, cplx b) { cplx o; o.r = a.r * b.r - a.i * b.i; o.i = a.r * b.i + a.i * b.r; return o; }
__device__ __forceinline__ cplx cexp_pos(float ar, float ai, float pos) {
  const float LOG2E = 1.44269504088896340736f;
  const float I2PI_HI = 0.15915494309189533577f;
  const float I2PI_LO = 6.4206383e-9f;
  float xr = ar * pos;
  float xi = ai * pos;
  float e = __builtin_amdgcn_exp2f(xr * LOG2E);
  float r = xi * I2PI_HI;
  float err = __builtin_fmaf(xi, I2PI_HI, -r);
  float f = __builtin_amdgcn_fractf(r);
  f += __builtin_fmaf(xi, I2PI_LO, err);
  cplx o;
  o.r = e * __builtin_amdgcn_cosf(f);
  o.i = e * __builtin_amdgcn_sinf(f);
  return o;
}
__device__ __forceinline__ void split_h(float a, float b, u32& hi, u32& lo) {
  hi = __builtin_bit_cast(u32, __builtin_amdgcn_cvt_pkrtz(a, b));
  u32 l;
  asm("v_fma_mixlo_f16 %0, %1, -1.0, %2 op_sel_hi:[1,0,0]" : "=v"(l) : "v"(hi), "v"(a));
  asm("v_fma_mixhi_f16 %0, %1, -1.0, %2 op_sel:[1,0,0] op_sel_hi:[1,0,0]" : "+v"(l) : "v"(hi), "v"(b));
  lo = l;
}

#define GT_ROW 288
#define GT_TAB (16 * GT_ROW)
#define GW_BYTES (2 * GT_TAB)
#define PREP_LDS (8 * GW_BYTES + 12288)

__global__ __launch_bounds__(512) void prep_kernel(
    const u16* __restrict__ u, const u16* __restrict__ lar, const u16* __restrict__ aim,
    const u16* __restrict__ cre, const u16* __restrict__ cim, const u16* __restrict__ ldt,
    const u16* __restrict__ kin, const u16* __restrict__ vin, const u16* __restrict__ shk,
    u16* __restrict__ uT, u16* __restrict__ kvT, u16* __restrict__ h16, int* __restrict__ hexp) {
  extern __shared__ __attribute__((aligned(16))) char smem[];
  const int b = blockIdx.x;
  const int t = threadIdx.x;
  const int lane = t & 63;
  const int wv = __builtin_amdgcn_readfirstlane(t >> 6);
  const int xcd = b & 7, idx = b >> 3;
  const int cb = xcd + 8 * (idx & 1);
  const int rblk = idx >> 1;
  const int d = cb * 64 + rblk * 4 + (wv >> 1);
  const int n = (wv & 1) * 64 + lane;
  const u16 p_ldt = ldt[d], p_lar = lar[d * N_MODE + n], p_aim = aim[d * N_MODE + n];
  const u16 p_cre = cre[d * N_MODE + n], p_cim = cim[d * N_MODE + n];
  const int l0 = rblk * 128 + (wv & 3) * 32 + (lane >> 4) * 8;
  const int c0 = cb * 64 + (lane & 15) * 4;
  u32x2 kk[11], vv[8];
  u32x4 sw[2];
  if (wv < 4) {
#pragma unroll
    for (int i = 0; i < 11; i++) {
      int l = l0 + i - 3;
      u32x2 z = {0, 0};
      kk[i] = (l >= 0) ? *(const u32x2*)(kin + (size_t)l * D_CH + c0) : z;
    }
#pragma unroll
    for (int i = 0; i < 8; i++) vv[i] = *(const u32x2*)(vin + (size_t)(l0 + i) * D_CH + c0);
#pragma unroll
    for (int i = 0; i < 2; i++) sw[i] = *(const u32x4*)(shk + (size_t)c0 * 4 + i * 8);
  } else {
#pragma unroll
    for (int i = 0; i < 8; i++) vv[i] = *(const u32x2*)(u + (size_t)(l0 + i) * D_CH + c0);
  }
  __builtin_amdgcn_sched_barrier(0);
  char* tab = smem + wv * GW_BYTES;
  float ar, ai;
  cplx C;
  int ec;
  {
    float dt = rbf(expf(bf2f(p_ldt)));
    float Ar = -rbf(expf(bf2f(p_lar)));
    float Ai = bf2f(p_aim);
    ar = Ar * dt;
    ai = Ai * dt;
    C.r = 2.0f * (bf2f(p_cre) * dt);
    C.i = 2.0f * (bf2f(p_cim) * dt);
    float m = fmaxf(fabsf(C.r), fabsf(C.i));
#pragma unroll
    for (int o = 32; o > 0; o >>= 1) m = fmaxf(m, __shfl_xor(m, o));
    ec = (m > 0.f && m < 3.0e38f) ? __builtin_amdgcn_frexp_expf(m) - 14 : 0;
    C.r = __builtin_ldexpf(C.r, -ec);
    C.i = __builtin_ldexpf(C.i, -ec);
  }
  const cplx e1 = cexp_pos(ar, ai, 1.0f);
  const cplx f8 = cexp_pos(ar, ai, 8.0f);
  const cplx E = cexp_pos(ar, ai, 128.0f);
  char* wp = tab + 4 * lane;
  const int c16 = lane & 15, q = lane >> 4;
  const char* rp = tab + c16 * GT_ROW + 16 * q;
#define PUT_ROW(row, re, im) { u32 hi_, lo_; split_h((re), (im), hi_, lo_); \
    *(u32*)(wp + (row) * GT_ROW) = hi_; *(u32*)(wp + (row) * GT_ROW + GT_TAB) = lo_; }
  {
    cplx F; F.r = 16384.0f; F.i = 0.f;
#pragma unroll
    for (int w = 0; w < 16; w++) {
      PUT_ROW(w, F.r, F.i);
      if (w < 15) F = cmul(F, f8);
    }
  }
  __builtin_amdgcn_wave_barrier();
  u32x4 Fh[4], Fl[4];
#pragma unroll
  for (int ks = 0; ks < 4; ks++) {
    Fh[ks] = *(const u32x4*)(rp + 64 * ks);
    Fl[ks] = *(const u32x4*)(rp + 64 * ks + GT_TAB);
  }
  __builtin_amdgcn_wave_barrier();
  f32x4 acc[3];
#define TILE_MMA(ti) { __builtin_amdgcn_wave_barrier();                                              \
    f32x4 a_ = {0.f, 0.f, 0.f, 0.f};                                                               \
    _Pragma("unroll") for (int ks = 0; ks < 4; ks++) {                                             \
      u32x4 Gh = *(const u32x4*)(rp + 64 * ks), Gl = *(const u32x4*)(rp + 64 * ks + GT_TAB);       \
      a_ = __builtin_amdgcn_mfma_f32_16x16x32_f16(__builtin_bit_cast(f16x8, Gl), __builtin_bit_cast(f16x8, Fh[ks]), a_, 0, 0, 0); \
      a_ = __builtin_amdgcn_mfma_f32_16x16x32_f16(__builtin_bit_cast(f16x8, Gh), __builtin_bit_cast(f16x8, Fl[ks]), a_, 0, 0, 0); \
      a_ = __builtin_amdgcn_mfma_f32_16x16x32_f16(__builtin_bit_cast(f16x8, Gh), __builtin_bit_cast(f16x8, Fh[ks]), a_, 0, 0, 0); \
    }                                                                                              \
    acc[ti] = a_; __builtin_amdgcn_wave_barrier(); }
  cplx g[8];
  g[0] = C;
#pragma unroll
  for (int be = 1; be < 8; be++) g[be] = cmul(g[be - 1], e1);
#pragma unroll
  for (int be = 0; be < 8; be++) PUT_ROW(be, g[be].r, -g[be].i);
#pragma unroll
  for (int be = 0; be < 8; be++) { g[be] = cmul(g[be], E); PUT_ROW(8 + be, g[be].r, -g[be].i); }
  TILE_MMA(0);
#pragma unroll
  for (int al = 0; al < 2; al++)
#pragma unroll
    for (int be = 0; be < 8; be += 2) { g[be] = cmul(g[be], E); PUT_ROW(4 * al + (be >> 1), g[be].r, -g[be].i); }
#pragma unroll
  for (int al = 0; al < 4; al++)
#pragma unroll
    for (int be = 0; be < 8; be += 4) { g[be] = cmul(g[be], E); PUT_ROW(8 + 2 * al + (be >> 2), g[be].r, -g[be].i); }
  TILE_MMA(1);
#pragma unroll
  for (int al = 0; al < 9; al++) { g[0] = cmul(g[0], E); PUT_ROW(al, g[0].r, -g[0].i); }
  TILE_MMA(2);
  {
    float* rw = (float*)tab;
#pragma unroll
    for (int rg = 0; rg < 4; rg++) {
      const int row = 4 * q + rg;
      rw[128 * (row >> 3) + (row & 7) + 8 * c16] = __builtin_ldexpf(acc[0][rg], ec - 14);
      int i1 = (row < 8) ? 256 + 64 * (row >> 2) + (row & 3) + 4 * c16 : 384 + 32 * ((row - 8) >> 1) + (row & 1) + 2 * c16;
      rw[i1] = __builtin_ldexpf(acc[1][rg], ec - 14);
      if (row < 9) rw[512 + 16 * row + c16] = __builtin_ldexpf(acc[2][rg], ec - 14);
    }
  }
  __builtin_amdgcn_sched_barrier(0);
  if (wv >= 4) {
#pragma unroll
    for (int c = 0; c < 4; c++) {
      u32x4 o;
#pragma unroll
      for (int p = 0; p < 4; p++)
        o[p] = __builtin_amdgcn_perm(vv[2 * p + 1][c >> 1], vv[2 * p][c >> 1], (c & 1) ? 0x07060302u : 0x05040100u);
      *(u32x4*)(uT + (size_t)(c0 + c) * L_SEQ + l0) = o;
    }
  } else {
#pragma unroll
    for (int c = 0; c < 4; c++) {
      u32 s01 = sw[c >> 1][(c & 1) * 2], s23 = sw[c >> 1][(c & 1) * 2 + 1];
      float w0 = bflo(s01), w1 = bfhi(s01), w2 = bflo(s23), w3 = bfhi(s23);
      float kf[11];
#pragma unroll
      for (int i = 0; i < 11; i++) kf[i] = (c & 1) ? bfhi(kk[i][c >> 1]) : bflo(kk[i][c >> 1]);
      float res[8];
#pragma unroll
      for (int i = 0; i < 8; i += 2) {
        u32 p3 = pk_bf16(kf[i] * w3, kf[i + 1] * w3);
        u32 p2 = pk_bf16(kf[i + 1] * w2, kf[i + 2] * w2);
        u32 p1 = pk_bf16(kf[i + 2] * w1, kf[i + 3] * w1);
        u32 p0 = pk_bf16(kf[i + 3] * w0, kf[i + 4] * w0);
        u32 a = pk_bf16(bflo(p3) + bflo(p2), bfhi(p3) + bfhi(p2));
        a = pk_bf16(bflo(a) + bflo(p1), bfhi(a) + bfhi(p1));
        a = pk_bf16(bflo(a) + bflo(p0), bfhi(a) + bfhi(p0));
        float v0 = (c & 1) ? bfhi(vv[i][c >> 1]) : bflo(vv[i][c >> 1]);
        float v1 = (c & 1) ? bfhi(vv[i + 1][c >> 1]) : bflo(vv[i + 1][c >> 1]);
        res[i] = bflo(a) * v0;
        res[i + 1] = bfhi(a) * v1;
      }
      u32x4 o;
#pragma unroll
      for (int p = 0; p < 4; p++) o[p] = pk_bf16(res[7 - 2 * p], res[6 - 2 * p]);
      *(u32x4*)(kvT + (size_t)(c0 + c) * L_SEQ + l0) = o;
    }
  }
  __syncthreads();
  const int tc = t & 127;
  const float* r0 = (const float*)(smem + (wv & 6) * GW_BYTES);
  const float* r1 = (const float*)(smem + ((wv & 6) + 1) * GW_BYTES);
  float kvl[6];
  float m = 0.f;
#pragma unroll
  for (int i = 0; i < 5; i++) { kvl[i] = r0[tc + 128 * i] + r1[tc + 128 * i]; m = fmaxf(m, fabsf(kvl[i])); }
  kvl[5] = r0[640] + r1[640];
  m = fmaxf(m, fabsf(kvl[5]));
#pragma unroll
  for (int o = 32; o > 0; o >>= 1) m = fmaxf(m, __shfl_xor(m, o));
  float* wmax = (float*)(smem + 8 * GW_BYTES);
  if (lane == 0) wmax[wv] = m;
  __syncthreads();
  m = fmaxf(wmax[wv & 6], wmax[(wv & 6) + 1]);
  int e = 0;
  if (m > 0.f && m < 3.0e38f) e = __builtin_amdgcn_frexp_expf(m) - 15;
  u16* kc = (u16*)(smem + (wv & 6) * GW_BYTES);
#pragma unroll
  for (int i = 0; i < 5; i++) kc[tc + 128 * i] = __builtin_bit_cast(u16, (_Float16)__builtin_ldexpf(kvl[i], -e));
  if (tc == 0) { kc[640] = __builtin_bit_cast(u16, (_Float16)__builtin_ldexpf(kvl[5], -e)); hexp[d] = e; }
  __syncthreads();
#pragma unroll
  for (int hb = 0; hb < 2; hb++) {
    u32x4 o;
#pragma unroll
    for (int p = 0; p < 4; p++) {
      const int l = 1024 * hb + 8 * tc + 2 * p;
      u32 pb = pk_bf16((float)l, (float)(l + 1));
      int i0 = (l < 256) ? l : (int)(pb & 0xffffu) - 17024;
      int i1 = (l < 256) ? l + 1 : (int)(pb >> 16) - 17024;
      o[p] = (u32)kc[i0] | ((u32)kc[i1] << 16);
    }
    *(u32x4*)(h16 + (size_t)d * L_SEQ + 1024 * hb + 8 * tc) = o;
  }
}

#define XS_BYTES 4608
#define HC_BYTES 2368
#define HS_BYTES (4 * HC_BYTES)
#define LDS_BYTES (8 * XS_BYTES + 8 * HS_BYTES)

template <bool IS_A>
__device__ __forceinline__ f32x4 mma(u32x4 a, u32x4 b, f32x4 c) {
  if (IS_A) return __builtin_amdgcn_mfma_f32_16x16x32_f16(__builtin_bit_cast(f16x8, a), __builtin_bit_cast(f16x8, b), c, 0, 0, 0);
  else return __builtin_amdgcn_mfma_f32_16x16x32_bf16(__builtin_bit_cast(bf16x8, a), __builtin_bit_cast(bf16x8, b), c, 0, 0, 0);
}

__device__ __forceinline__ u32 lds_off(const char* p) {
  return (u32)(uintptr_t)(const __attribute__((address_space(3))) char*)p;
}
#define DSR64(dst, addr, off) asm volatile("ds_read_b64 %0, %1 offset:%2" : "=v"(dst) : "v"(addr), "i"(off) : "memory")
#define DSR128(dst, addr, off) asm volatile("ds_read_b128 %0, %1 offset:%2" : "=v"(dst) : "v"(addr), "i"(off) : "memory")
#define LOAD_FRAGS(it, A, B)                                                   \
  _Pragma("unroll") for (int a_ = 0; a_ < 8; a_++) {                           \
    u32x2 lo_, hi_2;                                                           \
    DSR64(lo_, abase, 64 * ((it) & 3) + 288 * a_);                             \
    DSR64(hi_2, abase, 64 * ((it) & 3) + 288 * a_ + 8);                        \
    A[a_] = (u32x4){lo_[0], lo_[1], hi_2[0], hi_2[1]};                         \
    DSR128(B[a_], bbase, 448 + 512 * a_ - 64 * (it));                          \
  }                                                                            \
  __builtin_amdgcn_sched_barrier(0);
#define WAIT_FRAGS(A, B)                                                                              \
  __builtin_amdgcn_sched_barrier(0);                                                                  \
  asm volatile("s_waitcnt lgkmcnt(0)"                                                                 \
               : "+v"(A[0]), "+v"(A[1]), "+v"(A[2]), "+v"(A[3]), "+v"(A[4]), "+v"(A[5]), "+v"(A[6]), "+v"(A[7]), \
                 "+v"(B[0]), "+v"(B[1]), "+v"(B[2]), "+v"(B[3]), "+v"(B[4]), "+v"(B[5]), "+v"(B[6]), "+v"(B[7])  \
               :: "memory")

template <bool IS_A>
__device__ __forceinline__ void conv_body(char* smem, int g, const u16* __restrict__ xT, const u16* __restrict__ hT,
                                          const int* __restrict__ hexp, const u16* __restrict__ dvec,
                                          const u16* __restrict__ qin, u16* __restrict__ out) {
  const int t = threadIdx.x, lane = t & 63;
  const int w = __builtin_amdgcn_readfirstlane(t >> 6);
  const int ch = (g & 127) * 8 + w;
  char* xs = smem + w * XS_BYTES;
  char* hs = smem + 8 * XS_BYTES + w * HS_BYTES;
  const u16* xrow = xT + (size_t)ch * L_SEQ;
  const u16* hrow = hT + (size_t)ch * L_SEQ;
  u32x4 xr[4];
#pragma unroll
  for (int j = 0; j < 4; j++) xr[j] = *(const u32x4*)(xrow + 8 * (64 * j + lane));
  u32x4 hd[6];
  u32x2 he[6];
  const int ha = lane / 6, hi_ = lane - 6 * ha;
  const bool hact = lane < 48;
#pragma unroll
  for (int s = 0; s < 6; s++) {
    int hc = 32 * ha + 16 * (s / 3) + 6 * (s % 3) + hi_ - 2;
    u32x4 z4 = {0, 0, 0, 0};
    u32x2 z2 = {0, 0};
    hd[s] = (hact && hc >= 0 && hc < 256) ? *(const u32x4*)(hrow + 8 * hc) : z4;
    he[s] = (hact && hc + 1 >= 0 && hc + 1 < 256) ? *(const u32x2*)(hrow + 8 * hc + 8) : z2;
  }
  int ex = 0;
  if (lane < 30) *(u32x4*)(xs + 16 * lane) = (u32x4){0, 0, 0, 0};
  if (IS_A) {
    u32 mx = 0;
#pragma unroll
    for (int j = 0; j < 4; j++)
#pragma unroll
      for (int p = 0; p < 4; p++) {
        u32 dd = xr[j][p];
        mx = max(mx, max(dd & 0x7fffu, (dd >> 16) & 0x7fffu));
      }
#pragma unroll
    for (int o = 32; o > 0; o >>= 1) mx = max(mx, (u32)__shfl_xor((int)mx, o));
    ex = (int)(mx >> 7) - 127 - 14;
#pragma unroll
    for (int j = 0; j < 4; j++) {
      u32x4 o;
#pragma unroll
      for (int p = 0; p < 4; p++) {
        u32 dd = xr[j][3 - p];
        float lo = __builtin_ldexpf(bfhi(dd), -ex), hi = __builtin_ldexpf(bflo(dd), -ex);
        o[p] = __builtin_bit_cast(u32, __builtin_amdgcn_cvt_pkrtz(lo, hi));
      }
      *(u32x4*)(xs + 480 + 16 * (64 * j + lane)) = o;
    }
  } else {
#pragma unroll
    for (int j = 0; j < 4; j++) *(u32x4*)(xs + 480 + 16 * (64 * j + lane)) = xr[j];
  }
  const int r = lane & 15, q = lane >> 4;
  f32x4 acc[8];
#pragma unroll
  for (int i = 0; i < 8; i++) acc[i] = (f32x4){0.f, 0.f, 0.f, 0.f};
  const int lam = 25 + r - 8 * q;
  const u32 abase = lds_off(hs) + (lam & 3) * HC_BYTES + 2 * (lam & ~3);
  const u32 bbase = lds_off(xs) + 32 * r + 16 * q;
  char* hw = hs + ha * 288 + hi_ * 16;
  auto stage_h = [&](int hf) {
    if (hact) {
#pragma unroll
      for (int j = 0; j < 3; j++) {
        u32x4 d4 = hd[3 * hf + j];
        u32x2 e2 = he[3 * hf + j];
        u32 a01 = __builtin_amdgcn_alignbit(d4[1], d4[0], 16);
        u32 a12 = __builtin_amdgcn_alignbit(d4[2], d4[1], 16);
        u32 a23 = __builtin_amdgcn_alignbit(d4[3], d4[2], 16);
        u32 a3e = __builtin_amdgcn_alignbit(e2[0], d4[3], 16);
        u32 aee = __builtin_amdgcn_alignbit(e2[1], e2[0], 16);
        *(u32x4*)(hw + 96 * j) = d4;
        *(u32x4*)(hw + 96 * j + HC_BYTES) = (u32x4){a01, a12, a23, a3e};
        *(u32x4*)(hw + 96 * j + 2 * HC_BYTES) = (u32x4){d4[1], d4[2], d4[3], e2[0]};
        *(u32x4*)(hw + 96 * j + 3 * HC_BYTES) = (u32x4){a12, a23, a3e, aee};
      }
    }
  };
  u32x4 A0[8], B0[8], A1[8], B1[8];
  stage_h(0);
  LOAD_FRAGS(0, A0, B0);
  u32x4 qv[4];
  u32x2 uv[8];
  if (IS_A) {
#pragma unroll
    for (int ct = 0; ct < 8; ct++) uv[ct] = *(const u32x2*)(xrow + 256 * ct + 16 * r + 4 * q);
  } else {
#pragma unroll
    for (int i = 0; i < 4; i++) qv[i] = *(const u32x4*)(qin + (size_t)(4 * t + i) * D_CH + (g & 127) * 8);
  }
#define MMA_ALL(A, B)                                                                   \
  _Pragma("unroll") for (int a = 0; a < 8; a++)                                         \
  _Pragma("unroll") for (int e = 0; e + a < 8; e++) acc[a + e] = mma<IS_A>(A[a], B[e], acc[a + e]); \
  asm volatile("" : "+v"(acc[0]), "+v"(acc[1]), "+v"(acc[2]), "+v"(acc[3]), "+v"(acc[4]), "+v"(acc[5]), "+v"(acc[6]), "+v"(acc[7]));
  WAIT_FRAGS(A0, B0); LOAD_FRAGS(1, A1, B1); MMA_ALL(A0, B0);
  WAIT_FRAGS(A1, B1); LOAD_FRAGS(2, A0, B0); MMA_ALL(A1, B1);
  WAIT_FRAGS(A0, B0); LOAD_FRAGS(3, A1, B1);
  stage_h(1);
  MMA_ALL(A0, B0);
  WAIT_FRAGS(A1, B1); LOAD_FRAGS(4, A0, B0); MMA_ALL(A1, B1);
  WAIT_FRAGS(A0, B0); LOAD_FRAGS(5, A1, B1); MMA_ALL(A0, B0);
  WAIT_FRAGS(A1, B1); LOAD_FRAGS(6, A0, B0); MMA_ALL(A1, B1);
  WAIT_FRAGS(A0, B0); LOAD_FRAGS(7, A1, B1); MMA_ALL(A0, B0);
  WAIT_FRAGS(A1, B1); MMA_ALL(A1, B1);
  const float dsk = bf2f(dvec[ch]);
  int esc = 0;
  if (IS_A) esc = hexp[ch] + ex;
  char* ys = hs;
#pragma unroll
  for (int ct = 0; ct < 8; ct++) {
    const int l = 256 * ct + 16 * r + 4 * q;
    float x0, x1, x2, x3;
    if (IS_A) {
      u32x2 xv = uv[ct];
      x0 = bflo(xv[0]); x1 = bfhi(xv[0]); x2 = bflo(xv[1]); x3 = bfhi(xv[1]);
    } else {
      u32x2 xv = *(const u32x2*)(xs + 480 + 2 * ((l & ~7) + 4 - (l & 4)));
      x3 = bflo(xv[0]); x2 = bfhi(xv[0]); x1 = bflo(xv[1]); x0 = bfhi(xv[1]);
    }
    float y0 = acc[ct][0], y1 = acc[ct][1], y2 = acc[ct][2], y3 = acc[ct][3];
    if (IS_A) {
      y0 = __builtin_ldexpf(y0, esc); y1 = __builtin_ldexpf(y1, esc);
      y2 = __builtin_ldexpf(y2, esc); y3 = __builtin_ldexpf(y3, esc);
    }
    u32 yb01 = pk_bf16(y0, y1), yb23 = pk_bf16(y2, y3);
    u32 s01 = pk_bf16(x0 * dsk, x1 * dsk), s23 = pk_bf16(x2 * dsk, x3 * dsk);
    u32x2 o;
    o[0] = pk_bf16(bflo(yb01) + bflo(s01), bfhi(yb01) + bfhi(s01));
    o[1] = pk_bf16(bflo(yb23) + bflo(s23), bfhi(yb23) + bfhi(s23));
    *(u32x2*)(ys + 2 * l) = o;
  }
  __syncthreads();
  {
    u32x2 yv[8];
#pragma unroll
    for (int c = 0; c < 8; c++) yv[c] = *(const u32x2*)(smem + 8 * XS_BYTES + c * HS_BYTES + 8 * t);
#pragma unroll
    for (int i = 0; i < 4; i++) {
      const int l = 4 * t + i;
      u32x4 o;
#pragma unroll
      for (int p = 0; p < 4; p++) {
        u32 a = yv[2 * p][i >> 1], bq = yv[2 * p + 1][i >> 1];
        o[p] = (i & 1) ? ((a >> 16) | (bq & 0xffff0000u)) : ((a & 0xffffu) | (bq << 16));
      }
      if (!IS_A) {
#pragma unroll
        for (int p = 0; p < 4; p++) o[p] = pk_bf16(bflo(o[p]) * bflo(qv[i][p]), bfhi(o[p]) * bfhi(qv[i][p]));
      }
      *(u32x4*)(out + (size_t)l * (2 * D_CH) + g * 8) = o;
    }
  }
}

__global__ __launch_bounds__(512) void conv_kernel(const u16* __restrict__ uT, const u16* __restrict__ kvT,
                                                   const u16* __restrict__ h16, const int* __restrict__ hexp,
                                                   const u16* __restrict__ dskip, const u16* __restrict__ ssmk,
                                                   const u16* __restrict__ ssmd, const u16* __restrict__ qin,
                                                   u16* __restrict__ out) {
  extern __shared__ __attribute__((aligned(16))) char smem[];
  const int g = xcd_group(blockIdx.x);
  if (g < 128) conv_body<true>(smem, g, uT, h16, hexp, dskip, qin, out);
  else conv_body<false>(smem, g, kvT, ssmk, hexp, ssmd, qin, out);
}

extern "C" void kernel_launch(void* const* d_in, const int* in_sizes, int n_in, void* d_out, int out_size,
                              void* d_ws, size_t ws_size, hipStream_t stream) {
  const u16* u = (const u16*)d_in[0];
  const u16* lar = (const u16*)d_in[1];
  const u16* aim = (const u16*)d_in[2];
  const u16* cre = (const u16*)d_in[3];
  const u16* cim = (const u16*)d_in[4];
  const u16* ldt = (const u16*)d_in[5];
  const u16* dsk = (const u16*)d_in[6];
  const u16* q = (const u16*)d_in[7];
  const u16* k = (const u16*)d_in[8];
  const u16* v = (const u16*)d_in[9];
  const u16* shk = (const u16*)d_in[10];
  const u16* ssmk = (const u16*)d_in[11];
  const u16* ssmd = (const u16*)d_in[12];
  char* ws = (char*)d_ws;
  u16* uT = (u16*)ws;
  u16* kvT = (u16*)(ws + (size_t)4 * 1024 * 1024);
  u16* h16 = (u16*)(ws + (size_t)8 * 1024 * 1024);
  int* hexp = (int*)(ws + (size_t)12 * 1024 * 1024);
  hipFuncSetAttribute((const void*)conv_kernel, hipFuncAttributeMaxDynamicSharedMemorySize, LDS_BYTES);
  hipFuncSetAttribute((const void*)prep_kernel, hipFuncAttributeMaxDynamicSharedMemorySize, PREP_LDS);
  prep_kernel<<<256, 512, PREP_LDS, stream>>>(u, lar, aim, cre, cim, ldt, k, v, shk, uT, kvT, h16, hexp);
  conv_kernel<<<256, 512, LDS_BYTES, stream>>>(uT, kvT, h16, hexp, dsk, ssmk, ssmd, q, (u16*)d_out);
}
```

```cpp
#include <hip/hip_runtime.h>
#include <hip/hip_bf16.h>
#include <stdint.h>

typedef unsigned int u32;
typedef unsigned short u16;
typedef u32 u32x4 __attribute__((ext_vector_type(4)));
typedef u32 u32x2 __attribute__((ext_vector_type(2)));
typedef float f32x4 __attribute__((ext_vector_type(4)));
typedef __bf16 bf16x8 __attribute__((ext_vector_type(8)));
typedef _Float16 f16x8 __attribute__((ext_vector_type(8)));

#define L_SEQ 2048
#define D_CH 1024
#define N_MODE 128

__device__ __forceinline__ float bflo(u32 d) { return __uint_as_float(d << 16); }
__device__ __forceinline__ float bfhi(u32 d) { return __uint_as_float(d & 0xffff0000u); }
__device__ __forceinline__ float bf2f(u16 h) { return __uint_as_float(((u32)h) << 16); }
__device__ __forceinline__ u32 pk_bf16(float lo, float hi) {
  typedef __bf16 bf16x2 __attribute__((ext_vector_type(2)));
  typedef float f32x2 __attribute__((ext_vector_type(2)));
  f32x2 v = {lo, hi};
  bf16x2 r = __builtin_convertvector(v, bf16x2);
  return __builtin_bit_cast(u32, r);
}
__device__ __forceinline__ float rbf(float x) { return bflo(pk_bf16(x, 0.f)); }

__device__ __forceinline__ int xcd_group(int b) {
  int xcd = b & 7, idx = b >> 3;
  return ((xcd + 8 * (idx >> 3)) << 3) | (idx & 7);
}

struct cplx { float r, i; };
__device__ __forceinline__ cplx csq(cplx a) { cplx o; o.r = a.r * a.r - a.i * a.i; o.i = (a.r + a.r) * a.i; return o; }
__device__ __forceinline__ cplx cmul(cplx a, cplx b) { cplx o; o.r = a.r * b.r - a.i * b.i; o.i = a.r * b.i + a.i * b.r; return o; }
__device__ __forceinline__ cplx cexp_pos(float ar, float ai, float pos) {
  const float LOG2E = 1.44269504088896340736f;
  const float I2PI_HI = 0.15915494309189533577f;
  const float I2PI_LO = 6.4206383e-9f;
  float xr = ar * pos;
  float xi = ai * pos;
  float e = __builtin_amdgcn_exp2f(xr * LOG2E);
  float r = xi * I2PI_HI;
  float err = __builtin_fmaf(xi, I2PI_HI, -r);
  float f = __builtin_amdgcn_fractf(r);
  f += __builtin_fmaf(xi, I2PI_LO, err);
  cplx o;
  o.r = e * __builtin_amdgcn_cosf(f);
  o.i = e * __builtin_amdgcn_sinf(f);
  return o;
}
__device__ __forceinline__ void split_h(float a, float b, u32& hi, u32& lo) {
  hi = __builtin_bit_cast(u32, __builtin_amdgcn_cvt_pkrtz(a, b));
  u32 l;
  asm("v_fma_mixlo_f16 %0, %1, -1.0, %2 op_sel_hi:[1,0,0]" : "=v"(l) : "v"(hi), "v"(a));
  asm("v_fma_mixhi_f16 %0, %1, -1.0, %2 op_sel:[1,0,0] op_sel_hi:[1,0,0]" : "+v"(l) : "v"(hi), "v"(b));
  lo = l;
}

#define GT_ROW 288
#define GT_TAB (16 * GT_ROW)
#define GW_BYTES (2 * GT_TAB)
#define PREP_LDS (8 * GW_BYTES + 12288)

__global__ __launch_bounds__(512) void prep_kernel(
    const u16* __restrict__ u, const u16* __restrict__ lar, const u16* __restrict__ aim,
    const u16* __restrict__ cre, const u16* __restrict__ cim, const u16* __restrict__ ldt,
    const u16* __restrict__ kin, const u16* __restrict__ vin, const u16* __restrict__ shk,
    u16* __restrict__ uT, u16* __restrict__ kvT, u16* __restrict__ h16, int* __restrict__ hexp) {
  extern __shared__ __attribute__((aligned(16))) char smem[];
  const int b = blockIdx.x;
  const int t = threadIdx.x;
  const int lane = t & 63;
  const int wv = __builtin_amdgcn_readfirstlane(t >> 6);
  const int xcd = b & 7, idx = b >> 3;
  const int cb = xcd + 8 * (idx & 1);
  const int rblk = idx >> 1;
  const int d = cb * 64 + rblk * 4 + (wv >> 1);
  const int n = (wv & 1) * 64 + lane;
  const u16 p_ldt = ldt[d], p_lar = lar[d * N_MODE + n], p_aim = aim[d * N_MODE + n];
  const u16 p_cre = cre[d * N_MODE + n], p_cim = cim[d * N_MODE + n];
  __builtin_amdgcn_sched_barrier(0);
  const int l0 = rblk * 128 + (wv & 3) * 32 + (lane >> 4) * 8;
  const int c0 = cb * 64 + (lane & 15) * 4;
  u32x2 kk[11], vv[8];
  u32x4 sw[2];
  {
    const __amdgpu_buffer_rsrc_t rk = __builtin_amdgcn_make_buffer_rsrc((void*)kin, 0, (wv < 4) ? L_SEQ * D_CH * 2 : 0, 0x00020000);
    const __amdgpu_buffer_rsrc_t rv = __builtin_amdgcn_make_buffer_rsrc((void*)((wv < 4) ? vin : u), 0, L_SEQ * D_CH * 2, 0x00020000);
    const int vo = l0 * (D_CH * 2) + c0 * 2;
#pragma unroll
    for (int i = 0; i < 8; i++) vv[i] = __builtin_amdgcn_raw_buffer_load_b64(rv, vo + i * (D_CH * 2), 0, 0);
#pragma unroll
    for (int i = 0; i < 11; i++) {
      int o = vo + (i - 3) * (D_CH * 2);
      if (i < 3) o = (l0 > 0) ? o : (int)0x40000000;
      kk[i] = __builtin_amdgcn_raw_buffer_load_b64(rk, o, 0, 0);
    }
#pragma unroll
    for (int i = 0; i < 2; i++) sw[i] = *(const u32x4*)(shk + (size_t)c0 * 4 + i * 8);
  }
  __builtin_amdgcn_sched_barrier(0);
  char* tab = smem + wv * GW_BYTES;
  float ar, ai;
  cplx C;
  int ec;
  {
    float dt = rbf(expf(bf2f(p_ldt)));
    float Ar = -rbf(expf(bf2f(p_lar)));
    float Ai = bf2f(p_aim);
    ar = Ar * dt;
    ai = Ai * dt;
    C.r = 2.0f * (bf2f(p_cre) * dt);
    C.i = 2.0f * (bf2f(p_cim) * dt);
    float m = fmaxf(fabsf(C.r), fabsf(C.i));
#pragma unroll
    for (int o = 32; o > 0; o >>= 1) m = fmaxf(m, __shfl_xor(m, o));
    ec = (m > 0.f && m < 3.0e38f) ? __builtin_amdgcn_frexp_expf(m) - 14 : 0;
    C.r = __builtin_ldexpf(C.r, -ec);
    C.i = __builtin_ldexpf(C.i, -ec);
  }
  const cplx e1 = cexp_pos(ar, ai, 1.0f);
  const cplx f8 = cexp_pos(ar, ai, 8.0f);
  const cplx E = cexp_pos(ar, ai, 128.0f);
  char* wp = tab + 4 * lane;
  const int c16 = lane & 15, q = lane >> 4;
  const char* rp = tab + c16 * GT_ROW + 16 * q;
#define PUT_ROW(row, re, im) { u32 hi_, lo_; split_h((re), (im), hi_, lo_); \
    *(u32*)(wp + (row) * GT_ROW) = hi_; *(u32*)(wp + (row) * GT_ROW + GT_TAB) = lo_; }
  {
    cplx F; F.r = 16384.0f; F.i = 0.f;
#pragma unroll
    for (int w = 0; w < 16; w++) {
      PUT_ROW(w, F.r, F.i);
      if (w < 15) F = cmul(F, f8);
    }
  }
  __builtin_amdgcn_wave_barrier();
  u32x4 Fh[4], Fl[4];
#pragma unroll
  for (int ks = 0; ks < 4; ks++) {
    Fh[ks] = *(const u32x4*)(rp + 64 * ks);
    Fl[ks] = *(const u32x4*)(rp + 64 * ks + GT_TAB);
  }
  __builtin_amdgcn_wave_barrier();
  f32x4 acc[3];
#define TILE_MMA(ti) { __builtin_amdgcn_wave_barrier();                                              \
    f32x4 a_ = {0.f, 0.f, 0.f, 0.f};                                                               \
    _Pragma("unroll") for (int ks = 0; ks < 4; ks++) {                                             \
      u32x4 Gh = *(const u32x4*)(rp + 64 * ks), Gl = *(const u32x4*)(rp + 64 * ks + GT_TAB);       \
      a_ = __builtin_amdgcn_mfma_f32_16x16x32_f16(__builtin_bit_cast(f16x8, Gl), __builtin_bit_cast(f16x8, Fh[ks]), a_, 0, 0, 0); \
      a_ = __builtin_amdgcn_mfma_f32_16x16x32_f16(__builtin_bit_cast(f16x8, Gh), __builtin_bit_cast(f16x8, Fl[ks]), a_, 0, 0, 0); \
      a_ = __builtin_amdgcn_mfma_f32_16x16x32_f16(__builtin_bit_cast(f16x8, Gh), __builtin_bit_cast(f16x8, Fh[ks]), a_, 0, 0, 0); \
    }                                                                                              \
    acc[ti] = a_; __builtin_amdgcn_wave_barrier(); }
  cplx g[8];
  g[0] = C;
#pragma unroll
  for (int be = 1; be < 8; be++) g[be] = cmul(g[be - 1], e1);
#pragma unroll
  for (int be = 0; be < 8; be++) PUT_ROW(be, g[be].r, -g[be].i);
#pragma unroll
  for (int be = 0; be < 8; be++) { g[be] = cmul(g[be], E); PUT_ROW(8 + be, g[be].r, -g[be].i); }
  TILE_MMA(0);
#pragma unroll
  for (int al = 0; al < 2; al++)
#pragma unroll
    for (int be = 0; be < 8; be += 2) { g[be] = cmul(g[be], E); PUT_ROW(4 * al + (be >> 1), g[be].r, -g[be].i); }
#pragma unroll
  for (int al = 0; al < 4; al++)
#pragma unroll
    for (int be = 0; be < 8; be += 4) { g[be] = cmul(g[be], E); PUT_ROW(8 + 2 * al + (be >> 2), g[be].r, -g[be].i); }
  TILE_MMA(1);
#pragma unroll
  for (int al = 0; al < 9; al++) { g[0] = cmul(g[0], E); PUT_ROW(al, g[0].r, -g[0].i); }
  TILE_MMA(2);
  {
    float* rw = (float*)tab;
#pragma unroll
    for (int rg = 0; rg < 4; rg++) {
      const int row = 4 * q + rg;
      rw[128 * (row >> 3) + (row & 7) + 8 * c16] = __builtin_ldexpf(acc[0][rg], ec - 14);
      int i1 = (row < 8) ? 256 + 64 * (row >> 2) + (row & 3) + 4 * c16 : 384 + 32 * ((row - 8) >> 1) + (row & 1) + 2 * c16;
      rw[i1] = __builtin_ldexpf(acc[1][rg], ec - 14);
      if (row < 9) rw[512 + 16 * row + c16] = __builtin_ldexpf(acc[2][rg], ec - 14);
    }
  }
  __builtin_amdgcn_sched_barrier(0);
  if (wv >= 4) {
#pragma unroll
    for (int c = 0; c < 4; c++) {
      u32x4 o;
#pragma unroll
      for (int p = 0; p < 4; p++)
        o[p] = __builtin_amdgcn_perm(vv[2 * p + 1][c >> 1], vv[2 * p][c >> 1], (c & 1) ? 0x07060302u : 0x05040100u);
      *(u32x4*)(uT + (size_t)(c0 + c) * L_SEQ + l0) = o;
    }
  } else {
#pragma unroll
    for (int c = 0; c < 4; c++) {
      u32 s01 = sw[c >> 1][(c & 1) * 2], s23 = sw[c >> 1][(c & 1) * 2 + 1];
      float w0 = bflo(s01), w1 = bfhi(s01), w2 = bflo(s23), w3 = bfhi(s23);
      float kf[11];
#pragma unroll
      for (int i = 0; i < 11; i++) kf[i] = (c & 1) ? bfhi(kk[i][c >> 1]) : bflo(kk[i][c >> 1]);
      float res[8];
#pragma unroll
      for (int i = 0; i < 8; i += 2) {
        u32 p3 = pk_bf16(kf[i] * w3, kf[i + 1] * w3);
        u32 p2 = pk_bf16(kf[i + 1] * w2, kf[i + 2] * w2);
        u32 p1 = pk_bf16(kf[i + 2] * w1, kf[i + 3] * w1);
        u32 p0 = pk_bf16(kf[i + 3] * w0, kf[i + 4] * w0);
        u32 a = pk_bf16(bflo(p3) + bflo(p2), bfhi(p3) + bfhi(p2));
        a = pk_bf16(bflo(a) + bflo(p1), bfhi(a) + bfhi(p1));
        a = pk_bf16(bflo(a) + bflo(p0), bfhi(a) + bfhi(p0));
        float v0 = (c & 1) ? bfhi(vv[i][c >> 1]) : bflo(vv[i][c >> 1]);
        float v1 = (c & 1) ? bfhi(vv[i + 1][c >> 1]) : bflo(vv[i + 1][c >> 1]);
        res[i] = bflo(a) * v0;
        res[i + 1] = bfhi(a) * v1;
      }
      u32x4 o;
#pragma unroll
      for (int p = 0; p < 4; p++) o[p] = pk_bf16(res[7 - 2 * p], res[6 - 2 * p]);
      *(u32x4*)(kvT + (size_t)(c0 + c) * L_SEQ + l0) = o;
    }
  }
  __syncthreads();
  const int tc = t & 127;
  const float* r0 = (const float*)(smem + (wv & 6) * GW_BYTES);
  const float* r1 = (const float*)(smem + ((wv & 6) + 1) * GW_BYTES);
  float kvl[6];
  float m = 0.f;
#pragma unroll
  for (int i = 0; i < 5; i++) { kvl[i] = r0[tc + 128 * i] + r1[tc + 128 * i]; m = fmaxf(m, fabsf(kvl[i])); }
  kvl[5] = r0[640] + r1[640];
  m = fmaxf(m, fabsf(kvl[5]));
#pragma unroll
  for (int o = 32; o > 0; o >>= 1) m = fmaxf(m, __shfl_xor(m, o));
  float* wmax = (float*)(smem + 8 * GW_BYTES);
  if (lane == 0) wmax[wv] = m;
  __syncthreads();
  m = fmaxf(wmax[wv & 6], wmax[(wv & 6) + 1]);
  int e = 0;
  if (m > 0.f && m < 3.0e38f) e = __builtin_amdgcn_frexp_expf(m) - 15;
  u16* kc = (u16*)(smem + (wv & 6) * GW_BYTES);
#pragma unroll
  for (int i = 0; i < 5; i++) kc[tc + 128 * i] = __builtin_bit_cast(u16, (_Float16)__builtin_ldexpf(kvl[i], -e));
  if (tc == 0) { kc[640] = __builtin_bit_cast(u16, (_Float16)__builtin_ldexpf(kvl[5], -e)); hexp[d] = e; }
  __syncthreads();
#pragma unroll
  for (int hb = 0; hb < 2; hb++) {
    u32x4 o;
#pragma unroll
    for (int p = 0; p < 4; p++) {
      const int l = 1024 * hb + 8 * tc + 2 * p;
      u32 pb = pk_bf16((float)l, (float)(l + 1));
      int i0 = (l < 256) ? l : (int)(pb & 0xffffu) - 17024;
      int i1 = (l < 256) ? l + 1 : (int)(pb >> 16) - 17024;
      o[p] = (u32)kc[i0] | ((u32)kc[i1] << 16);
    }
    *(u32x4*)(h16 + (size_t)d * L_SEQ + 1024 * hb + 8 * tc) = o;
  }
}

#define XS_BYTES 4608
#define HC_BYTES 2368
#define HS_BYTES (4 * HC_BYTES)
#define LDS_BYTES (8 * XS_BYTES + 8 * HS_BYTES)

template <bool IS_A>
__device__ __forceinline__ f32x4 mma(u32x4 a, u32x4 b, f32x4 c) {
  if (IS_A) return __builtin_amdgcn_mfma_f32_16x16x32_f16(__builtin_bit_cast(f16x8, a), __builtin_bit_cast(f16x8, b), c, 0, 0, 0);
  else return __builtin_amdgcn_mfma_f32_16x16x32_bf16(__builtin_bit_cast(bf16x8, a), __builtin_bit_cast(bf16x8, b), c, 0, 0, 0);
}

__device__ __forceinline__ u32 lds_off(const char* p) {
  return (u32)(uintptr_t)(const __attribute__((address_space(3))) char*)p;
}
#define DSR64(dst, addr, off) asm volatile("ds_read_b64 %0, %1 offset:%2" : "=v"(dst) : "v"(addr), "i"(off) : "memory")
#define DSR128(dst, addr, off) asm volatile("ds_read_b128 %0, %1 offset:%2" : "=v"(dst) : "v"(addr), "i"(off) : "memory")
#define LOAD_FRAGS(it, A, B)                                                   \
  _Pragma("unroll") for (int a_ = 0; a_ < 8; a_++) {                           \
    u32x2 lo_, hi_2;                                                           \
    DSR64(lo_, abase, 64 * ((it) & 3) + 288 * a_);                             \
    DSR64(hi_2, abase, 64 * ((it) & 3) + 288 * a_ + 8);                        \
    A[a_] = (u32x4){lo_[0], lo_[1], hi_2[0], hi_2[1]};                         \
    DSR128(B[a_], bbase, 448 + 512 * a_ - 64 * (it));                          \
  }                                                                            \
  __builtin_amdgcn_sched_barrier(0);
#define WAIT_FRAGS(A, B)                                                                              \
  __builtin_amdgcn_sched_barrier(0);                                                                  \
  asm volatile("s_waitcnt lgkmcnt(0)"                                                                 \
               : "+v"(A[0]), "+v"(A[1]), "+v"(A[2]), "+v"(A[3]), "+v"(A[4]), "+v"(A[5]), "+v"(A[6]), "+v"(A[7]), \
                 "+v"(B[0]), "+v"(B[1]), "+v"(B[2]), "+v"(B[3]), "+v"(B[4]), "+v"(B[5]), "+v"(B[6]), "+v"(B[7])  \
               :: "memory")

template <bool IS_A>
__device__ __forceinline__ void conv_body(char* smem, int g, const u16* __restrict__ xT, const u16* __restrict__ hT,
                                          const int* __restrict__ hexp, const u16* __restrict__ dvec,
                                          const u16* __restrict__ qin, u16* __restrict__ out) {
  const int t = threadIdx.x, lane = t & 63;
  const int w = __builtin_amdgcn_readfirstlane(t >> 6);
  const int ch = (g & 127) * 8 + w;
  char* xs = smem + w * XS_BYTES;
  char* hs = smem + 8 * XS_BYTES + w * HS_BYTES;
  const u16* xrow = xT + (size_t)ch * L_SEQ;
  const u16* hrow = hT + (size_t)ch * L_SEQ;
  u32x4 xr[4];
#pragma unroll
  for (int j = 0; j < 4; j++) xr[j] = *(const u32x4*)(xrow + 8 * (64 * j + lane));
  u32x4 hd[6];
  u32x2 he[6];
  const int ha = lane / 6, hi_ = lane - 6 * ha;
  const bool hact = lane < 48;
#pragma unroll
  for (int s = 0; s < 6; s++) {
    int hc = 32 * ha + 16 * (s / 3) + 6 * (s % 3) + hi_ - 2;
    u32x4 z4 = {0, 0, 0, 0};
    u32x2 z2 = {0, 0};
    hd[s] = (hact && hc >= 0 && hc < 256) ? *(const u32x4*)(hrow + 8 * hc) : z4;
    he[s] = (hact && hc + 1 >= 0 && hc + 1 < 256) ? *(const u32x2*)(hrow + 8 * hc + 8) : z2;
  }
  int ex = 0;
  if (lane < 30) *(u32x4*)(xs + 16 * lane) = (u32x4){0, 0, 0, 0};
  if (IS_A) {
    u32 mx = 0;
#pragma unroll
    for (int j = 0; j < 4; j++)
#pragma unroll
      for (int p = 0; p < 4; p++) {
        u32 dd = xr[j][p];
        mx = max(mx, max(dd & 0x7fffu, (dd >> 16) & 0x7fffu));
      }
#pragma unroll
    for (int o = 32; o > 0; o >>= 1) mx = max(mx, (u32)__shfl_xor((int)mx, o));
    ex = (int)(mx >> 7) - 127 - 14;
#pragma unroll
    for (int j = 0; j < 4; j++) {
      u32x4 o;
#pragma unroll
      for (int p = 0; p < 4; p++) {
        u32 dd = xr[j][3 - p];
        float lo = __builtin_ldexpf(bfhi(dd), -ex), hi = __builtin_ldexpf(bflo(dd), -ex);
        o[p] = __builtin_bit_cast(u32, __builtin_amdgcn_cvt_pkrtz(lo, hi));
      }
      *(u32x4*)(xs + 480 + 16 * (64 * j + lane)) = o;
    }
  } else {
#pragma unroll
    for (int j = 0; j < 4; j++) *(u32x4*)(xs + 480 + 16 * (64 * j + lane)) = xr[j];
  }
  const int r = lane & 15, q = lane >> 4;
  f32x4 acc[8];
#pragma unroll
  for (int i = 0; i < 8; i++) acc[i] = (f32x4){0.f, 0.f, 0.f, 0.f};
  const int lam = 25 + r - 8 * q;
  const u32 abase = lds_off(hs) + (lam & 3) * HC_BYTES + 2 * (lam & ~3);
  const u32 bbase = lds_off(xs) + 32 * r + 16 * q;
  char* hw = hs + ha * 288 + hi_ * 16;
  auto stage_h = [&](int hf) {
    if (hact) {
#pragma unroll
      for (int j = 0; j < 3; j++) {
        u32x4 d4 = hd[3 * hf + j];
        u32x2 e2 = he[3 * hf + j];
        u32 a01 = __builtin_amdgcn_alignbit(d4[1], d4[0], 16);
        u32 a12 = __builtin_amdgcn_alignbit(d4[2], d4[1], 16);
        u32 a23 = __builtin_amdgcn_alignbit(d4[3], d4[2], 16);
        u32 a3e = __builtin_amdgcn_alignbit(e2[0], d4[3], 16);
        u32 aee = __builtin_amdgcn_alignbit(e2[1], e2[0], 16);
        *(u32x4*)(hw + 96 * j) = d4;
        *(u32x4*)(hw + 96 * j + HC_BYTES) = (u32x4){a01, a12, a23, a3e};
        *(u32x4*)(hw + 96 * j + 2 * HC_BYTES) = (u32x4){d4[1], d4[2], d4[3], e2[0]};
        *(u32x4*)(hw + 96 * j + 3 * HC_BYTES) = (u32x4){a12, a23, a3e, aee};
      }
    }
  };
  u32x4 A0[8], B0[8], A1[8], B1[8];
  stage_h(0);
  LOAD_FRAGS(0, A0, B0);
  u32x4 qv[4];
  u32x2 uv[8];
  if (IS_A) {
#pragma unroll
    for (int ct = 0; ct < 8; ct++) uv[ct] = *(const u32x2*)(xrow + 256 * ct + 16 * r + 4 * q);
  } else {
#pragma unroll
    for (int i = 0; i < 4; i++) qv[i] = *(const u32x4*)(qin + (size_t)(4 * t + i) * D_CH + (g & 127) * 8);
  }
#define MMA_ALL(A, B)                                                                   \
  _Pragma("unroll") for (int a = 0; a < 8; a++)                                         \
  _Pragma("unroll") for (int e = 0; e + a < 8; e++) acc[a + e] = mma<IS_A>(A[a], B[e], acc[a + e]); \
  asm volatile("" : "+v"(acc[0]), "+v"(acc[1]), "+v"(acc[2]), "+v"(acc[3]), "+v"(acc[4]), "+v"(acc[5]), "+v"(acc[6]), "+v"(acc[7]));
  WAIT_FRAGS(A0, B0); LOAD_FRAGS(1, A1, B1); MMA_ALL(A0, B0);
  WAIT_FRAGS(A1, B1); LOAD_FRAGS(2, A0, B0); MMA_ALL(A1, B1);
  WAIT_FRAGS(A0, B0); LOAD_FRAGS(3, A1, B1);
  stage_h(1);
  MMA_ALL(A0, B0);
  WAIT_FRAGS(A1, B1); LOAD_FRAGS(4, A0, B0); MMA_ALL(A1, B1);
  WAIT_FRAGS(A0, B0); LOAD_FRAGS(5, A1, B1); MMA_ALL(A0, B0);
  WAIT_FRAGS(A1, B1); LOAD_FRAGS(6, A0, B0); MMA_ALL(A1, B1);
  WAIT_FRAGS(A0, B0); LOAD_FRAGS(7, A1, B1); MMA_ALL(A0, B0);
  WAIT_FRAGS(A1, B1); MMA_ALL(A1, B1);
  const float dsk = bf2f(dvec[ch]);
  int esc = 0;
  if (IS_A) esc = hexp[ch] + ex;
  char* ys = hs;
#pragma unroll
  for (int ct = 0; ct < 8; ct++) {
    const int l = 256 * ct + 16 * r + 4 * q;
    float x0, x1, x2, x3;
    if (IS_A) {
      u32x2 xv = uv[ct];
      x0 = bflo(xv[0]); x1 = bfhi(xv[0]); x2 = bflo(xv[1]); x3 = bfhi(xv[1]);
    } else {
      u32x2 xv = *(const u32x2*)(xs + 480 + 2 * ((l & ~7) + 4 - (l & 4)));
      x3 = bflo(xv[0]); x2 = bfhi(xv[0]); x1 = bflo(xv[1]); x0 = bfhi(xv[1]);
    }
    float y0 = acc[ct][0], y1 = acc[ct][1], y2 = acc[ct][2], y3 = acc[ct][3];
    if (IS_A) {
      y0 = __builtin_ldexpf(y0, esc); y1 = __builtin_ldexpf(y1, esc);
      y2 = __builtin_ldexpf(y2, esc); y3 = __builtin_ldexpf(y3, esc);
    }
    u32 yb01 = pk_bf16(y0, y1), yb23 = pk_bf16(y2, y3);
    u32 s01 = pk_bf16(x0 * dsk, x1 * dsk), s23 = pk_bf16(x2 * dsk, x3 * dsk);
    u32x2 o;
    o[0] = pk_bf16(bflo(yb01) + bflo(s01), bfhi(yb01) + bfhi(s01));
    o[1] = pk_bf16(bflo(yb23) + bflo(s23), bfhi(yb23) + bfhi(s23));
    *(u32x2*)(ys + 2 * l) = o;
  }
  __syncthreads();
  {
    u32x2 yv[8];
#pragma unroll
    for (int c = 0; c < 8; c++) yv[c] = *(const u32x2*)(smem + 8 * XS_BYTES + c * HS_BYTES + 8 * t);
#pragma unroll
    for (int i = 0; i < 4; i++) {
      const int l = 4 * t + i;
      u32x4 o;
#pragma unroll
      for (int p = 0; p < 4; p++) {
        u32 a = yv[2 * p][i >> 1], bq = yv[2 * p + 1][i >> 1];
        o[p] = (i & 1) ? ((a >> 16) | (bq & 0xffff0000u)) : ((a & 0xffffu) | (bq << 16));
      }
      if (!IS_A) {
#pragma unroll
        for (int p = 0; p < 4; p++) o[p] = pk_bf16(bflo(o[p]) * bflo(qv[i][p]), bfhi(o[p]) * bfhi(qv[i][p]));
      }
      *(u32x4*)(out + (size_t)l * (2 * D_CH) + g * 8) = o;
    }
  }
}

__global__ __launch_bounds__(512) void conv_kernel(const u16* __restrict__ uT, const u16* __restrict__ kvT,
                                                   const u16* __restrict__ h16, const int* __restrict__ hexp,
                                                   const u16* __restrict__ dskip, const u16* __restrict__ ssmk,
                                                   const u16* __restrict__ ssmd, const u16* __restrict__ qin,
                                                   u16* __restrict__ out) {
  extern __shared__ __attribute__((aligned(16))) char smem[];
  const int g = xcd_group(blockIdx.x);
  if (g < 128) conv_body<true>(smem, g, uT, h16, hexp, dskip, qin, out);
  else conv_body<false>(smem, g, kvT, ssmk, hexp, ssmd, qin, out);
}

extern "C" void kernel_launch(void* const* d_in, const int* in_sizes, int n_in, void* d_out, int out_size,
                              void* d_ws, size_t ws_size, hipStream_t stream) {
  const u16* u = (const u16*)d_in[0];
  const u16* lar = (const u16*)d_in[1];
  const u16* aim = (const u16*)d_in[2];
  const u16* cre = (const u16*)d_in[3];
  const u16* cim = (const u16*)d_in[4];
  const u16* ldt = (const u16*)d_in[5];
  const u16* dsk = (const u16*)d_in[6];
  const u16* q = (const u16*)d_in[7];
  const u16* k = (const u16*)d_in[8];
  const u16* v = (const u16*)d_in[9];
  const u16* shk = (const u16*)d_in[10];
  const u16* ssmk = (const u16*)d_in[11];
  const u16* ssmd = (const u16*)d_in[12];
  char* ws = (char*)d_ws;
  u16* uT = (u16*)ws;
  u16* kvT = (u16*)(ws + (size_t)4 * 1024 * 1024);
  u16* h16 = (u16*)(ws + (size_t)8 * 1024 * 1024);
  int* hexp = (int*)(ws + (size_t)12 * 1024 * 1024);
  hipFuncSetAttribute((const void*)conv_kernel, hipFuncAttributeMaxDynamicSharedMemorySize, LDS_BYTES);
  hipFuncSetAttribute((const void*)prep_kernel, hipFuncAttributeMaxDynamicSharedMemorySize, PREP_LDS);
  prep_kernel<<<256, 512, PREP_LDS, stream>>>(u, lar, aim, cre, cim, ldt, k, v, shk, uT, kvT, h16, hexp);
  conv_kernel<<<256, 512, LDS_BYTES, stream>>>(uT, kvT, h16, hexp, dsk, ssmk, ssmd, q, (u16*)d_out);
}
```

```cpp
#include <hip/hip_runtime.h>
#include <hip/hip_bf16.h>
#include <stdint.h>

typedef unsigned int u32;
typedef unsigned short u16;
typedef u32 u32x4 __attribute__((ext_vector_type(4)));
typedef u32 u32x2 __attribute__((ext_vector_type(2)));
typedef float f32x4 __attribute__((ext_vector_type(4)));
typedef __bf16 bf16x8 __attribute__((ext_vector_type(8)));
typedef _Float16 f16x8 __attribute__((ext_vector_type(8)));

#define L_SEQ 2048
#define D_CH 1024
#define N_MODE 128

__device__ __forceinline__ float bflo(u32 d) { return __uint_as_float(d << 16); }
__device__ __forceinline__ float bfhi(u32 d) { return __uint_as_float(d & 0xffff0000u); }
__device__ __forceinline__ float bf2f(u16 h) { return __uint_as_float(((u32)h) << 16); }
__device__ __forceinline__ u32 pk_bf16(float lo, float hi) {
  typedef __bf16 bf16x2 __attribute__((ext_vector_type(2)));
  typedef float f32x2 __attribute__((ext_vector_type(2)));
  f32x2 v = {lo, hi};
  bf16x2 r = __builtin_convertvector(v, bf16x2);
  return __builtin_bit_cast(u32, r);
}
__device__ __forceinline__ float rbf(float x) { return __uint_as_float(pk_bf16(0.f, x)); }

__device__ __forceinline__ u32 wave_max_u32(u32 x) {
  x = max(x, (u32)__builtin_amdgcn_update_dpp(0, (int)x, 0x111, 0xf, 0xf, true));
  x = max(x, (u32)__builtin_amdgcn_update_dpp(0, (int)x, 0x112, 0xf, 0xf, true));
  x = max(x, (u32)__builtin_amdgcn_update_dpp(0, (int)x, 0x114, 0xf, 0xf, true));
  x = max(x, (u32)__builtin_amdgcn_update_dpp(0, (int)x, 0x118, 0xf, 0xf, true));
  x = max(x, (u32)__builtin_amdgcn_update_dpp(0, (int)x, 0x142, 0xa, 0xf, true));
  x = max(x, (u32)__builtin_amdgcn_update_dpp(0, (int)x, 0x143, 0xc, 0xf, true));
  return (u32)__builtin_amdgcn_readlane((int)x, 63);
}

__global__ __launch_bounds__(512) void prep_kernel(const u16* __restrict__ u, const u16* __restrict__ kin,
                                                   const u16* __restrict__ vin, const u16* __restrict__ shk,
                                                   u16* __restrict__ uT, u16* __restrict__ kvT) {
  const int b = blockIdx.x;
  const int t = threadIdx.x;
  const int lane = t & 63;
  const int wv = t >> 6;
  const int xcd = b & 7, idx = b >> 3;
  const int c0 = (xcd + 8 * (idx & 1)) * 64 + (wv & 1) * 32 + (lane & 15) * 2;
  const int l0 = (idx >> 1) * 128 + (wv >> 1) * 32 + (lane >> 4) * 8;
  const __amdgpu_buffer_rsrc_t rk = __builtin_amdgcn_make_buffer_rsrc((void*)kin, 0, L_SEQ * D_CH * 2, 0x00020000);
  const int vo = l0 * (D_CH * 2) + c0 * 2;
  u32 uu[8], kk[11], vv[8];
#pragma unroll
  for (int i = 0; i < 8; i++) uu[i] = *(const u32*)((const char*)u + vo + i * (D_CH * 2));
#pragma unroll
  for (int i = 0; i < 11; i++) {
    int o = vo + (i - 3) * (D_CH * 2);
    if (i < 3) o = (l0 > 0) ? o : (int)0x40000000;
    kk[i] = __builtin_amdgcn_raw_buffer_load_b32(rk, o, 0, 0);
  }
#pragma unroll
  for (int i = 0; i < 8; i++) vv[i] = *(const u32*)((const char*)vin + vo + i * (D_CH * 2));
  const u32x4 sw = *(const u32x4*)(shk + (size_t)c0 * 4);
  __builtin_amdgcn_sched_barrier(0);
#pragma unroll
  for (int c = 0; c < 2; c++) {
    u32x4 o;
#pragma unroll
    for (int p = 0; p < 4; p++) o[p] = __builtin_amdgcn_perm(uu[2 * p + 1], uu[2 * p], c ? 0x07060302u : 0x05040100u);
    *(u32x4*)(uT + (size_t)(c0 + c) * L_SEQ + l0) = o;
  }
#pragma unroll
  for (int c = 0; c < 2; c++) {
    const u32 s01 = sw[2 * c], s23 = sw[2 * c + 1];
    const float w0 = bflo(s01), w1 = bfhi(s01), w2 = bflo(s23), w3 = bfhi(s23);
    float kf[11];
#pragma unroll
    for (int i = 0; i < 11; i++) kf[i] = c ? bfhi(kk[i]) : bflo(kk[i]);
    float res[8];
#pragma unroll
    for (int i = 0; i < 8; i++) {
      float a = rbf(rbf(kf[i] * w3) + rbf(kf[i + 1] * w2));
      a = rbf(a + rbf(kf[i + 2] * w1));
      a = rbf(a + rbf(kf[i + 3] * w0));
      res[i] = a * (c ? bfhi(vv[i]) : bflo(vv[i]));
    }
    u32x4 o;
#pragma unroll
    for (int p = 0; p < 4; p++) o[p] = pk_bf16(res[7 - 2 * p], res[6 - 2 * p]);
    *(u32x4*)(kvT + (size_t)(c0 + c) * L_SEQ + l0) = o;
  }
}

#define XS_BYTES 4608
#define HC_BYTES 2368
#define HS_BYTES (4 * HC_BYTES)
#define BW_BYTES (XS_BYTES + HS_BYTES)
#define GT_ROW 544
#define GT_TAB (16 * GT_ROW)
#define KC_OFF (2 * GT_TAB)
#define GEN_BYTES 20480
#define AW_BYTES (XS_BYTES + GEN_BYTES)
#define CNT_OFF (4 * BW_BYTES + 4 * AW_BYTES)
#define LDS_BYTES (CNT_OFF + 64)

struct cplx { float r, i; };
__device__ __forceinline__ cplx cmul(cplx a, cplx b) { cplx o; o.r = a.r * b.r - a.i * b.i; o.i = a.r * b.i + a.i * b.r; return o; }
__device__ __forceinline__ cplx cexp_pos(float ar, float ai, float pos) {
  const float LOG2E = 1.44269504088896340736f;
  const float I2PI_HI = 0.15915494309189533577f;
  const float I2PI_LO = 6.4206383e-9f;
  float xr = ar * pos;
  float xi = ai * pos;
  float e = __builtin_amdgcn_exp2f(xr * LOG2E);
  float r = xi * I2PI_HI;
  float err = __builtin_fmaf(xi, I2PI_HI, -r);
  float f = __builtin_amdgcn_fractf(r);
  f += __builtin_fmaf(xi, I2PI_LO, err);
  cplx o;
  o.r = e * __builtin_amdgcn_cosf(f);
  o.i = e * __builtin_amdgcn_sinf(f);
  return o;
}
__device__ __forceinline__ void split_h(float a, float b, u32& hi, u32& lo) {
  hi = __builtin_bit_cast(u32, __builtin_amdgcn_cvt_pkrtz(a, b));
  u32 l;
  asm("v_fma_mixlo_f16 %0, %1, -1.0, %2 op_sel_hi:[1,0,0]" : "=v"(l) : "v"(hi), "v"(a));
  asm("v_fma_mixhi_f16 %0, %1, -1.0, %2 op_sel:[1,0,0] op_sel_hi:[1,0,0]" : "+v"(l) : "v"(hi), "v"(b));
  lo = l;
}

template <bool IS_A>
__device__ __forceinline__ f32x4 mma(u32x4 a, u32x4 b, f32x4 c) {
  if (IS_A) return __builtin_amdgcn_mfma_f32_16x16x32_f16(__builtin_bit_cast(f16x8, a), __builtin_bit_cast(f16x8, b), c, 0, 0, 0);
  else return __builtin_amdgcn_mfma_f32_16x16x32_bf16(__builtin_bit_cast(bf16x8, a), __builtin_bit_cast(bf16x8, b), c, 0, 0, 0);
}

__device__ __forceinline__ u32 lds_off(const char* p) {
  return (u32)(uintptr_t)(const __attribute__((address_space(3))) char*)p;
}
#define DSR64(dst, addr, off) asm volatile("ds_read_b64 %0, %1 offset:%2" : "=v"(dst) : "v"(addr), "i"(off) : "memory")
#define DSR128(dst, addr, off) asm volatile("ds_read_b128 %0, %1 offset:%2" : "=v"(dst) : "v"(addr), "i"(off) : "memory")
#define WAIT_FRAGS(AL, AH, B)                                                                         \
  __builtin_amdgcn_sched_barrier(0);                                                                  \
  asm volatile("s_waitcnt lgkmcnt(0)"                                                                 \
               : "+v"(AL[0]), "+v"(AL[1]), "+v"(AL[2]), "+v"(AL[3]), "+v"(AL[4]), "+v"(AL[5]), "+v"(AL[6]), "+v"(AL[7]), \
                 "+v"(AH[0]), "+v"(AH[1]), "+v"(AH[2]), "+v"(AH[3]), "+v"(AH[4]), "+v"(AH[5]), "+v"(AH[6]), "+v"(AH[7]), \
                 "+v"(B[0]), "+v"(B[1]), "+v"(B[2]), "+v"(B[3]), "+v"(B[4]), "+v"(B[5]), "+v"(B[6]), "+v"(B[7])  \
               :: "memory");                                                                          \
  __builtin_amdgcn_sched_barrier(0);

__device__ __forceinline__ int gen_taps(char* tab, char* lin, int lane, u16 p_ldt, const u16 (&p_lar)[2], const u16 (&p_aim)[2],
                                        const u16 (&p_cre)[2], const u16 (&p_cim)[2]) {
  cplx C[2], e1[2], f8[2], E[2];
  const float dt = rbf(expf(bf2f(p_ldt)));
  u32 mc = 0;
#pragma unroll
  for (int h = 0; h < 2; h++) {
    float ar = -rbf(expf(bf2f(p_lar[h]))) * dt;
    float ai = bf2f(p_aim[h]) * dt;
    C[h].r = 2.0f * (bf2f(p_cre[h]) * dt);
    C[h].i = -2.0f * (bf2f(p_cim[h]) * dt);
    mc = max(mc, max(__float_as_uint(C[h].r) & 0x7fffffffu, __float_as_uint(C[h].i) & 0x7fffffffu));
    e1[h] = cexp_pos(ar, ai, 1.0f);
    f8[h] = cexp_pos(ar, ai, 8.0f);
    E[h] = cexp_pos(ar, ai, 128.0f);
    e1[h].i = -e1[h].i;
    E[h].i = -E[h].i;
  }
  mc = wave_max_u32(mc);
  const int ec = (mc > 0u && mc < 0x7f800000u) ? __builtin_amdgcn_frexp_expf(__uint_as_float(mc)) - 14 : 0;
#pragma unroll
  for (int h = 0; h < 2; h++) { C[h].r = __builtin_ldexpf(C[h].r, -ec); C[h].i = __builtin_ldexpf(C[h].i, -ec); }
  char* wp = tab + 4 * lane;
  const int c16 = lane & 15, q = lane >> 4;
  const char* rp = tab + c16 * GT_ROW + 16 * q;
#define PUT_ROW(row, h, z) { u32 hi_, lo_; split_h((z).r, (z).i, hi_, lo_); \
    *(u32*)(wp + (row) * GT_ROW + 256 * (h)) = hi_; *(u32*)(wp + (row) * GT_ROW + 256 * (h) + GT_TAB) = lo_; }
  {
    cplx F[2];
    F[0].r = F[1].r = 16384.0f; F[0].i = F[1].i = 0.f;
#pragma unroll
    for (int w = 0; w < 16; w++)
#pragma unroll
      for (int h = 0; h < 2; h++) {
        PUT_ROW(w, h, F[h]);
        if (w < 15) F[h] = cmul(F[h], f8[h]);
      }
  }
  __builtin_amdgcn_wave_barrier();
  u32x4 Fh[8], Fl[8];
#pragma unroll
  for (int ks = 0; ks < 8; ks++) {
    Fh[ks] = *(const u32x4*)(rp + 64 * ks);
    Fl[ks] = *(const u32x4*)(rp + 64 * ks + GT_TAB);
  }
  __builtin_amdgcn_wave_barrier();
  f32x4 acc[3];
#define TILE_MMA(ti) { __builtin_amdgcn_wave_barrier();                                              \
    f32x4 a_ = {0.f, 0.f, 0.f, 0.f}, b_ = {0.f, 0.f, 0.f, 0.f};                                    \
    _Pragma("unroll") for (int ks = 0; ks < 8; ks++) {                                             \
      u32x4 Gh = *(const u32x4*)(rp + 64 * ks), Gl = *(const u32x4*)(rp + 64 * ks + GT_TAB);       \
      b_ = mma<true>(Gl, Fh[ks], b_);                                                              \
      b_ = mma<true>(Gh, Fl[ks], b_);                                                              \
      a_ = mma<true>(Gh, Fh[ks], a_);                                                              \
    }                                                                                              \
    acc[ti] = a_ + b_; __builtin_amdgcn_wave_barrier(); }
  cplx g[2][8];
#pragma unroll
  for (int h = 0; h < 2; h++) g[h][0] = C[h];
#pragma unroll
  for (int be = 1; be < 8; be++)
#pragma unroll
    for (int h = 0; h < 2; h++) g[h][be] = cmul(g[h][be - 1], e1[h]);
#pragma unroll
  for (int be = 0; be < 8; be++)
#pragma unroll
    for (int h = 0; h < 2; h++) PUT_ROW(be, h, g[h][be]);
#pragma unroll
  for (int be = 0; be < 8; be++)
#pragma unroll
    for (int h = 0; h < 2; h++) { g[h][be] = cmul(g[h][be], E[h]); PUT_ROW(8 + be, h, g[h][be]); }
  TILE_MMA(0);
#pragma unroll
  for (int al = 0; al < 2; al++)
#pragma unroll
    for (int be = 0; be < 8; be += 2)
#pragma unroll
      for (int h = 0; h < 2; h++) { g[h][be] = cmul(g[h][be], E[h]); PUT_ROW(4 * al + (be >> 1), h, g[h][be]); }
#pragma unroll
  for (int al = 0; al < 4; al++)
#pragma unroll
    for (int be = 0; be < 8; be += 4)
#pragma unroll
      for (int h = 0; h < 2; h++) { g[h][be] = cmul(g[h][be], E[h]); PUT_ROW(8 + 2 * al + (be >> 2), h, g[h][be]); }
  TILE_MMA(1);
#pragma unroll
  for (int al = 0; al < 9; al++)
#pragma unroll
    for (int h = 0; h < 2; h++) { g[h][0] = cmul(g[h][0], E[h]); PUT_ROW(al, h, g[h][0]); }
  TILE_MMA(2);
  const bool v2 = q < 2, v28 = (q == 2) && (c16 == 0);
  u32 mk = 0;
#pragma unroll
  for (int rg = 0; rg < 4; rg++) {
    mk = max(mk, max(__float_as_uint(acc[0][rg]) & 0x7fffffffu, __float_as_uint(acc[1][rg]) & 0x7fffffffu));
    u32 m2 = __float_as_uint(acc[2][rg]) & 0x7fffffffu;
    mk = max(mk, (v2 || (rg == 0 && v28)) ? m2 : 0u);
  }
  mk = wave_max_u32(mk);
  const int es = (mk > 0u && mk < 0x7f800000u) ? __builtin_amdgcn_frexp_expf(__uint_as_float(mk)) - 15 : 0;
  u16* kc = (u16*)(tab + KC_OFF);
#pragma unroll
  for (int rg = 0; rg < 4; rg++) {
    const int row = 4 * q + rg;
    kc[128 * (row >> 3) + (row & 7) + 8 * c16] = __builtin_bit_cast(u16, (_Float16)__builtin_ldexpf(acc[0][rg], -es));
    int i1 = (row < 8) ? 256 + 64 * (row >> 2) + (row & 3) + 4 * c16 : 384 + 32 * ((row - 8) >> 1) + (row & 1) + 2 * c16;
    kc[i1] = __builtin_bit_cast(u16, (_Float16)__builtin_ldexpf(acc[1][rg], -es));
    kc[512 + 16 * row + c16] = __builtin_bit_cast(u16, (_Float16)__builtin_ldexpf(acc[2][rg], -es));
  }
  __builtin_amdgcn_wave_barrier();
  {
    const char* kb = (const char*)kc;
    const bool dir = lane < 32;
    const int a0 = dir ? 16 * lane : 512 + 8 * (lane - 32);
    u32x2 d01 = *(const u32x2*)(kb + a0), d23 = *(const u32x2*)(kb + a0 + 8);
    u32x4 o;
    o[0] = __builtin_amdgcn_perm(d01[0], d01[0], dir ? 0x03020100u : 0x01000100u);
    o[1] = __builtin_amdgcn_perm(d01[1], d01[0], dir ? 0x07060504u : 0x05040302u);
    o[2] = __builtin_amdgcn_perm(d23[0], d01[1], dir ? 0x07060504u : 0x01000100u);
    o[3] = dir ? d23[1] : __builtin_amdgcn_perm(d23[0], d01[1], 0x05040302u);
    *(u32x4*)(lin + 32 + 16 * lane) = o;
    u32 d0 = *(const u32*)(kb + 768 + 4 * lane), d1 = *(const u32*)(kb + 772 + 4 * lane);
    o[0] = __builtin_amdgcn_perm(d0, d0, 0x01000100u);
    o[1] = d0;
    o[2] = __builtin_amdgcn_perm(d0, d0, 0x03020302u);
    o[3] = __builtin_amdgcn_perm(d1, d1, 0x01000100u);
    *(u32x4*)(lin + 32 + 1024 + 16 * lane) = o;
#pragma unroll
    for (int i = 0; i < 2; i++) {
      u32 x0 = kc[512 + 64 * i + lane], x1 = kc[513 + 64 * i + lane];
      o[0] = o[1] = x0 | (x0 << 16);
      o[3] = x1 | (x1 << 16);
      o[2] = (lane & 1) ? o[3] : (x0 | (x1 << 16));
      *(u32x4*)(lin + 32 + 2048 + 1024 * i + 16 * lane) = o;
    }
    if (lane < 4) *(u32x4*)(lin + ((lane & 2) ? 4096 : 0) + 16 * lane) = (u32x4){0, 0, 0, 0};
  }
  __builtin_amdgcn_wave_barrier();
  return es + ec - 14;
}

template <bool IS_A>
__device__ __forceinline__ void conv_wave(char* smem, int w, int lane, int ch, const u16* __restrict__ xT, const u16* __restrict__ hT,
                                          const u16* __restrict__ lar, const u16* __restrict__ aim, const u16* __restrict__ cre,
                                          const u16* __restrict__ cim, const u16* __restrict__ ldt,
                                          const u16* __restrict__ dvec, const u16* __restrict__ qin, u16* __restrict__ out) {
  char* xs = IS_A ? smem + 4 * BW_BYTES + w * AW_BYTES : smem + w * BW_BYTES;
  char* hs = xs + XS_BYTES;
  const u16* xrow = xT + (size_t)ch * L_SEQ;
  u16 p_ldt, p_lar[2], p_aim[2], p_cre[2], p_cim[2];
  if (IS_A) {
    p_ldt = ldt[ch];
#pragma unroll
    for (int h = 0; h < 2; h++) {
      const int o = ch * N_MODE + 64 * h + lane;
      p_lar[h] = lar[o]; p_aim[h] = aim[o]; p_cre[h] = cre[o]; p_cim[h] = cim[o];
    }
    __builtin_amdgcn_sched_barrier(0);
  }
  u32x4 xr[4];
#pragma unroll
  for (int j = 0; j < 4; j++) xr[j] = *(const u32x4*)(xrow + 8 * (64 * j + lane));
  u32x4 hd[6];
  u32x2 he[6];
  const int hl = lane < 48 ? lane : lane - 48;
  const int ha = hl / 6, hi_ = hl - 6 * ha;
  if (!IS_A) {
    const __amdgpu_buffer_rsrc_t rh = __builtin_amdgcn_make_buffer_rsrc((void*)(hT + (size_t)ch * L_SEQ), 0, L_SEQ * 2, 0x00020000);
#pragma unroll
    for (int s = 0; s < 6; s++) {
      const int hc = 32 * ha + 16 * (s / 3) + 6 * (s % 3) + hi_ - 2;
      int o1 = 16 * hc, o2 = 16 * hc + 16;
      if (s == 0) { o1 = hc >= 0 ? o1 : 0x40000000; o2 = hc >= -1 ? o2 : 0x40000000; }
      hd[s] = __builtin_amdgcn_raw_buffer_load_b128(rh, o1, 0, 0);
      he[s] = __builtin_amdgcn_raw_buffer_load_b64(rh, o2, 0, 0);
    }
  }
  const u16 p_d = dvec[ch];
  __builtin_amdgcn_sched_barrier(0);
  int esc = 0;
  if (IS_A) {
    esc = gen_taps(hs, xs, lane, p_ldt, p_lar, p_aim, p_cre, p_cim);
#pragma unroll
    for (int s = 0; s < 6; s++) {
      const int hc = 32 * ha + 16 * (s / 3) + 6 * (s % 3) + hi_;
      hd[s] = *(const u32x4*)(xs + 16 * hc);
      he[s] = *(const u32x2*)(xs + 16 * hc + 16);
    }
    __builtin_amdgcn_wave_barrier();
  }
  if (lane < 30) *(u32x4*)(xs + 16 * lane) = (u32x4){0, 0, 0, 0};
  if (IS_A) {
    u32 mx = 0;
#pragma unroll
    for (int j = 0; j < 4; j++)
#pragma unroll
      for (int p = 0; p < 4; p++) {
        u32 dd = xr[j][p];
        mx = max(mx, max(dd & 0x7fffu, (dd >> 16) & 0x7fffu));
      }
    mx = wave_max_u32(mx);
    const int ex = (int)(mx >> 7) - 127 - 14;
    esc += ex;
#pragma unroll
    for (int j = 0; j < 4; j++) {
      u32x4 o;
#pragma unroll
      for (int p = 0; p < 4; p++) {
        u32 dd = xr[j][3 - p];
        float lo = __builtin_ldexpf(bfhi(dd), -ex), hi = __builtin_ldexpf(bflo(dd), -ex);
        o[p] = __builtin_bit_cast(u32, __builtin_amdgcn_cvt_pkrtz(lo, hi));
      }
      *(u32x4*)(xs + 480 + 16 * (64 * j + lane)) = o;
    }
  } else {
#pragma unroll
    for (int j = 0; j < 4; j++) *(u32x4*)(xs + 480 + 16 * (64 * j + lane)) = xr[j];
  }
  const int r = lane & 15, q = lane >> 4;
  f32x4 acc[8];
#pragma unroll
  for (int i = 0; i < 8; i++) acc[i] = (f32x4){0.f, 0.f, 0.f, 0.f};
  const int lam = 25 + r - 8 * q;
  const u32 abase = lds_off(hs) + (lam & 3) * HC_BYTES + 2 * (lam & ~3);
  const u32 bbase = lds_off(xs) + 32 * r + 16 * q;
  char* hw = hs + ha * 288 + hi_ * 16;
#define STAGE_PIECE(hf, k) {                                                           \
    const u32x4 d4 = hd[3 * (hf) + ((k) >> 2)];                                        \
    const u32x2 e2 = he[3 * (hf) + ((k) >> 2)];                                        \
    u32x4 o_;                                                                          \
    if (((k) & 3) == 0) o_ = d4;                                                       \
    else if (((k) & 3) == 1) o_ = (u32x4){__builtin_amdgcn_alignbit(d4[1], d4[0], 16), __builtin_amdgcn_alignbit(d4[2], d4[1], 16), \
                                          __builtin_amdgcn_alignbit(d4[3], d4[2], 16), __builtin_amdgcn_alignbit(e2[0], d4[3], 16)}; \
    else if (((k) & 3) == 2) o_ = (u32x4){d4[1], d4[2], d4[3], e2[0]};                 \
    else o_ = (u32x4){__builtin_amdgcn_alignbit(d4[2], d4[1], 16), __builtin_amdgcn_alignbit(d4[3], d4[2], 16), \
                      __builtin_amdgcn_alignbit(e2[0], d4[3], 16), __builtin_amdgcn_alignbit(e2[1], e2[0], 16)}; \
    *(u32x4*)(hw + 96 * ((k) >> 2) + ((k) & 3) * HC_BYTES) = o_; }
  u32x2 AL0[8], AH0[8], AL1[8], AH1[8];
  u32x4 B0[8], B1[8];
#define LOAD_PIECE(it, k, AL, AH, B) {                                                  \
    if ((k) % 3 == 0) DSR64(AL[(k) / 3], abase, 64 * ((it) & 3) + 288 * ((k) / 3));     \
    else if ((k) % 3 == 1) DSR64(AH[(k) / 3], abase, 64 * ((it) & 3) + 288 * ((k) / 3) + 8); \
    else DSR128(B[(k) / 3], bbase, 448 + 512 * ((k) / 3) - 64 * (it)); }
#pragma unroll
  for (int k = 0; k < 12; k++) STAGE_PIECE(0, k);
  __builtin_amdgcn_sched_barrier(0);
#pragma unroll
  for (int k = 0; k < 24; k++) LOAD_PIECE(0, k, AL0, AH0, B0);
  __builtin_amdgcn_sched_barrier(0);
  u32x2 qv[8];
  if (IS_A) {
#pragma unroll
    for (int ct = 0; ct < 8; ct++) qv[ct] = *(const u32x2*)(xrow + 256 * ct + 16 * r + 4 * q);
  } else {
#pragma unroll
    for (int i = 0; i < 8; i++) qv[i] = *(const u32x2*)(qin + (size_t)(8 * (64 * w + lane) + i) * D_CH + (ch - w));
  }
  __builtin_amdgcn_s_setprio(2);
#define ITER(it, AL, AH, B, NAL, NAH, NB)                                               \
  WAIT_FRAGS(AL, AH, B);                                                                \
  {                                                                                     \
    int k_ = 0;                                                                         \
    _Pragma("unroll") for (int a = 0; a < 8; a++)                                       \
    _Pragma("unroll") for (int e = 0; e + a < 8; e++) {                                 \
      acc[a + e] = mma<IS_A>((u32x4){AL[a][0], AL[a][1], AH[a][0], AH[a][1]}, B[e], acc[a + e]); \
      if ((it) < 7 && k_ < 24) LOAD_PIECE((it) + 1, k_, NAL, NAH, NB);                  \
      if ((it) == 2 && k_ >= 24) STAGE_PIECE(1, k_ - 24);                               \
      __builtin_amdgcn_sched_barrier(0);                                                \
      k_++;                                                                             \
    }                                                                                   \
  }                                                                                     \
  asm volatile("" : "+v"(acc[0]), "+v"(acc[1]), "+v"(acc[2]), "+v"(acc[3]), "+v"(acc[4]), "+v"(acc[5]), "+v"(acc[6]), "+v"(acc[7]));
  ITER(0, AL0, AH0, B0, AL1, AH1, B1);
  ITER(1, AL1, AH1, B1, AL0, AH0, B0);
  ITER(2, AL0, AH0, B0, AL1, AH1, B1);
  ITER(3, AL1, AH1, B1, AL0, AH0, B0);
  ITER(4, AL0, AH0, B0, AL1, AH1, B1);
  ITER(5, AL1, AH1, B1, AL0, AH0, B0);
  ITER(6, AL0, AH0, B0, AL1, AH1, B1);
  ITER(7, AL1, AH1, B1, AL0, AH0, B0);
  __builtin_amdgcn_s_setprio(0);
  const float dsk = bf2f(p_d);
  char* ys = hs;
#pragma unroll
  for (int ct = 0; ct < 8; ct++) {
    const int l = 256 * ct + 16 * r + 4 * q;
    float x0, x1, x2, x3;
    if (IS_A) {
      u32x2 xv = qv[ct];
      x0 = bflo(xv[0]); x1 = bfhi(xv[0]); x2 = bflo(xv[1]); x3 = bfhi(xv[1]);
    } else {
      u32x2 xv = *(const u32x2*)(xs + 480 + 2 * ((l & ~7) + 4 - (l & 4)));
      x3 = bflo(xv[0]); x2 = bfhi(xv[0]); x1 = bflo(xv[1]); x0 = bfhi(xv[1]);
    }
    float y0 = acc[ct][0], y1 = acc[ct][1], y2 = acc[ct][2], y3 = acc[ct][3];
    if (IS_A) {
      y0 = __builtin_ldexpf(y0, esc); y1 = __builtin_ldexpf(y1, esc);
      y2 = __builtin_ldexpf(y2, esc); y3 = __builtin_ldexpf(y3, esc);
    }
    u32x2 o;
    o[0] = pk_bf16(rbf(y0) + rbf(x0 * dsk), rbf(y1) + rbf(x1 * dsk));
    o[1] = pk_bf16(rbf(y2) + rbf(x2 * dsk), rbf(y3) + rbf(x3 * dsk));
    *(u32x2*)(ys + 2 * l) = o;
  }
  if (!IS_A) {
    const u32 cnt = lds_off(smem + CNT_OFF);
    asm volatile("s_waitcnt lgkmcnt(0)" ::: "memory");
    if (lane == 0) asm volatile("ds_add_u32 %0, %1" :: "v"(cnt), "v"(1u) : "memory");
    for (;;) {
      u32 cv;
      asm volatile("ds_read_b32 %0, %1\n\ts_waitcnt lgkmcnt(0)" : "=v"(cv) : "v"(cnt) : "memory");
      if (__builtin_amdgcn_readfirstlane(cv) >= 4u) break;
      __builtin_amdgcn_s_sleep(1);
    }
    const int tb = 64 * w + lane;
    u32x4 yv[4];
#pragma unroll
    for (int c = 0; c < 4; c++) yv[c] = *(const u32x4*)(smem + c * BW_BYTES + XS_BYTES + 16 * tb);
#pragma unroll
    for (int i = 0; i < 8; i++) {
      u32x2 o;
#pragma unroll
      for (int p = 0; p < 2; p++) {
        u32 a = yv[2 * p][i >> 1], bq = yv[2 * p + 1][i >> 1];
        float ya = (i & 1) ? bfhi(a) : bflo(a), yb = (i & 1) ? bfhi(bq) : bflo(bq);
        o[p] = pk_bf16(ya * bflo(qv[i][p]), yb * bfhi(qv[i][p]));
      }
      *(u32x2*)(out + (size_t)(8 * tb + i) * (2 * D_CH) + D_CH + (ch - w)) = o;
    }
  }
}

__global__ __launch_bounds__(512) void conv_kernel(const u16* __restrict__ uT, const u16* __restrict__ kvT,
                                                   const u16* __restrict__ lar, const u16* __restrict__ aim,
                                                   const u16* __restrict__ cre, const u16* __restrict__ cim,
                                                   const u16* __restrict__ ldt, const u16* __restrict__ dskip,
                                                   const u16* __restrict__ ssmk, const u16* __restrict__ ssmd,
                                                   const u16* __restrict__ qin, u16* __restrict__ out) {
  extern __shared__ __attribute__((aligned(16))) char smem[];
  const int t = threadIdx.x, lane = t & 63;
  const int wv = __builtin_amdgcn_readfirstlane(t >> 6);
  const int xcd = blockIdx.x & 7, idx = blockIdx.x >> 3;
  const int c4 = (xcd + 8 * (idx >> 4)) * 64 + (idx & 15) * 4;
  if (t == 0) *(u32*)(smem + CNT_OFF) = 0u;
  __syncthreads();
  if (wv < 4) conv_wave<false>(smem, wv, lane, c4 + wv, kvT, ssmk, lar, aim, cre, cim, ldt, ssmd, qin, out);
  else conv_wave<true>(smem, wv - 4, lane, c4 + wv - 4, uT, ssmk, lar, aim, cre, cim, ldt, dskip, qin, out);
  __syncthreads();
  {
    u32x2 yv[4];
#pragma unroll
    for (int c = 0; c < 4; c++) yv[c] = *(const u32x2*)(smem + 4 * BW_BYTES + c * AW_BYTES + XS_BYTES + 8 * t);
#pragma unroll
    for (int i = 0; i < 4; i++) {
      u32x2 o;
#pragma unroll
      for (int p = 0; p < 2; p++)
        o[p] = __builtin_amdgcn_perm(yv[2 * p + 1][i >> 1], yv[2 * p][i >> 1], (i & 1) ? 0x07060302u : 0x05040100u);
      *(u32x2*)(out + (size_t)(4 * t + i) * (2 * D_CH) + c4) = o;
    }
  }
}

extern "C" void kernel_launch(void* const* d_in, const int* in_sizes, int n_in, void* d_out, int out_size,
                              void* d_ws, size_t ws_size, hipStream_t stream) {
  const u16* u = (const u16*)d_in[0];
  const u16* lar = (const u16*)d_in[1];
  const u16* aim = (const u16*)d_in[2];
  const u16* cre = (const u16*)d_in[3];
  const u16* cim = (const u16*)d_in[4];
  const u16* ldt = (const u16*)d_in[5];
  const u16* dsk = (const u16*)d_in[6];
  const u16* q = (const u16*)d_in[7];
  const u16* k = (const u16*)d_in[8];
  const u16* v = (const u16*)d_in[9];
  const u16* shk = (const u16*)d_in[10];
  const u16* ssmk = (const u16*)d_in[11];
  const u16* ssmd = (const u16*)d_in[12];
  char* ws = (char*)d_ws;
  u16* uT = (u16*)ws;
  u16* kvT = (u16*)(ws + (size_t)4 * 1024 * 1024);
  (void)hipFuncSetAttribute((const void*)conv_kernel, hipFuncAttributeMaxDynamicSharedMemorySize, LDS_BYTES);
  prep_kernel<<<256, 512, 0, stream>>>(u, k, v, shk, uT, kvT);
  conv_kernel<<<256, 512, LDS_BYTES, stream>>>(uT, kvT, lar, aim, cre, cim, ldt, dsk, ssmk, ssmd, q, (u16*)d_out);
}
```

```cpp
#include <hip/hip_runtime.h>
#include <hip/hip_bf16.h>
#include <stdint.h>

typedef unsigned int u32;
typedef unsigned short u16;
typedef u32 u32x4 __attribute__((ext_vector_type(4)));
typedef u32 u32x2 __attribute__((ext_vector_type(2)));
typedef float f32x4 __attribute__((ext_vector_type(4)));
typedef __bf16 bf16x8 __attribute__((ext_vector_type(8)));
typedef _Float16 f16x8 __attribute__((ext_vector_type(8)));

#define L_SEQ 2048
#define D_CH 1024
#define N_MODE 128

__device__ __forceinline__ float bflo(u32 d) { return __uint_as_float(d << 16); }
__device__ __forceinline__ float bfhi(u32 d) { return __uint_as_float(d & 0xffff0000u); }
__device__ __forceinline__ float bf2f(u16 h) { return __uint_as_float(((u32)h) << 16); }
__device__ __forceinline__ u32 pk_bf16(float lo, float hi) {
  typedef __bf16 bf16x2 __attribute__((ext_vector_type(2)));
  typedef float f32x2 __attribute__((ext_vector_type(2)));
  f32x2 v = {lo, hi};
  bf16x2 r = __builtin_convertvector(v, bf16x2);
  return __builtin_bit_cast(u32, r);
}
__device__ __forceinline__ float rbf(float x) { return __uint_as_float(pk_bf16(0.f, x)); }

__device__ __forceinline__ u32 wave_max_u32(u32 x) {
  x = max(x, (u32)__builtin_amdgcn_update_dpp(0, (int)x, 0x111, 0xf, 0xf, true));
  x = max(x, (u32)__builtin_amdgcn_update_dpp(0, (int)x, 0x112, 0xf, 0xf, true));
  x = max(x, (u32)__builtin_amdgcn_update_dpp(0, (int)x, 0x114, 0xf, 0xf, true));
  x = max(x, (u32)__builtin_amdgcn_update_dpp(0, (int)x, 0x118, 0xf, 0xf, true));
  x = max(x, (u32)__builtin_amdgcn_update_dpp(0, (int)x, 0x142, 0xa, 0xf, true));
  x = max(x, (u32)__builtin_amdgcn_update_dpp(0, (int)x, 0x143, 0xc, 0xf, true));
  return (u32)__builtin_amdgcn_readlane((int)x, 63);
}

__global__ __launch_bounds__(512) void prep_kernel(const u16* __restrict__ u, const u16* __restrict__ kin,
                                                   const u16* __restrict__ vin, const u16* __restrict__ shk,
                                                   u16* __restrict__ uT, u16* __restrict__ kvT) {
  const int b = blockIdx.x;
  const int t = threadIdx.x;
  const int lane = t & 63;
  const int wv = t >> 6;
  const int xcd = b & 7, idx = b >> 3;
  const int c0 = (xcd + 8 * (idx & 1)) * 64 + (wv & 1) * 32 + (lane & 15) * 2;
  const int l0 = (idx >> 1) * 128 + (wv >> 1) * 32 + (lane >> 4) * 8;
  const __amdgpu_buffer_rsrc_t rk = __builtin_amdgcn_make_buffer_rsrc((void*)kin, 0, L_SEQ * D_CH * 2, 0x00020000);
  const int vo = l0 * (D_CH * 2) + c0 * 2;
  u32 uu[8], kk[11], vv[8];
#pragma unroll
  for (int i = 0; i < 8; i++) uu[i] = *(const u32*)((const char*)u + vo + i * (D_CH * 2));
#pragma unroll
  for (int i = 0; i < 11; i++) {
    int o = vo + (i - 3) * (D_CH * 2);
    if (i < 3) o = (l0 > 0) ? o : (int)0x40000000;
    kk[i] = __builtin_amdgcn_raw_buffer_load_b32(rk, o, 0, 0);
  }
#pragma unroll
  for (int i = 0; i < 8; i++) vv[i] = *(const u32*)((const char*)vin + vo + i * (D_CH * 2));
  const u32x4 sw = *(const u32x4*)(shk + (size_t)c0 * 4);
  __builtin_amdgcn_sched_barrier(0);
#pragma unroll
  for (int c = 0; c < 2; c++) {
    u32x4 o;
#pragma unroll
    for (int p = 0; p < 4; p++) o[p] = __builtin_amdgcn_perm(uu[2 * p + 1], uu[2 * p], c ? 0x07060302u : 0x05040100u);
    *(u32x4*)(uT + (size_t)(c0 + c) * L_SEQ + l0) = o;
  }
#pragma unroll
  for (int c = 0; c < 2; c++) {
    const u32 s01 = sw[2 * c], s23 = sw[2 * c + 1];
    const float w0 = bflo(s01), w1 = bfhi(s01), w2 = bflo(s23), w3 = bfhi(s23);
    float kf[11];
#pragma unroll
    for (int i = 0; i < 11; i++) kf[i] = c ? bfhi(kk[i]) : bflo(kk[i]);
    float res[8];
#pragma unroll
    for (int i = 0; i < 8; i++) {
      float a = rbf(rbf(kf[i] * w3) + rbf(kf[i + 1] * w2));
      a = rbf(a + rbf(kf[i + 2] * w1));
      a = rbf(a + rbf(kf[i + 3] * w0));
      res[i] = a * (c ? bfhi(vv[i]) : bflo(vv[i]));
    }
    u32x4 o;
#pragma unroll
    for (int p = 0; p < 4; p++) o[p] = pk_bf16(res[7 - 2 * p], res[6 - 2 * p]);
    *(u32x4*)(kvT + (size_t)(c0 + c) * L_SEQ + l0) = o;
  }
}

#define XS_BYTES 4608
#define HC_BYTES 2368
#define HS_BYTES (4 * HC_BYTES)
#define W_BYTES (XS_BYTES + HS_BYTES)
#define GT_ROW 288
#define GT_TAB (16 * GT_ROW)
#define EX_OFF (8 * W_BYTES)
#define KC_OFF (EX_OFF + 4 * 3072)
#define CNT_OFF (KC_OFF + 4 * 1536)
#define LDS_BYTES (CNT_OFF + 64)

typedef float f32x2 __attribute__((ext_vector_type(2)));
__device__ __forceinline__ f32x2 cmul(f32x2 g, f32x2 e) {
  f32x2 t, o;
  asm("v_pk_mul_f32 %0, %1, %2 op_sel:[0,0] op_sel_hi:[0,1]" : "=v"(t) : "v"(g), "v"(e));
  asm("v_pk_fma_f32 %0, %1, %2, %3 op_sel:[1,1,0] op_sel_hi:[1,0,1] neg_lo:[1,0,0]" : "=v"(o) : "v"(g), "v"(e), "v"(t));
  return o;
}
__device__ __forceinline__ f32x2 cexp_pos(float ar, float ai, float pos) {
  const float LOG2E = 1.44269504088896340736f;
  const float I2PI_HI = 0.15915494309189533577f;
  const float I2PI_LO = 6.4206383e-9f;
  float xr = ar * pos;
  float xi = ai * pos;
  float e = __builtin_amdgcn_exp2f(xr * LOG2E);
  float r = xi * I2PI_HI;
  float err = __builtin_fmaf(xi, I2PI_HI, -r);
  float f = __builtin_amdgcn_fractf(r);
  f += __builtin_fmaf(xi, I2PI_LO, err);
  f32x2 o;
  o[0] = e * __builtin_amdgcn_cosf(f);
  o[1] = e * __builtin_amdgcn_sinf(f);
  return o;
}
__device__ __forceinline__ void split_h(float a, float b, u32& hi, u32& lo) {
  hi = __builtin_bit_cast(u32, __builtin_amdgcn_cvt_pkrtz(a, b));
  u32 l;
  asm("v_fma_mixlo_f16 %0, %1, -1.0, %2 op_sel_hi:[1,0,0]" : "=v"(l) : "v"(hi), "v"(a));
  asm("v_fma_mixhi_f16 %0, %1, -1.0, %2 op_sel:[1,0,0] op_sel_hi:[1,0,0]" : "+v"(l) : "v"(hi), "v"(b));
  lo = l;
}

template <bool IS_A>
__device__ __forceinline__ f32x4 mma(u32x4 a, u32x4 b, f32x4 c) {
  if (IS_A) return __builtin_amdgcn_mfma_f32_16x16x32_f16(__builtin_bit_cast(f16x8, a), __builtin_bit_cast(f16x8, b), c, 0, 0, 0);
  else return __builtin_amdgcn_mfma_f32_16x16x32_bf16(__builtin_bit_cast(bf16x8, a), __builtin_bit_cast(bf16x8, b), c, 0, 0, 0);
}

__device__ __forceinline__ u32 lds_off(const char* p) {
  return (u32)(uintptr_t)(const __attribute__((address_space(3))) char*)p;
}
#define DSR64(dst, addr, off) asm volatile("ds_read_b64 %0, %1 offset:%2" : "=v"(dst) : "v"(addr), "i"(off) : "memory")
#define DSR128(dst, addr, off) asm volatile("ds_read_b128 %0, %1 offset:%2" : "=v"(dst) : "v"(addr), "i"(off) : "memory")
#define WAIT_FRAGS(AL, AH, B)                                                                         \
  __builtin_amdgcn_sched_barrier(0);                                                                  \
  asm volatile("s_waitcnt lgkmcnt(0)"                                                                 \
               : "+v"(AL[0]), "+v"(AL[1]), "+v"(AL[2]), "+v"(AL[3]), "+v"(AL[4]), "+v"(AL[5]), "+v"(AL[6]), "+v"(AL[7]), \
                 "+v"(AH[0]), "+v"(AH[1]), "+v"(AH[2]), "+v"(AH[3]), "+v"(AH[4]), "+v"(AH[5]), "+v"(AH[6]), "+v"(AH[7]), \
                 "+v"(B[0]), "+v"(B[1]), "+v"(B[2]), "+v"(B[3]), "+v"(B[4]), "+v"(B[5]), "+v"(B[6]), "+v"(B[7])  \
               :: "memory");                                                                          \
  __builtin_amdgcn_sched_barrier(0);

__device__ __forceinline__ void gen_partial(char* tab, int lane, u16 p_ldt, u16 p_lar, u16 p_aim, u16 p_cre, u16 p_cim,
                                            f32x4 (&acc)[3]) {
  const float dt = rbf(expf(bf2f(p_ldt)));
  const float ar = -rbf(expf(bf2f(p_lar))) * dt;
  const float ai = bf2f(p_aim) * dt;
  f32x2 C;
  C[0] = 2.0f * (bf2f(p_cre) * dt);
  C[1] = -2.0f * (bf2f(p_cim) * dt);
  const u32 mc = wave_max_u32(max(__float_as_uint(C[0]) & 0x7fffffffu, __float_as_uint(C[1]) & 0x7fffffffu));
  const f32x2 e1 = cexp_pos(ar, -ai, 1.0f);
  const f32x2 f8 = cexp_pos(ar, ai, 8.0f);
  const f32x2 E = cexp_pos(ar, -ai, 128.0f);
  const int ec = (mc > 0u && mc < 0x7f800000u) ? __builtin_amdgcn_frexp_expf(__uint_as_float(mc)) - 14 : 0;
  C[0] = __builtin_ldexpf(C[0], -ec) * 1.0003662109375f;
  C[1] = __builtin_ldexpf(C[1], -ec) * 1.0003662109375f;
  char* wp = tab + 4 * lane;
  const int c16 = lane & 15, q = lane >> 4;
  const char* rp = tab + c16 * GT_ROW + 16 * q;
  {
    f32x2 F = {16384.0f, 0.f};
#pragma unroll
    for (int w = 0; w < 16; w++) {
      u32 hi_, lo_;
      split_h(F[0], F[1], hi_, lo_);
      *(u32*)(wp + w * GT_ROW) = hi_;
      *(u32*)(wp + w * GT_ROW + GT_TAB) = lo_;
      if (w < 15) F = cmul(F, f8);
    }
  }
  __builtin_amdgcn_wave_barrier();
  u32x4 Fh[4], Fl[4];
#pragma unroll
  for (int ks = 0; ks < 4; ks++) {
    Fh[ks] = *(const u32x4*)(rp + 64 * ks);
    Fl[ks] = *(const u32x4*)(rp + 64 * ks + GT_TAB);
  }
  __builtin_amdgcn_wave_barrier();
#define PUT_ROW(tb, row, z) *(u32*)(wp + (tb) * GT_TAB + (row) * GT_ROW) = __builtin_bit_cast(u32, __builtin_amdgcn_cvt_pkrtz((z)[0], (z)[1]))
#define TILE_MMA(ti, tb) { __builtin_amdgcn_wave_barrier();                                          \
    f32x4 a_ = {0.f, 0.f, 0.f, 0.f}, b_ = {0.f, 0.f, 0.f, 0.f};                                    \
    _Pragma("unroll") for (int ks = 0; ks < 4; ks++) {                                             \
      u32x4 Gh = *(const u32x4*)(rp + 64 * ks + (tb) * GT_TAB);                                    \
      b_ = mma<true>(Gh, Fl[ks], b_);                                                              \
      a_ = mma<true>(Gh, Fh[ks], a_);                                                              \
    }                                                                                              \
    acc[ti] = a_ + b_; __builtin_amdgcn_wave_barrier(); }
  f32x2 g[8];
  g[0] = C;
#pragma unroll
  for (int be = 1; be < 8; be++) g[be] = cmul(g[be - 1], e1);
#pragma unroll
  for (int be = 0; be < 8; be++) PUT_ROW(0, be, g[be]);
#pragma unroll
  for (int be = 0; be < 8; be++) { g[be] = cmul(g[be], E); PUT_ROW(0, 8 + be, g[be]); }
  TILE_MMA(0, 0);
#pragma unroll
  for (int al = 0; al < 2; al++)
#pragma unroll
    for (int be = 0; be < 8; be += 2) { g[be] = cmul(g[be], E); PUT_ROW(1, 4 * al + (be >> 1), g[be]); }
#pragma unroll
  for (int al = 0; al < 4; al++)
#pragma unroll
    for (int be = 0; be < 8; be += 4) { g[be] = cmul(g[be], E); PUT_ROW(1, 8 + 2 * al + (be >> 2), g[be]); }
  TILE_MMA(1, 1);
#pragma unroll
  for (int al = 0; al < 9; al++) { g[0] = cmul(g[0], E); PUT_ROW(0, al, g[0]); }
  TILE_MMA(2, 0);
#pragma unroll
  for (int i = 0; i < 3; i++)
#pragma unroll
    for (int rg = 0; rg < 4; rg++) acc[i][rg] = __builtin_ldexpf(acc[i][rg], ec - 14);
}

__device__ __forceinline__ int gen_expand(u16* kc, char* lin, int lane, const f32x4 (&acc)[3]) {
  const int c16 = lane & 15, q = lane >> 4;
  const bool v2 = q < 2, v28 = (q == 2) && (c16 == 0);
  u32 mk = 0;
#pragma unroll
  for (int rg = 0; rg < 4; rg++) {
    mk = max(mk, max(__float_as_uint(acc[0][rg]) & 0x7fffffffu, __float_as_uint(acc[1][rg]) & 0x7fffffffu));
    u32 m2 = __float_as_uint(acc[2][rg]) & 0x7fffffffu;
    mk = max(mk, (v2 || (rg == 0 && v28)) ? m2 : 0u);
  }
  mk = wave_max_u32(mk);
  const int es = (mk > 0u && mk < 0x7f800000u) ? __builtin_amdgcn_frexp_expf(__uint_as_float(mk)) - 15 : 0;
#pragma unroll
  for (int rg = 0; rg < 4; rg++) {
    const int row = 4 * q + rg;
    kc[128 * (row >> 3) + (row & 7) + 8 * c16] = __builtin_bit_cast(u16, (_Float16)__builtin_ldexpf(acc[0][rg], -es));
    int i1 = (row < 8) ? 256 + 64 * (row >> 2) + (row & 3) + 4 * c16 : 384 + 32 * ((row - 8) >> 1) + (row & 1) + 2 * c16;
    kc[i1] = __builtin_bit_cast(u16, (_Float16)__builtin_ldexpf(acc[1][rg], -es));
    kc[512 + 16 * row + c16] = __builtin_bit_cast(u16, (_Float16)__builtin_ldexpf(acc[2][rg], -es));
  }
  __builtin_amdgcn_wave_barrier();
  {
    const char* kb = (const char*)kc;
    const bool dir = lane < 32;
    const int a0 = dir ? 16 * lane : 512 + 8 * (lane - 32);
    u32x2 d01 = *(const u32x2*)(kb + a0), d23 = *(const u32x2*)(kb + a0 + 8);
    u32x4 o;
    o[0] = __builtin_amdgcn_perm(d01[0], d01[0], dir ? 0x03020100u : 0x01000100u);
    o[1] = __builtin_amdgcn_perm(d01[1], d01[0], dir ? 0x07060504u : 0x05040302u);
    o[2] = __builtin_amdgcn_perm(d23[0], d01[1], dir ? 0x07060504u : 0x01000100u);
    o[3] = dir ? d23[1] : __builtin_amdgcn_perm(d23[0], d01[1], 0x05040302u);
    *(u32x4*)(lin + 32 + 16 * lane) = o;
    u32 d0 = *(const u32*)(kb + 768 + 4 * lane), d1 = *(const u32*)(kb + 772 + 4 * lane);
    o[0] = __builtin_amdgcn_perm(d0, d0, 0x01000100u);
    o[1] = d0;
    o[2] = __builtin_amdgcn_perm(d0, d0, 0x03020302u);
    o[3] = __builtin_amdgcn_perm(d1, d1, 0x01000100u);
    *(u32x4*)(lin + 32 + 1024 + 16 * lane) = o;
#pragma unroll
    for (int i = 0; i < 2; i++) {
      u32 x0 = kc[512 + 64 * i + lane], x1 = kc[513 + 64 * i + lane];
      o[0] = o[1] = x0 | (x0 << 16);
      o[3] = x1 | (x1 << 16);
      o[2] = (lane & 1) ? o[3] : (x0 | (x1 << 16));
      *(u32x4*)(lin + 32 + 2048 + 1024 * i + 16 * lane) = o;
    }
    if (lane < 4) *(u32x4*)(lin + ((lane & 2) ? 4096 : 0) + 16 * lane) = (u32x4){0, 0, 0, 0};
  }
  __builtin_amdgcn_wave_barrier();
  return es;
}

template <bool IS_A>
__device__ __forceinline__ void conv_wave(char* smem, int w, int lane, int ch, const u16* __restrict__ xT, const u16* __restrict__ hT,
                                          const u16* __restrict__ lar, const u16* __restrict__ aim, const u16* __restrict__ cre,
                                          const u16* __restrict__ cim, const u16* __restrict__ ldt,
                                          const u16* __restrict__ dvec, const u16* __restrict__ qin, u16* __restrict__ out) {
  char* xs = smem + (IS_A ? w + 4 : w) * W_BYTES;
  char* hs = xs + XS_BYTES;
  const u16* xrow = xT + (size_t)ch * L_SEQ;
  const int po = ch * N_MODE + (IS_A ? 0 : 64) + lane;
  const u16 p_ldt = ldt[ch], p_lar = lar[po], p_aim = aim[po], p_cre = cre[po], p_cim = cim[po];
  __builtin_amdgcn_sched_barrier(0);
  u32x4 xr[4];
#pragma unroll
  for (int j = 0; j < 4; j++) xr[j] = *(const u32x4*)(xrow + 8 * (64 * j + lane));
  u32x4 hd[6];
  u32x2 he[6];
  const int hl = lane < 48 ? lane : lane - 48;
  const int ha = hl / 6, hi_ = hl - 6 * ha;
  if (!IS_A) {
    const __amdgpu_buffer_rsrc_t rh = __builtin_amdgcn_make_buffer_rsrc((void*)(hT + (size_t)ch * L_SEQ), 0, L_SEQ * 2, 0x00020000);
#pragma unroll
    for (int s = 0; s < 6; s++) {
      const int hc = 32 * ha + 16 * (s / 3) + 6 * (s % 3) + hi_ - 2;
      int o1 = 16 * hc, o2 = 16 * hc + 16;
      if (s == 0) { o1 = hc >= 0 ? o1 : 0x40000000; o2 = hc >= -1 ? o2 : 0x40000000; }
      hd[s] = __builtin_amdgcn_raw_buffer_load_b128(rh, o1, 0, 0);
      he[s] = __builtin_amdgcn_raw_buffer_load_b64(rh, o2, 0, 0);
    }
  }
  const u16 p_d = dvec[ch];
  __builtin_amdgcn_sched_barrier(0);
  int esc = 0;
  {
    f32x4 ka[3];
    gen_partial(hs, lane, p_ldt, p_lar, p_aim, p_cre, p_cim, ka);
    char* ex = smem + EX_OFF + w * 3072 + 16 * lane;
    if (!IS_A) {
#pragma unroll
      for (int i = 0; i < 3; i++) *(f32x4*)(ex + 1024 * i) = ka[i];
    }
    __syncthreads();
    if (IS_A) {
#pragma unroll
      for (int i = 0; i < 3; i++) ka[i] += *(const f32x4*)(ex + 1024 * i);
      esc = gen_expand((u16*)(smem + KC_OFF + w * 1536), xs, lane, ka);
    }
  }
  if (IS_A) {
#pragma unroll
    for (int s = 0; s < 6; s++) {
      const int hc = 32 * ha + 16 * (s / 3) + 6 * (s % 3) + hi_;
      hd[s] = *(const u32x4*)(xs + 16 * hc);
      he[s] = *(const u32x2*)(xs + 16 * hc + 16);
    }
    __builtin_amdgcn_wave_barrier();
  }
  if (lane < 30) *(u32x4*)(xs + 16 * lane) = (u32x4){0, 0, 0, 0};
  if (IS_A) {
    u32 mx = 0;
#pragma unroll
    for (int j = 0; j < 4; j++)
#pragma unroll
      for (int p = 0; p < 4; p++) {
        u32 dd = xr[j][p];
        mx = max(mx, max(dd & 0x7fffu, (dd >> 16) & 0x7fffu));
      }
    mx = wave_max_u32(mx);
    const int ex = (int)(mx >> 7) - 127 - 14;
    esc += ex;
#pragma unroll
    for (int j = 0; j < 4; j++) {
      u32x4 o;
#pragma unroll
      for (int p = 0; p < 4; p++) {
        u32 dd = xr[j][3 - p];
        float lo = __builtin_ldexpf(bfhi(dd), -ex), hi = __builtin_ldexpf(bflo(dd), -ex);
        o[p] = __builtin_bit_cast(u32, __builtin_amdgcn_cvt_pkrtz(lo, hi));
      }
      *(u32x4*)(xs + 480 + 16 * (64 * j + lane)) = o;
    }
  } else {
#pragma unroll
    for (int j = 0; j < 4; j++) *(u32x4*)(xs + 480 + 16 * (64 * j + lane)) = xr[j];
  }
  const int r = lane & 15, q = lane >> 4;
  f32x4 acc[8];
#pragma unroll
  for (int i = 0; i < 8; i++) acc[i] = (f32x4){0.f, 0.f, 0.f, 0.f};
  const int lam = 25 + r - 8 * q;
  const u32 abase = lds_off(hs) + (lam & 3) * HC_BYTES + 2 * (lam & ~3);
  const u32 bbase = lds_off(xs) + 32 * r + 16 * q;
  char* hw = hs + ha * 288 + hi_ * 16;
#define STAGE_PIECE(hf, k) {                                                           \
    const u32x4 d4 = hd[3 * (hf) + ((k) >> 2)];                                        \
    const u32x2 e2 = he[3 * (hf) + ((k) >> 2)];                                        \
    u32x4 o_;                                                                          \
    if (((k) & 3) == 0) o_ = d4;                                                       \
    else if (((k) & 3) == 1) o_ = (u32x4){__builtin_amdgcn_alignbit(d4[1], d4[0], 16), __builtin_amdgcn_alignbit(d4[2], d4[1], 16), \
                                          __builtin_amdgcn_alignbit(d4[3], d4[2], 16), __builtin_amdgcn_alignbit(e2[0], d4[3], 16)}; \
    else if (((k) & 3) == 2) o_ = (u32x4){d4[1], d4[2], d4[3], e2[0]};                 \
    else o_ = (u32x4){__builtin_amdgcn_alignbit(d4[2], d4[1], 16), __builtin_amdgcn_alignbit(d4[3], d4[2], 16), \
                      __builtin_amdgcn_alignbit(e2[0], d4[3], 16), __builtin_amdgcn_alignbit(e2[1], e2[0], 16)}; \
    *(u32x4*)(hw + 96 * ((k) >> 2) + ((k) & 3) * HC_BYTES) = o_; }
  u32x2 AL0[8], AH0[8], AL1[8], AH1[8];
  u32x4 B0[8], B1[8];
#define LOAD_PIECE(it, k, AL, AH, B) {                                                  \
    if ((k) % 3 == 0) DSR64(AL[(k) / 3], abase, 64 * ((it) & 3) + 288 * ((k) / 3));     \
    else if ((k) % 3 == 1) DSR64(AH[(k) / 3], abase, 64 * ((it) & 3) + 288 * ((k) / 3) + 8); \
    else DSR128(B[(k) / 3], bbase, 448 + 512 * ((k) / 3) - 64 * (it)); }
#pragma unroll
  for (int k = 0; k < 12; k++) STAGE_PIECE(0, k);
  __builtin_amdgcn_sched_barrier(0);
#pragma unroll
  for (int k = 0; k < 24; k++) LOAD_PIECE(0, k, AL0, AH0, B0);
  __builtin_amdgcn_sched_barrier(0);
  u32x2 qv[8];
  if (IS_A) {
#pragma unroll
    for (int ct = 0; ct < 8; ct++) qv[ct] = *(const u32x2*)(xrow + 256 * ct + 16 * r + 4 * q);
  } else {
#pragma unroll
    for (int i = 0; i < 8; i++) qv[i] = *(const u32x2*)(qin + (size_t)(8 * (64 * w + lane) + i) * D_CH + (ch - w));
  }
  __builtin_amdgcn_s_setprio(2);
#define ITER(it, AL, AH, B, NAL, NAH, NB)                                               \
  WAIT_FRAGS(AL, AH, B);                                                                \
  {                                                                                     \
    int k_ = 0;                                                                         \
    _Pragma("unroll") for (int a = 0; a < 8; a++)                                       \
    _Pragma("unroll") for (int e = 0; e + a < 8; e++) {                                 \
      acc[a + e] = mma<IS_A>((u32x4){AL[a][0], AL[a][1], AH[a][0], AH[a][1]}, B[e], acc[a + e]); \
      if ((it) < 7 && k_ < 24) LOAD_PIECE((it) + 1, k_, NAL, NAH, NB);                  \
      if ((it) == 2 && k_ >= 24) STAGE_PIECE(1, k_ - 24);                               \
      __builtin_amdgcn_sched_barrier(0);                                                \
      k_++;                                                                             \
    }                                                                                   \
  }                                                                                     \
  asm volatile("" : "+v"(acc[0]), "+v"(acc[1]), "+v"(acc[2]), "+v"(acc[3]), "+v"(acc[4]), "+v"(acc[5]), "+v"(acc[6]), "+v"(acc[7]));
  ITER(0, AL0, AH0, B0, AL1, AH1, B1);
  ITER(1, AL1, AH1, B1, AL0, AH0, B0);
  ITER(2, AL0, AH0, B0, AL1, AH1, B1);
  ITER(3, AL1, AH1, B1, AL0, AH0, B0);
  ITER(4, AL0, AH0, B0, AL1, AH1, B1);
  ITER(5, AL1, AH1, B1, AL0, AH0, B0);
  ITER(6, AL0, AH0, B0, AL1, AH1, B1);
  ITER(7, AL1, AH1, B1, AL0, AH0, B0);
  __builtin_amdgcn_s_setprio(0);
  const float dsk = bf2f(p_d);
  char* ys = hs;
#pragma unroll
  for (int ct = 0; ct < 8; ct++) {
    const int l = 256 * ct + 16 * r + 4 * q;
    float x0, x1, x2, x3;
    if (IS_A) {
      u32x2 xv = qv[ct];
      x0 = bflo(xv[0]); x1 = bfhi(xv[0]); x2 = bflo(xv[1]); x3 = bfhi(xv[1]);
    } else {
      u32x2 xv = *(const u32x2*)(xs + 480 + 2 * ((l & ~7) + 4 - (l & 4)));
      x3 = bflo(xv[0]); x2 = bfhi(xv[0]); x1 = bflo(xv[1]); x0 = bfhi(xv[1]);
    }
    float y0 = acc[ct][0], y1 = acc[ct][1], y2 = acc[ct][2], y3 = acc[ct][3];
    if (IS_A) {
      y0 = __builtin_ldexpf(y0, esc); y1 = __builtin_ldexpf(y1, esc);
      y2 = __builtin_ldexpf(y2, esc); y3 = __builtin_ldexpf(y3, esc);
    }
    u32x2 o;
    o[0] = pk_bf16(rbf(y0) + rbf(x0 * dsk), rbf(y1) + rbf(x1 * dsk));
    o[1] = pk_bf16(rbf(y2) + rbf(x2 * dsk), rbf(y3) + rbf(x3 * dsk));
    *(u32x2*)(ys + 2 * l) = o;
  }
  if (!IS_A) {
    const u32 cnt = lds_off(smem + CNT_OFF);
    asm volatile("s_waitcnt lgkmcnt(0)" ::: "memory");
    if (lane == 0) asm volatile("ds_add_u32 %0, %1" :: "v"(cnt), "v"(1u) : "memory");
    for (;;) {
      u32 cv;
      asm volatile("ds_read_b32 %0, %1\n\ts_waitcnt lgkmcnt(0)" : "=v"(cv) : "v"(cnt) : "memory");
      if (__builtin_amdgcn_readfirstlane(cv) >= 4u) break;
      __builtin_amdgcn_s_sleep(1);
    }
    const int tb = 64 * w + lane;
    u32x4 yv[4];
#pragma unroll
    for (int c = 0; c < 4; c++) yv[c] = *(const u32x4*)(smem + c * W_BYTES + XS_BYTES + 16 * tb);
#pragma unroll
    for (int i = 0; i < 8; i++) {
      u32x2 o;
#pragma unroll
      for (int p = 0; p < 2; p++) {
        u32 a = yv[2 * p][i >> 1], bq = yv[2 * p + 1][i >> 1];
        float ya = (i & 1) ? bfhi(a) : bflo(a), yb = (i & 1) ? bfhi(bq) : bflo(bq);
        o[p] = pk_bf16(ya * bflo(qv[i][p]), yb * bfhi(qv[i][p]));
      }
      *(u32x2*)(out + (size_t)(8 * tb + i) * (2 * D_CH) + D_CH + (ch - w)) = o;
    }
  }
}

__global__ __launch_bounds__(512) void conv_kernel(const u16* __restrict__ uT, const u16* __restrict__ kvT,
                                                   const u16* __restrict__ lar, const u16* __restrict__ aim,
                                                   const u16* __restrict__ cre, const u16* __restrict__ cim,
                                                   const u16* __restrict__ ldt, const u16* __restrict__ dskip,
                                                   const u16* __restrict__ ssmk, const u16* __restrict__ ssmd,
                                                   const u16* __restrict__ qin, u16* __restrict__ out) {
  extern __shared__ __attribute__((aligned(16))) char smem[];
  const int t = threadIdx.x, lane = t & 63;
  const int wv = __builtin_amdgcn_readfirstlane(t >> 6);
  const int xcd = blockIdx.x & 7, idx = blockIdx.x >> 3;
  const int c4 = (xcd + 8 * (idx >> 4)) * 64 + (idx & 15) * 4;
  if (t == 0) *(u32*)(smem + CNT_OFF) = 0u;
  if (wv < 4) conv_wave<false>(smem, wv, lane, c4 + wv, kvT, ssmk, lar, aim, cre, cim, ldt, ssmd, qin, out);
  else conv_wave<true>(smem, wv - 4, lane, c4 + wv - 4, uT, ssmk, lar, aim, cre, cim, ldt, dskip, qin, out);
  __syncthreads();
  {
    u32x2 yv[4];
#pragma unroll
    for (int c = 0; c < 4; c++) yv[c] = *(const u32x2*)(smem + (4 + c) * W_BYTES + XS_BYTES + 8 * t);
#pragma unroll
    for (int i = 0; i < 4; i++) {
      u32x2 o;
#pragma unroll
      for (int p = 0; p < 2; p++)
        o[p] = __builtin_amdgcn_perm(yv[2 * p + 1][i >> 1], yv[2 * p][i >> 1], (i & 1) ? 0x07060302u : 0x05040100u);
      *(u32x2*)(out + (size_t)(4 * t + i) * (2 * D_CH) + c4) = o;
    }
  }
}

extern "C" void kernel_launch(void* const* d_in, const int* in_sizes, int n_in, void* d_out, int out_size,
                              void* d_ws, size_t ws_size, hipStream_t stream) {
  const u16* u = (const u16*)d_in[0];
  const u16* lar = (const u16*)d_in[1];
  const u16* aim = (const u16*)d_in[2];
  const u16* cre = (const u16*)d_in[3];
  const u16* cim = (const u16*)d_in[4];
  const u16* ldt = (const u16*)d_in[5];
  const u16* dsk = (const u16*)d_in[6];
  const u16* q = (const u16*)d_in[7];
  const u16* k = (const u16*)d_in[8];
  const u16* v = (const u16*)d_in[9];
  const u16* shk = (const u16*)d_in[10];
  const u16* ssmk = (const u16*)d_in[11];
  const u16* ssmd = (const u16*)d_in[12];
  char* ws = (char*)d_ws;
  u16* uT = (u16*)ws;
  u16* kvT = (u16*)(ws + (size_t)4 * 1024 * 1024);
  (void)hipFuncSetAttribute((const void*)conv_kernel, hipFuncAttributeMaxDynamicSharedMemorySize, LDS_BYTES);
  prep_kernel<<<256, 512, 0, stream>>>(u, k, v, shk, uT, kvT);
  conv_kernel<<<256, 512, LDS_BYTES, stream>>>(uT, kvT, lar, aim, cre, cim, ldt, dsk, ssmk, ssmd, q, (u16*)d_out);
}
```

```cpp
#include <hip/hip_runtime.h>
#include <hip/hip_bf16.h>
#include <stdint.h>
#include <utility>

typedef unsigned int u32;
typedef unsigned short u16;
typedef u32 u32x4 __attribute__((ext_vector_type(4)));
typedef u32 u32x2 __attribute__((ext_vector_type(2)));
typedef float f32x4 __attribute__((ext_vector_type(4)));
typedef __bf16 bf16x8 __attribute__((ext_vector_type(8)));
typedef _Float16 f16x8 __attribute__((ext_vector_type(8)));

#define L_SEQ 2048
#define D_CH 1024
#define N_MODE 128

__device__ __forceinline__ float bflo(u32 d) { return __uint_as_float(d << 16); }
__device__ __forceinline__ float bfhi(u32 d) { return __uint_as_float(d & 0xffff0000u); }
__device__ __forceinline__ float bf2f(u16 h) { return __uint_as_float(((u32)h) << 16); }
__device__ __forceinline__ u32 pk_bf16(float lo, float hi) {
  typedef __bf16 bf16x2 __attribute__((ext_vector_type(2)));
  typedef float f32x2 __attribute__((ext_vector_type(2)));
  f32x2 v = {lo, hi};
  bf16x2 r = __builtin_convertvector(v, bf16x2);
  return __builtin_bit_cast(u32, r);
}
__device__ __forceinline__ float rbf(float x) { return __uint_as_float(pk_bf16(0.f, x)); }

__device__ __forceinline__ u32 wave_max_u32(u32 x) {
  x = max(x, (u32)__builtin_amdgcn_update_dpp(0, (int)x, 0x111, 0xf, 0xf, true));
  x = max(x, (u32)__builtin_amdgcn_update_dpp(0, (int)x, 0x112, 0xf, 0xf, true));
  x = max(x, (u32)__builtin_amdgcn_update_dpp(0, (int)x, 0x114, 0xf, 0xf, true));
  x = max(x, (u32)__builtin_amdgcn_update_dpp(0, (int)x, 0x118, 0xf, 0xf, true));
  x = max(x, (u32)__builtin_amdgcn_update_dpp(0, (int)x, 0x142, 0xa, 0xf, true));
  x = max(x, (u32)__builtin_amdgcn_update_dpp(0, (int)x, 0x143, 0xc, 0xf, true));
  return (u32)__builtin_amdgcn_readlane((int)x, 63);
}


#define GT_ROW 288
#define GT_TAB (16 * GT_ROW)
#define KC_STRIDE 768

typedef float f32x2 __attribute__((ext_vector_type(2)));
__device__ __forceinline__ f32x2 cmul(f32x2 g, f32x2 e) {
  f32x2 t, o;
  asm("v_pk_mul_f32 %0, %1, %2 op_sel:[0,0] op_sel_hi:[0,1]" : "=v"(t) : "v"(g), "v"(e));
  asm("v_pk_fma_f32 %0, %1, %2, %3 op_sel:[1,1,0] op_sel_hi:[1,0,1] neg_lo:[1,0,0]" : "=v"(o) : "v"(g), "v"(e), "v"(t));
  return o;
}
__device__ __forceinline__ f32x2 cexp_pos(float ar, float ai, float pos) {
  const float LOG2E = 1.44269504088896340736f;
  const float I2PI_HI = 0.15915494309189533577f;
  const float I2PI_LO = 6.4206383e-9f;
  float xr = ar * pos;
  float xi = ai * pos;
  float e = __builtin_amdgcn_exp2f(xr * LOG2E);
  float r = xi * I2PI_HI;
  float err = __builtin_fmaf(xi, I2PI_HI, -r);
  float f = __builtin_amdgcn_fractf(r);
  f += __builtin_fmaf(xi, I2PI_LO, err);
  f32x2 o;
  o[0] = e * __builtin_amdgcn_cosf(f);
  o[1] = e * __builtin_amdgcn_sinf(f);
  return o;
}
__device__ __forceinline__ void split_h(float a, float b, u32& hi, u32& lo) {
  hi = __builtin_bit_cast(u32, __builtin_amdgcn_cvt_pkrtz(a, b));
  u32 l;
  asm("v_fma_mixlo_f16 %0, %1, -1.0, %2 op_sel_hi:[1,0,0]" : "=v"(l) : "v"(hi), "v"(a));
  asm("v_fma_mixhi_f16 %0, %1, -1.0, %2 op_sel:[1,0,0] op_sel_hi:[1,0,0]" : "+v"(l) : "v"(hi), "v"(b));
  lo = l;
}

template <bool IS_A>
__device__ __forceinline__ f32x4 mma(u32x4 a, u32x4 b, f32x4 c) {
  if (IS_A) return __builtin_amdgcn_mfma_f32_16x16x32_f16(__builtin_bit_cast(f16x8, a), __builtin_bit_cast(f16x8, b), c, 0, 0, 0);
  else return __builtin_amdgcn_mfma_f32_16x16x32_bf16(__builtin_bit_cast(bf16x8, a), __builtin_bit_cast(bf16x8, b), c, 0, 0, 0);
}

__device__ __forceinline__ u32 lds_off(const char* p) {
  return (u32)(uintptr_t)(const __attribute__((address_space(3))) char*)p;
}
__device__ __forceinline__ void gen_partial(char* tab, int lane, u16 p_ldt, u16 p_lar, u16 p_aim, u16 p_cre, u16 p_cim,
                                            f32x4 (&acc)[3]) {
  const float dt = rbf(expf(bf2f(p_ldt)));
  const float ar = -rbf(expf(bf2f(p_lar))) * dt;
  const float ai = bf2f(p_aim) * dt;
  f32x2 C;
  C[0] = 2.0f * (bf2f(p_cre) * dt);
  C[1] = -2.0f * (bf2f(p_cim) * dt);
  const u32 mc = wave_max_u32(max(__float_as_uint(C[0]) & 0x7fffffffu, __float_as_uint(C[1]) & 0x7fffffffu));
  const f32x2 e1 = cexp_pos(ar, -ai, 1.0f);
  const f32x2 f8 = cexp_pos(ar, ai, 8.0f);
  const f32x2 E = cexp_pos(ar, -ai, 128.0f);
  const int ec = (mc > 0u && mc < 0x7f800000u) ? __builtin_amdgcn_frexp_expf(__uint_as_float(mc)) - 14 : 0;
  C[0] = __builtin_ldexpf(C[0], -ec) * 1.0003662109375f;
  C[1] = __builtin_ldexpf(C[1], -ec) * 1.0003662109375f;
  char* wp = tab + 4 * lane;
  const int c16 = lane & 15, q = lane >> 4;
  const char* rp = tab + c16 * GT_ROW + 16 * q;
  {
    f32x2 F = {16384.0f, 0.f};
#pragma unroll
    for (int w = 0; w < 16; w++) {
      u32 hi_, lo_;
      split_h(F[0], F[1], hi_, lo_);
      *(u32*)(wp + w * GT_ROW) = hi_;
      *(u32*)(wp + w * GT_ROW + GT_TAB) = lo_;
      if (w < 15) F = cmul(F, f8);
    }
  }
  __builtin_amdgcn_wave_barrier();
  u32x4 Fh[4], Fl[4];
#pragma unroll
  for (int ks = 0; ks < 4; ks++) {
    Fh[ks] = *(const u32x4*)(rp + 64 * ks);
    Fl[ks] = *(const u32x4*)(rp + 64 * ks + GT_TAB);
  }
  __builtin_amdgcn_wave_barrier();
#define PUT_ROW(tb, row, z) *(u32*)(wp + (tb) * GT_TAB + (row) * GT_ROW) = __builtin_bit_cast(u32, __builtin_amdgcn_cvt_pkrtz((z)[0], (z)[1]))
#define TILE_MMA(ti, tb) { __builtin_amdgcn_wave_barrier();                                          \
    f32x4 a_ = {0.f, 0.f, 0.f, 0.f}, b_ = {0.f, 0.f, 0.f, 0.f};                                    \
    _Pragma("unroll") for (int ks = 0; ks < 4; ks++) {                                             \
      u32x4 Gh = *(const u32x4*)(rp + 64 * ks + (tb) * GT_TAB);                                    \
      b_ = mma<true>(Gh, Fl[ks], b_);                                                              \
      a_ = mma<true>(Gh, Fh[ks], a_);                                                              \
    }                                                                                              \
    acc[ti] = a_ + b_; __builtin_amdgcn_wave_barrier(); }
  f32x2 g[8];
  g[0] = C;
#pragma unroll
  for (int be = 1; be < 8; be++) g[be] = cmul(g[be - 1], e1);
#pragma unroll
  for (int be = 0; be < 8; be++) PUT_ROW(0, be, g[be]);
#pragma unroll
  for (int be = 0; be < 8; be++) { g[be] = cmul(g[be], E); PUT_ROW(0, 8 + be, g[be]); }
  TILE_MMA(0, 0);
#pragma unroll
  for (int al = 0; al < 2; al++)
#pragma unroll
    for (int be = 0; be < 8; be += 2) { g[be] = cmul(g[be], E); PUT_ROW(1, 4 * al + (be >> 1), g[be]); }
#pragma unroll
  for (int al = 0; al < 4; al++)
#pragma unroll
    for (int be = 0; be < 8; be += 4) { g[be] = cmul(g[be], E); PUT_ROW(1, 8 + 2 * al + (be >> 2), g[be]); }
  TILE_MMA(1, 1);
#pragma unroll
  for (int al = 0; al < 9; al++) { g[0] = cmul(g[0], E); PUT_ROW(0, al, g[0]); }
  TILE_MMA(2, 0);
#pragma unroll
  for (int i = 0; i < 3; i++)
#pragma unroll
    for (int rg = 0; rg < 4; rg++) acc[i][rg] = __builtin_ldexpf(acc[i][rg], ec - 14);
}


#define PW_BYTES (2 * GT_TAB)
#define PREP_LDS (8 * PW_BYTES + 12288)

__global__ __launch_bounds__(512) void prep_kernel(
    const u16* __restrict__ u, const u16* __restrict__ lar, const u16* __restrict__ aim,
    const u16* __restrict__ cre, const u16* __restrict__ cim, const u16* __restrict__ ldt,
    const u16* __restrict__ kin, const u16* __restrict__ vin, const u16* __restrict__ shk,
    u16* __restrict__ uT, u16* __restrict__ kvT, u16* __restrict__ kcg, int* __restrict__ hexp) {
  extern __shared__ __attribute__((aligned(16))) char smem[];
  const int b = blockIdx.x;
  const int t = threadIdx.x;
  const int lane = t & 63;
  const int wv = __builtin_amdgcn_readfirstlane(t >> 6);
  const int xcd = b & 7, idx = b >> 3;
  const int cb = xcd + 8 * (idx & 1);
  const int d = cb * 64 + (idx >> 1) * 4 + (wv & 3);
  const int po = d * N_MODE + (wv >> 2) * 64 + lane;
  const u16 p_ldt = ldt[d], p_lar = lar[po], p_aim = aim[po], p_cre = cre[po], p_cim = cim[po];
  __builtin_amdgcn_sched_barrier(0);
  const int c0 = cb * 64 + (wv & 1) * 32 + (lane & 15) * 2;
  const int l0 = (idx >> 1) * 128 + (wv >> 1) * 32 + (lane >> 4) * 8;
  const __amdgpu_buffer_rsrc_t rk = __builtin_amdgcn_make_buffer_rsrc((void*)kin, 0, L_SEQ * D_CH * 2, 0x00020000);
  const int vo = l0 * (D_CH * 2) + c0 * 2;
  u32 uu[8], kk[11], vv[8];
#pragma unroll
  for (int i = 0; i < 8; i++) uu[i] = *(const u32*)((const char*)u + vo + i * (D_CH * 2));
#pragma unroll
  for (int i = 0; i < 11; i++) {
    int o = vo + (i - 3) * (D_CH * 2);
    if (i < 3) o = (l0 > 0) ? o : (int)0x40000000;
    kk[i] = __builtin_amdgcn_raw_buffer_load_b32(rk, o, 0, 0);
  }
#pragma unroll
  for (int i = 0; i < 8; i++) vv[i] = *(const u32*)((const char*)vin + vo + i * (D_CH * 2));
  const u32x4 sw = *(const u32x4*)(shk + (size_t)c0 * 4);
  __builtin_amdgcn_sched_barrier(0);
  f32x4 ka[3];
  char* tab = smem + wv * PW_BYTES;
  gen_partial(tab, lane, p_ldt, p_lar, p_aim, p_cre, p_cim, ka);
  if (wv >= 4) {
#pragma unroll
    for (int i = 0; i < 3; i++) *(f32x4*)(tab + 1024 * i + 16 * lane) = ka[i];
  }
  __builtin_amdgcn_sched_barrier(0);
#pragma unroll
  for (int c = 0; c < 2; c++) {
    u32x4 o;
#pragma unroll
    for (int p = 0; p < 4; p++) o[p] = __builtin_amdgcn_perm(uu[2 * p + 1], uu[2 * p], c ? 0x07060302u : 0x05040100u);
    *(u32x4*)(uT + (size_t)(c0 + c) * L_SEQ + l0) = o;
  }
#pragma unroll
  for (int c = 0; c < 2; c++) {
    const u32 s01 = sw[2 * c], s23 = sw[2 * c + 1];
    const float w0 = bflo(s01), w1 = bfhi(s01), w2 = bflo(s23), w3 = bfhi(s23);
    float kf[11];
#pragma unroll
    for (int i = 0; i < 11; i++) kf[i] = c ? bfhi(kk[i]) : bflo(kk[i]);
    float res[8];
#pragma unroll
    for (int i = 0; i < 8; i++) {
      float a = rbf(rbf(kf[i] * w3) + rbf(kf[i + 1] * w2));
      a = rbf(a + rbf(kf[i + 2] * w1));
      a = rbf(a + rbf(kf[i + 3] * w0));
      res[i] = a * (c ? bfhi(vv[i]) : bflo(vv[i]));
    }
    u32x4 o;
#pragma unroll
    for (int p = 0; p < 4; p++) o[p] = pk_bf16(res[7 - 2 * p], res[6 - 2 * p]);
    *(u32x4*)(kvT + (size_t)(c0 + c) * L_SEQ + l0) = o;
  }
  asm volatile("s_waitcnt lgkmcnt(0)\n\ts_barrier" ::: "memory");
  if (wv >= 4) return;
  {
    const char* ex = tab + 4 * PW_BYTES + 16 * lane;
#pragma unroll
    for (int i = 0; i < 3; i++) ka[i] += *(const f32x4*)(ex + 1024 * i);
    const int c16 = lane & 15, q = lane >> 4;
    const bool v2 = q < 2, v28 = (q == 2) && (c16 == 0);
    u32 mk = 0;
#pragma unroll
    for (int rg = 0; rg < 4; rg++) {
      mk = max(mk, max(__float_as_uint(ka[0][rg]) & 0x7fffffffu, __float_as_uint(ka[1][rg]) & 0x7fffffffu));
      u32 m2 = __float_as_uint(ka[2][rg]) & 0x7fffffffu;
      mk = max(mk, (v2 || (rg == 0 && v28)) ? m2 : 0u);
    }
    mk = wave_max_u32(mk);
    const int es = (mk > 0u && mk < 0x7f800000u) ? __builtin_amdgcn_frexp_expf(__uint_as_float(mk)) - 15 : 0;
    u16* kc = (u16*)tab;
#pragma unroll
    for (int rg = 0; rg < 4; rg++) {
      const int row = 4 * q + rg;
      kc[128 * (row >> 3) + (row & 7) + 8 * c16] = __builtin_bit_cast(u16, (_Float16)__builtin_ldexpf(ka[0][rg], -es));
      int i1 = (row < 8) ? 256 + 64 * (row >> 2) + (row & 3) + 4 * c16 : 384 + 32 * ((row - 8) >> 1) + (row & 1) + 2 * c16;
      kc[i1] = __builtin_bit_cast(u16, (_Float16)__builtin_ldexpf(ka[1][rg], -es));
      kc[512 + 16 * row + c16] = __builtin_bit_cast(u16, (_Float16)__builtin_ldexpf(ka[2][rg], -es));
    }
    __builtin_amdgcn_wave_barrier();
    u16* dst = kcg + (size_t)d * KC_STRIDE;
    *(u32x4*)(dst + 8 * lane) = *(const u32x4*)(tab + 16 * lane);
    if (lane < 32) *(u32x4*)(dst + 512 + 8 * lane) = *(const u32x4*)(tab + 1024 + 16 * lane);
    if (lane == 0) hexp[d] = es;
  }
}

#define XS_BYTES 4608
#define HC_BYTES 2368
#define HS_BYTES (4 * HC_BYTES)
#define W_BYTES (XS_BYTES + HS_BYTES)
#define YS_OFF (8 * W_BYTES)
#define YS_BYTES 2064
#define LDS_BYTES (YS_OFF + 8 * YS_BYTES)

__device__ __forceinline__ int xcd_group(int b) {
  int xcd = b & 7, idx = b >> 3;
  return ((xcd + 8 * (idx >> 3)) << 3) | (idx & 7);
}

template <class F, int... I>
__device__ __forceinline__ void sfor_impl(F&& f, std::integer_sequence<int, I...>) { (f(std::integral_constant<int, I>{}), ...); }
template <int N, class F>
__device__ __forceinline__ void sfor(F&& f) { sfor_impl(f, std::make_integer_sequence<int, N>{}); }
constexpr int p1a(int k) { return k < 4 ? 0 : k < 7 ? 1 : k < 9 ? 2 : 3; }
constexpr int p1e(int k) { return k < 4 ? k : k < 7 ? k - 4 : k < 9 ? k - 7 : 0; }
constexpr int p2a(int k) { return k < 20 ? k / 4 : k < 23 ? 5 : k < 25 ? 6 : 7; }
constexpr int p2e(int k) { return k < 20 ? 4 - k / 4 + (k & 3) : k < 23 ? k - 20 : k < 25 ? k - 23 : 0; }

template <int OFF> __device__ __forceinline__ void dsr64(u32x2& dst, u32 addr) {
  asm volatile("ds_read_b64 %0, %1 offset:%2" : "=v"(dst) : "v"(addr), "i"(OFF) : "memory");
}
template <int OFF> __device__ __forceinline__ void dsr128(u32x4& dst, u32 addr) {
  asm volatile("ds_read_b128 %0, %1 offset:%2" : "=v"(dst) : "v"(addr), "i"(OFF) : "memory");
}
#define DSR64(dst, addr, off) dsr64<(off)>(dst, addr)
#define DSR128(dst, addr, off) dsr128<(off)>(dst, addr)
#define WAIT_FRAGS(AL, AH, B)                                                                         \
  __builtin_amdgcn_sched_barrier(0);                                                                  \
  asm volatile("s_waitcnt lgkmcnt(0)"                                                                 \
               : "+v"(AL[0]), "+v"(AL[1]), "+v"(AL[2]), "+v"(AL[3]), "+v"(AL[4]), "+v"(AL[5]), "+v"(AL[6]), "+v"(AL[7]), \
                 "+v"(AH[0]), "+v"(AH[1]), "+v"(AH[2]), "+v"(AH[3]), "+v"(AH[4]), "+v"(AH[5]), "+v"(AH[6]), "+v"(AH[7]), \
                 "+v"(B[0]), "+v"(B[1]), "+v"(B[2]), "+v"(B[3]), "+v"(B[4]), "+v"(B[5]), "+v"(B[6]), "+v"(B[7])  \
               :: "memory");                                                                          \
  __builtin_amdgcn_sched_barrier(0);
#define WAIT_FRAGS4(AL, AH, B)                                                                        \
  __builtin_amdgcn_sched_barrier(0);                                                                  \
  asm volatile("s_waitcnt lgkmcnt(0)"                                                                 \
               : "+v"(AL[0]), "+v"(AL[1]), "+v"(AL[2]), "+v"(AL[3]),                                  \
                 "+v"(AH[0]), "+v"(AH[1]), "+v"(AH[2]), "+v"(AH[3]),                                  \
                 "+v"(B[0]), "+v"(B[1]), "+v"(B[2]), "+v"(B[3])                                       \
               :: "memory");                                                                          \
  __builtin_amdgcn_sched_barrier(0);

template <bool IS_A>
__device__ __forceinline__ void conv_body(char* smem, int g, const u16* __restrict__ xT, const u16* __restrict__ hT,
                                          const int* __restrict__ hexp, const u16* __restrict__ dvec,
                                          const u16* __restrict__ qin, u16* __restrict__ out) {
  const int t = threadIdx.x, lane = t & 63;
  const int w = __builtin_amdgcn_readfirstlane(t >> 6);
  const int ch = (g & 127) * 8 + w;
  char* xs = smem + w * W_BYTES;
  char* hs = xs + XS_BYTES;
  const u16* xrow = xT + (size_t)ch * L_SEQ;
  u32x4 hd[6];
  u32x2 he[6];
  const int hl = lane < 48 ? lane : lane - 48;
  const int ha = hl / 6, hi_ = hl - 6 * ha;
  u32x4 kc0, kc1;
  int esc = 0;
  if (IS_A) {
    const u16* krow = hT + (size_t)ch * KC_STRIDE;
    kc0 = *(const u32x4*)(krow + 8 * lane);
    kc1 = *(const u32x4*)(krow + 512 + 8 * (lane & 31));
    esc = hexp[ch];
  } else {
    const __amdgpu_buffer_rsrc_t rh = __builtin_amdgcn_make_buffer_rsrc((void*)(hT + (size_t)ch * L_SEQ), 0, L_SEQ * 2, 0x00020000);
#pragma unroll
    for (int s = 0; s < 6; s++) {
      const int hc = 32 * ha + 16 * (s / 3) + 6 * (s % 3) + hi_ - 2;
      int o1 = 16 * hc, o2 = 16 * hc + 16;
      if (s == 0) { o1 = hc >= 0 ? o1 : 0x40000000; o2 = hc >= -1 ? o2 : 0x40000000; }
      hd[s] = __builtin_amdgcn_raw_buffer_load_b128(rh, o1, 0, 0);
      he[s] = __builtin_amdgcn_raw_buffer_load_b64(rh, o2, 0, 0);
    }
  }
  u32x4 xr[4];
#pragma unroll
  for (int j = 0; j < 4; j++) xr[j] = *(const u32x4*)(xrow + 8 * (64 * j + lane));
  const u16 p_d = dvec[ch];
  __builtin_amdgcn_sched_barrier(0);
  if (IS_A) {
    char* kb = hs;
    char* lin = xs;
    const u16* kc = (const u16*)kb;
    *(u32x4*)(kb + 16 * lane) = kc0;
    *(u32x4*)(kb + 1024 + 16 * (lane & 31)) = kc1;
    __builtin_amdgcn_wave_barrier();
    const bool dir = lane < 32;
    const int a0 = dir ? 16 * lane : 512 + 8 * (lane - 32);
    u32x2 d01 = *(const u32x2*)(kb + a0), d23 = *(const u32x2*)(kb + a0 + 8);
    u32x4 o;
    o[0] = __builtin_amdgcn_perm(d01[0], d01[0], dir ? 0x03020100u : 0x01000100u);
    o[1] = __builtin_amdgcn_perm(d01[1], d01[0], dir ? 0x07060504u : 0x05040302u);
    o[2] = __builtin_amdgcn_perm(d23[0], d01[1], dir ? 0x07060504u : 0x01000100u);
    o[3] = dir ? d23[1] : __builtin_amdgcn_perm(d23[0], d01[1], 0x05040302u);
    *(u32x4*)(lin + 32 + 16 * lane) = o;
    u32 d0 = *(const u32*)(kb + 768 + 4 * lane), d1 = *(const u32*)(kb + 772 + 4 * lane);
    o[0] = __builtin_amdgcn_perm(d0, d0, 0x01000100u);
    o[1] = d0;
    o[2] = __builtin_amdgcn_perm(d0, d0, 0x03020302u);
    o[3] = __builtin_amdgcn_perm(d1, d1, 0x01000100u);
    *(u32x4*)(lin + 32 + 1024 + 16 * lane) = o;
#pragma unroll
    for (int i = 0; i < 2; i++) {
      u32 x0 = kc[512 + 64 * i + lane], x1 = kc[513 + 64 * i + lane];
      o[0] = o[1] = x0 | (x0 << 16);
      o[3] = x1 | (x1 << 16);
      o[2] = (lane & 1) ? o[3] : (x0 | (x1 << 16));
      *(u32x4*)(lin + 32 + 2048 + 1024 * i + 16 * lane) = o;
    }
    if (lane < 4) *(u32x4*)(lin + ((lane & 2) ? 4096 : 0) + 16 * lane) = (u32x4){0, 0, 0, 0};
    __builtin_amdgcn_wave_barrier();
#pragma unroll
    for (int s = 0; s < 6; s++) {
      const int hc = 32 * ha + 16 * (s / 3) + 6 * (s % 3) + hi_;
      hd[s] = *(const u32x4*)(lin + 16 * hc);
      he[s] = *(const u32x2*)(lin + 16 * hc + 16);
    }
    __builtin_amdgcn_wave_barrier();
  }
#define STAGE_PIECE(hwp, hf, k) {                                                      \
    const u32x4 d4 = hd[3 * (hf) + ((k) >> 2)];                                        \
    const u32x2 e2 = he[3 * (hf) + ((k) >> 2)];                                        \
    u32x4 o_;                                                                          \
    if (((k) & 3) == 0) o_ = d4;                                                       \
    else if (((k) & 3) == 1) o_ = (u32x4){__builtin_amdgcn_alignbit(d4[1], d4[0], 16), __builtin_amdgcn_alignbit(d4[2], d4[1], 16), \
                                          __builtin_amdgcn_alignbit(d4[3], d4[2], 16), __builtin_amdgcn_alignbit(e2[0], d4[3], 16)}; \
    else if (((k) & 3) == 2) o_ = (u32x4){d4[1], d4[2], d4[3], e2[0]};                 \
    else o_ = (u32x4){__builtin_amdgcn_alignbit(d4[2], d4[1], 16), __builtin_amdgcn_alignbit(d4[3], d4[2], 16), \
                      __builtin_amdgcn_alignbit(e2[0], d4[3], 16), __builtin_amdgcn_alignbit(e2[1], e2[0], 16)}; \
    *(u32x4*)((hwp) + 96 * ((k) >> 2) + ((k) & 3) * HC_BYTES) = o_; }
  char* hw = hs + ha * 288 + hi_ * 16;
  if (ha < 4) {
#pragma unroll
    for (int k = 0; k < 12; k++) STAGE_PIECE(hw, 0, k);
#pragma unroll
    for (int k = 0; k < 12; k++) STAGE_PIECE(hw + 4 * 288, 1, k);
  }
  if (lane < 30) *(u32x4*)(xs + 16 * lane) = (u32x4){0, 0, 0, 0};
  if (IS_A) {
    u32 mx = 0;
#pragma unroll
    for (int j = 0; j < 4; j++)
#pragma unroll
      for (int p = 0; p < 4; p++) {
        u32 dd = xr[j][p];
        mx = max(mx, max(dd & 0x7fffu, (dd >> 16) & 0x7fffu));
      }
    mx = wave_max_u32(mx);
    const int ex = (int)(mx >> 7) - 127 - 14;
    esc += ex;
#pragma unroll
    for (int j = 0; j < 4; j++) {
      u32x4 o;
#pragma unroll
      for (int p = 0; p < 4; p++) {
        u32 dd = xr[j][3 - p];
        float lo = __builtin_ldexpf(bfhi(dd), -ex), hi = __builtin_ldexpf(bflo(dd), -ex);
        o[p] = __builtin_bit_cast(u32, __builtin_amdgcn_cvt_pkrtz(lo, hi));
      }
      *(u32x4*)(xs + 480 + 16 * (64 * j + lane)) = o;
    }
  } else {
#pragma unroll
    for (int j = 0; j < 4; j++) *(u32x4*)(xs + 480 + 16 * (64 * j + lane)) = xr[j];
  }
  const int r = lane & 15, q = lane >> 4;
  f32x4 acc[4];
#pragma unroll
  for (int i = 0; i < 4; i++) acc[i] = (f32x4){0.f, 0.f, 0.f, 0.f};
  const int lam = 25 + r - 8 * q;
  const u32 abase = lds_off(hs) + (lam & 3) * HC_BYTES + 2 * (lam & ~3);
  const u32 bbase = lds_off(xs) + 32 * r + 16 * q;
  u32x2 AL0[8], AH0[8], AL1[8], AH1[8];
  u32x4 B0[8], B1[8];
#define LOAD_PIECE(it, sl, k, AL, AH, B) {                                                      \
    if constexpr ((k) % 3 == 0) DSR64(AL[(k) / 3], abase, 64 * ((it) & 3) + 288 * ((k) / 3 + (sl)));      \
    else if constexpr ((k) % 3 == 1) DSR64(AH[(k) / 3], abase, 64 * ((it) & 3) + 288 * ((k) / 3 + (sl)) + 8); \
    else DSR128(B[(k) / 3], bbase, 448 + 512 * ((k) / 3) - 64 * (it)); }
  __builtin_amdgcn_sched_barrier(0);
  sfor<12>([&](auto kc_) { constexpr int k = decltype(kc_)::value; LOAD_PIECE(0, 0, k, AL0, AH0, B0); });
  __builtin_amdgcn_sched_barrier(0);
  u32x4 qv[4];
  u32x2 uv[8];
  if (IS_A) {
#pragma unroll
    for (int ct = 0; ct < 8; ct++) uv[ct] = *(const u32x2*)(xrow + 256 * ct + 16 * r + 4 * q);
  } else {
#pragma unroll
    for (int i = 0; i < 4; i++) qv[i] = *(const u32x4*)(qin + (size_t)(1024 * (i >> 1) + 2 * t + (i & 1)) * D_CH + (g & 127) * 8);
  }
#define PIN_ACC asm volatile("" : "+v"(acc[0]), "+v"(acc[1]), "+v"(acc[2]), "+v"(acc[3]));
#define ITER1(it, AL, AH, B, NAL, NAH, NB)                                              \
  WAIT_FRAGS4(AL, AH, B);                                                               \
  sfor<10>([&](auto kc_) {                                                              \
    constexpr int k_ = decltype(kc_)::value, a = p1a(k_), e = p1e(k_);                  \
    acc[a + e] = mma<IS_A>((u32x4){AL[a][0], AL[a][1], AH[a][0], AH[a][1]}, B[e], acc[a + e]); \
    if constexpr ((it) < 7) {                                                           \
      LOAD_PIECE((it) + 1, (((it) + 1) >> 2) * 4, k_, NAL, NAH, NB);                    \
      if constexpr (k_ < 2) LOAD_PIECE((it) + 1, (((it) + 1) >> 2) * 4, 10 + k_, NAL, NAH, NB); \
    }                                                                                   \
    __builtin_amdgcn_sched_barrier(0);                                                  \
  });                                                                                   \
  PIN_ACC
#define ITER2(it, AL, AH, B, NAL, NAH, NB)                                              \
  WAIT_FRAGS(AL, AH, B);                                                                \
  sfor<26>([&](auto kc_) {                                                              \
    constexpr int k_ = decltype(kc_)::value, a = p2a(k_), e = p2e(k_);                  \
    acc[a + e - 4] = mma<IS_A>((u32x4){AL[a][0], AL[a][1], AH[a][0], AH[a][1]}, B[e], acc[a + e - 4]); \
    if constexpr ((it) == 2) {                                                          \
      if constexpr (k_ < 12) { LOAD_PIECE(3, 0, 2 * k_, NAL, NAH, NB); LOAD_PIECE(3, 0, 2 * k_ + 1, NAL, NAH, NB); } \
      else if constexpr (k_ < 24) STAGE_PIECE(hw, 1, k_ - 12);                          \
    } else if constexpr ((it) < 7 && k_ < 24) LOAD_PIECE((it) + 1, 0, k_, NAL, NAH, NB); \
    __builtin_amdgcn_sched_barrier(0);                                                  \
  });                                                                                   \
  PIN_ACC
  const float dsk = bf2f(p_d);
#define EPI1(c0_, ysp)                                                                  \
  _Pragma("unroll") for (int ci = 0; ci < 4; ci++) {                                    \
    const int l = 256 * ((c0_) + ci) + 16 * r + 4 * q;                                  \
    float x0, x1, x2, x3;                                                               \
    if (IS_A) {                                                                         \
      u32x2 xv = uv[(c0_) + ci];                                                        \
      x0 = bflo(xv[0]); x1 = bfhi(xv[0]); x2 = bflo(xv[1]); x3 = bfhi(xv[1]);           \
    } else {                                                                            \
      u32x2 xv = *(const u32x2*)(xs + 480 + 2 * ((l & ~7) + 4 - (l & 4)));              \
      x3 = bflo(xv[0]); x2 = bfhi(xv[0]); x1 = bflo(xv[1]); x0 = bfhi(xv[1]);           \
    }                                                                                   \
    float y0 = acc[ci][0], y1 = acc[ci][1], y2 = acc[ci][2], y3 = acc[ci][3];           \
    if (IS_A) {                                                                         \
      y0 = __builtin_ldexpf(y0, esc); y1 = __builtin_ldexpf(y1, esc);                   \
      y2 = __builtin_ldexpf(y2, esc); y3 = __builtin_ldexpf(y3, esc);                   \
    }                                                                                   \
    u32x2 o;                                                                            \
    o[0] = pk_bf16(rbf(y0) + rbf(x0 * dsk), rbf(y1) + rbf(x1 * dsk));                   \
    o[1] = pk_bf16(rbf(y2) + rbf(x2 * dsk), rbf(y3) + rbf(x3 * dsk));                   \
    *(u32x2*)((ysp) + 2 * (l & 1023)) = o;                                              \
  }
#define EPI2(hf, ysb, ystride)                                                          \
  {                                                                                     \
    u32 yv[8];                                                                          \
    _Pragma("unroll") for (int c = 0; c < 8; c++) yv[c] = *(const u32*)((ysb) + c * (ystride) + 4 * t); \
    _Pragma("unroll") for (int i = 0; i < 2; i++) {                                     \
      u32x4 o;                                                                          \
      _Pragma("unroll") for (int p = 0; p < 4; p++) {                                   \
        if (IS_A) o[p] = __builtin_amdgcn_perm(yv[2 * p + 1], yv[2 * p], i ? 0x07060302u : 0x05040100u); \
        else {                                                                          \
          float ya = i ? bfhi(yv[2 * p]) : bflo(yv[2 * p]), yb = i ? bfhi(yv[2 * p + 1]) : bflo(yv[2 * p + 1]); \
          o[p] = pk_bf16(ya * bflo(qv[2 * (hf) + i][p]), yb * bfhi(qv[2 * (hf) + i][p])); \
        }                                                                               \
      }                                                                                 \
      *(u32x4*)(out + (size_t)(1024 * (hf) + 2 * t + i) * (2 * D_CH) + g * 8) = o;      \
    }                                                                                   \
  }
  ITER1(0, AL0, AH0, B0, AL1, AH1, B1);
  ITER1(1, AL1, AH1, B1, AL0, AH0, B0);
  ITER1(2, AL0, AH0, B0, AL1, AH1, B1);
  ITER1(3, AL1, AH1, B1, AL0, AH0, B0);
  ITER1(4, AL0, AH0, B0, AL1, AH1, B1);
  ITER1(5, AL1, AH1, B1, AL0, AH0, B0);
  ITER1(6, AL0, AH0, B0, AL1, AH1, B1);
  ITER1(7, AL1, AH1, B1, AL0, AH0, B0);
#pragma unroll
  for (int k = 0; k < 12; k++) STAGE_PIECE(hw, 0, k);
  __builtin_amdgcn_sched_barrier(0);
  sfor<24>([&](auto kc_) { constexpr int k = decltype(kc_)::value; LOAD_PIECE(0, 0, k, AL0, AH0, B0); });
  __builtin_amdgcn_sched_barrier(0);
  EPI1(0, smem + YS_OFF + w * YS_BYTES);
#pragma unroll
  for (int i = 0; i < 4; i++) acc[i] = (f32x4){0.f, 0.f, 0.f, 0.f};
  asm volatile("s_waitcnt lgkmcnt(0)\n\ts_barrier" ::: "memory");
  EPI2(0, smem + YS_OFF, YS_BYTES);
  ITER2(0, AL0, AH0, B0, AL1, AH1, B1);
  ITER2(1, AL1, AH1, B1, AL0, AH0, B0);
  ITER2(2, AL0, AH0, B0, AL1, AH1, B1);
  ITER2(3, AL1, AH1, B1, AL0, AH0, B0);
  ITER2(4, AL0, AH0, B0, AL1, AH1, B1);
  ITER2(5, AL1, AH1, B1, AL0, AH0, B0);
  ITER2(6, AL0, AH0, B0, AL1, AH1, B1);
  ITER2(7, AL1, AH1, B1, AL0, AH0, B0);
  EPI1(4, hs);
  asm volatile("s_waitcnt lgkmcnt(0)\n\ts_barrier" ::: "memory");
  EPI2(1, smem + XS_BYTES, W_BYTES);
}

__global__ __launch_bounds__(512) void conv_kernel(const u16* __restrict__ uT, const u16* __restrict__ kvT,
                                                   const u16* __restrict__ kcg, const int* __restrict__ hexp,
                                                   const u16* __restrict__ dskip, const u16* __restrict__ ssmk,
                                                   const u16* __restrict__ ssmd, const u16* __restrict__ qin,
                                                   u16* __restrict__ out) {
  extern __shared__ __attribute__((aligned(16))) char smem[];
  const int g = xcd_group(blockIdx.x);
  if (g < 128) conv_body<true>(smem, g, uT, kcg, hexp, dskip, qin, out);
  else conv_body<false>(smem, g, kvT, ssmk, hexp, ssmd, qin, out);
}

extern "C" void kernel_launch(void* const* d_in, const int* in_sizes, int n_in, void* d_out, int out_size,
                              void* d_ws, size_t ws_size, hipStream_t stream) {
  const u16* u = (const u16*)d_in[0];
  const u16* lar = (const u16*)d_in[1];
  const u16* aim = (const u16*)d_in[2];
  const u16* cre = (const u16*)d_in[3];
  const u16* cim = (const u16*)d_in[4];
  const u16* ldt = (const u16*)d_in[5];
  const u16* dsk = (const u16*)d_in[6];
  const u16* q = (const u16*)d_in[7];
  const u16* k = (const u16*)d_in[8];
  const u16* v = (const u16*)d_in[9];
  const u16* shk = (const u16*)d_in[10];
  const u16* ssmk = (const u16*)d_in[11];
  const u16* ssmd = (const u16*)d_in[12];
  char* ws = (char*)d_ws;
  u16* uT = (u16*)ws;
  u16* kvT = (u16*)(ws + (size_t)4 * 1024 * 1024);
  u16* kcg = (u16*)(ws + (size_t)8 * 1024 * 1024);
  int* hexp = (int*)(ws + (size_t)12 * 1024 * 1024);
  (void)hipFuncSetAttribute((const void*)prep_kernel, hipFuncAttributeMaxDynamicSharedMemorySize, PREP_LDS);
  (void)hipFuncSetAttribute((const void*)conv_kernel, hipFuncAttributeMaxDynamicSharedMemorySize, LDS_BYTES);
  prep_kernel<<<256, 512, PREP_LDS, stream>>>(u, lar, aim, cre, cim, ldt, k, v, shk, uT, kvT, kcg, hexp);
  conv_kernel<<<256, 512, LDS_BYTES, stream>>>(uT, kvT, kcg, hexp, dsk, ssmk, ssmd, q, (u16*)d_out);
}
```

```cpp
#include <hip/hip_runtime.h>
#include <hip/hip_bf16.h>
#include <stdint.h>
#include <utility>

typedef unsigned int u32;
typedef unsigned short u16;
typedef u32 u32x4 __attribute__((ext_vector_type(4)));
typedef u32 u32x2 __attribute__((ext_vector_type(2)));
typedef float f32x4 __attribute__((ext_vector_type(4)));
typedef __bf16 bf16x8 __attribute__((ext_vector_type(8)));
typedef _Float16 f16x8 __attribute__((ext_vector_type(8)));

#define ST_AUX 16
#define L_SEQ 2048
#define D_CH 1024
#define N_MODE 128

__device__ __forceinline__ float bflo(u32 d) { return __uint_as_float(d << 16); }
__device__ __forceinline__ float bfhi(u32 d) { return __uint_as_float(d & 0xffff0000u); }
__device__ __forceinline__ float bf2f(u16 h) { return __uint_as_float(((u32)h) << 16); }
__device__ __forceinline__ u32 pk_bf16(float lo, float hi) {
  typedef __bf16 bf16x2 __attribute__((ext_vector_type(2)));
  typedef float f32x2 __attribute__((ext_vector_type(2)));
  f32x2 v = {lo, hi};
  bf16x2 r = __builtin_convertvector(v, bf16x2);
  return __builtin_bit_cast(u32, r);
}
__device__ __forceinline__ float rbf(float x) { return __uint_as_float(pk_bf16(0.f, x)); }

__device__ __forceinline__ u32 wave_max_u32(u32 x) {
  x = max(x, (u32)__builtin_amdgcn_update_dpp(0, (int)x, 0x111, 0xf, 0xf, true));
  x = max(x, (u32)__builtin_amdgcn_update_dpp(0, (int)x, 0x112, 0xf, 0xf, true));
  x = max(x, (u32)__builtin_amdgcn_update_dpp(0, (int)x, 0x114, 0xf, 0xf, true));
  x = max(x, (u32)__builtin_amdgcn_update_dpp(0, (int)x, 0x118, 0xf, 0xf, true));
  x = max(x, (u32)__builtin_amdgcn_update_dpp(0, (int)x, 0x142, 0xa, 0xf, true));
  x = max(x, (u32)__builtin_amdgcn_update_dpp(0, (int)x, 0x143, 0xc, 0xf, true));
  return (u32)__builtin_amdgcn_readlane((int)x, 63);
}


#define GT_ROW 288
#define GT_TAB (16 * GT_ROW)
#define KC_STRIDE 768

typedef float f32x2 __attribute__((ext_vector_type(2)));
__device__ __forceinline__ f32x2 cmul(f32x2 g, f32x2 e) {
  f32x2 t, o;
  asm("v_pk_mul_f32 %0, %1, %2 op_sel:[0,0] op_sel_hi:[0,1]" : "=v"(t) : "v"(g), "v"(e));
  asm("v_pk_fma_f32 %0, %1, %2, %3 op_sel:[1,1,0] op_sel_hi:[1,0,1] neg_lo:[1,0,0]" : "=v"(o) : "v"(g), "v"(e), "v"(t));
  return o;
}
__device__ __forceinline__ f32x2 cexp_pos(float ar, float ai, float pos) {
  const float LOG2E = 1.44269504088896340736f;
  const float I2PI_HI = 0.15915494309189533577f;
  const float I2PI_LO = 6.4206383e-9f;
  float xr = ar * pos;
  float xi = ai * pos;
  float e = __builtin_amdgcn_exp2f(xr * LOG2E);
  float r = xi * I2PI_HI;
  float err = __builtin_fmaf(xi, I2PI_HI, -r);
  float f = __builtin_amdgcn_fractf(r);
  f += __builtin_fmaf(xi, I2PI_LO, err);
  f32x2 o;
  o[0] = e * __builtin_amdgcn_cosf(f);
  o[1] = e * __builtin_amdgcn_sinf(f);
  return o;
}
__device__ __forceinline__ void split_h(float a, float b, u32& hi, u32& lo) {
  hi = __builtin_bit_cast(u32, __builtin_amdgcn_cvt_pkrtz(a, b));
  u32 l;
  asm("v_fma_mixlo_f16 %0, %1, -1.0, %2 op_sel_hi:[1,0,0]" : "=v"(l) : "v"(hi), "v"(a));
  asm("v_fma_mixhi_f16 %0, %1, -1.0, %2 op_sel:[1,0,0] op_sel_hi:[1,0,0]" : "+v"(l) : "v"(hi), "v"(b));
  lo = l;
}

template <bool IS_A>
__device__ __forceinline__ f32x4 mma(u32x4 a, u32x4 b, f32x4 c) {
  if (IS_A) return __builtin_amdgcn_mfma_f32_16x16x32_f16(__builtin_bit_cast(f16x8, a), __builtin_bit_cast(f16x8, b), c, 0, 0, 0);
  else return __builtin_amdgcn_mfma_f32_16x16x32_bf16(__builtin_bit_cast(bf16x8, a), __builtin_bit_cast(bf16x8, b), c, 0, 0, 0);
}

__device__ __forceinline__ u32 lds_off(const char* p) {
  return (u32)(uintptr_t)(const __attribute__((address_space(3))) char*)p;
}
__device__ __forceinline__ void gen_partial(char* tab, int lane, u16 p_ldt, u16 p_lar, u16 p_aim, u16 p_cre, u16 p_cim,
                                            f32x4 (&acc)[3]) {
  const float dt = rbf(expf(bf2f(p_ldt)));
  const float ar = -rbf(expf(bf2f(p_lar))) * dt;
  const float ai = bf2f(p_aim) * dt;
  f32x2 C;
  C[0] = 2.0f * (bf2f(p_cre) * dt);
  C[1] = -2.0f * (bf2f(p_cim) * dt);
  const u32 mc = wave_max_u32(max(__float_as_uint(C[0]) & 0x7fffffffu, __float_as_uint(C[1]) & 0x7fffffffu));
  const f32x2 e1 = cexp_pos(ar, -ai, 1.0f);
  const f32x2 f8 = cexp_pos(ar, ai, 8.0f);
  const f32x2 E = cexp_pos(ar, -ai, 128.0f);
  const int ec = (mc > 0u && mc < 0x7f800000u) ? __builtin_amdgcn_frexp_expf(__uint_as_float(mc)) - 14 : 0;
  C[0] = __builtin_ldexpf(C[0], -ec) * 1.0003662109375f;
  C[1] = __builtin_ldexpf(C[1], -ec) * 1.0003662109375f;
  char* wp = tab + 4 * lane;
  const int c16 = lane & 15, q = lane >> 4;
  const char* rp = tab + c16 * GT_ROW + 16 * q;
  {
    f32x2 F = {16384.0f, 0.f};
#pragma unroll
    for (int w = 0; w < 16; w++) {
      u32 hi_, lo_;
      split_h(F[0], F[1], hi_, lo_);
      *(u32*)(wp + w * GT_ROW) = hi_;
      *(u32*)(wp + w * GT_ROW + GT_TAB) = lo_;
      if (w < 15) F = cmul(F, f8);
    }
  }
  __builtin_amdgcn_wave_barrier();
  u32x4 Fh[4], Fl[4];
#pragma unroll
  for (int ks = 0; ks < 4; ks++) {
    Fh[ks] = *(const u32x4*)(rp + 64 * ks);
    Fl[ks] = *(const u32x4*)(rp + 64 * ks + GT_TAB);
  }
  __builtin_amdgcn_wave_barrier();
#define PUT_ROW(tb, row, z) *(u32*)(wp + (tb) * GT_TAB + (row) * GT_ROW) = __builtin_bit_cast(u32, __builtin_amdgcn_cvt_pkrtz((z)[0], (z)[1]))
#define TILE_MMA(ti, tb) { __builtin_amdgcn_wave_barrier();                                          \
    f32x4 a_ = {0.f, 0.f, 0.f, 0.f}, b_ = {0.f, 0.f, 0.f, 0.f};                                    \
    _Pragma("unroll") for (int ks = 0; ks < 4; ks++) {                                             \
      u32x4 Gh = *(const u32x4*)(rp + 64 * ks + (tb) * GT_TAB);                                    \
      b_ = mma<true>(Gh, Fl[ks], b_);                                                              \
      a_ = mma<true>(Gh, Fh[ks], a_);                                                              \
    }                                                                                              \
    acc[ti] = a_ + b_; __builtin_amdgcn_wave_barrier(); }
  f32x2 g[8];
  g[0] = C;
#pragma unroll
  for (int be = 1; be < 8; be++) g[be] = cmul(g[be - 1], e1);
#pragma unroll
  for (int be = 0; be < 8; be++) PUT_ROW(0, be, g[be]);
#pragma unroll
  for (int be = 0; be < 8; be++) { g[be] = cmul(g[be], E); PUT_ROW(0, 8 + be, g[be]); }
  TILE_MMA(0, 0);
#pragma unroll
  for (int al = 0; al < 2; al++)
#pragma unroll
    for (int be = 0; be < 8; be += 2) { g[be] = cmul(g[be], E); PUT_ROW(1, 4 * al + (be >> 1), g[be]); }
#pragma unroll
  for (int al = 0; al < 4; al++)
#pragma unroll
    for (int be = 0; be < 8; be += 4) { g[be] = cmul(g[be], E); PUT_ROW(1, 8 + 2 * al + (be >> 2), g[be]); }
  TILE_MMA(1, 1);
#pragma unroll
  for (int al = 0; al < 9; al++) { g[0] = cmul(g[0], E); PUT_ROW(0, al, g[0]); }
  TILE_MMA(2, 0);
#pragma unroll
  for (int i = 0; i < 3; i++)
#pragma unroll
    for (int rg = 0; rg < 4; rg++) acc[i][rg] = __builtin_ldexpf(acc[i][rg], ec - 14);
}


#define PW_BYTES (2 * GT_TAB)
#define PREP_LDS (8 * PW_BYTES + 12288)

__global__ __launch_bounds__(512) void prep_kernel(
    const u16* __restrict__ u, const u16* __restrict__ lar, const u16* __restrict__ aim,
    const u16* __restrict__ cre, const u16* __restrict__ cim, const u16* __restrict__ ldt,
    const u16* __restrict__ kin, const u16* __restrict__ vin, const u16* __restrict__ shk,
    u16* __restrict__ uT, u16* __restrict__ kvT, u16* __restrict__ kcg, int* __restrict__ hexp) {
  extern __shared__ __attribute__((aligned(16))) char smem[];
  const int b = blockIdx.x;
  const int t = threadIdx.x;
  const int lane = t & 63;
  const int wv = __builtin_amdgcn_readfirstlane(t >> 6);
  const int xcd = b & 7, idx = b >> 3;
  const int cb = xcd + 8 * (idx & 1);
  const int d = cb * 64 + (idx >> 1) * 4 + (wv & 3);
  const int po = d * N_MODE + (wv >> 2) * 64 + lane;
  const u16 p_ldt = ldt[d], p_lar = lar[po], p_aim = aim[po], p_cre = cre[po], p_cim = cim[po];
  __builtin_amdgcn_sched_barrier(0);
  const int c0 = cb * 64 + (wv & 1) * 32 + (lane & 15) * 2;
  const int l0 = (idx >> 1) * 128 + (wv >> 1) * 32 + (lane >> 4) * 8;
  const __amdgpu_buffer_rsrc_t rk = __builtin_amdgcn_make_buffer_rsrc((void*)kin, 0, L_SEQ * D_CH * 2, 0x00020000);
  const __amdgpu_buffer_rsrc_t ru = __builtin_amdgcn_make_buffer_rsrc((void*)uT, 0, 12 * 1024 * 1024, 0x00020000);
  const int vo = l0 * (D_CH * 2) + c0 * 2;
  u32 uu[8], kk[11], vv[8];
#pragma unroll
  for (int i = 0; i < 8; i++) uu[i] = *(const u32*)((const char*)u + vo + i * (D_CH * 2));
#pragma unroll
  for (int i = 0; i < 11; i++) {
    int o = vo + (i - 3) * (D_CH * 2);
    if (i < 3) o = (l0 > 0) ? o : (int)0x40000000;
    kk[i] = __builtin_amdgcn_raw_buffer_load_b32(rk, o, 0, 0);
  }
#pragma unroll
  for (int i = 0; i < 8; i++) vv[i] = *(const u32*)((const char*)vin + vo + i * (D_CH * 2));
  const u32x4 sw = *(const u32x4*)(shk + (size_t)c0 * 4);
  __builtin_amdgcn_sched_barrier(0);
  f32x4 ka[3];
  char* tab = smem + wv * PW_BYTES;
  gen_partial(tab, lane, p_ldt, p_lar, p_aim, p_cre, p_cim, ka);
  if (wv >= 4) {
#pragma unroll
    for (int i = 0; i < 3; i++) *(f32x4*)(tab + 1024 * i + 16 * lane) = ka[i];
  }
  __builtin_amdgcn_sched_barrier(0);
#pragma unroll
  for (int c = 0; c < 2; c++) {
    u32x4 o;
#pragma unroll
    for (int p = 0; p < 4; p++) o[p] = __builtin_amdgcn_perm(uu[2 * p + 1], uu[2 * p], c ? 0x07060302u : 0x05040100u);
    __builtin_amdgcn_raw_buffer_store_b128(o, ru, ((c0 + c) * L_SEQ + l0) * 2, 0, ST_AUX);
  }
#pragma unroll
  for (int c = 0; c < 2; c++) {
    const u32 s01 = sw[2 * c], s23 = sw[2 * c + 1];
    const float w0 = bflo(s01), w1 = bfhi(s01), w2 = bflo(s23), w3 = bfhi(s23);
    float kf[11];
#pragma unroll
    for (int i = 0; i < 11; i++) kf[i] = c ? bfhi(kk[i]) : bflo(kk[i]);
    float res[8];
#pragma unroll
    for (int i = 0; i < 8; i++) {
      float a = rbf(rbf(kf[i] * w3) + rbf(kf[i + 1] * w2));
      a = rbf(a + rbf(kf[i + 2] * w1));
      a = rbf(a + rbf(kf[i + 3] * w0));
      res[i] = a * (c ? bfhi(vv[i]) : bflo(vv[i]));
    }
    u32x4 o;
#pragma unroll
    for (int p = 0; p < 4; p++) o[p] = pk_bf16(res[7 - 2 * p], res[6 - 2 * p]);
    __builtin_amdgcn_raw_buffer_store_b128(o, ru, (L_SEQ * D_CH + (c0 + c) * L_SEQ + l0) * 2, 0, ST_AUX);
  }
  asm volatile("s_waitcnt lgkmcnt(0)\n\ts_barrier" ::: "memory");
  if (wv >= 4) return;
  {
    const char* ex = tab + 4 * PW_BYTES + 16 * lane;
#pragma unroll
    for (int i = 0; i < 3; i++) ka[i] += *(const f32x4*)(ex + 1024 * i);
    const int c16 = lane & 15, q = lane >> 4;
    const bool v2 = q < 2, v28 = (q == 2) && (c16 == 0);
    u32 mk = 0;
#pragma unroll
    for (int rg = 0; rg < 4; rg++) {
      mk = max(mk, max(__float_as_uint(ka[0][rg]) & 0x7fffffffu, __float_as_uint(ka[1][rg]) & 0x7fffffffu));
      u32 m2 = __float_as_uint(ka[2][rg]) & 0x7fffffffu;
      mk = max(mk, (v2 || (rg == 0 && v28)) ? m2 : 0u);
    }
    mk = wave_max_u32(mk);
    const int es = (mk > 0u && mk < 0x7f800000u) ? __builtin_amdgcn_frexp_expf(__uint_as_float(mk)) - 15 : 0;
    u16* kc = (u16*)tab;
#pragma unroll
    for (int rg = 0; rg < 4; rg++) {
      const int row = 4 * q + rg;
      kc[128 * (row >> 3) + (row & 7) + 8 * c16] = __builtin_bit_cast(u16, (_Float16)__builtin_ldexpf(ka[0][rg], -es));
      int i1 = (row < 8) ? 256 + 64 * (row >> 2) + (row & 3) + 4 * c16 : 384 + 32 * ((row - 8) >> 1) + (row & 1) + 2 * c16;
      kc[i1] = __builtin_bit_cast(u16, (_Float16)__builtin_ldexpf(ka[1][rg], -es));
      kc[512 + 16 * row + c16] = __builtin_bit_cast(u16, (_Float16)__builtin_ldexpf(ka[2][rg], -es));
    }
    __builtin_amdgcn_wave_barrier();
    u16* dst = kcg + (size_t)d * KC_STRIDE;
    __builtin_amdgcn_raw_buffer_store_b128(*(const u32x4*)(tab + 16 * lane), ru, (2 * L_SEQ * D_CH + d * KC_STRIDE + 8 * lane) * 2, 0, ST_AUX);
    if (lane < 32) __builtin_amdgcn_raw_buffer_store_b128(*(const u32x4*)(tab + 1024 + 16 * lane), ru, (2 * L_SEQ * D_CH + d * KC_STRIDE + 512 + 8 * lane) * 2, 0, ST_AUX);
    if (lane == 0) hexp[d] = es;
  }
}

#define XS_BYTES 4608
#define HC_BYTES 2368
#define HS_BYTES (4 * HC_BYTES)
#define W_BYTES (XS_BYTES + HS_BYTES)
#define YS_OFF (8 * W_BYTES)
#define YS_BYTES 2064
#define LDS_BYTES (YS_OFF + 8 * YS_BYTES)

__device__ __forceinline__ int xcd_group(int b) {
  int xcd = b & 7, idx = b >> 3;
  return ((xcd + 8 * (idx >> 3)) << 3) | (idx & 7);
}

template <class F, int... I>
__device__ __forceinline__ void sfor_impl(F&& f, std::integer_sequence<int, I...>) { (f(std::integral_constant<int, I>{}), ...); }
template <int N, class F>
__device__ __forceinline__ void sfor(F&& f) { sfor_impl(f, std::make_integer_sequence<int, N>{}); }
constexpr int p1a(int k) { return k < 4 ? 0 : k < 7 ? 1 : k < 9 ? 2 : 3; }
constexpr int p1e(int k) { return k < 4 ? k : k < 7 ? k - 4 : k < 9 ? k - 7 : 0; }
constexpr int p2a(int k) { return k < 20 ? k / 4 : k < 23 ? 5 : k < 25 ? 6 : 7; }
constexpr int p2e(int k) { return k < 20 ? 4 - k / 4 + (k & 3) : k < 23 ? k - 20 : k < 25 ? k - 23 : 0; }

template <int OFF> __device__ __forceinline__ void dsr64(u32x2& dst, u32 addr) {
  asm volatile("ds_read_b64 %0, %1 offset:%2" : "=v"(dst) : "v"(addr), "i"(OFF) : "memory");
}
template <int OFF> __device__ __forceinline__ void dsr128(u32x4& dst, u32 addr) {
  asm volatile("ds_read_b128 %0, %1 offset:%2" : "=v"(dst) : "v"(addr), "i"(OFF) : "memory");
}
#define DSR64(dst, addr, off) dsr64<(off)>(dst, addr)
#define DSR128(dst, addr, off) dsr128<(off)>(dst, addr)
#define WAIT_FRAGS(AL, AH, B)                                                                         \
  __builtin_amdgcn_sched_barrier(0);                                                                  \
  asm volatile("s_waitcnt lgkmcnt(0)"                                                                 \
               : "+v"(AL[0]), "+v"(AL[1]), "+v"(AL[2]), "+v"(AL[3]), "+v"(AL[4]), "+v"(AL[5]), "+v"(AL[6]), "+v"(AL[7]), \
                 "+v"(AH[0]), "+v"(AH[1]), "+v"(AH[2]), "+v"(AH[3]), "+v"(AH[4]), "+v"(AH[5]), "+v"(AH[6]), "+v"(AH[7]), \
                 "+v"(B[0]), "+v"(B[1]), "+v"(B[2]), "+v"(B[3]), "+v"(B[4]), "+v"(B[5]), "+v"(B[6]), "+v"(B[7])  \
               :: "memory");                                                                          \
  __builtin_amdgcn_sched_barrier(0);
#define WAIT_FRAGS4(AL, AH, B)                                                                        \
  __builtin_amdgcn_sched_barrier(0);                                                                  \
  asm volatile("s_waitcnt lgkmcnt(0)"                                                                 \
               : "+v"(AL[0]), "+v"(AL[1]), "+v"(AL[2]), "+v"(AL[3]),                                  \
                 "+v"(AH[0]), "+v"(AH[1]), "+v"(AH[2]), "+v"(AH[3]),                                  \
                 "+v"(B[0]), "+v"(B[1]), "+v"(B[2]), "+v"(B[3])                                       \
               :: "memory");                                                                          \
  __builtin_amdgcn_sched_barrier(0);

template <bool IS_A>
__device__ __forceinline__ void conv_body(char* smem, int g, const u16* __restrict__ xT, const u16* __restrict__ hT,
                                          const int* __restrict__ hexp, const u16* __restrict__ dvec,
                                          const u16* __restrict__ qin, u16* __restrict__ out) {
  const int t = threadIdx.x, lane = t & 63;
  const int w = __builtin_amdgcn_readfirstlane(t >> 6);
  const int ch = (g & 127) * 8 + w;
  char* xs = smem + w * W_BYTES;
  char* hs = xs + XS_BYTES;
  const u16* xrow = xT + (size_t)ch * L_SEQ;
  u32x4 hd[6];
  u32x2 he[6];
  const int hl = lane < 48 ? lane : lane - 48;
  const int ha = hl / 6, hi_ = hl - 6 * ha;
  u32x4 kc0, kc1;
  int esc = 0;
  if (IS_A) {
    const u16* krow = hT + (size_t)ch * KC_STRIDE;
    kc0 = *(const u32x4*)(krow + 8 * lane);
    kc1 = *(const u32x4*)(krow + 512 + 8 * (lane & 31));
    esc = hexp[ch];
  } else {
    const __amdgpu_buffer_rsrc_t rh = __builtin_amdgcn_make_buffer_rsrc((void*)(hT + (size_t)ch * L_SEQ), 0, L_SEQ * 2, 0x00020000);
#pragma unroll
    for (int s = 0; s < 6; s++) {
      const int hc = 32 * ha + 16 * (s / 3) + 6 * (s % 3) + hi_ - 2;
      int o1 = 16 * hc, o2 = 16 * hc + 16;
      if (s == 0) { o1 = hc >= 0 ? o1 : 0x40000000; o2 = hc >= -1 ? o2 : 0x40000000; }
      hd[s] = __builtin_amdgcn_raw_buffer_load_b128(rh, o1, 0, 0);
      he[s] = __builtin_amdgcn_raw_buffer_load_b64(rh, o2, 0, 0);
    }
  }
  u32x4 xr[4];
#pragma unroll
  for (int j = 0; j < 4; j++) xr[j] = *(const u32x4*)(xrow + 8 * (64 * j + lane));
  const u16 p_d = dvec[ch];
  __builtin_amdgcn_sched_barrier(0);
  if (IS_A) {
    char* kb = hs;
    char* lin = xs;
    const u16* kc = (const u16*)kb;
    *(u32x4*)(kb + 16 * lane) = kc0;
    *(u32x4*)(kb + 1024 + 16 * (lane & 31)) = kc1;
    __builtin_amdgcn_wave_barrier();
    const bool dir = lane < 32;
    const int a0 = dir ? 16 * lane : 512 + 8 * (lane - 32);
    u32x2 d01 = *(const u32x2*)(kb + a0), d23 = *(const u32x2*)(kb + a0 + 8);
    u32x4 o;
    o[0] = __builtin_amdgcn_perm(d01[0], d01[0], dir ? 0x03020100u : 0x01000100u);
    o[1] = __builtin_amdgcn_perm(d01[1], d01[0], dir ? 0x07060504u : 0x05040302u);
    o[2] = __builtin_amdgcn_perm(d23[0], d01[1], dir ? 0x07060504u : 0x01000100u);
    o[3] = dir ? d23[1] : __builtin_amdgcn_perm(d23[0], d01[1], 0x05040302u);
    *(u32x4*)(lin + 32 + 16 * lane) = o;
    u32 d0 = *(const u32*)(kb + 768 + 4 * lane), d1 = *(const u32*)(kb + 772 + 4 * lane);
    o[0] = __builtin_amdgcn_perm(d0, d0, 0x01000100u);
    o[1] = d0;
    o[2] = __builtin_amdgcn_perm(d0, d0, 0x03020302u);
    o[3] = __builtin_amdgcn_perm(d1, d1, 0x01000100u);
    *(u32x4*)(lin + 32 + 1024 + 16 * lane) = o;
#pragma unroll
    for (int i = 0; i < 2; i++) {
      u32 x0 = kc[512 + 64 * i + lane], x1 = kc[513 + 64 * i + lane];
      o[0] = o[1] = x0 | (x0 << 16);
      o[3] = x1 | (x1 << 16);
      o[2] = (lane & 1) ? o[3] : (x0 | (x1 << 16));
      *(u32x4*)(lin + 32 + 2048 + 1024 * i + 16 * lane) = o;
    }
    if (lane < 4) *(u32x4*)(lin + ((lane & 2) ? 4096 : 0) + 16 * lane) = (u32x4){0, 0, 0, 0};
    __builtin_amdgcn_wave_barrier();
#pragma unroll
    for (int s = 0; s < 6; s++) {
      const int hc = 32 * ha + 16 * (s / 3) + 6 * (s % 3) + hi_;
      hd[s] = *(const u32x4*)(lin + 16 * hc);
      he[s] = *(const u32x2*)(lin + 16 * hc + 16);
    }
    __builtin_amdgcn_wave_barrier();
  }
#define STAGE_PIECE(hwp, hf, k) {                                                      \
    const u32x4 d4 = hd[3 * (hf) + ((k) >> 2)];                                        \
    const u32x2 e2 = he[3 * (hf) + ((k) >> 2)];                                        \
    u32x4 o_;                                                                          \
    if (((k) & 3) == 0) o_ = d4;                                                       \
    else if (((k) & 3) == 1) o_ = (u32x4){__builtin_amdgcn_alignbit(d4[1], d4[0], 16), __builtin_amdgcn_alignbit(d4[2], d4[1], 16), \
                                          __builtin_amdgcn_alignbit(d4[3], d4[2], 16), __builtin_amdgcn_alignbit(e2[0], d4[3], 16)}; \
    else if (((k) & 3) == 2) o_ = (u32x4){d4[1], d4[2], d4[3], e2[0]};                 \
    else o_ = (u32x4){__builtin_amdgcn_alignbit(d4[2], d4[1], 16), __builtin_amdgcn_alignbit(d4[3], d4[2], 16), \
                      __builtin_amdgcn_alignbit(e2[0], d4[3], 16), __builtin_amdgcn_alignbit(e2[1], e2[0], 16)}; \
    *(u32x4*)((hwp) + 96 * ((k) >> 2) + ((k) & 3) * HC_BYTES) = o_; }
  char* hw = hs + ha * 288 + hi_ * 16;
  if (ha < 4) {
#pragma unroll
    for (int k = 0; k < 12; k++) STAGE_PIECE(hw, 0, k);
#pragma unroll
    for (int k = 0; k < 12; k++) STAGE_PIECE(hw + 4 * 288, 1, k);
  }
  if (lane < 30) *(u32x4*)(xs + 16 * lane) = (u32x4){0, 0, 0, 0};
  if (IS_A) {
    u32 mx = 0;
#pragma unroll
    for (int j = 0; j < 4; j++)
#pragma unroll
      for (int p = 0; p < 4; p++) {
        u32 dd = xr[j][p];
        mx = max(mx, max(dd & 0x7fffu, (dd >> 16) & 0x7fffu));
      }
    mx = wave_max_u32(mx);
    const int ex = (int)(mx >> 7) - 127 - 14;
    esc += ex;
#pragma unroll
    for (int j = 0; j < 4; j++) {
      u32x4 o;
#pragma unroll
      for (int p = 0; p < 4; p++) {
        u32 dd = xr[j][3 - p];
        float lo = __builtin_ldexpf(bfhi(dd), -ex), hi = __builtin_ldexpf(bflo(dd), -ex);
        o[p] = __builtin_bit_cast(u32, __builtin_amdgcn_cvt_pkrtz(lo, hi));
      }
      *(u32x4*)(xs + 480 + 16 * (64 * j + lane)) = o;
    }
  } else {
#pragma unroll
    for (int j = 0; j < 4; j++) *(u32x4*)(xs + 480 + 16 * (64 * j + lane)) = xr[j];
  }
  const int r = lane & 15, q = lane >> 4;
  f32x4 acc[4];
#pragma unroll
  for (int i = 0; i < 4; i++) acc[i] = (f32x4){0.f, 0.f, 0.f, 0.f};
  const int lam = 25 + r - 8 * q;
  const u32 abase = lds_off(hs) + (lam & 3) * HC_BYTES + 2 * (lam & ~3);
  const u32 bbase = lds_off(xs) + 32 * r + 16 * q;
  u32x2 AL0[8], AH0[8], AL1[8], AH1[8];
  u32x4 B0[8], B1[8];
#define LOAD_PIECE(it, sl, k, AL, AH, B) {                                                      \
    if constexpr ((k) % 3 == 0) DSR64(AL[(k) / 3], abase, 64 * ((it) & 3) + 288 * ((k) / 3 + (sl)));      \
    else if constexpr ((k) % 3 == 1) DSR64(AH[(k) / 3], abase, 64 * ((it) & 3) + 288 * ((k) / 3 + (sl)) + 8); \
    else DSR128(B[(k) / 3], bbase, 448 + 512 * ((k) / 3) - 64 * (it)); }
  __builtin_amdgcn_sched_barrier(0);
  sfor<12>([&](auto kc_) { constexpr int k = decltype(kc_)::value; LOAD_PIECE(0, 0, k, AL0, AH0, B0); });
  __builtin_amdgcn_sched_barrier(0);
  u32x4 qv[4];
  u32x2 uv[8];
  if (IS_A) {
#pragma unroll
    for (int ct = 0; ct < 8; ct++) uv[ct] = *(const u32x2*)(xrow + 256 * ct + 16 * r + 4 * q);
  } else {
#pragma unroll
    for (int i = 0; i < 4; i++) qv[i] = *(const u32x4*)(qin + (size_t)(1024 * (i >> 1) + 2 * t + (i & 1)) * D_CH + (g & 127) * 8);
  }
#define PIN_ACC asm volatile("" : "+v"(acc[0]), "+v"(acc[1]), "+v"(acc[2]), "+v"(acc[3]));
#define ITER1(it, AL, AH, B, NAL, NAH, NB)                                              \
  WAIT_FRAGS4(AL, AH, B);                                                               \
  sfor<10>([&](auto kc_) {                                                              \
    constexpr int k_ = decltype(kc_)::value, a = p1a(k_), e = p1e(k_);                  \
    acc[a + e] = mma<IS_A>((u32x4){AL[a][0], AL[a][1], AH[a][0], AH[a][1]}, B[e], acc[a + e]); \
    if constexpr ((it) < 7) {                                                           \
      LOAD_PIECE((it) + 1, (((it) + 1) >> 2) * 4, k_, NAL, NAH, NB);                    \
      if constexpr (k_ < 2) LOAD_PIECE((it) + 1, (((it) + 1) >> 2) * 4, 10 + k_, NAL, NAH, NB); \
    }                                                                                   \
    __builtin_amdgcn_sched_barrier(0);                                                  \
  });                                                                                   \
  PIN_ACC
#define ITER2(it, AL, AH, B, NAL, NAH, NB)                                              \
  WAIT_FRAGS(AL, AH, B);                                                                \
  sfor<26>([&](auto kc_) {                                                              \
    constexpr int k_ = decltype(kc_)::value, a = p2a(k_), e = p2e(k_);                  \
    acc[a + e - 4] = mma<IS_A>((u32x4){AL[a][0], AL[a][1], AH[a][0], AH[a][1]}, B[e], acc[a + e - 4]); \
    if constexpr ((it) == 2) {                                                          \
      if constexpr (k_ < 12) { LOAD_PIECE(3, 0, 2 * k_, NAL, NAH, NB); LOAD_PIECE(3, 0, 2 * k_ + 1, NAL, NAH, NB); } \
      else if constexpr (k_ < 24) STAGE_PIECE(hw, 1, k_ - 12);                          \
    } else if constexpr ((it) < 7 && k_ < 24) LOAD_PIECE((it) + 1, 0, k_, NAL, NAH, NB); \
    __builtin_amdgcn_sched_barrier(0);                                                  \
  });                                                                                   \
  PIN_ACC
  const float dsk = bf2f(p_d);
  const __amdgpu_buffer_rsrc_t ro = __builtin_amdgcn_make_buffer_rsrc((void*)out, 0, 8 * 1024 * 1024, 0x00020000);
#define EPI1(c0_, ysp)                                                                  \
  _Pragma("unroll") for (int ci = 0; ci < 4; ci++) {                                    \
    const int l = 256 * ((c0_) + ci) + 16 * r + 4 * q;                                  \
    float x0, x1, x2, x3;                                                               \
    if (IS_A) {                                                                         \
      u32x2 xv = uv[(c0_) + ci];                                                        \
      x0 = bflo(xv[0]); x1 = bfhi(xv[0]); x2 = bflo(xv[1]); x3 = bfhi(xv[1]);           \
    } else {                                                                            \
      u32x2 xv = *(const u32x2*)(xs + 480 + 2 * ((l & ~7) + 4 - (l & 4)));              \
      x3 = bflo(xv[0]); x2 = bfhi(xv[0]); x1 = bflo(xv[1]); x0 = bfhi(xv[1]);           \
    }                                                                                   \
    float y0 = acc[ci][0], y1 = acc[ci][1], y2 = acc[ci][2], y3 = acc[ci][3];           \
    if (IS_A) {                                                                         \
      y0 = __builtin_ldexpf(y0, esc); y1 = __builtin_ldexpf(y1, esc);                   \
      y2 = __builtin_ldexpf(y2, esc); y3 = __builtin_ldexpf(y3, esc);                   \
    }                                                                                   \
    u32x2 o;                                                                            \
    o[0] = pk_bf16(rbf(y0) + rbf(x0 * dsk), rbf(y1) + rbf(x1 * dsk));                   \
    o[1] = pk_bf16(rbf(y2) + rbf(x2 * dsk), rbf(y3) + rbf(x3 * dsk));                   \
    *(u32x2*)((ysp) + 2 * (l & 1023)) = o;                                              \
  }
#define EPI2(hf, ysb, ystride)                                                          \
  {                                                                                     \
    u32 yv[8];                                                                          \
    _Pragma("unroll") for (int c = 0; c < 8; c++) yv[c] = *(const u32*)((ysb) + c * (ystride) + 4 * t); \
    _Pragma("unroll") for (int i = 0; i < 2; i++) {                                     \
      u32x4 o;                                                                          \
      _Pragma("unroll") for (int p = 0; p < 4; p++) {                                   \
        if (IS_A) o[p] = __builtin_amdgcn_perm(yv[2 * p + 1], yv[2 * p], i ? 0x07060302u : 0x05040100u); \
        else {                                                                          \
          float ya = i ? bfhi(yv[2 * p]) : bflo(yv[2 * p]), yb = i ? bfhi(yv[2 * p + 1]) : bflo(yv[2 * p + 1]); \
          o[p] = pk_bf16(ya * bflo(qv[2 * (hf) + i][p]), yb * bfhi(qv[2 * (hf) + i][p])); \
        }                                                                               \
      }                                                                                 \
      __builtin_amdgcn_raw_buffer_store_b128(o, ro, ((1024 * (hf) + 2 * t + i) * (2 * D_CH) + g * 8) * 2, 0, (hf) == 0 ? 16 : 0); \
    }                                                                                   \
  }
  ITER1(0, AL0, AH0, B0, AL1, AH1, B1);
  ITER1(1, AL1, AH1, B1, AL0, AH0, B0);
  ITER1(2, AL0, AH0, B0, AL1, AH1, B1);
  ITER1(3, AL1, AH1, B1, AL0, AH0, B0);
  ITER1(4, AL0, AH0, B0, AL1, AH1, B1);
  ITER1(5, AL1, AH1, B1, AL0, AH0, B0);
  ITER1(6, AL0, AH0, B0, AL1, AH1, B1);
  ITER1(7, AL1, AH1, B1, AL0, AH0, B0);
#pragma unroll
  for (int k = 0; k < 12; k++) STAGE_PIECE(hw, 0, k);
  __builtin_amdgcn_sched_barrier(0);
  sfor<24>([&](auto kc_) { constexpr int k = decltype(kc_)::value; LOAD_PIECE(0, 0, k, AL0, AH0, B0); });
  __builtin_amdgcn_sched_barrier(0);
  EPI1(0, smem + YS_OFF + w * YS_BYTES);
#pragma unroll
  for (int i = 0; i < 4; i++) acc[i] = (f32x4){0.f, 0.f, 0.f, 0.f};
  asm volatile("s_waitcnt lgkmcnt(0)\n\ts_barrier" ::: "memory");
  EPI2(0, smem + YS_OFF, YS_BYTES);
  ITER2(0, AL0, AH0, B0, AL1, AH1, B1);
  ITER2(1, AL1, AH1, B1, AL0, AH0, B0);
  ITER2(2, AL0, AH0, B0, AL1, AH1, B1);
  ITER2(3, AL1, AH1, B1, AL0, AH0, B0);
  ITER2(4, AL0, AH0, B0, AL1, AH1, B1);
  ITER2(5, AL1, AH1, B1, AL0, AH0, B0);
  ITER2(6, AL0, AH0, B0, AL1, AH1, B1);
  ITER2(7, AL1, AH1, B1, AL0, AH0, B0);
  EPI1(4, hs);
  asm volatile("s_waitcnt lgkmcnt(0)\n\ts_barrier" ::: "memory");
  EPI2(1, smem + XS_BYTES, W_BYTES);
}

__global__ __launch_bounds__(512) void conv_kernel(const u16* __restrict__ uT, const u16* __restrict__ kvT,
                                                   const u16* __restrict__ kcg, const int* __restrict__ hexp,
                                                   const u16* __restrict__ dskip, const u16* __restrict__ ssmk,
                                                   const u16* __restrict__ ssmd, const u16* __restrict__ qin,
                                                   u16* __restrict__ out) {
  extern __shared__ __attribute__((aligned(16))) char smem[];
  const int g = xcd_group(blockIdx.x);
  if (g < 128) conv_body<true>(smem, g, uT, kcg, hexp, dskip, qin, out);
  else conv_body<false>(smem, g, kvT, ssmk, hexp, ssmd, qin, out);
}

extern "C" void kernel_launch(void* const* d_in, const int* in_sizes, int n_in, void* d_out, int out_size,
                              void* d_ws, size_t ws_size, hipStream_t stream) {
  const u16* u = (const u16*)d_in[0];
  const u16* lar = (const u16*)d_in[1];
  const u16* aim = (const u16*)d_in[2];
  const u16* cre = (const u16*)d_in[3];
  const u16* cim = (const u16*)d_in[4];
  const u16* ldt = (const u16*)d_in[5];
  const u16* dsk = (const u16*)d_in[6];
  const u16* q = (const u16*)d_in[7];
  const u16* k = (const u16*)d_in[8];
  const u16* v = (const u16*)d_in[9];
  const u16* shk = (const u16*)d_in[10];
  const u16* ssmk = (const u16*)d_in[11];
  const u16* ssmd = (const u16*)d_in[12];
  char* ws = (char*)d_ws;
  u16* uT = (u16*)ws;
  u16* kvT = (u16*)(ws + (size_t)4 * 1024 * 1024);
  u16* kcg = (u16*)(ws + (size_t)8 * 1024 * 1024);
  int* hexp = (int*)(ws + (size_t)12 * 1024 * 1024);
  (void)hipFuncSetAttribute((const void*)prep_kernel, hipFuncAttributeMaxDynamicSharedMemorySize, PREP_LDS);
  (void)hipFuncSetAttribute((const void*)conv_kernel, hipFuncAttributeMaxDynamicSharedMemorySize, LDS_BYTES);
  prep_kernel<<<256, 512, PREP_LDS, stream>>>(u, lar, aim, cre, cim, ldt, k, v, shk, uT, kvT, kcg, hexp);
  conv_kernel<<<256, 512, LDS_BYTES, stream>>>(uT, kvT, kcg, hexp, dsk, ssmk, ssmd, q, (u16*)d_out);
}
```

```cpp
#include <hip/hip_runtime.h>
#include <hip/hip_bf16.h>
#include <stdint.h>
#include <utility>

typedef unsigned int u32;
typedef unsigned short u16;
typedef u32 u32x4 __attribute__((ext_vector_type(4)));
typedef u32 u32x2 __attribute__((ext_vector_type(2)));
typedef float f32x4 __attribute__((ext_vector_type(4)));
typedef __bf16 bf16x8 __attribute__((ext_vector_type(8)));
typedef _Float16 f16x8 __attribute__((ext_vector_type(8)));

#define ST_AUX 16
#define AUX0 0
#define L_SEQ 2048
#define D_CH 1024
#define N_MODE 128

__device__ __forceinline__ float bflo(u32 d) { return __uint_as_float(d << 16); }
__device__ __forceinline__ float bfhi(u32 d) { return __uint_as_float(d & 0xffff0000u); }
__device__ __forceinline__ float bf2f(u16 h) { return __uint_as_float(((u32)h) << 16); }
__device__ __forceinline__ u32 pk_bf16(float lo, float hi) {
  typedef __bf16 bf16x2 __attribute__((ext_vector_type(2)));
  typedef float f32x2 __attribute__((ext_vector_type(2)));
  f32x2 v = {lo, hi};
  bf16x2 r = __builtin_convertvector(v, bf16x2);
  return __builtin_bit_cast(u32, r);
}
__device__ __forceinline__ float rbf(float x) { return __uint_as_float(pk_bf16(0.f, x)); }

__device__ __forceinline__ u32 wave_max_u32(u32 x) {
  x = max(x, (u32)__builtin_amdgcn_update_dpp(0, (int)x, 0x111, 0xf, 0xf, true));
  x = max(x, (u32)__builtin_amdgcn_update_dpp(0, (int)x, 0x112, 0xf, 0xf, true));
  x = max(x, (u32)__builtin_amdgcn_update_dpp(0, (int)x, 0x114, 0xf, 0xf, true));
  x = max(x, (u32)__builtin_amdgcn_update_dpp(0, (int)x, 0x118, 0xf, 0xf, true));
  x = max(x, (u32)__builtin_amdgcn_update_dpp(0, (int)x, 0x142, 0xa, 0xf, true));
  x = max(x, (u32)__builtin_amdgcn_update_dpp(0, (int)x, 0x143, 0xc, 0xf, true));
  return (u32)__builtin_amdgcn_readlane((int)x, 63);
}


#define GT_ROW 288
#define GT_TAB (16 * GT_ROW)
#define KC_STRIDE 768

typedef float f32x2 __attribute__((ext_vector_type(2)));
__device__ __forceinline__ f32x2 cmul(f32x2 g, f32x2 e) {
  f32x2 t, o;
  asm("v_pk_mul_f32 %0, %1, %2 op_sel:[0,0] op_sel_hi:[0,1]" : "=v"(t) : "v"(g), "v"(e));
  asm("v_pk_fma_f32 %0, %1, %2, %3 op_sel:[1,1,0] op_sel_hi:[1,0,1] neg_lo:[1,0,0]" : "=v"(o) : "v"(g), "v"(e), "v"(t));
  return o;
}
__device__ __forceinline__ f32x2 cexp_pos(float ar, float ai, float pos) {
  const float LOG2E = 1.44269504088896340736f;
  const float I2PI_HI = 0.15915494309189533577f;
  const float I2PI_LO = 6.4206383e-9f;
  float xr = ar * pos;
  float xi = ai * pos;
  float e = __builtin_amdgcn_exp2f(xr * LOG2E);
  float r = xi * I2PI_HI;
  float err = __builtin_fmaf(xi, I2PI_HI, -r);
  float f = __builtin_amdgcn_fractf(r);
  f += __builtin_fmaf(xi, I2PI_LO, err);
  f32x2 o;
  o[0] = e * __builtin_amdgcn_cosf(f);
  o[1] = e * __builtin_amdgcn_sinf(f);
  return o;
}
__device__ __forceinline__ void split_h(float a, float b, u32& hi, u32& lo) {
  hi = __builtin_bit_cast(u32, __builtin_amdgcn_cvt_pkrtz(a, b));
  u32 l;
  asm("v_fma_mixlo_f16 %0, %1, -1.0, %2 op_sel_hi:[1,0,0]" : "=v"(l) : "v"(hi), "v"(a));
  asm("v_fma_mixhi_f16 %0, %1, -1.0, %2 op_sel:[1,0,0] op_sel_hi:[1,0,0]" : "+v"(l) : "v"(hi), "v"(b));
  lo = l;
}

template <bool IS_A>
__device__ __forceinline__ f32x4 mma(u32x4 a, u32x4 b, f32x4 c) {
  if (IS_A) return __builtin_amdgcn_mfma_f32_16x16x32_f16(__builtin_bit_cast(f16x8, a), __builtin_bit_cast(f16x8, b), c, 0, 0, 0);
  else return __builtin_amdgcn_mfma_f32_16x16x32_bf16(__builtin_bit_cast(bf16x8, a), __builtin_bit_cast(bf16x8, b), c, 0, 0, 0);
}

__device__ __forceinline__ u32 lds_off(const char* p) {
  return (u32)(uintptr_t)(const __attribute__((address_space(3))) char*)p;
}
__device__ __forceinline__ void gen_partial(char* tab, int lane, u16 p_ldt, u16 p_lar, u16 p_aim, u16 p_cre, u16 p_cim,
                                            f32x4 (&acc)[3]) {
  const float dt = rbf(expf(bf2f(p_ldt)));
  const float ar = -rbf(expf(bf2f(p_lar))) * dt;
  const float ai = bf2f(p_aim) * dt;
  f32x2 C;
  C[0] = 2.0f * (bf2f(p_cre) * dt);
  C[1] = -2.0f * (bf2f(p_cim) * dt);
  const u32 mc = wave_max_u32(max(__float_as_uint(C[0]) & 0x7fffffffu, __float_as_uint(C[1]) & 0x7fffffffu));
  const f32x2 e1 = cexp_pos(ar, -ai, 1.0f);
  const f32x2 f8 = cexp_pos(ar, ai, 8.0f);
  const f32x2 E = cexp_pos(ar, -ai, 128.0f);
  const int ec = (mc > 0u && mc < 0x7f800000u) ? __builtin_amdgcn_frexp_expf(__uint_as_float(mc)) - 14 : 0;
  C[0] = __builtin_ldexpf(C[0], -ec) * 1.0003662109375f;
  C[1] = __builtin_ldexpf(C[1], -ec) * 1.0003662109375f;
  char* wp = tab + 4 * lane;
  const int c16 = lane & 15, q = lane >> 4;
  const char* rp = tab + c16 * GT_ROW + 16 * q;
  {
    f32x2 F = {16384.0f, 0.f};
#pragma unroll
    for (int w = 0; w < 16; w++) {
      u32 hi_, lo_;
      split_h(F[0], F[1], hi_, lo_);
      *(u32*)(wp + w * GT_ROW) = hi_;
      *(u32*)(wp + w * GT_ROW + GT_TAB) = lo_;
      if (w < 15) F = cmul(F, f8);
    }
  }
  __builtin_amdgcn_wave_barrier();
  u32x4 Fh[4], Fl[4];
#pragma unroll
  for (int ks = 0; ks < 4; ks++) {
    Fh[ks] = *(const u32x4*)(rp + 64 * ks);
    Fl[ks] = *(const u32x4*)(rp + 64 * ks + GT_TAB);
  }
  __builtin_amdgcn_wave_barrier();
#define PUT_ROW(tb, row, z) *(u32*)(wp + (tb) * GT_TAB + (row) * GT_ROW) = __builtin_bit_cast(u32, __builtin_amdgcn_cvt_pkrtz((z)[0], (z)[1]))
#define TILE_MMA(ti, tb) { __builtin_amdgcn_wave_barrier();                                          \
    f32x4 a_ = {0.f, 0.f, 0.f, 0.f}, b_ = {0.f, 0.f, 0.f, 0.f};                                    \
    _Pragma("unroll") for (int ks = 0; ks < 4; ks++) {                                             \
      u32x4 Gh = *(const u32x4*)(rp + 64 * ks + (tb) * GT_TAB);                                    \
      b_ = mma<true>(Gh, Fl[ks], b_);                                                              \
      a_ = mma<true>(Gh, Fh[ks], a_);                                                              \
    }                                                                                              \
    acc[ti] = a_ + b_; __builtin_amdgcn_wave_barrier(); }
  f32x2 g[8];
  g[0] = C;
#pragma unroll
  for (int be = 1; be < 8; be++) g[be] = cmul(g[be - 1], e1);
#pragma unroll
  for (int be = 0; be < 8; be++) PUT_ROW(0, be, g[be]);
#pragma unroll
  for (int be = 0; be < 8; be++) { g[be] = cmul(g[be], E); PUT_ROW(0, 8 + be, g[be]); }
  TILE_MMA(0, 0);
#pragma unroll
  for (int al = 0; al < 2; al++)
#pragma unroll
    for (int be = 0; be < 8; be += 2) { g[be] = cmul(g[be], E); PUT_ROW(1, 4 * al + (be >> 1), g[be]); }
#pragma unroll
  for (int al = 0; al < 4; al++)
#pragma unroll
    for (int be = 0; be < 8; be += 4) { g[be] = cmul(g[be], E); PUT_ROW(1, 8 + 2 * al + (be >> 2), g[be]); }
  TILE_MMA(1, 1);
#pragma unroll
  for (int al = 0; al < 9; al++) { g[0] = cmul(g[0], E); PUT_ROW(0, al, g[0]); }
  TILE_MMA(2, 0);
#pragma unroll
  for (int i = 0; i < 3; i++)
#pragma unroll
    for (int rg = 0; rg < 4; rg++) acc[i][rg] = __builtin_ldexpf(acc[i][rg], ec - 14);
}


#define PW_BYTES (2 * GT_TAB)
#define PREP_LDS (8 * PW_BYTES + 12288)

__global__ __launch_bounds__(512) void prep_kernel(
    const u16* __restrict__ u, const u16* __restrict__ lar, const u16* __restrict__ aim,
    const u16* __restrict__ cre, const u16* __restrict__ cim, const u16* __restrict__ ldt,
    const u16* __restrict__ kin, const u16* __restrict__ vin, const u16* __restrict__ shk,
    u16* __restrict__ uT, u16* __restrict__ kvT, u16* __restrict__ kcg, int* __restrict__ hexp) {
  extern __shared__ __attribute__((aligned(16))) char smem[];
  const int b = blockIdx.x;
  const int t = threadIdx.x;
  const int lane = t & 63;
  const int wv = __builtin_amdgcn_readfirstlane(t >> 6);
  const int xcd = b & 7, idx = b >> 3;
  const int cb = xcd + 8 * (idx & 1);
  const int d = cb * 64 + (idx >> 1) * 4 + (wv & 3);
  const int po = d * N_MODE + (wv >> 2) * 64 + lane;
  const u16 p_ldt = ldt[d], p_lar = lar[po], p_aim = aim[po], p_cre = cre[po], p_cim = cim[po];
  __builtin_amdgcn_sched_barrier(0);
  const int c0 = cb * 64 + (wv & 1) * 32 + (lane & 15) * 2;
  const int l0 = (idx >> 1) * 128 + (wv >> 1) * 32 + (lane >> 4) * 8;
  const __amdgpu_buffer_rsrc_t rk = __builtin_amdgcn_make_buffer_rsrc((void*)kin, 0, L_SEQ * D_CH * 2, 0x00020000);
  const __amdgpu_buffer_rsrc_t ru = __builtin_amdgcn_make_buffer_rsrc((void*)uT, 0, 12 * 1024 * 1024, 0x00020000);
  const int vo = l0 * (D_CH * 2) + c0 * 2;
  u32 uu[8], kk[11], vv[8];
#pragma unroll
  for (int i = 0; i < 8; i++) uu[i] = *(const u32*)((const char*)u + vo + i * (D_CH * 2));
#pragma unroll
  for (int i = 0; i < 11; i++) {
    int o = vo + (i - 3) * (D_CH * 2);
    if (i < 3) o = (l0 > 0) ? o : (int)0x40000000;
    kk[i] = __builtin_amdgcn_raw_buffer_load_b32(rk, o, 0, 0);
  }
#pragma unroll
  for (int i = 0; i < 8; i++) vv[i] = *(const u32*)((const char*)vin + vo + i * (D_CH * 2));
  const u32x4 sw = *(const u32x4*)(shk + (size_t)c0 * 4);
  __builtin_amdgcn_sched_barrier(0);
  f32x4 ka[3];
  char* tab = smem + wv * PW_BYTES;
  gen_partial(tab, lane, p_ldt, p_lar, p_aim, p_cre, p_cim, ka);
  if (wv >= 4) {
#pragma unroll
    for (int i = 0; i < 3; i++) *(f32x4*)(tab + 1024 * i + 16 * lane) = ka[i];
  }
  __builtin_amdgcn_sched_barrier(0);
#pragma unroll
  for (int c = 0; c < 2; c++) {
    u32x4 o;
#pragma unroll
    for (int p = 0; p < 4; p++) o[p] = __builtin_amdgcn_perm(uu[2 * p + 1], uu[2 * p], c ? 0x07060302u : 0x05040100u);
    __builtin_amdgcn_raw_buffer_store_b128(o, ru, ((c0 + c) * L_SEQ + l0) * 2, 0, ST_AUX);
  }
#pragma unroll
  for (int c = 0; c < 2; c++) {
    const u32 s01 = sw[2 * c], s23 = sw[2 * c + 1];
    const float w0 = bflo(s01), w1 = bfhi(s01), w2 = bflo(s23), w3 = bfhi(s23);
    float kf[11];
#pragma unroll
    for (int i = 0; i < 11; i++) kf[i] = c ? bfhi(kk[i]) : bflo(kk[i]);
    float res[8];
#pragma unroll
    for (int i = 0; i < 8; i++) {
      float a = rbf(rbf(kf[i] * w3) + rbf(kf[i + 1] * w2));
      a = rbf(a + rbf(kf[i + 2] * w1));
      a = rbf(a + rbf(kf[i + 3] * w0));
      res[i] = a * (c ? bfhi(vv[i]) : bflo(vv[i]));
    }
    u32x4 o;
#pragma unroll
    for (int p = 0; p < 4; p++) o[p] = pk_bf16(res[7 - 2 * p], res[6 - 2 * p]);
    __builtin_amdgcn_raw_buffer_store_b128(o, ru, (L_SEQ * D_CH + (c0 + c) * L_SEQ + l0) * 2, 0, ST_AUX);
  }
  asm volatile("s_waitcnt lgkmcnt(0)\n\ts_barrier" ::: "memory");
  if (wv >= 4) return;
  {
    const char* ex = tab + 4 * PW_BYTES + 16 * lane;
#pragma unroll
    for (int i = 0; i < 3; i++) ka[i] += *(const f32x4*)(ex + 1024 * i);
    const int c16 = lane & 15, q = lane >> 4;
    const bool v2 = q < 2, v28 = (q == 2) && (c16 == 0);
    u32 mk = 0;
#pragma unroll
    for (int rg = 0; rg < 4; rg++) {
      mk = max(mk, max(__float_as_uint(ka[0][rg]) & 0x7fffffffu, __float_as_uint(ka[1][rg]) & 0x7fffffffu));
      u32 m2 = __float_as_uint(ka[2][rg]) & 0x7fffffffu;
      mk = max(mk, (v2 || (rg == 0 && v28)) ? m2 : 0u);
    }
    mk = wave_max_u32(mk);
    const int es = (mk > 0u && mk < 0x7f800000u) ? __builtin_amdgcn_frexp_expf(__uint_as_float(mk)) - 15 : 0;
    u16* kc = (u16*)tab;
#pragma unroll
    for (int rg = 0; rg < 4; rg++) {
      const int row = 4 * q + rg;
      kc[128 * (row >> 3) + (row & 7) + 8 * c16] = __builtin_bit_cast(u16, (_Float16)__builtin_ldexpf(ka[0][rg], -es));
      int i1 = (row < 8) ? 256 + 64 * (row >> 2) + (row & 3) + 4 * c16 : 384 + 32 * ((row - 8) >> 1) + (row & 1) + 2 * c16;
      kc[i1] = __builtin_bit_cast(u16, (_Float16)__builtin_ldexpf(ka[1][rg], -es));
      kc[512 + 16 * row + c16] = __builtin_bit_cast(u16, (_Float16)__builtin_ldexpf(ka[2][rg], -es));
    }
    __builtin_amdgcn_wave_barrier();
    u16* dst = kcg + (size_t)d * KC_STRIDE;
    __builtin_amdgcn_raw_buffer_store_b128(*(const u32x4*)(tab + 16 * lane), ru, (2 * L_SEQ * D_CH + d * KC_STRIDE + 8 * lane) * 2, 0, ST_AUX);
    if (lane < 32) __builtin_amdgcn_raw_buffer_store_b128(*(const u32x4*)(tab + 1024 + 16 * lane), ru, (2 * L_SEQ * D_CH + d * KC_STRIDE + 512 + 8 * lane) * 2, 0, ST_AUX);
    if (lane == 0) hexp[d] = es;
  }
}

#define XS_BYTES 4608
#define HC_BYTES 2368
#define HS_BYTES (4 * HC_BYTES)
#define W_BYTES (XS_BYTES + HS_BYTES)
#define YS_OFF (8 * W_BYTES)
#define YS_BYTES 2064
#define FL_OFF (YS_OFF + 8 * YS_BYTES)
#define LDS_BYTES (FL_OFF + 32)

__device__ __forceinline__ int xcd_group(int b) {
  int xcd = b & 7, idx = b >> 3;
  return ((xcd + 8 * (idx >> 3)) << 3) | (idx & 7);
}

template <class F, int... I>
__device__ __forceinline__ void sfor_impl(F&& f, std::integer_sequence<int, I...>) { (f(std::integral_constant<int, I>{}), ...); }
template <int N, class F>
__device__ __forceinline__ void sfor(F&& f) { sfor_impl(f, std::make_integer_sequence<int, N>{}); }
constexpr int pxa(int k) { return k < 8 ? 0 : k < 15 ? 1 : k < 21 ? 2 : 3; }
constexpr int pxe(int k) { return k < 8 ? k : k < 15 ? k - 8 : k < 21 ? k - 15 : k - 21; }
constexpr int pya(int k) { return k < 4 ? 0 : k < 7 ? 1 : k < 9 ? 2 : 3; }
constexpr int pye(int k) { return k < 4 ? k : k < 7 ? k - 4 : k < 9 ? k - 7 : 0; }

template <int OFF> __device__ __forceinline__ void dsr64(u32x2& dst, u32 addr) {
  asm volatile("ds_read_b64 %0, %1 offset:%2" : "=v"(dst) : "v"(addr), "i"(OFF) : "memory");
}
template <int OFF> __device__ __forceinline__ void dsr128(u32x4& dst, u32 addr) {
  asm volatile("ds_read_b128 %0, %1 offset:%2" : "=v"(dst) : "v"(addr), "i"(OFF) : "memory");
}
#define DSR64(dst, addr, off) dsr64<(off)>(dst, addr)
#define DSR128(dst, addr, off) dsr128<(off)>(dst, addr)
#define WAIT_FRAGS(AL, AH, B)                                                                         \
  __builtin_amdgcn_sched_barrier(0);                                                                  \
  asm volatile("s_waitcnt lgkmcnt(0)"                                                                 \
               : "+v"(AL[0]), "+v"(AL[1]), "+v"(AL[2]), "+v"(AL[3]),                                  \
                 "+v"(AH[0]), "+v"(AH[1]), "+v"(AH[2]), "+v"(AH[3]),                                  \
                 "+v"(B[0]), "+v"(B[1]), "+v"(B[2]), "+v"(B[3]), "+v"(B[4]), "+v"(B[5]), "+v"(B[6]), "+v"(B[7])  \
               :: "memory");                                                                          \
  __builtin_amdgcn_sched_barrier(0);
#define WAIT_FRAGS4(AL, AH, B)                                                                        \
  __builtin_amdgcn_sched_barrier(0);                                                                  \
  asm volatile("s_waitcnt lgkmcnt(0)"                                                                 \
               : "+v"(AL[0]), "+v"(AL[1]), "+v"(AL[2]), "+v"(AL[3]),                                  \
                 "+v"(AH[0]), "+v"(AH[1]), "+v"(AH[2]), "+v"(AH[3]),                                  \
                 "+v"(B[0]), "+v"(B[1]), "+v"(B[2]), "+v"(B[3])                                       \
               :: "memory");                                                                          \
  __builtin_amdgcn_sched_barrier(0);

template <bool IS_A>
__device__ __forceinline__ void conv_body(char* smem, int g, const u16* __restrict__ xT, const u16* __restrict__ hT,
                                          const int* __restrict__ hexp, const u16* __restrict__ dvec,
                                          const u16* __restrict__ qin, u16* __restrict__ out) {
  const int t = threadIdx.x, lane = t & 63;
  const int w = __builtin_amdgcn_readfirstlane(t >> 6);
  const int ch = (g & 127) * 8 + w;
  char* xs = smem + w * W_BYTES;
  char* hs = xs + XS_BYTES;
  const u16* xrow = xT + (size_t)ch * L_SEQ;
  const int hl = lane < 48 ? lane : lane - 48;
  int hc[3];
#pragma unroll
  for (int j = 0; j < 3; j++) {
    const int c = 48 * j + hl, sg = (c * 57) >> 10;
    hc[j] = 32 * (sg & 3) + 16 * (sg >> 2) + c - 18 * sg;
  }
  u32x4 hd[6];
  u32x2 he[6];
  u32x4 kc0, kc1;
  u32x4 xr[4];
  int esc = 0;
  if (IS_A) {
    const u16* krow = hT + (size_t)ch * KC_STRIDE;
    kc0 = *(const u32x4*)(krow + 8 * lane);
    kc1 = *(const u32x4*)(krow + 512 + 8 * (lane & 31));
    esc = hexp[ch];
#pragma unroll
    for (int j = 0; j < 4; j++) xr[j] = *(const u32x4*)(xrow + 8 * (64 * j + lane));
  } else {
    const u32 xl = lds_off(xs) + 480;
    const u16* xp = xrow + 8 * lane;
    asm volatile("s_mov_b32 m0, %1\n\ts_nop 0\n\tglobal_load_lds_dwordx4 %0, off\n\tglobal_load_lds_dwordx4 %0, off offset:1024\n\t"
                 "global_load_lds_dwordx4 %0, off offset:2048\n\tglobal_load_lds_dwordx4 %0, off offset:3072"
                 :: "v"(xp), "s"(xl) : "memory", "m0");
    const __amdgpu_buffer_rsrc_t rh = __builtin_amdgcn_make_buffer_rsrc((void*)(hT + (size_t)ch * L_SEQ), 0, L_SEQ * 2, 0x00020000);
#pragma unroll
    for (int s = 0; s < 6; s++) {
      int o1 = 16 * (hc[s % 3] - 2) + 2048 * (s / 3), o2 = o1 + 16;
      if (s == 0) { o1 = o1 >= 0 ? o1 : 0x40000000; o2 = o2 >= 0 ? o2 : 0x40000000; }
      hd[s] = __builtin_amdgcn_raw_buffer_load_b128(rh, o1, 0, 0);
      he[s] = __builtin_amdgcn_raw_buffer_load_b64(rh, o2, 0, 0);
    }
  }
  const u16 p_d = dvec[ch];
  if (lane == 0) *(u32*)(smem + FL_OFF + 4 * w) = 0u;
  __builtin_amdgcn_sched_barrier(0);
  asm volatile("s_waitcnt lgkmcnt(0)\n\ts_barrier" ::: "memory");
  __builtin_amdgcn_sched_barrier(0);
  if (IS_A) {
    char* kb = hs;
    char* lin = xs;
    const u16* kc = (const u16*)kb;
    *(u32x4*)(kb + 16 * lane) = kc0;
    *(u32x4*)(kb + 1024 + 16 * (lane & 31)) = kc1;
    __builtin_amdgcn_wave_barrier();
    const bool dir = lane < 32;
    const int a0 = dir ? 16 * lane : 512 + 8 * (lane - 32);
    u32x2 d01 = *(const u32x2*)(kb + a0), d23 = *(const u32x2*)(kb + a0 + 8);
    u32x4 o;
    o[0] = __builtin_amdgcn_perm(d01[0], d01[0], dir ? 0x03020100u : 0x01000100u);
    o[1] = __builtin_amdgcn_perm(d01[1], d01[0], dir ? 0x07060504u : 0x05040302u);
    o[2] = __builtin_amdgcn_perm(d23[0], d01[1], dir ? 0x07060504u : 0x01000100u);
    o[3] = dir ? d23[1] : __builtin_amdgcn_perm(d23[0], d01[1], 0x05040302u);
    *(u32x4*)(lin + 32 + 16 * lane) = o;
    u32 d0 = *(const u32*)(kb + 768 + 4 * lane), d1 = *(const u32*)(kb + 772 + 4 * lane);
    o[0] = __builtin_amdgcn_perm(d0, d0, 0x01000100u);
    o[1] = d0;
    o[2] = __builtin_amdgcn_perm(d0, d0, 0x03020302u);
    o[3] = __builtin_amdgcn_perm(d1, d1, 0x01000100u);
    *(u32x4*)(lin + 32 + 1024 + 16 * lane) = o;
#pragma unroll
    for (int i = 0; i < 2; i++) {
      u32 x0 = kc[512 + 64 * i + lane], x1 = kc[513 + 64 * i + lane];
      o[0] = o[1] = x0 | (x0 << 16);
      o[3] = x1 | (x1 << 16);
      o[2] = (lane & 1) ? o[3] : (x0 | (x1 << 16));
      *(u32x4*)(lin + 32 + 2048 + 1024 * i + 16 * lane) = o;
    }
    if (lane < 4) *(u32x4*)(lin + ((lane & 2) ? 4096 : 0) + 16 * lane) = (u32x4){0, 0, 0, 0};
    __builtin_amdgcn_wave_barrier();
#pragma unroll
    for (int s = 0; s < 6; s++) {
      hd[s] = *(const u32x4*)(lin + 16 * hc[s % 3] + 2048 * (s / 3));
      he[s] = *(const u32x2*)(lin + 16 * hc[s % 3] + 2048 * (s / 3) + 16);
    }
    __builtin_amdgcn_wave_barrier();
  }
#define STAGE_PIECE(set, k) {                                                          \
    const u32x4 d4 = hd[3 * (set) + ((k) >> 2)];                                       \
    const u32x2 e2 = he[3 * (set) + ((k) >> 2)];                                       \
    u32x4 o_;                                                                          \
    if (((k) & 3) == 0) o_ = d4;                                                       \
    else if (((k) & 3) == 1) o_ = (u32x4){__builtin_amdgcn_alignbit(d4[1], d4[0], 16), __builtin_amdgcn_alignbit(d4[2], d4[1], 16), \
                                          __builtin_amdgcn_alignbit(d4[3], d4[2], 16), __builtin_amdgcn_alignbit(e2[0], d4[3], 16)}; \
    else if (((k) & 3) == 2) o_ = (u32x4){d4[1], d4[2], d4[3], e2[0]};                 \
    else o_ = (u32x4){__builtin_amdgcn_alignbit(d4[2], d4[1], 16), __builtin_amdgcn_alignbit(d4[3], d4[2], 16), \
                      __builtin_amdgcn_alignbit(e2[0], d4[3], 16), __builtin_amdgcn_alignbit(e2[1], e2[0], 16)}; \
    *(u32x4*)(hw + 768 * ((k) >> 2) + ((k) & 3) * HC_BYTES) = o_; }
  char* hw = hs + 16 * hl;
#pragma unroll
  for (int k = 0; k < 12; k++) STAGE_PIECE(0, k);
  if (lane < 30) *(u32x4*)(xs + 16 * lane) = (u32x4){0, 0, 0, 0};
  if (IS_A) {
    u32 mx = 0;
#pragma unroll
    for (int j = 0; j < 4; j++)
#pragma unroll
      for (int p = 0; p < 4; p++) {
        u32 dd = xr[j][p];
        mx = max(mx, max(dd & 0x7fffu, (dd >> 16) & 0x7fffu));
      }
    mx = wave_max_u32(mx);
    const int ex = (int)(mx >> 7) - 127 - 14;
    esc += ex;
#pragma unroll
    for (int j = 0; j < 4; j++) {
      u32x4 o;
#pragma unroll
      for (int p = 0; p < 4; p++) {
        u32 dd = xr[j][3 - p];
        float lo = __builtin_ldexpf(bfhi(dd), -ex), hi = __builtin_ldexpf(bflo(dd), -ex);
        o[p] = __builtin_bit_cast(u32, __builtin_amdgcn_cvt_pkrtz(lo, hi));
      }
      *(u32x4*)(xs + 480 + 16 * (64 * j + lane)) = o;
    }
  }
  const int r = lane & 15, q = lane >> 4;
  f32x4 acc[8];
#pragma unroll
  for (int i = 0; i < 8; i++) acc[i] = (f32x4){0.f, 0.f, 0.f, 0.f};
  const int lam = 25 + r - 8 * q;
  const u32 abase = lds_off(hs) + (lam & 3) * HC_BYTES + 2 * (lam & ~3);
  const u32 bbase = lds_off(xs) + 32 * r + 16 * q;
  u32x2 AL0[4], AH0[4], AL1[4], AH1[4];
  u32x4 B0[8], B1[8];
#define LOAD_PIECE(it, k, AL, AH, B) {                                                          \
    if constexpr ((k) < 8 && ((k) & 1) == 0) DSR64(AL[((k) >> 1) & 3], abase, 64 * ((it) & 3) + 288 * (((k) >> 1) + 4 * ((it) >> 2)));   \
    else if constexpr ((k) < 8) DSR64(AH[((k) >> 1) & 3], abase, 64 * ((it) & 3) + 288 * (((k) >> 1) + 4 * ((it) >> 2)) + 8); \
    else DSR128(B[((k) - 8) & 7], bbase, 448 + 512 * ((k) - 8) - 64 * (it)); }
  __builtin_amdgcn_sched_barrier(0);
  sfor<16>([&](auto kc_) { constexpr int k = decltype(kc_)::value; LOAD_PIECE(0, k, AL0, AH0, B0); });
  __builtin_amdgcn_sched_barrier(0);
  u32x4 qv[6];
  u32x2 uv[8];
  if (IS_A) {
#pragma unroll
    for (int ct = 0; ct < 8; ct++) uv[ct] = *(const u32x2*)(xrow + 256 * ct + 16 * r + 4 * q);
  } else {
#pragma unroll
    for (int i = 0; i < 2; i++) qv[4 + i] = *(const u32x4*)(qin + (size_t)(1024 + 2 * t + i) * D_CH + (g & 127) * 8);
#pragma unroll
    for (int i = 0; i < 4; i++) qv[i] = *(const u32x4*)(qin + (size_t)(4 * (t & 255) + i) * D_CH + (g & 127) * 8);
  }
#define PIN_LO asm volatile("" : "+v"(acc[0]), "+v"(acc[1]), "+v"(acc[2]), "+v"(acc[3]));
#define PIN_HI asm volatile("" : "+v"(acc[4]), "+v"(acc[5]), "+v"(acc[6]), "+v"(acc[7]));
#define ITERX(it, AL, AH, B, NAL, NAH, NB)                                              \
  WAIT_FRAGS(AL, AH, B);                                                                \
  sfor<26>([&](auto kc_) {                                                              \
    constexpr int k_ = decltype(kc_)::value, a = pxa(k_), e = pxe(k_);                  \
    acc[a + e] = mma<IS_A>((u32x4){AL[a][0], AL[a][1], AH[a][0], AH[a][1]}, B[e], acc[a + e]); \
    if constexpr ((it) < 7) { if constexpr (k_ < 16) LOAD_PIECE((it) + 1, k_, NAL, NAH, NB); } \
    else if constexpr (k_ < 12) STAGE_PIECE(1, k_)       \
    else if constexpr (k_ < 24) LOAD_PIECE(0, k_ - 12, NAL, NAH, NB);     \
    __builtin_amdgcn_sched_barrier(0);                                                  \
  });                                                                                   \
  PIN_LO PIN_HI
#define ITERY(it, AL, AH, B, NAL, NAH, NB)                                              \
  WAIT_FRAGS4(AL, AH, B);                                                               \
  sfor<10>([&](auto kc_) {                                                              \
    constexpr int k_ = decltype(kc_)::value, a = pya(k_), e = pye(k_);                  \
    acc[4 + a + e] = mma<IS_A>((u32x4){AL[a][0], AL[a][1], AH[a][0], AH[a][1]}, B[e], acc[4 + a + e]); \
    if constexpr ((it) < 7 && k_ < 6) {                                                 \
      LOAD_PIECE((it) + 1, 2 * k_, NAL, NAH, NB);                                       \
      LOAD_PIECE((it) + 1, 2 * k_ + 1, NAL, NAH, NB);                                   \
    }                                                                                   \
    __builtin_amdgcn_sched_barrier(0);                                                  \
  });                                                                                   \
  PIN_HI
  const float dsk = bf2f(p_d);
  const __amdgpu_buffer_rsrc_t ro = __builtin_amdgcn_make_buffer_rsrc((void*)out, 0, 8 * 1024 * 1024, 0x00020000);
#define EPI1(c0_, ysp)                                                                  \
  _Pragma("unroll") for (int ci = 0; ci < 4; ci++) {                                    \
    const int l = 256 * ((c0_) + ci) + 16 * r + 4 * q;                                  \
    float x0, x1, x2, x3;                                                               \
    if (IS_A) {                                                                         \
      u32x2 xv = uv[(c0_) + ci];                                                        \
      x0 = bflo(xv[0]); x1 = bfhi(xv[0]); x2 = bflo(xv[1]); x3 = bfhi(xv[1]);           \
    } else {                                                                            \
      u32x2 xv = *(const u32x2*)(xs + 480 + 2 * ((l & ~7) + 4 - (l & 4)));              \
      x3 = bflo(xv[0]); x2 = bfhi(xv[0]); x1 = bflo(xv[1]); x0 = bfhi(xv[1]);           \
    }                                                                                   \
    float y0 = acc[(c0_) + ci][0], y1 = acc[(c0_) + ci][1], y2 = acc[(c0_) + ci][2], y3 = acc[(c0_) + ci][3]; \
    if (IS_A) {                                                                         \
      y0 = __builtin_ldexpf(y0, esc); y1 = __builtin_ldexpf(y1, esc);                   \
      y2 = __builtin_ldexpf(y2, esc); y3 = __builtin_ldexpf(y3, esc);                   \
    }                                                                                   \
    u32x2 o;                                                                            \
    o[0] = pk_bf16(rbf(y0) + rbf(x0 * dsk), rbf(y1) + rbf(x1 * dsk));                   \
    o[1] = pk_bf16(rbf(y2) + rbf(x2 * dsk), rbf(y3) + rbf(x3 * dsk));                   \
    *(u32x2*)((ysp) + 2 * (l & 1023)) = o;                                              \
  }
#define EPI2(NR, row0, qb, ysb, ystride, aux)                                           \
  {                                                                                     \
    u32 yv[8][(NR) / 2];                                                                \
    _Pragma("unroll") for (int c = 0; c < 8; c++) {                                     \
      if ((NR) == 2) yv[c][0] = *(const u32*)((ysb) + c * (ystride) + 4 * t);           \
      else { u32x2 y2 = *(const u32x2*)((ysb) + c * (ystride) + 8 * t); yv[c][0] = y2[0]; yv[c][(NR) / 2 - 1] = y2[1]; } \
    }                                                                                   \
    _Pragma("unroll") for (int i = 0; i < (NR); i++) {                                  \
      u32x4 o;                                                                          \
      _Pragma("unroll") for (int p = 0; p < 4; p++) {                                   \
        const u32 ya_ = yv[2 * p][i >> 1], yb_ = yv[2 * p + 1][i >> 1];                 \
        if (IS_A) o[p] = __builtin_amdgcn_perm(yb_, ya_, (i & 1) ? 0x07060302u : 0x05040100u); \
        else {                                                                          \
          float ya = (i & 1) ? bfhi(ya_) : bflo(ya_), yb = (i & 1) ? bfhi(yb_) : bflo(yb_); \
          o[p] = pk_bf16(ya * bflo(qv[(qb) + i][p]), yb * bfhi(qv[(qb) + i][p]));       \
        }                                                                               \
      }                                                                                 \
      __builtin_amdgcn_raw_buffer_store_b128(o, ro, (((row0) + i) * (2 * D_CH) + g * 8) * 2, 0, aux); \
    }                                                                                   \
  }
  ITERX(0, AL0, AH0, B0, AL1, AH1, B1);
  ITERX(1, AL1, AH1, B1, AL0, AH0, B0);
  ITERX(2, AL0, AH0, B0, AL1, AH1, B1);
  ITERX(3, AL1, AH1, B1, AL0, AH0, B0);
  ITERX(4, AL0, AH0, B0, AL1, AH1, B1);
  ITERX(5, AL1, AH1, B1, AL0, AH0, B0);
  ITERX(6, AL0, AH0, B0, AL1, AH1, B1);
  ITERX(7, AL1, AH1, B1, AL0, AH0, B0);
  EPI1(0, smem + YS_OFF + w * YS_BYTES);
  asm volatile("ds_write_b32 %0, %1" :: "v"(lds_off(smem + FL_OFF) + 4 * w), "v"(1u) : "memory");
  ITERY(0, AL0, AH0, B0, AL1, AH1, B1);
  ITERY(1, AL1, AH1, B1, AL0, AH0, B0);
  ITERY(2, AL0, AH0, B0, AL1, AH1, B1);
  ITERY(3, AL1, AH1, B1, AL0, AH0, B0);
  ITERY(4, AL0, AH0, B0, AL1, AH1, B1);
  ITERY(5, AL1, AH1, B1, AL0, AH0, B0);
  ITERY(6, AL0, AH0, B0, AL1, AH1, B1);
  ITERY(7, AL1, AH1, B1, AL0, AH0, B0);
  EPI1(4, hs);
  if (w < 4) {
    u32 fl;
    do {
      asm volatile("ds_read_b32 %0, %1\n\ts_waitcnt lgkmcnt(0)" : "=v"(fl) : "v"(lds_off(smem + FL_OFF) + 4 * (lane & 7)) : "memory");
    } while (__builtin_amdgcn_ballot_w64(fl == 0u) != 0ull);
    EPI2(4, 4 * t, 0, smem + YS_OFF, YS_BYTES, AUX0);
  }
  asm volatile("s_waitcnt lgkmcnt(0)\n\ts_barrier" ::: "memory");
  EPI2(2, 1024 + 2 * t, 4, smem + XS_BYTES, W_BYTES, 0);
}

__global__ __launch_bounds__(512) void conv_kernel(const u16* __restrict__ uT, const u16* __restrict__ kvT,
                                                   const u16* __restrict__ kcg, const int* __restrict__ hexp,
                                                   const u16* __restrict__ dskip, const u16* __restrict__ ssmk,
                                                   const u16* __restrict__ ssmd, const u16* __restrict__ qin,
                                                   u16* __restrict__ out) {
  extern __shared__ __attribute__((aligned(16))) char smem[];
  const int g = xcd_group(blockIdx.x);
  if (g < 128) conv_body<true>(smem, g, uT, kcg, hexp, dskip, qin, out);
  else conv_body<false>(smem, g, kvT, ssmk, hexp, ssmd, qin, out);
}

extern "C" void kernel_launch(void* const* d_in, const int* in_sizes, int n_in, void* d_out, int out_size,
                              void* d_ws, size_t ws_size, hipStream_t stream) {
  const u16* u = (const u16*)d_in[0];
  const u16* lar = (const u16*)d_in[1];
  const u16* aim = (const u16*)d_in[2];
  const u16* cre = (const u16*)d_in[3];
  const u16* cim = (const u16*)d_in[4];
  const u16* ldt = (const u16*)d_in[5];
  const u16* dsk = (const u16*)d_in[6];
  const u16* q = (const u16*)d_in[7];
  const u16* k = (const u16*)d_in[8];
  const u16* v = (const u16*)d_in[9];
  const u16* shk = (const u16*)d_in[10];
  const u16* ssmk = (const u16*)d_in[11];
  const u16* ssmd = (const u16*)d_in[12];
  char* ws = (char*)d_ws;
  u16* uT = (u16*)ws;
  u16* kvT = (u16*)(ws + (size_t)4 * 1024 * 1024);
  u16* kcg = (u16*)(ws + (size_t)8 * 1024 * 1024);
  int* hexp = (int*)(ws + (size_t)12 * 1024 * 1024);
  (void)hipFuncSetAttribute((const void*)prep_kernel, hipFuncAttributeMaxDynamicSharedMemorySize, PREP_LDS);
  (void)hipFuncSetAttribute((const void*)conv_kernel, hipFuncAttributeMaxDynamicSharedMemorySize, LDS_BYTES);
  prep_kernel<<<256, 512, PREP_LDS, stream>>>(u, lar, aim, cre, cim, ldt, k, v, shk, uT, kvT, kcg, hexp);
  conv_kernel<<<256, 512, LDS_BYTES, stream>>>(uT, kvT, kcg, hexp, dsk, ssmk, ssmd, q, (u16*)d_out);
}
```

```cpp
#include <hip/hip_runtime.h>
#include <hip/hip_bf16.h>
#include <stdint.h>
#include <utility>

typedef unsigned int u32;
typedef unsigned short u16;
typedef u32 u32x4 __attribute__((ext_vector_type(4)));
typedef u32 u32x2 __attribute__((ext_vector_type(2)));
typedef float f32x4 __attribute__((ext_vector_type(4)));
typedef __bf16 bf16x8 __attribute__((ext_vector_type(8)));
typedef _Float16 f16x8 __attribute__((ext_vector_type(8)));

#define ST_AUX 16
#define AUX0 0
#define L_SEQ 2048
#define D_CH 1024
#define N_MODE 128

__device__ __forceinline__ float bflo(u32 d) { return __uint_as_float(d << 16); }
__device__ __forceinline__ float bfhi(u32 d) { return __uint_as_float(d & 0xffff0000u); }
__device__ __forceinline__ float bf2f(u16 h) { return __uint_as_float(((u32)h) << 16); }
__device__ __forceinline__ u32 pk_bf16(float lo, float hi) {
  typedef __bf16 bf16x2 __attribute__((ext_vector_type(2)));
  typedef float f32x2 __attribute__((ext_vector_type(2)));
  f32x2 v = {lo, hi};
  bf16x2 r = __builtin_convertvector(v, bf16x2);
  return __builtin_bit_cast(u32, r);
}
__device__ __forceinline__ float rbf(float x) { return __uint_as_float(pk_bf16(0.f, x)); }

__device__ __forceinline__ u32 wave_max_u32(u32 x) {
  x = max(x, (u32)__builtin_amdgcn_update_dpp(0, (int)x, 0x111, 0xf, 0xf, true));
  x = max(x, (u32)__builtin_amdgcn_update_dpp(0, (int)x, 0x112, 0xf, 0xf, true));
  x = max(x, (u32)__builtin_amdgcn_update_dpp(0, (int)x, 0x114, 0xf, 0xf, true));
  x = max(x, (u32)__builtin_amdgcn_update_dpp(0, (int)x, 0x118, 0xf, 0xf, true));
  x = max(x, (u32)__builtin_amdgcn_update_dpp(0, (int)x, 0x142, 0xa, 0xf, true));
  x = max(x, (u32)__builtin_amdgcn_update_dpp(0, (int)x, 0x143, 0xc, 0xf, true));
  return (u32)__builtin_amdgcn_readlane((int)x, 63);
}


#define GT_ROW 288
#define GT_TAB (16 * GT_ROW)
#define KC_STRIDE 768

typedef float f32x2 __attribute__((ext_vector_type(2)));
__device__ __forceinline__ f32x2 cmul(f32x2 g, f32x2 e) {
  f32x2 t, o;
  asm("v_pk_mul_f32 %0, %1, %2 op_sel:[0,0] op_sel_hi:[0,1]" : "=v"(t) : "v"(g), "v"(e));
  asm("v_pk_fma_f32 %0, %1, %2, %3 op_sel:[1,1,0] op_sel_hi:[1,0,1] neg_lo:[1,0,0]" : "=v"(o) : "v"(g), "v"(e), "v"(t));
  return o;
}
__device__ __forceinline__ f32x2 cexp_pos(float ar, float ai, float pos) {
  const float LOG2E = 1.44269504088896340736f;
  const float I2PI_HI = 0.15915494309189533577f;
  const float I2PI_LO = 6.4206383e-9f;
  float xr = ar * pos;
  float xi = ai * pos;
  float e = __builtin_amdgcn_exp2f(xr * LOG2E);
  float r = xi * I2PI_HI;
  float err = __builtin_fmaf(xi, I2PI_HI, -r);
  float f = __builtin_amdgcn_fractf(r);
  f += __builtin_fmaf(xi, I2PI_LO, err);
  f32x2 o;
  o[0] = e * __builtin_amdgcn_cosf(f);
  o[1] = e * __builtin_amdgcn_sinf(f);
  return o;
}
__device__ __forceinline__ void split_h(float a, float b, u32& hi, u32& lo) {
  hi = __builtin_bit_cast(u32, __builtin_amdgcn_cvt_pkrtz(a, b));
  u32 l;
  asm("v_fma_mixlo_f16 %0, %1, -1.0, %2 op_sel_hi:[1,0,0]" : "=v"(l) : "v"(hi), "v"(a));
  asm("v_fma_mixhi_f16 %0, %1, -1.0, %2 op_sel:[1,0,0] op_sel_hi:[1,0,0]" : "+v"(l) : "v"(hi), "v"(b));
  lo = l;
}

template <bool IS_A>
__device__ __forceinline__ f32x4 mma(u32x4 a, u32x4 b, f32x4 c) {
  if (IS_A) return __builtin_amdgcn_mfma_f32_16x16x32_f16(__builtin_bit_cast(f16x8, a), __builtin_bit_cast(f16x8, b), c, 0, 0, 0);
  else return __builtin_amdgcn_mfma_f32_16x16x32_bf16(__builtin_bit_cast(bf16x8, a), __builtin_bit_cast(bf16x8, b), c, 0, 0, 0);
}

__device__ __forceinline__ u32 lds_off(const char* p) {
  return (u32)(uintptr_t)(const __attribute__((address_space(3))) char*)p;
}
__device__ __forceinline__ void gen_partial(char* tab, int lane, u16 p_ldt, u16 p_lar, u16 p_aim, u16 p_cre, u16 p_cim,
                                            f32x4 (&acc)[3]) {
  const float dt = rbf(expf(bf2f(p_ldt)));
  const float ar = -rbf(expf(bf2f(p_lar))) * dt;
  const float ai = bf2f(p_aim) * dt;
  f32x2 C;
  C[0] = 2.0f * (bf2f(p_cre) * dt);
  C[1] = -2.0f * (bf2f(p_cim) * dt);
  const u32 mc = wave_max_u32(max(__float_as_uint(C[0]) & 0x7fffffffu, __float_as_uint(C[1]) & 0x7fffffffu));
  const f32x2 e1 = cexp_pos(ar, -ai, 1.0f);
  const f32x2 f8 = cexp_pos(ar, ai, 8.0f);
  const f32x2 E = cexp_pos(ar, -ai, 128.0f);
  const int ec = (mc > 0u && mc < 0x7f800000u) ? __builtin_amdgcn_frexp_expf(__uint_as_float(mc)) - 14 : 0;
  C[0] = __builtin_ldexpf(C[0], -ec) * 1.0003662109375f;
  C[1] = __builtin_ldexpf(C[1], -ec) * 1.0003662109375f;
  char* wp = tab + 4 * lane;
  const int c16 = lane & 15, q = lane >> 4;
  const char* rp = tab + c16 * GT_ROW + 16 * q;
  {
    f32x2 F = {16384.0f, 0.f};
#pragma unroll
    for (int w = 0; w < 16; w++) {
      u32 hi_, lo_;
      split_h(F[0], F[1], hi_, lo_);
      *(u32*)(wp + w * GT_ROW) = hi_;
      *(u32*)(wp + w * GT_ROW + GT_TAB) = lo_;
      if (w < 15) F = cmul(F, f8);
    }
  }
  __builtin_amdgcn_wave_barrier();
  u32x4 Fh[4], Fl[4];
#pragma unroll
  for (int ks = 0; ks < 4; ks++) {
    Fh[ks] = *(const u32x4*)(rp + 64 * ks);
    Fl[ks] = *(const u32x4*)(rp + 64 * ks + GT_TAB);
  }
  __builtin_amdgcn_wave_barrier();
#define PUT_ROW(tb, row, z) *(u32*)(wp + (tb) * GT_TAB + (row) * GT_ROW) = __builtin_bit_cast(u32, __builtin_amdgcn_cvt_pkrtz((z)[0], (z)[1]))
#define TILE_MMA(ti, tb) { __builtin_amdgcn_wave_barrier();                                          \
    f32x4 a_ = {0.f, 0.f, 0.f, 0.f}, b_ = {0.f, 0.f, 0.f, 0.f};                                    \
    _Pragma("unroll") for (int ks = 0; ks < 4; ks++) {                                             \
      u32x4 Gh = *(const u32x4*)(rp + 64 * ks + (tb) * GT_TAB);                                    \
      b_ = mma<true>(Gh, Fl[ks], b_);                                                              \
      a_ = mma<true>(Gh, Fh[ks], a_);                                                              \
    }                                                                                              \
    acc[ti] = a_ + b_; __builtin_amdgcn_wave_barrier(); }
  f32x2 g[8];
  g[0] = C;
#pragma unroll
  for (int be = 1; be < 8; be++) g[be] = cmul(g[be - 1], e1);
#pragma unroll
  for (int be = 0; be < 8; be++) PUT_ROW(0, be, g[be]);
#pragma unroll
  for (int be = 0; be < 8; be++) { g[be] = cmul(g[be], E); PUT_ROW(0, 8 + be, g[be]); }
  TILE_MMA(0, 0);
#pragma unroll
  for (int al = 0; al < 2; al++)
#pragma unroll
    for (int be = 0; be < 8; be += 2) { g[be] = cmul(g[be], E); PUT_ROW(1, 4 * al + (be >> 1), g[be]); }
#pragma unroll
  for (int al = 0; al < 4; al++)
#pragma unroll
    for (int be = 0; be < 8; be += 4) { g[be] = cmul(g[be], E); PUT_ROW(1, 8 + 2 * al + (be >> 2), g[be]); }
  TILE_MMA(1, 1);
#pragma unroll
  for (int al = 0; al < 9; al++) { g[0] = cmul(g[0], E); PUT_ROW(0, al, g[0]); }
  TILE_MMA(2, 0);
#pragma unroll
  for (int i = 0; i < 3; i++)
#pragma unroll
    for (int rg = 0; rg < 4; rg++) acc[i][rg] = __builtin_ldexpf(acc[i][rg], ec - 14);
}


#define PW_BYTES (2 * GT_TAB)
#define PREP_LDS (8 * PW_BYTES + 12288)

__global__ __launch_bounds__(512) void prep_kernel(
    const u16* __restrict__ u, const u16* __restrict__ lar, const u16* __restrict__ aim,
    const u16* __restrict__ cre, const u16* __restrict__ cim, const u16* __restrict__ ldt,
    const u16* __restrict__ kin, const u16* __restrict__ vin, const u16* __restrict__ shk,
    u16* __restrict__ uT, u16* __restrict__ kvT, u16* __restrict__ kcg, int* __restrict__ hexp) {
  extern __shared__ __attribute__((aligned(16))) char smem[];
  const int b = blockIdx.x;
  const int t = threadIdx.x;
  const int lane = t & 63;
  const int wv = __builtin_amdgcn_readfirstlane(t >> 6);
  const int xcd = b & 7, idx = b >> 3;
  const int cb = xcd + 8 * (idx & 1);
  const int d = cb * 64 + (idx >> 1) * 4 + (wv & 3);
  const int po = d * N_MODE + (wv >> 2) * 64 + lane;
  const u16 p_ldt = ldt[d], p_lar = lar[po], p_aim = aim[po], p_cre = cre[po], p_cim = cim[po];
  __builtin_amdgcn_sched_barrier(0);
  const int c0 = cb * 64 + (wv & 1) * 32 + (lane & 15) * 2;
  const int l0 = (idx >> 1) * 128 + (wv >> 1) * 32 + (lane >> 4) * 8;
  const __amdgpu_buffer_rsrc_t rk = __builtin_amdgcn_make_buffer_rsrc((void*)kin, 0, L_SEQ * D_CH * 2, 0x00020000);
  const __amdgpu_buffer_rsrc_t ru = __builtin_amdgcn_make_buffer_rsrc((void*)uT, 0, 12 * 1024 * 1024, 0x00020000);
  const int vo = l0 * (D_CH * 2) + c0 * 2;
  u32 uu[8], kk[11], vv[8];
#pragma unroll
  for (int i = 0; i < 8; i++) uu[i] = *(const u32*)((const char*)u + vo + i * (D_CH * 2));
#pragma unroll
  for (int i = 0; i < 11; i++) {
    int o = vo + (i - 3) * (D_CH * 2);
    if (i < 3) o = (l0 > 0) ? o : (int)0x40000000;
    kk[i] = __builtin_amdgcn_raw_buffer_load_b32(rk, o, 0, 0);
  }
#pragma unroll
  for (int i = 0; i < 8; i++) vv[i] = *(const u32*)((const char*)vin + vo + i * (D_CH * 2));
  const u32x4 sw = *(const u32x4*)(shk + (size_t)c0 * 4);
  __builtin_amdgcn_sched_barrier(0);
  f32x4 ka[3];
  char* tab = smem + wv * PW_BYTES;
  gen_partial(tab, lane, p_ldt, p_lar, p_aim, p_cre, p_cim, ka);
  if (wv >= 4) {
#pragma unroll
    for (int i = 0; i < 3; i++) *(f32x4*)(tab + 1024 * i + 16 * lane) = ka[i];
  }
  __builtin_amdgcn_sched_barrier(0);
#pragma unroll
  for (int c = 0; c < 2; c++) {
    u32x4 o;
#pragma unroll
    for (int p = 0; p < 4; p++) o[p] = __builtin_amdgcn_perm(uu[2 * p + 1], uu[2 * p], c ? 0x07060302u : 0x05040100u);
    __builtin_amdgcn_raw_buffer_store_b128(o, ru, ((c0 + c) * L_SEQ + l0) * 2, 0, ST_AUX);
  }
#pragma unroll
  for (int c = 0; c < 2; c++) {
    const u32 s01 = sw[2 * c], s23 = sw[2 * c + 1];
    const float w0 = bflo(s01), w1 = bfhi(s01), w2 = bflo(s23), w3 = bfhi(s23);
    float kf[11];
#pragma unroll
    for (int i = 0; i < 11; i++) kf[i] = c ? bfhi(kk[i]) : bflo(kk[i]);
    float res[8];
#pragma unroll
    for (int i = 0; i < 8; i++) {
      float a = rbf(rbf(kf[i] * w3) + rbf(kf[i + 1] * w2));
      a = rbf(a + rbf(kf[i + 2] * w1));
      a = rbf(a + rbf(kf[i + 3] * w0));
      res[i] = a * (c ? bfhi(vv[i]) : bflo(vv[i]));
    }
    u32x4 o;
#pragma unroll
    for (int p = 0; p < 4; p++) o[p] = pk_bf16(res[7 - 2 * p], res[6 - 2 * p]);
    __builtin_amdgcn_raw_buffer_store_b128(o, ru, (L_SEQ * D_CH + (c0 + c) * L_SEQ + l0) * 2, 0, ST_AUX);
  }
  asm volatile("s_waitcnt lgkmcnt(0)\n\ts_barrier" ::: "memory");
  if (wv >= 4) return;
  {
    const char* ex = tab + 4 * PW_BYTES + 16 * lane;
#pragma unroll
    for (int i = 0; i < 3; i++) ka[i] += *(const f32x4*)(ex + 1024 * i);
    const int c16 = lane & 15, q = lane >> 4;
    const bool v2 = q < 2, v28 = (q == 2) && (c16 == 0);
    u32 mk = 0;
#pragma unroll
    for (int rg = 0; rg < 4; rg++) {
      mk = max(mk, max(__float_as_uint(ka[0][rg]) & 0x7fffffffu, __float_as_uint(ka[1][rg]) & 0x7fffffffu));
      u32 m2 = __float_as_uint(ka[2][rg]) & 0x7fffffffu;
      mk = max(mk, (v2 || (rg == 0 && v28)) ? m2 : 0u);
    }
    mk = wave_max_u32(mk);
    const int es = (mk > 0u && mk < 0x7f800000u) ? __builtin_amdgcn_frexp_expf(__uint_as_float(mk)) - 15 : 0;
    u16* kc = (u16*)tab;
#pragma unroll
    for (int rg = 0; rg < 4; rg++) {
      const int row = 4 * q + rg;
      kc[128 * (row >> 3) + (row & 7) + 8 * c16] = __builtin_bit_cast(u16, (_Float16)__builtin_ldexpf(ka[0][rg], -es));
      int i1 = (row < 8) ? 256 + 64 * (row >> 2) + (row & 3) + 4 * c16 : 384 + 32 * ((row - 8) >> 1) + (row & 1) + 2 * c16;
      kc[i1] = __builtin_bit_cast(u16, (_Float16)__builtin_ldexpf(ka[1][rg], -es));
      kc[512 + 16 * row + c16] = __builtin_bit_cast(u16, (_Float16)__builtin_ldexpf(ka[2][rg], -es));
    }
    __builtin_amdgcn_wave_barrier();
    u16* dst = kcg + (size_t)d * KC_STRIDE;
    __builtin_amdgcn_raw_buffer_store_b128(*(const u32x4*)(tab + 16 * lane), ru, (2 * L_SEQ * D_CH + d * KC_STRIDE + 8 * lane) * 2, 0, ST_AUX);
    if (lane < 32) __builtin_amdgcn_raw_buffer_store_b128(*(const u32x4*)(tab + 1024 + 16 * lane), ru, (2 * L_SEQ * D_CH + d * KC_STRIDE + 512 + 8 * lane) * 2, 0, ST_AUX);
    if (lane == 0) hexp[d] = es;
  }
}

#define XS_BYTES 4608
#define HC_BYTES 2368
#define HS_BYTES (4 * HC_BYTES)
#define W_BYTES (XS_BYTES + HS_BYTES)
#define YS_OFF (8 * W_BYTES)
#define YS_BYTES 2064
#define FL_OFF (YS_OFF + 8 * YS_BYTES)
#define LDS_BYTES (FL_OFF + 32)

__device__ __forceinline__ int xcd_group(int b) {
  int xcd = b & 7, idx = b >> 3;
  return ((xcd + 8 * (idx >> 3)) << 3) | (idx & 7);
}

template <class F, int... I>
__device__ __forceinline__ void sfor_impl(F&& f, std::integer_sequence<int, I...>) { (f(std::integral_constant<int, I>{}), ...); }
template <int N, class F>
__device__ __forceinline__ void sfor(F&& f) { sfor_impl(f, std::make_integer_sequence<int, N>{}); }
constexpr int pxa(int k) { return k < 8 ? 0 : k < 15 ? 1 : k < 21 ? 2 : 3; }
constexpr int pxe(int k) { return k < 8 ? k : k < 15 ? k - 8 : k < 21 ? k - 15 : k - 21; }
constexpr int pya(int k) { return k < 4 ? 0 : k < 7 ? 1 : k < 9 ? 2 : 3; }
constexpr int pye(int k) { return k < 4 ? k : k < 7 ? k - 4 : k < 9 ? k - 7 : 0; }

template <int OFF> __device__ __forceinline__ void dsr64(u32x2& dst, u32 addr) {
  asm volatile("ds_read_b64 %0, %1 offset:%2" : "=v"(dst) : "v"(addr), "i"(OFF) : "memory");
}
template <int OFF> __device__ __forceinline__ void dsr128(u32x4& dst, u32 addr) {
  asm volatile("ds_read_b128 %0, %1 offset:%2" : "=v"(dst) : "v"(addr), "i"(OFF) : "memory");
}
#define DSR64(dst, addr, off) dsr64<(off)>(dst, addr)
#define DSR128(dst, addr, off) dsr128<(off)>(dst, addr)
#define WAIT_FRAGS(AL, AH, B)                                                                         \
  __builtin_amdgcn_sched_barrier(0);                                                                  \
  asm volatile("s_waitcnt lgkmcnt(0)"                                                                 \
               : "+v"(AL[0]), "+v"(AL[1]), "+v"(AL[2]), "+v"(AL[3]),                                  \
                 "+v"(AH[0]), "+v"(AH[1]), "+v"(AH[2]), "+v"(AH[3]),                                  \
                 "+v"(B[0]), "+v"(B[1]), "+v"(B[2]), "+v"(B[3]), "+v"(B[4]), "+v"(B[5]), "+v"(B[6]), "+v"(B[7])  \
               :: "memory");                                                                          \
  __builtin_amdgcn_sched_barrier(0);
#define WAIT_FRAGS4(AL, AH, B)                                                                        \
  __builtin_amdgcn_sched_barrier(0);                                                                  \
  asm volatile("s_waitcnt lgkmcnt(0)"                                                                 \
               : "+v"(AL[0]), "+v"(AL[1]), "+v"(AL[2]), "+v"(AL[3]),                                  \
                 "+v"(AH[0]), "+v"(AH[1]), "+v"(AH[2]), "+v"(AH[3]),                                  \
                 "+v"(B[0]), "+v"(B[1]), "+v"(B[2]), "+v"(B[3])                                       \
               :: "memory");                                                                          \
  __builtin_amdgcn_sched_barrier(0);

template <bool IS_A>
__device__ __forceinline__ void conv_body(char* smem, int g, const u16* __restrict__ xT, const u16* __restrict__ hT,
                                          const int* __restrict__ hexp, const u16* __restrict__ dvec,
                                          const u16* __restrict__ qin, u16* __restrict__ out) {
  const int t = threadIdx.x, lane = t & 63;
  const int w = __builtin_amdgcn_readfirstlane(t >> 6);
  const int ch = (g & 127) * 8 + w;
  char* xs = smem + w * W_BYTES;
  char* hs = xs + XS_BYTES;
  const u16* xrow = xT + (size_t)ch * L_SEQ;
  const int hl = lane < 48 ? lane : lane - 48;
  int hc[3];
#pragma unroll
  for (int j = 0; j < 3; j++) {
    const int c = 48 * j + hl, sg = (c * 57) >> 10;
    hc[j] = 32 * (sg & 3) + 16 * (sg >> 2) + c - 18 * sg;
  }
  u32x4 hd[6];
  u32x2 he[6];
  u32x4 kc0, kc1;
  u32x4 xr[4];
  int esc = 0;
  if (IS_A) {
    const u16* krow = hT + (size_t)ch * KC_STRIDE;
    kc0 = *(const u32x4*)(krow + 8 * lane);
    kc1 = *(const u32x4*)(krow + 512 + 8 * (lane & 31));
    esc = hexp[ch];
#pragma unroll
    for (int j = 0; j < 4; j++) xr[j] = *(const u32x4*)(xrow + 8 * (64 * j + lane));
  } else {
    const u32 xl = lds_off(xs) + 480;
    const u16* xp = xrow + 8 * lane;
    asm volatile("s_mov_b32 m0, %1\n\ts_nop 0\n\tglobal_load_lds_dwordx4 %0, off\n\tglobal_load_lds_dwordx4 %0, off offset:1024\n\t"
                 "global_load_lds_dwordx4 %0, off offset:2048\n\tglobal_load_lds_dwordx4 %0, off offset:3072"
                 :: "v"(xp), "s"(xl) : "memory", "m0");
    const __amdgpu_buffer_rsrc_t rh = __builtin_amdgcn_make_buffer_rsrc((void*)(hT + (size_t)ch * L_SEQ), 0, L_SEQ * 2, 0x00020000);
#pragma unroll
    for (int s = 0; s < 6; s++) {
      int o1 = 16 * (hc[s % 3] - 2) + 2048 * (s / 3), o2 = o1 + 16;
      if (s == 0) { o1 = o1 >= 0 ? o1 : 0x40000000; o2 = o2 >= 0 ? o2 : 0x40000000; }
      hd[s] = __builtin_amdgcn_raw_buffer_load_b128(rh, o1, 0, 0);
      he[s] = __builtin_amdgcn_raw_buffer_load_b64(rh, o2, 0, 0);
    }
  }
  const u16 p_d = dvec[ch];
  if (lane == 0) *(u32*)(smem + FL_OFF + 4 * w) = 0u;
  __builtin_amdgcn_sched_barrier(0);
  asm volatile("s_waitcnt lgkmcnt(0)\n\ts_barrier" ::: "memory");
  __builtin_amdgcn_sched_barrier(0);
  if (IS_A) {
    char* kb = hs;
    char* lin = xs;
    const u16* kc = (const u16*)kb;
    *(u32x4*)(kb + 16 * lane) = kc0;
    *(u32x4*)(kb + 1024 + 16 * (lane & 31)) = kc1;
    __builtin_amdgcn_wave_barrier();
    const bool dir = lane < 32;
    const int a0 = dir ? 16 * lane : 512 + 8 * (lane - 32);
    u32x2 d01 = *(const u32x2*)(kb + a0), d23 = *(const u32x2*)(kb + a0 + 8);
    u32x4 o;
    o[0] = __builtin_amdgcn_perm(d01[0], d01[0], dir ? 0x03020100u : 0x01000100u);
    o[1] = __builtin_amdgcn_perm(d01[1], d01[0], dir ? 0x07060504u : 0x05040302u);
    o[2] = __builtin_amdgcn_perm(d23[0], d01[1], dir ? 0x07060504u : 0x01000100u);
    o[3] = dir ? d23[1] : __builtin_amdgcn_perm(d23[0], d01[1], 0x05040302u);
    *(u32x4*)(lin + 32 + 16 * lane) = o;
    u32 d0 = *(const u32*)(kb + 768 + 4 * lane), d1 = *(const u32*)(kb + 772 + 4 * lane);
    o[0] = __builtin_amdgcn_perm(d0, d0, 0x01000100u);
    o[1] = d0;
    o[2] = __builtin_amdgcn_perm(d0, d0, 0x03020302u);
    o[3] = __builtin_amdgcn_perm(d1, d1, 0x01000100u);
    *(u32x4*)(lin + 32 + 1024 + 16 * lane) = o;
#pragma unroll
    for (int i = 0; i < 2; i++) {
      u32 x0 = kc[512 + 64 * i + lane], x1 = kc[513 + 64 * i + lane];
      o[0] = o[1] = x0 | (x0 << 16);
      o[3] = x1 | (x1 << 16);
      o[2] = (lane & 1) ? o[3] : (x0 | (x1 << 16));
      *(u32x4*)(lin + 32 + 2048 + 1024 * i + 16 * lane) = o;
    }
    if (lane < 4) *(u32x4*)(lin + ((lane & 2) ? 4096 : 0) + 16 * lane) = (u32x4){0, 0, 0, 0};
    __builtin_amdgcn_wave_barrier();
#pragma unroll
    for (int s = 0; s < 6; s++) {
      hd[s] = *(const u32x4*)(lin + 16 * hc[s % 3] + 2048 * (s / 3));
      he[s] = *(const u32x2*)(lin + 16 * hc[s % 3] + 2048 * (s / 3) + 16);
    }
    __builtin_amdgcn_wave_barrier();
  }
#define STAGE_PIECE(set, k) {                                                          \
    const u32x4 d4 = hd[3 * (set) + ((k) >> 2)];                                       \
    const u32x2 e2 = he[3 * (set) + ((k) >> 2)];                                       \
    u32x4 o_;                                                                          \
    if (((k) & 3) == 0) o_ = d4;                                                       \
    else if (((k) & 3) == 1) o_ = (u32x4){__builtin_amdgcn_alignbit(d4[1], d4[0], 16), __builtin_amdgcn_alignbit(d4[2], d4[1], 16), \
                                          __builtin_amdgcn_alignbit(d4[3], d4[2], 16), __builtin_amdgcn_alignbit(e2[0], d4[3], 16)}; \
    else if (((k) & 3) == 2) o_ = (u32x4){d4[1], d4[2], d4[3], e2[0]};                 \
    else o_ = (u32x4){__builtin_amdgcn_alignbit(d4[2], d4[1], 16), __builtin_amdgcn_alignbit(d4[3], d4[2], 16), \
                      __builtin_amdgcn_alignbit(e2[0], d4[3], 16), __builtin_amdgcn_alignbit(e2[1], e2[0], 16)}; \
    *(u32x4*)(hw + 768 * ((k) >> 2) + ((k) & 3) * HC_BYTES) = o_; }
  char* hw = hs + 16 * hl;
#pragma unroll
  for (int k = 0; k < 12; k++) STAGE_PIECE(0, k);
  if (lane < 30) *(u32x4*)(xs + 16 * lane) = (u32x4){0, 0, 0, 0};
  if (IS_A) {
    u32 mx = 0;
#pragma unroll
    for (int j = 0; j < 4; j++)
#pragma unroll
      for (int p = 0; p < 4; p++) {
        u32 dd = xr[j][p];
        mx = max(mx, max(dd & 0x7fffu, (dd >> 16) & 0x7fffu));
      }
    mx = wave_max_u32(mx);
    const int ex = (int)(mx >> 7) - 127 - 14;
    esc += ex;
#pragma unroll
    for (int j = 0; j < 4; j++) {
      u32x4 o;
#pragma unroll
      for (int p = 0; p < 4; p++) {
        u32 dd = xr[j][3 - p];
        float lo = __builtin_ldexpf(bfhi(dd), -ex), hi = __builtin_ldexpf(bflo(dd), -ex);
        o[p] = __builtin_bit_cast(u32, __builtin_amdgcn_cvt_pkrtz(lo, hi));
      }
      *(u32x4*)(xs + 480 + 16 * (64 * j + lane)) = o;
    }
  }
  float dsk = bf2f(p_d);
  asm volatile("" : "+v"(dsk));
  const int r = lane & 15, q = lane >> 4;
  f32x4 acc[8];
#pragma unroll
  for (int i = 0; i < 8; i++) acc[i] = (f32x4){0.f, 0.f, 0.f, 0.f};
  const int lam = 25 + r - 8 * q;
  const u32 abase = lds_off(hs) + (lam & 3) * HC_BYTES + 2 * (lam & ~3);
  const u32 bbase = lds_off(xs) + 32 * r + 16 * q;
  u32x2 AL0[4], AH0[4], AL1[4], AH1[4];
  u32x4 B0[8], B1[8];
#define LOAD_PIECE(it, k, AL, AH, B) {                                                          \
    if constexpr ((k) < 8 && ((k) & 1) == 0) DSR64(AL[((k) >> 1) & 3], abase, 64 * ((it) & 3) + 288 * (((k) >> 1) + 4 * ((it) >> 2)));   \
    else if constexpr ((k) < 8) DSR64(AH[((k) >> 1) & 3], abase, 64 * ((it) & 3) + 288 * (((k) >> 1) + 4 * ((it) >> 2)) + 8); \
    else DSR128(B[((k) - 8) & 7], bbase, 448 + 512 * ((k) - 8) - 64 * (it)); }
  __builtin_amdgcn_sched_barrier(0);
  sfor<16>([&](auto kc_) { constexpr int k = decltype(kc_)::value; LOAD_PIECE(0, k, AL0, AH0, B0); });
  __builtin_amdgcn_sched_barrier(0);
  const __amdgpu_buffer_rsrc_t rq = __builtin_amdgcn_make_buffer_rsrc((void*)qin, 0, 1024 * D_CH * 2, 0x00020000);
  u32x4 qv[6];
  u32x2 uv[8];
  if (IS_A) {
#pragma unroll
    for (int ct = 0; ct < 8; ct++) uv[ct] = *(const u32x2*)(xrow + 256 * ct + 16 * r + 4 * q);
  } else {
#pragma unroll
    for (int i = 0; i < 2; i++) qv[4 + i] = *(const u32x4*)(qin + (size_t)(1024 + 2 * t + i) * D_CH + (g & 127) * 8);
#pragma unroll
    for (int i = 0; i < 4; i++)
      qv[i] = __builtin_amdgcn_raw_buffer_load_b128(rq, ((4 * t + i) * D_CH + (g & 127) * 8) * 2, 0, 0);
  }
#define PIN_LO asm volatile("" : "+v"(acc[0]), "+v"(acc[1]), "+v"(acc[2]), "+v"(acc[3]));
#define PIN_HI asm volatile("" : "+v"(acc[4]), "+v"(acc[5]), "+v"(acc[6]), "+v"(acc[7]));
#define ITERX(it, AL, AH, B, NAL, NAH, NB)                                              \
  WAIT_FRAGS(AL, AH, B);                                                                \
  sfor<26>([&](auto kc_) {                                                              \
    constexpr int k_ = decltype(kc_)::value, a = pxa(k_), e = pxe(k_);                  \
    acc[a + e] = mma<IS_A>((u32x4){AL[a][0], AL[a][1], AH[a][0], AH[a][1]}, B[e], acc[a + e]); \
    if constexpr ((it) < 7) { if constexpr (k_ < 16) LOAD_PIECE((it) + 1, k_, NAL, NAH, NB); } \
    else if constexpr (k_ < 12) STAGE_PIECE(1, k_)       \
    else if constexpr (k_ < 24) LOAD_PIECE(0, k_ - 12, NAL, NAH, NB);     \
    __builtin_amdgcn_sched_barrier(0);                                                  \
  });                                                                                   \
  PIN_LO PIN_HI
#define ITERY(it, AL, AH, B, NAL, NAH, NB)                                              \
  WAIT_FRAGS4(AL, AH, B);                                                               \
  sfor<10>([&](auto kc_) {                                                              \
    constexpr int k_ = decltype(kc_)::value, a = pya(k_), e = pye(k_);                  \
    acc[4 + a + e] = mma<IS_A>((u32x4){AL[a][0], AL[a][1], AH[a][0], AH[a][1]}, B[e], acc[4 + a + e]); \
    if constexpr ((it) < 7 && k_ < 6) {                                                 \
      LOAD_PIECE((it) + 1, 2 * k_, NAL, NAH, NB);                                       \
      LOAD_PIECE((it) + 1, 2 * k_ + 1, NAL, NAH, NB);                                   \
    }                                                                                   \
    __builtin_amdgcn_sched_barrier(0);                                                  \
  });                                                                                   \
  PIN_HI
  const __amdgpu_buffer_rsrc_t ro = __builtin_amdgcn_make_buffer_rsrc((void*)out, 0, 8 * 1024 * 1024, 0x00020000);
#define EPI1(c0_, ysp)                                                                  \
  if (!IS_A) {                                                                          \
    _Pragma("unroll") for (int ci = 0; ci < 4; ci++)                                    \
      uv[(c0_) + ci] = *(const u32x2*)(xs + 480 + 512 * ((c0_) + ci) + 32 * r + 16 * (q >> 1) + 8 - 8 * (q & 1)); \
    __builtin_amdgcn_sched_barrier(0);                                                  \
  }                                                                                     \
  _Pragma("unroll") for (int ci = 0; ci < 4; ci++) {                                    \
    const int l = 256 * ((c0_) + ci) + 16 * r + 4 * q;                                  \
    float x0, x1, x2, x3;                                                               \
    const u32x2 xv = uv[(c0_) + ci];                                                    \
    if (IS_A) { x0 = bflo(xv[0]); x1 = bfhi(xv[0]); x2 = bflo(xv[1]); x3 = bfhi(xv[1]); } \
    else { x3 = bflo(xv[0]); x2 = bfhi(xv[0]); x1 = bflo(xv[1]); x0 = bfhi(xv[1]); }    \
    float y0 = acc[(c0_) + ci][0], y1 = acc[(c0_) + ci][1], y2 = acc[(c0_) + ci][2], y3 = acc[(c0_) + ci][3]; \
    if (IS_A) {                                                                         \
      y0 = __builtin_ldexpf(y0, esc); y1 = __builtin_ldexpf(y1, esc);                   \
      y2 = __builtin_ldexpf(y2, esc); y3 = __builtin_ldexpf(y3, esc);                   \
    }                                                                                   \
    u32x2 o;                                                                            \
    o[0] = pk_bf16(rbf(y0) + rbf(x0 * dsk), rbf(y1) + rbf(x1 * dsk));                   \
    o[1] = pk_bf16(rbf(y2) + rbf(x2 * dsk), rbf(y3) + rbf(x3 * dsk));                   \
    *(u32x2*)((ysp) + 2 * (l & 1023)) = o;                                              \
  }
#define EPI2(NR, row0, qb, ysb, ystride, aux)                                           \
  {                                                                                     \
    u32 yv[8][(NR) / 2];                                                                \
    _Pragma("unroll") for (int c = 0; c < 8; c++) {                                     \
      if ((NR) == 2) yv[c][0] = *(const u32*)((ysb) + c * (ystride) + 4 * t);           \
      else { u32x2 y2 = *(const u32x2*)((ysb) + c * (ystride) + 8 * t); yv[c][0] = y2[0]; yv[c][(NR) / 2 - 1] = y2[1]; } \
    }                                                                                   \
    _Pragma("unroll") for (int i = 0; i < (NR); i++) {                                  \
      u32x4 o;                                                                          \
      _Pragma("unroll") for (int p = 0; p < 4; p++) {                                   \
        const u32 ya_ = yv[2 * p][i >> 1], yb_ = yv[2 * p + 1][i >> 1];                 \
        if (IS_A) o[p] = __builtin_amdgcn_perm(yb_, ya_, (i & 1) ? 0x07060302u : 0x05040100u); \
        else {                                                                          \
          float ya = (i & 1) ? bfhi(ya_) : bflo(ya_), yb = (i & 1) ? bfhi(yb_) : bflo(yb_); \
          o[p] = pk_bf16(ya * bflo(qv[(qb) + i][p]), yb * bfhi(qv[(qb) + i][p]));       \
        }                                                                               \
      }                                                                                 \
      __builtin_amdgcn_raw_buffer_store_b128(o, ro, (((row0) + i) * (2 * D_CH) + g * 8) * 2, 0, aux); \
    }                                                                                   \
  }
  ITERX(0, AL0, AH0, B0, AL1, AH1, B1);
  ITERX(1, AL1, AH1, B1, AL0, AH0, B0);
  ITERX(2, AL0, AH0, B0, AL1, AH1, B1);
  ITERX(3, AL1, AH1, B1, AL0, AH0, B0);
  ITERX(4, AL0, AH0, B0, AL1, AH1, B1);
  ITERX(5, AL1, AH1, B1, AL0, AH0, B0);
  ITERX(6, AL0, AH0, B0, AL1, AH1, B1);
  ITERX(7, AL1, AH1, B1, AL0, AH0, B0);
  EPI1(0, smem + YS_OFF + w * YS_BYTES);
  asm volatile("ds_write_b32 %0, %1" :: "v"(lds_off(smem + FL_OFF) + 4 * w), "v"(1u) : "memory");
  ITERY(0, AL0, AH0, B0, AL1, AH1, B1);
  ITERY(1, AL1, AH1, B1, AL0, AH0, B0);
  ITERY(2, AL0, AH0, B0, AL1, AH1, B1);
  ITERY(3, AL1, AH1, B1, AL0, AH0, B0);
  ITERY(4, AL0, AH0, B0, AL1, AH1, B1);
  ITERY(5, AL1, AH1, B1, AL0, AH0, B0);
  ITERY(6, AL0, AH0, B0, AL1, AH1, B1);
  ITERY(7, AL1, AH1, B1, AL0, AH0, B0);
  EPI1(4, hs);
  if (w < 4) {
    u32 fl;
    do {
      asm volatile("ds_read_b32 %0, %1\n\ts_waitcnt lgkmcnt(0)" : "=v"(fl) : "v"(lds_off(smem + FL_OFF) + 4 * (lane & 7)) : "memory");
    } while (__builtin_amdgcn_ballot_w64(fl == 0u) != 0ull);
    EPI2(4, 4 * t, 0, smem + YS_OFF, YS_BYTES, AUX0);
  }
  asm volatile("s_waitcnt lgkmcnt(0)\n\ts_barrier" ::: "memory");
  EPI2(2, 1024 + 2 * t, 4, smem + XS_BYTES, W_BYTES, 0);
}

__global__ __launch_bounds__(512) void conv_kernel(const u16* __restrict__ uT, const u16* __restrict__ kvT,
                                                   const u16* __restrict__ kcg, const int* __restrict__ hexp,
                                                   const u16* __restrict__ dskip, const u16* __restrict__ ssmk,
                                                   const u16* __restrict__ ssmd, const u16* __restrict__ qin,
                                                   u16* __restrict__ out) {
  extern __shared__ __attribute__((aligned(16))) char smem[];
  const int g = xcd_group(blockIdx.x);
  if (g < 128) conv_body<true>(smem, g, uT, kcg, hexp, dskip, qin, out);
  else conv_body<false>(smem, g, kvT, ssmk, hexp, ssmd, qin, out);
}

extern "C" void kernel_launch(void* const* d_in, const int* in_sizes, int n_in, void* d_out, int out_size,
                              void* d_ws, size_t ws_size, hipStream_t stream) {
  const u16* u = (const u16*)d_in[0];
  const u16* lar = (const u16*)d_in[1];
  const u16* aim = (const u16*)d_in[2];
  const u16* cre = (const u16*)d_in[3];
  const u16* cim = (const u16*)d_in[4];
  const u16* ldt = (const u16*)d_in[5];
  const u16* dsk = (const u16*)d_in[6];
  const u16* q = (const u16*)d_in[7];
  const u16* k = (const u16*)d_in[8];
  const u16* v = (const u16*)d_in[9];
  const u16* shk = (const u16*)d_in[10];
  const u16* ssmk = (const u16*)d_in[11];
  const u16* ssmd = (const u16*)d_in[12];
  char* ws = (char*)d_ws;
  u16* uT = (u16*)ws;
  u16* kvT = (u16*)(ws + (size_t)4 * 1024 * 1024);
  u16* kcg = (u16*)(ws + (size_t)8 * 1024 * 1024);
  int* hexp = (int*)(ws + (size_t)12 * 1024 * 1024);
  (void)hipFuncSetAttribute((const void*)prep_kernel, hipFuncAttributeMaxDynamicSharedMemorySize, PREP_LDS);
  (void)hipFuncSetAttribute((const void*)conv_kernel, hipFuncAttributeMaxDynamicSharedMemorySize, LDS_BYTES);
  prep_kernel<<<256, 512, PREP_LDS, stream>>>(u, lar, aim, cre, cim, ldt, k, v, shk, uT, kvT, kcg, hexp);
  conv_kernel<<<256, 512, LDS_BYTES, stream>>>(uT, kvT, kcg, hexp, dsk, ssmk, ssmd, q, (u16*)d_out);
}
```

```cpp
#include <hip/hip_runtime.h>
#include <hip/hip_bf16.h>
#include <stdint.h>
#include <utility>

typedef unsigned int u32;
typedef unsigned short u16;
typedef u32 u32x4 __attribute__((ext_vector_type(4)));
typedef u32 u32x2 __attribute__((ext_vector_type(2)));
typedef float f32x4 __attribute__((ext_vector_type(4)));
typedef __bf16 bf16x8 __attribute__((ext_vector_type(8)));
typedef _Float16 f16x8 __attribute__((ext_vector_type(8)));

#define ST_AUX 16
#define AUX0 0
#define L_SEQ 2048
#define D_CH 1024
#define N_MODE 128

__device__ __forceinline__ float bflo(u32 d) { return __uint_as_float(d << 16); }
__device__ __forceinline__ float bfhi(u32 d) { return __uint_as_float(d & 0xffff0000u); }
__device__ __forceinline__ float bf2f(u16 h) { return __uint_as_float(((u32)h) << 16); }
__device__ __forceinline__ u32 pk_bf16(float lo, float hi) {
  typedef __bf16 bf16x2 __attribute__((ext_vector_type(2)));
  typedef float f32x2 __attribute__((ext_vector_type(2)));
  f32x2 v = {lo, hi};
  bf16x2 r = __builtin_convertvector(v, bf16x2);
  return __builtin_bit_cast(u32, r);
}
__device__ __forceinline__ float rbf(float x) { return __uint_as_float(pk_bf16(0.f, x)); }

__device__ __forceinline__ u32 wave_max_u32(u32 x) {
  x = max(x, (u32)__builtin_amdgcn_update_dpp(0, (int)x, 0x111, 0xf, 0xf, true));
  x = max(x, (u32)__builtin_amdgcn_update_dpp(0, (int)x, 0x112, 0xf, 0xf, true));
  x = max(x, (u32)__builtin_amdgcn_update_dpp(0, (int)x, 0x114, 0xf, 0xf, true));
  x = max(x, (u32)__builtin_amdgcn_update_dpp(0, (int)x, 0x118, 0xf, 0xf, true));
  x = max(x, (u32)__builtin_amdgcn_update_dpp(0, (int)x, 0x142, 0xa, 0xf, true));
  x = max(x, (u32)__builtin_amdgcn_update_dpp(0, (int)x, 0x143, 0xc, 0xf, true));
  return (u32)__builtin_amdgcn_readlane((int)x, 63);
}


#define GT_ROW 288
#define GT_TAB (16 * GT_ROW)
#define KC_STRIDE 768

typedef float f32x2 __attribute__((ext_vector_type(2)));
__device__ __forceinline__ f32x2 cmul(f32x2 g, f32x2 e) {
  f32x2 t, o;
  asm("v_pk_mul_f32 %0, %1, %2 op_sel:[0,0] op_sel_hi:[0,1]" : "=v"(t) : "v"(g), "v"(e));
  asm("v_pk_fma_f32 %0, %1, %2, %3 op_sel:[1,1,0] op_sel_hi:[1,0,1] neg_lo:[1,0,0]" : "=v"(o) : "v"(g), "v"(e), "v"(t));
  return o;
}
__device__ __forceinline__ f32x2 cexp_pos(float ar, float ai, float pos) {
  const float LOG2E = 1.44269504088896340736f;
  const float I2PI_HI = 0.15915494309189533577f;
  const float I2PI_LO = 6.4206383e-9f;
  float xr = ar * pos;
  float xi = ai * pos;
  float e = __builtin_amdgcn_exp2f(xr * LOG2E);
  float r = xi * I2PI_HI;
  float err = __builtin_fmaf(xi, I2PI_HI, -r);
  float f = __builtin_amdgcn_fractf(r);
  f += __builtin_fmaf(xi, I2PI_LO, err);
  f32x2 o;
  o[0] = e * __builtin_amdgcn_cosf(f);
  o[1] = e * __builtin_amdgcn_sinf(f);
  return o;
}
__device__ __forceinline__ void split_h(float a, float b, u32& hi, u32& lo) {
  hi = __builtin_bit_cast(u32, __builtin_amdgcn_cvt_pkrtz(a, b));
  u32 l;
  asm("v_fma_mixlo_f16 %0, %1, -1.0, %2 op_sel_hi:[1,0,0]" : "=v"(l) : "v"(hi), "v"(a));
  asm("v_fma_mixhi_f16 %0, %1, -1.0, %2 op_sel:[1,0,0] op_sel_hi:[1,0,0]" : "+v"(l) : "v"(hi), "v"(b));
  lo = l;
}

template <bool IS_A>
__device__ __forceinline__ f32x4 mma(u32x4 a, u32x4 b, f32x4 c) {
  if (IS_A) return __builtin_amdgcn_mfma_f32_16x16x32_f16(__builtin_bit_cast(f16x8, a), __builtin_bit_cast(f16x8, b), c, 0, 0, 0);
  else return __builtin_amdgcn_mfma_f32_16x16x32_bf16(__builtin_bit_cast(bf16x8, a), __builtin_bit_cast(bf16x8, b), c, 0, 0, 0);
}

__device__ __forceinline__ u32 lds_off(const char* p) {
  return (u32)(uintptr_t)(const __attribute__((address_space(3))) char*)p;
}
__device__ __forceinline__ void gen_partial(char* tab, int lane, u16 p_ldt, u16 p_lar, u16 p_aim, u16 p_cre, u16 p_cim,
                                            f32x4 (&acc)[3]) {
  const float dt = rbf(expf(bf2f(p_ldt)));
  const float ar = -rbf(expf(bf2f(p_lar))) * dt;
  const float ai = bf2f(p_aim) * dt;
  f32x2 C;
  C[0] = 2.0f * (bf2f(p_cre) * dt);
  C[1] = -2.0f * (bf2f(p_cim) * dt);
  const u32 mc = wave_max_u32(max(__float_as_uint(C[0]) & 0x7fffffffu, __float_as_uint(C[1]) & 0x7fffffffu));
  const f32x2 e1 = cexp_pos(ar, -ai, 1.0f);
  const f32x2 f8 = cexp_pos(ar, ai, 8.0f);
  const f32x2 E = cexp_pos(ar, -ai, 128.0f);
  const int ec = (mc > 0u && mc < 0x7f800000u) ? __builtin_amdgcn_frexp_expf(__uint_as_float(mc)) - 14 : 0;
  C[0] = __builtin_ldexpf(C[0], -ec) * 1.0003662109375f;
  C[1] = __builtin_ldexpf(C[1], -ec) * 1.0003662109375f;
  char* wp = tab + 4 * lane;
  const int c16 = lane & 15, q = lane >> 4;
  const char* rp = tab + c16 * GT_ROW + 16 * q;
  {
    f32x2 F = {16384.0f, 0.f};
#pragma unroll
    for (int w = 0; w < 16; w++) {
      u32 hi_, lo_;
      split_h(F[0], F[1], hi_, lo_);
      *(u32*)(wp + w * GT_ROW) = hi_;
      *(u32*)(wp + w * GT_ROW + GT_TAB) = lo_;
      if (w < 15) F = cmul(F, f8);
    }
  }
  __builtin_amdgcn_wave_barrier();
  u32x4 Fh[4], Fl[4];
#pragma unroll
  for (int ks = 0; ks < 4; ks++) {
    Fh[ks] = *(const u32x4*)(rp + 64 * ks);
    Fl[ks] = *(const u32x4*)(rp + 64 * ks + GT_TAB);
  }
  __builtin_amdgcn_wave_barrier();
#define PUT_ROW(tb, row, z) *(u32*)(wp + (tb) * GT_TAB + (row) * GT_ROW) = __builtin_bit_cast(u32, __builtin_amdgcn_cvt_pkrtz((z)[0], (z)[1]))
#define TILE_MMA(ti, tb) { __builtin_amdgcn_wave_barrier();                                          \
    f32x4 a_ = {0.f, 0.f, 0.f, 0.f}, b_ = {0.f, 0.f, 0.f, 0.f};                                    \
    _Pragma("unroll") for (int ks = 0; ks < 4; ks++) {                                             \
      u32x4 Gh = *(const u32x4*)(rp + 64 * ks + (tb) * GT_TAB);                                    \
      b_ = mma<true>(Gh, Fl[ks], b_);                                                              \
      a_ = mma<true>(Gh, Fh[ks], a_);                                                              \
    }                                                                                              \
    acc[ti] = a_ + b_; __builtin_amdgcn_wave_barrier(); }
  f32x2 g[8];
  g[0] = C;
#pragma unroll
  for (int be = 1; be < 8; be++) g[be] = cmul(g[be - 1], e1);
#pragma unroll
  for (int be = 0; be < 8; be++) PUT_ROW(0, be, g[be]);
#pragma unroll
  for (int be = 0; be < 8; be++) { g[be] = cmul(g[be], E); PUT_ROW(0, 8 + be, g[be]); }
  TILE_MMA(0, 0);
#pragma unroll
  for (int al = 0; al < 2; al++)
#pragma unroll
    for (int be = 0; be < 8; be += 2) { g[be] = cmul(g[be], E); PUT_ROW(1, 4 * al + (be >> 1), g[be]); }
#pragma unroll
  for (int al = 0; al < 4; al++)
#pragma unroll
    for (int be = 0; be < 8; be += 4) { g[be] = cmul(g[be], E); PUT_ROW(1, 8 + 2 * al + (be >> 2), g[be]); }
  TILE_MMA(1, 1);
#pragma unroll
  for (int al = 0; al < 9; al++) { g[0] = cmul(g[0], E); PUT_ROW(0, al, g[0]); }
  TILE_MMA(2, 0);
#pragma unroll
  for (int i = 0; i < 3; i++)
#pragma unroll
    for (int rg = 0; rg < 4; rg++) acc[i][rg] = __builtin_ldexpf(acc[i][rg], ec - 14);
}


#define PW_BYTES (2 * GT_TAB)
#define TU_OFF (8 * PW_BYTES)
#define TV_OFF (TU_OFF + 16384)
#define TK_OFF (TV_OFF + 16384 + 512)
#define PREP_LDS (TK_OFF + 16384)

__device__ __forceinline__ void dma16(const void* g, u32 lds_base) {
  asm volatile("s_mov_b32 m0, %1\n\ts_nop 0\n\tglobal_load_lds_dwordx4 %0, off" :: "v"(g), "s"(lds_base) : "memory", "m0");
}

__global__ __launch_bounds__(512) void prep_kernel(
    const u16* __restrict__ u, const u16* __restrict__ lar, const u16* __restrict__ aim,
    const u16* __restrict__ cre, const u16* __restrict__ cim, const u16* __restrict__ ldt,
    const u16* __restrict__ kin, const u16* __restrict__ vin, const u16* __restrict__ shk,
    u16* __restrict__ uT, u16* __restrict__ kvT, u16* __restrict__ kcg, int* __restrict__ hexp) {
  extern __shared__ __attribute__((aligned(16))) char smem[];
  const int b = blockIdx.x;
  const int t = threadIdx.x;
  const int lane = t & 63;
  const int wv = __builtin_amdgcn_readfirstlane(t >> 6);
  const int xcd = b & 7, idx = b >> 3;
  const int cb = xcd + 8 * (idx & 1);
  const int rt = idx >> 1;
  const u32 sb = lds_off(smem);
  const int c0 = cb * 64 + (wv & 1) * 32 + (lane & 15) * 2;
  const int rg = (wv >> 1) * 4 + (lane >> 4);
  const int l0 = rt * 128 + rg * 8;
  if (wv == 0 && lane < 24) {
    if (rt > 0) dma16((const char*)kin + (size_t)(rt * 128 - 3 + (lane >> 3)) * (D_CH * 2) + cb * 128 + ((lane & 7) ^ 4) * 16, sb + TK_OFF - 384);
    else *(u32x4*)(smem + TK_OFF - 384 + 16 * lane) = (u32x4){0, 0, 0, 0};
  }
  const int d = cb * 64 + rt * 4 + (wv & 3);
  const int po = (d * N_MODE + (wv >> 2) * 64 + lane) * 2;
  u32 p_ldt, p_lar, p_aim, p_cre, p_cim;
  u32x4 sw;
  asm volatile("global_load_dwordx4 %5, %12, %13\n\tglobal_load_ushort %0, %6, %7\n\tglobal_load_ushort %1, %8, %9\n\t"
               "global_load_ushort %2, %8, %10\n\tglobal_load_ushort %3, %8, %11\n\tglobal_load_ushort %4, %8, %14"
               : "=&v"(p_ldt), "=&v"(p_lar), "=&v"(p_aim), "=&v"(p_cre), "=&v"(p_cim), "=&v"(sw)
               : "v"(d * 2), "s"(ldt), "v"(po), "s"(lar), "s"(aim), "s"(cre), "v"(c0 * 8), "s"(shk), "s"(cim) : "memory");
  {
    const size_t go = (size_t)(rt * 128 + wv * 16 + (lane >> 3)) * (D_CH * 2) + cb * 128;
    const size_t g0 = go + (lane & 7) * 16, g1 = go + 8 * (D_CH * 2) + ((lane & 7) ^ 4) * 16;
    dma16((const char*)kin + g0, sb + TK_OFF + wv * 2048);
    dma16((const char*)kin + g1, sb + TK_OFF + wv * 2048 + 1024);
    dma16((const char*)vin + g0, sb + TV_OFF + wv * 2048);
    dma16((const char*)vin + g1, sb + TV_OFF + wv * 2048 + 1024);
    dma16((const char*)u + g0, sb + TU_OFF + wv * 2048);
    dma16((const char*)u + g1, sb + TU_OFF + wv * 2048 + 1024);
  }
  asm volatile("s_waitcnt vmcnt(6)" : "+v"(p_ldt), "+v"(p_lar), "+v"(p_aim), "+v"(p_cre), "+v"(p_cim), "+v"(sw) :: "memory");
  const __amdgpu_buffer_rsrc_t ru = __builtin_amdgcn_make_buffer_rsrc((void*)uT, 0, 12 * 1024 * 1024, 0x00020000);
  __builtin_amdgcn_sched_barrier(0);
  f32x4 ka[3];
  char* tab = smem + wv * PW_BYTES;
  gen_partial(tab, lane, (u16)p_ldt, (u16)p_lar, (u16)p_aim, (u16)p_cre, (u16)p_cim, ka);
  if (wv >= 4) {
#pragma unroll
    for (int i = 0; i < 3; i++) *(f32x4*)(tab + 1024 * i + 16 * lane) = ka[i];
  }
  __builtin_amdgcn_sched_barrier(0);
  asm volatile("s_waitcnt vmcnt(0) lgkmcnt(0)\n\ts_barrier" ::: "memory");
  u32 uu[8], kk[11], vv[8];
  {
    const int cd = ((wv & 1) * 64 + (lane & 15) * 4);
    const int to = rg * 1024 + (cd ^ ((rg & 1) * 64));
#pragma unroll
    for (int i = 0; i < 8; i++) uu[i] = *(const u32*)(smem + TU_OFF + to + 128 * i);
#pragma unroll
    for (int i = 0; i < 3; i++) kk[i] = *(const u32*)(smem + TK_OFF - 384 + (to ^ 64) + 128 * i);
#pragma unroll
    for (int i = 0; i < 8; i++) kk[3 + i] = *(const u32*)(smem + TK_OFF + to + 128 * i);
#pragma unroll
    for (int i = 0; i < 8; i++) vv[i] = *(const u32*)(smem + TV_OFF + to + 128 * i);
  }
#pragma unroll
  for (int c = 0; c < 2; c++) {
    u32x4 o;
#pragma unroll
    for (int p = 0; p < 4; p++) o[p] = __builtin_amdgcn_perm(uu[2 * p + 1], uu[2 * p], c ? 0x07060302u : 0x05040100u);
    __builtin_amdgcn_raw_buffer_store_b128(o, ru, ((c0 + c) * L_SEQ + l0) * 2, 0, ST_AUX);
  }
#pragma unroll
  for (int c = 0; c < 2; c++) {
    const u32 s01 = sw[2 * c], s23 = sw[2 * c + 1];
    const float w0 = bflo(s01), w1 = bfhi(s01), w2 = bflo(s23), w3 = bfhi(s23);
    float kf[11];
#pragma unroll
    for (int i = 0; i < 11; i++) kf[i] = c ? bfhi(kk[i]) : bflo(kk[i]);
    float res[8];
#pragma unroll
    for (int i = 0; i < 8; i++) {
      float a = rbf(rbf(kf[i] * w3) + rbf(kf[i + 1] * w2));
      a = rbf(a + rbf(kf[i + 2] * w1));
      a = rbf(a + rbf(kf[i + 3] * w0));
      res[i] = a * (c ? bfhi(vv[i]) : bflo(vv[i]));
    }
    u32x4 o;
#pragma unroll
    for (int p = 0; p < 4; p++) o[p] = pk_bf16(res[7 - 2 * p], res[6 - 2 * p]);
    __builtin_amdgcn_raw_buffer_store_b128(o, ru, (L_SEQ * D_CH + (c0 + c) * L_SEQ + l0) * 2, 0, ST_AUX);
  }
  if (wv >= 4) return;
  {
    const char* ex = tab + 4 * PW_BYTES + 16 * lane;
#pragma unroll
    for (int i = 0; i < 3; i++) ka[i] += *(const f32x4*)(ex + 1024 * i);
    const int c16 = lane & 15, q = lane >> 4;
    const bool v2 = q < 2, v28 = (q == 2) && (c16 == 0);
    u32 mk = 0;
#pragma unroll
    for (int rg = 0; rg < 4; rg++) {
      mk = max(mk, max(__float_as_uint(ka[0][rg]) & 0x7fffffffu, __float_as_uint(ka[1][rg]) & 0x7fffffffu));
      u32 m2 = __float_as_uint(ka[2][rg]) & 0x7fffffffu;
      mk = max(mk, (v2 || (rg == 0 && v28)) ? m2 : 0u);
    }
    mk = wave_max_u32(mk);
    const int es = (mk > 0u && mk < 0x7f800000u) ? __builtin_amdgcn_frexp_expf(__uint_as_float(mk)) - 15 : 0;
    u16* kc = (u16*)tab;
#pragma unroll
    for (int rg = 0; rg < 4; rg++) {
      const int row = 4 * q + rg;
      kc[128 * (row >> 3) + (row & 7) + 8 * c16] = __builtin_bit_cast(u16, (_Float16)__builtin_ldexpf(ka[0][rg], -es));
      int i1 = (row < 8) ? 256 + 64 * (row >> 2) + (row & 3) + 4 * c16 : 384 + 32 * ((row - 8) >> 1) + (row & 1) + 2 * c16;
      kc[i1] = __builtin_bit_cast(u16, (_Float16)__builtin_ldexpf(ka[1][rg], -es));
      kc[512 + 16 * row + c16] = __builtin_bit_cast(u16, (_Float16)__builtin_ldexpf(ka[2][rg], -es));
    }
    __builtin_amdgcn_wave_barrier();
    u16* dst = kcg + (size_t)d * KC_STRIDE;
    __builtin_amdgcn_raw_buffer_store_b128(*(const u32x4*)(tab + 16 * lane), ru, (2 * L_SEQ * D_CH + d * KC_STRIDE + 8 * lane) * 2, 0, ST_AUX);
    if (lane < 32) __builtin_amdgcn_raw_buffer_store_b128(*(const u32x4*)(tab + 1024 + 16 * lane), ru, (2 * L_SEQ * D_CH + d * KC_STRIDE + 512 + 8 * lane) * 2, 0, ST_AUX);
    if (lane == 0) hexp[d] = es;
  }
}

#define XS_BYTES 4608
#define HC_BYTES 2368
#define HS_BYTES (4 * HC_BYTES)
#define W_BYTES (XS_BYTES + HS_BYTES)
#define YS_OFF (8 * W_BYTES)
#define YS_BYTES 2064
#define FL_OFF (YS_OFF + 8 * YS_BYTES)
#define LDS_BYTES (FL_OFF + 32)

__device__ __forceinline__ int xcd_group(int b) {
  int xcd = b & 7, idx = b >> 3;
  return ((xcd + 8 * (idx >> 3)) << 3) | (idx & 7);
}

template <class F, int... I>
__device__ __forceinline__ void sfor_impl(F&& f, std::integer_sequence<int, I...>) { (f(std::integral_constant<int, I>{}), ...); }
template <int N, class F>
__device__ __forceinline__ void sfor(F&& f) { sfor_impl(f, std::make_integer_sequence<int, N>{}); }
constexpr int pxa(int k) { return k < 8 ? 0 : k < 15 ? 1 : k < 21 ? 2 : 3; }
constexpr int pxe(int k) { return k < 8 ? k : k < 15 ? k - 8 : k < 21 ? k - 15 : k - 21; }
constexpr int pya(int k) { return k < 4 ? 0 : k < 7 ? 1 : k < 9 ? 2 : 3; }
constexpr int pye(int k) { return k < 4 ? k : k < 7 ? k - 4 : k < 9 ? k - 7 : 0; }

template <int OFF> __device__ __forceinline__ void dsr64(u32x2& dst, u32 addr) {
  asm volatile("ds_read_b64 %0, %1 offset:%2" : "=v"(dst) : "v"(addr), "i"(OFF) : "memory");
}
template <int OFF> __device__ __forceinline__ void dsr128(u32x4& dst, u32 addr) {
  asm volatile("ds_read_b128 %0, %1 offset:%2" : "=v"(dst) : "v"(addr), "i"(OFF) : "memory");
}
#define DSR64(dst, addr, off) dsr64<(off)>(dst, addr)
#define DSR128(dst, addr, off) dsr128<(off)>(dst, addr)
#define WAIT_FRAGS(AL, AH, B)                                                                         \
  __builtin_amdgcn_sched_barrier(0);                                                                  \
  asm volatile("s_waitcnt lgkmcnt(0)"                                                                 \
               : "+v"(AL[0]), "+v"(AL[1]), "+v"(AL[2]), "+v"(AL[3]),                                  \
                 "+v"(AH[0]), "+v"(AH[1]), "+v"(AH[2]), "+v"(AH[3]),                                  \
                 "+v"(B[0]), "+v"(B[1]), "+v"(B[2]), "+v"(B[3]), "+v"(B[4]), "+v"(B[5]), "+v"(B[6]), "+v"(B[7])  \
               :: "memory");                                                                          \
  __builtin_amdgcn_sched_barrier(0);
#define WAIT_FRAGS4(AL, AH, B)                                                                        \
  __builtin_amdgcn_sched_barrier(0);                                                                  \
  asm volatile("s_waitcnt lgkmcnt(0)"                                                                 \
               : "+v"(AL[0]), "+v"(AL[1]), "+v"(AL[2]), "+v"(AL[3]),                                  \
                 "+v"(AH[0]), "+v"(AH[1]), "+v"(AH[2]), "+v"(AH[3]),                                  \
                 "+v"(B[0]), "+v"(B[1]), "+v"(B[2]), "+v"(B[3])                                       \
               :: "memory");                                                                          \
  __builtin_amdgcn_sched_barrier(0);

template <bool IS_A>
__device__ __forceinline__ void conv_body(char* smem, int g, const u16* __restrict__ xT, const u16* __restrict__ hT,
                                          const int* __restrict__ hexp, const u16* __restrict__ dvec,
                                          const u16* __restrict__ qin, u16* __restrict__ out) {
  const int t = threadIdx.x, lane = t & 63;
  const int w = __builtin_amdgcn_readfirstlane(t >> 6);
  const int ch = (g & 127) * 8 + w;
  char* xs = smem + w * W_BYTES;
  char* hs = xs + XS_BYTES;
  const u16* xrow = xT + (size_t)ch * L_SEQ;
  const int hl = lane < 48 ? lane : lane - 48;
  int hc[3];
#pragma unroll
  for (int j = 0; j < 3; j++) {
    const int c = 48 * j + hl, sg = (c * 57) >> 10;
    hc[j] = 32 * (sg & 3) + 16 * (sg >> 2) + c - 18 * sg;
  }
  u32x4 hd[6];
  u32x2 he[6];
  u32x4 kc0, kc1;
  u32x4 xr[4];
  int esc = 0;
  if (IS_A) {
    const u16* krow = hT + (size_t)ch * KC_STRIDE;
    kc0 = *(const u32x4*)(krow + 8 * lane);
    kc1 = *(const u32x4*)(krow + 512 + 8 * (lane & 31));
    esc = hexp[ch];
#pragma unroll
    for (int j = 0; j < 4; j++) xr[j] = *(const u32x4*)(xrow + 8 * (64 * j + lane));
  } else {
    const u32 xl = lds_off(xs) + 480;
    const u16* xp = xrow + 8 * lane;
    asm volatile("s_mov_b32 m0, %1\n\ts_nop 0\n\tglobal_load_lds_dwordx4 %0, off\n\tglobal_load_lds_dwordx4 %0, off offset:1024\n\t"
                 "global_load_lds_dwordx4 %0, off offset:2048\n\tglobal_load_lds_dwordx4 %0, off offset:3072"
                 :: "v"(xp), "s"(xl) : "memory", "m0");
    const __amdgpu_buffer_rsrc_t rh = __builtin_amdgcn_make_buffer_rsrc((void*)(hT + (size_t)ch * L_SEQ), 0, L_SEQ * 2, 0x00020000);
#pragma unroll
    for (int s = 0; s < 6; s++) {
      int o1 = 16 * (hc[s % 3] - 2) + 2048 * (s / 3), o2 = o1 + 16;
      if (s == 0) { o1 = o1 >= 0 ? o1 : 0x40000000; o2 = o2 >= 0 ? o2 : 0x40000000; }
      hd[s] = __builtin_amdgcn_raw_buffer_load_b128(rh, o1, 0, 0);
      he[s] = __builtin_amdgcn_raw_buffer_load_b64(rh, o2, 0, 0);
    }
  }
  const u16 p_d = dvec[ch];
  if (lane == 0) *(u32*)(smem + FL_OFF + 4 * w) = 0u;
  __builtin_amdgcn_sched_barrier(0);
  asm volatile("s_waitcnt lgkmcnt(0)\n\ts_barrier" ::: "memory");
  __builtin_amdgcn_sched_barrier(0);
  if (IS_A) {
    char* kb = hs;
    char* lin = xs;
    const u16* kc = (const u16*)kb;
    *(u32x4*)(kb + 16 * lane) = kc0;
    *(u32x4*)(kb + 1024 + 16 * (lane & 31)) = kc1;
    __builtin_amdgcn_wave_barrier();
    const bool dir = lane < 32;
    const int a0 = dir ? 16 * lane : 512 + 8 * (lane - 32);
    u32x2 d01 = *(const u32x2*)(kb + a0), d23 = *(const u32x2*)(kb + a0 + 8);
    u32x4 o;
    o[0] = __builtin_amdgcn_perm(d01[0], d01[0], dir ? 0x03020100u : 0x01000100u);
    o[1] = __builtin_amdgcn_perm(d01[1], d01[0], dir ? 0x07060504u : 0x05040302u);
    o[2] = __builtin_amdgcn_perm(d23[0], d01[1], dir ? 0x07060504u : 0x01000100u);
    o[3] = dir ? d23[1] : __builtin_amdgcn_perm(d23[0], d01[1], 0x05040302u);
    *(u32x4*)(lin + 32 + 16 * lane) = o;
    u32 d0 = *(const u32*)(kb + 768 + 4 * lane), d1 = *(const u32*)(kb + 772 + 4 * lane);
    o[0] = __builtin_amdgcn_perm(d0, d0, 0x01000100u);
    o[1] = d0;
    o[2] = __builtin_amdgcn_perm(d0, d0, 0x03020302u);
    o[3] = __builtin_amdgcn_perm(d1, d1, 0x01000100u);
    *(u32x4*)(lin + 32 + 1024 + 16 * lane) = o;
#pragma unroll
    for (int i = 0; i < 2; i++) {
      u32 x0 = kc[512 + 64 * i + lane], x1 = kc[513 + 64 * i + lane];
      o[0] = o[1] = x0 | (x0 << 16);
      o[3] = x1 | (x1 << 16);
      o[2] = (lane & 1) ? o[3] : (x0 | (x1 << 16));
      *(u32x4*)(lin + 32 + 2048 + 1024 * i + 16 * lane) = o;
    }
    if (lane < 4) *(u32x4*)(lin + ((lane & 2) ? 4096 : 0) + 16 * lane) = (u32x4){0, 0, 0, 0};
    __builtin_amdgcn_wave_barrier();
#pragma unroll
    for (int s = 0; s < 6; s++) {
      hd[s] = *(const u32x4*)(lin + 16 * hc[s % 3] + 2048 * (s / 3));
      he[s] = *(const u32x2*)(lin + 16 * hc[s % 3] + 2048 * (s / 3) + 16);
    }
    __builtin_amdgcn_wave_barrier();
  }
#define STAGE_PIECE(set, k) {                                                          \
    const u32x4 d4 = hd[3 * (set) + ((k) >> 2)];                                       \
    const u32x2 e2 = he[3 * (set) + ((k) >> 2)];                                       \
    u32x4 o_;                                                                          \
    if (((k) & 3) == 0) o_ = d4;                                                       \
    else if (((k) & 3) == 1) o_ = (u32x4){__builtin_amdgcn_alignbit(d4[1], d4[0], 16), __builtin_amdgcn_alignbit(d4[2], d4[1], 16), \
                                          __builtin_amdgcn_alignbit(d4[3], d4[2], 16), __builtin_amdgcn_alignbit(e2[0], d4[3], 16)}; \
    else if (((k) & 3) == 2) o_ = (u32x4){d4[1], d4[2], d4[3], e2[0]};                 \
    else o_ = (u32x4){__builtin_amdgcn_alignbit(d4[2], d4[1], 16), __builtin_amdgcn_alignbit(d4[3], d4[2], 16), \
                      __builtin_amdgcn_alignbit(e2[0], d4[3], 16), __builtin_amdgcn_alignbit(e2[1], e2[0], 16)}; \
    *(u32x4*)(hw + 768 * ((k) >> 2) + ((k) & 3) * HC_BYTES) = o_; }
  char* hw = hs + 16 * hl;
#pragma unroll
  for (int k = 0; k < 12; k++) STAGE_PIECE(0, k);
  if (lane < 30) *(u32x4*)(xs + 16 * lane) = (u32x4){0, 0, 0, 0};
  if (IS_A) {
    u32 mx = 0;
#pragma unroll
    for (int j = 0; j < 4; j++)
#pragma unroll
      for (int p = 0; p < 4; p++) {
        u32 dd = xr[j][p];
        mx = max(mx, max(dd & 0x7fffu, (dd >> 16) & 0x7fffu));
      }
    mx = wave_max_u32(mx);
    const int ex = (int)(mx >> 7) - 127 - 14;
    esc += ex;
#pragma unroll
    for (int j = 0; j < 4; j++) {
      u32x4 o;
#pragma unroll
      for (int p = 0; p < 4; p++) {
        u32 dd = xr[j][3 - p];
        float lo = __builtin_ldexpf(bfhi(dd), -ex), hi = __builtin_ldexpf(bflo(dd), -ex);
        o[p] = __builtin_bit_cast(u32, __builtin_amdgcn_cvt_pkrtz(lo, hi));
      }
      *(u32x4*)(xs + 480 + 16 * (64 * j + lane)) = o;
    }
  }
  float dsk = bf2f(p_d);
  asm volatile("" : "+v"(dsk));
  const int r = lane & 15, q = lane >> 4;
  f32x4 acc[8];
#pragma unroll
  for (int i = 0; i < 8; i++) acc[i] = (f32x4){0.f, 0.f, 0.f, 0.f};
  const int lam = 25 + r - 8 * q;
  const u32 abase = lds_off(hs) + (lam & 3) * HC_BYTES + 2 * (lam & ~3);
  const u32 bbase = lds_off(xs) + 32 * r + 16 * q;
  u32x2 AL0[4], AH0[4], AL1[4], AH1[4];
  u32x4 B0[8], B1[8];
#define LOAD_PIECE(it, k, AL, AH, B) {                                                          \
    if constexpr ((k) < 8 && ((k) & 1) == 0) DSR64(AL[((k) >> 1) & 3], abase, 64 * ((it) & 3) + 288 * (((k) >> 1) + 4 * ((it) >> 2)));   \
    else if constexpr ((k) < 8) DSR64(AH[((k) >> 1) & 3], abase, 64 * ((it) & 3) + 288 * (((k) >> 1) + 4 * ((it) >> 2)) + 8); \
    else DSR128(B[((k) - 8) & 7], bbase, 448 + 512 * ((k) - 8) - 64 * (it)); }
  __builtin_amdgcn_sched_barrier(0);
  sfor<16>([&](auto kc_) { constexpr int k = decltype(kc_)::value; LOAD_PIECE(0, k, AL0, AH0, B0); });
  __builtin_amdgcn_sched_barrier(0);
  const __amdgpu_buffer_rsrc_t rq = __builtin_amdgcn_make_buffer_rsrc((void*)qin, 0, 1024 * D_CH * 2, 0x00020000);
  u32x4 qv[6];
  u32x2 uv[8];
  if (IS_A) {
#pragma unroll
    for (int ct = 0; ct < 8; ct++) uv[ct] = *(const u32x2*)(xrow + 256 * ct + 16 * r + 4 * q);
  } else {
#pragma unroll
    for (int i = 0; i < 2; i++) qv[4 + i] = *(const u32x4*)(qin + (size_t)(1024 + 2 * t + i) * D_CH + (g & 127) * 8);
#pragma unroll
    for (int i = 0; i < 4; i++)
      qv[i] = __builtin_amdgcn_raw_buffer_load_b128(rq, ((4 * t + i) * D_CH + (g & 127) * 8) * 2, 0, 0);
  }
#define PIN_LO asm volatile("" : "+v"(acc[0]), "+v"(acc[1]), "+v"(acc[2]), "+v"(acc[3]));
#define PIN_HI asm volatile("" : "+v"(acc[4]), "+v"(acc[5]), "+v"(acc[6]), "+v"(acc[7]));
#define ITERX(it, AL, AH, B, NAL, NAH, NB)                                              \
  WAIT_FRAGS(AL, AH, B);                                                                \
  sfor<26>([&](auto kc_) {                                                              \
    constexpr int k_ = decltype(kc_)::value, a = pxa(k_), e = pxe(k_);                  \
    acc[a + e] = mma<IS_A>((u32x4){AL[a][0], AL[a][1], AH[a][0], AH[a][1]}, B[e], acc[a + e]); \
    if constexpr ((it) < 7) { if constexpr (k_ < 16) LOAD_PIECE((it) + 1, k_, NAL, NAH, NB); } \
    else if constexpr (k_ < 12) STAGE_PIECE(1, k_)       \
    else if constexpr (k_ < 24) LOAD_PIECE(0, k_ - 12, NAL, NAH, NB);     \
    __builtin_amdgcn_sched_barrier(0);                                                  \
  });                                                                                   \
  PIN_LO PIN_HI
#define ITERY(it, AL, AH, B, NAL, NAH, NB)                                              \
  WAIT_FRAGS4(AL, AH, B);                                                               \
  sfor<10>([&](auto kc_) {                                                              \
    constexpr int k_ = decltype(kc_)::value, a = pya(k_), e = pye(k_);                  \
    acc[4 + a + e] = mma<IS_A>((u32x4){AL[a][0], AL[a][1], AH[a][0], AH[a][1]}, B[e], acc[4 + a + e]); \
    if constexpr ((it) < 7 && k_ < 6) {                                                 \
      LOAD_PIECE((it) + 1, 2 * k_, NAL, NAH, NB);                                       \
      LOAD_PIECE((it) + 1, 2 * k_ + 1, NAL, NAH, NB);                                   \
    }                                                                                   \
    __builtin_amdgcn_sched_barrier(0);                                                  \
  });                                                                                   \
  PIN_HI
  const __amdgpu_buffer_rsrc_t ro = __builtin_amdgcn_make_buffer_rsrc((void*)out, 0, 8 * 1024 * 1024, 0x00020000);
#define EPI1(c0_, ysp)                                                                  \
  if (!IS_A) {                                                                          \
    _Pragma("unroll") for (int ci = 0; ci < 4; ci++)                                    \
      uv[(c0_) + ci] = *(const u32x2*)(xs + 480 + 512 * ((c0_) + ci) + 32 * r + 16 * (q >> 1) + 8 - 8 * (q & 1)); \
    __builtin_amdgcn_sched_barrier(0);                                                  \
  }                                                                                     \
  _Pragma("unroll") for (int ci = 0; ci < 4; ci++) {                                    \
    const int l = 256 * ((c0_) + ci) + 16 * r + 4 * q;                                  \
    float x0, x1, x2, x3;                                                               \
    const u32x2 xv = uv[(c0_) + ci];                                                    \
    if (IS_A) { x0 = bflo(xv[0]); x1 = bfhi(xv[0]); x2 = bflo(xv[1]); x3 = bfhi(xv[1]); } \
    else { x3 = bflo(xv[0]); x2 = bfhi(xv[0]); x1 = bflo(xv[1]); x0 = bfhi(xv[1]); }    \
    float y0 = acc[(c0_) + ci][0], y1 = acc[(c0_) + ci][1], y2 = acc[(c0_) + ci][2], y3 = acc[(c0_) + ci][3]; \
    if (IS_A) {                                                                         \
      y0 = __builtin_ldexpf(y0, esc); y1 = __builtin_ldexpf(y1, esc);                   \
      y2 = __builtin_ldexpf(y2, esc); y3 = __builtin_ldexpf(y3, esc);                   \
    }                                                                                   \
    u32x2 o;                                                                            \
    o[0] = pk_bf16(rbf(y0) + rbf(x0 * dsk), rbf(y1) + rbf(x1 * dsk));                   \
    o[1] = pk_bf16(rbf(y2) + rbf(x2 * dsk), rbf(y3) + rbf(x3 * dsk));                   \
    *(u32x2*)((ysp) + 2 * (l & 1023)) = o;                                              \
  }
#define EPI2(NR, row0, qb, ysb, ystride, aux)                                           \
  {                                                                                     \
    u32 yv[8][(NR) / 2];                                                                \
    _Pragma("unroll") for (int c = 0; c < 8; c++) {                                     \
      if ((NR) == 2) yv[c][0] = *(const u32*)((ysb) + c * (ystride) + 4 * t);           \
      else { u32x2 y2 = *(const u32x2*)((ysb) + c * (ystride) + 8 * t); yv[c][0] = y2[0]; yv[c][(NR) / 2 - 1] = y2[1]; } \
    }                                                                                   \
    _Pragma("unroll") for (int i = 0; i < (NR); i++) {                                  \
      u32x4 o;                                                                          \
      _Pragma("unroll") for (int p = 0; p < 4; p++) {                                   \
        const u32 ya_ = yv[2 * p][i >> 1], yb_ = yv[2 * p + 1][i >> 1];                 \
        if (IS_A) o[p] = __builtin_amdgcn_perm(yb_, ya_, (i & 1) ? 0x07060302u : 0x05040100u); \
        else {                                                                          \
          float ya = (i & 1) ? bfhi(ya_) : bflo(ya_), yb = (i & 1) ? bfhi(yb_) : bflo(yb_); \
          o[p] = pk_bf16(ya * bflo(qv[(qb) + i][p]), yb * bfhi(qv[(qb) + i][p]));       \
        }                                                                               \
      }                                                                                 \
      __builtin_amdgcn_raw_buffer_store_b128(o, ro, (((row0) + i) * (2 * D_CH) + g * 8) * 2, 0, aux); \
    }                                                                                   \
  }
  ITERX(0, AL0, AH0, B0, AL1, AH1, B1);
  ITERX(1, AL1, AH1, B1, AL0, AH0, B0);
  ITERX(2, AL0, AH0, B0, AL1, AH1, B1);
  ITERX(3, AL1, AH1, B1, AL0, AH0, B0);
  ITERX(4, AL0, AH0, B0, AL1, AH1, B1);
  ITERX(5, AL1, AH1, B1, AL0, AH0, B0);
  ITERX(6, AL0, AH0, B0, AL1, AH1, B1);
  ITERX(7, AL1, AH1, B1, AL0, AH0, B0);
  EPI1(0, smem + YS_OFF + w * YS_BYTES);
  asm volatile("ds_write_b32 %0, %1" :: "v"(lds_off(smem + FL_OFF) + 4 * w), "v"(1u) : "memory");
  ITERY(0, AL0, AH0, B0, AL1, AH1, B1);
  ITERY(1, AL1, AH1, B1, AL0, AH0, B0);
  ITERY(2, AL0, AH0, B0, AL1, AH1, B1);
  ITERY(3, AL1, AH1, B1, AL0, AH0, B0);
  ITERY(4, AL0, AH0, B0, AL1, AH1, B1);
  ITERY(5, AL1, AH1, B1, AL0, AH0, B0);
  ITERY(6, AL0, AH0, B0, AL1, AH1, B1);
  ITERY(7, AL1, AH1, B1, AL0, AH0, B0);
  EPI1(4, hs);
  if (w < 4) {
    u32 fl;
    do {
      asm volatile("ds_read_b32 %0, %1\n\ts_waitcnt lgkmcnt(0)" : "=v"(fl) : "v"(lds_off(smem + FL_OFF) + 4 * (lane & 7)) : "memory");
    } while (__builtin_amdgcn_ballot_w64(fl == 0u) != 0ull);
    EPI2(4, 4 * t, 0, smem + YS_OFF, YS_BYTES, AUX0);
  }
  asm volatile("s_waitcnt lgkmcnt(0)\n\ts_barrier" ::: "memory");
  EPI2(2, 1024 + 2 * t, 4, smem + XS_BYTES, W_BYTES, 0);
}

__global__ __launch_bounds__(512) void conv_kernel(const u16* __restrict__ uT, const u16* __restrict__ kvT,
                                                   const u16* __restrict__ kcg, const int* __restrict__ hexp,
                                                   const u16* __restrict__ dskip, const u16* __restrict__ ssmk,
                                                   const u16* __restrict__ ssmd, const u16* __restrict__ qin,
                                                   u16* __restrict__ out) {
  extern __shared__ __attribute__((aligned(16))) char smem[];
  const int g = xcd_group(blockIdx.x);
  if (g < 128) conv_body<true>(smem, g, uT, kcg, hexp, dskip, qin, out);
  else conv_body<false>(smem, g, kvT, ssmk, hexp, ssmd, qin, out);
}

extern "C" void kernel_launch(void* const* d_in, const int* in_sizes, int n_in, void* d_out, int out_size,
                              void* d_ws, size_t ws_size, hipStream_t stream) {
  const u16* u = (const u16*)d_in[0];
  const u16* lar = (const u16*)d_in[1];
  const u16* aim = (const u16*)d_in[2];
  const u16* cre = (const u16*)d_in[3];
  const u16* cim = (const u16*)d_in[4];
  const u16* ldt = (const u16*)d_in[5];
  const u16* dsk = (const u16*)d_in[6];
  const u16* q = (const u16*)d_in[7];
  const u16* k = (const u16*)d_in[8];
  const u16* v = (const u16*)d_in[9];
  const u16* shk = (const u16*)d_in[10];
  const u16* ssmk = (const u16*)d_in[11];
  const u16* ssmd = (const u16*)d_in[12];
  char* ws = (char*)d_ws;
  u16* uT = (u16*)ws;
  u16* kvT = (u16*)(ws + (size_t)4 * 1024 * 1024);
  u16* kcg = (u16*)(ws + (size_t)8 * 1024 * 1024);
  int* hexp = (int*)(ws + (size_t)12 * 1024 * 1024);
  (void)hipFuncSetAttribute((const void*)prep_kernel, hipFuncAttributeMaxDynamicSharedMemorySize, PREP_LDS);
  (void)hipFuncSetAttribute((const void*)conv_kernel, hipFuncAttributeMaxDynamicSharedMemorySize, LDS_BYTES);
  prep_kernel<<<256, 512, PREP_LDS, stream>>>(u, lar, aim, cre, cim, ldt, k, v, shk, uT, kvT, kcg, hexp);
  conv_kernel<<<256, 512, LDS_BYTES, stream>>>(uT, kvT, kcg, hexp, dsk, ssmk, ssmd, q, (u16*)d_out);
}
```

```cpp
#include <hip/hip_runtime.h>
#include <hip/hip_bf16.h>
#include <stdint.h>
#include <utility>

typedef unsigned int u32;
typedef unsigned short u16;
typedef u32 u32x4 __attribute__((ext_vector_type(4)));
typedef u32 u32x2 __attribute__((ext_vector_type(2)));
typedef float f32x4 __attribute__((ext_vector_type(4)));
typedef __bf16 bf16x8 __attribute__((ext_vector_type(8)));
typedef _Float16 f16x8 __attribute__((ext_vector_type(8)));

#define ST_AUX 16
#define AUX0 0
#define AUX1 0
#define H_AUX 0
#define Q_AUX 0
#define PF 1
#define L_SEQ 2048
#define D_CH 1024
#define N_MODE 128

__device__ __forceinline__ float bflo(u32 d) { return __uint_as_float(d << 16); }
__device__ __forceinline__ float bfhi(u32 d) { return __uint_as_float(d & 0xffff0000u); }
__device__ __forceinline__ float bf2f(u16 h) { return __uint_as_float(((u32)h) << 16); }
__device__ __forceinline__ u32 pk_bf16(float lo, float hi) {
  typedef __bf16 bf16x2 __attribute__((ext_vector_type(2)));
  typedef float f32x2 __attribute__((ext_vector_type(2)));
  f32x2 v = {lo, hi};
  bf16x2 r = __builtin_convertvector(v, bf16x2);
  return __builtin_bit_cast(u32, r);
}
__device__ __forceinline__ float rbf(float x) { return __uint_as_float(pk_bf16(0.f, x)); }

__device__ __forceinline__ u32 wave_max_u32(u32 x) {
  x = max(x, (u32)__builtin_amdgcn_update_dpp(0, (int)x, 0x111, 0xf, 0xf, true));
  x = max(x, (u32)__builtin_amdgcn_update_dpp(0, (int)x, 0x112, 0xf, 0xf, true));
  x = max(x, (u32)__builtin_amdgcn_update_dpp(0, (int)x, 0x114, 0xf, 0xf, true));
  x = max(x, (u32)__builtin_amdgcn_update_dpp(0, (int)x, 0x118, 0xf, 0xf, true));
  x = max(x, (u32)__builtin_amdgcn_update_dpp(0, (int)x, 0x142, 0xa, 0xf, true));
  x = max(x, (u32)__builtin_amdgcn_update_dpp(0, (int)x, 0x143, 0xc, 0xf, true));
  return (u32)__builtin_amdgcn_readlane((int)x, 63);
}

#define GT_ROW 288
#define GT_TAB (16 * GT_ROW)
#define KC_STRIDE 768

typedef float f32x2 __attribute__((ext_vector_type(2)));
__device__ __forceinline__ f32x2 cmul(f32x2 g, f32x2 e) {
  f32x2 t, o;
  asm("v_pk_mul_f32 %0, %1, %2 op_sel:[0,0] op_sel_hi:[0,1]" : "=v"(t) : "v"(g), "v"(e));
  asm("v_pk_fma_f32 %0, %1, %2, %3 op_sel:[1,1,0] op_sel_hi:[1,0,1] neg_lo:[1,0,0]" : "=v"(o) : "v"(g), "v"(e), "v"(t));
  return o;
}
__device__ __forceinline__ f32x2 cexp_pos(float ar, float ai, float pos) {
  const float LOG2E = 1.44269504088896340736f;
  const float I2PI_HI = 0.15915494309189533577f;
  const float I2PI_LO = 6.4206383e-9f;
  float xr = ar * pos;
  float xi = ai * pos;
  float e = __builtin_amdgcn_exp2f(xr * LOG2E);
  float r = xi * I2PI_HI;
  float err = __builtin_fmaf(xi, I2PI_HI, -r);
  float f = __builtin_amdgcn_fractf(r);
  f += __builtin_fmaf(xi, I2PI_LO, err);
  f32x2 o;
  o[0] = e * __builtin_amdgcn_cosf(f);
  o[1] = e * __builtin_amdgcn_sinf(f);
  return o;
}
__device__ __forceinline__ void split_h(float a, float b, u32& hi, u32& lo) {
  hi = __builtin_bit_cast(u32, __builtin_amdgcn_cvt_pkrtz(a, b));
  u32 l;
  asm("v_fma_mixlo_f16 %0, %1, -1.0, %2 op_sel_hi:[1,0,0]" : "=v"(l) : "v"(hi), "v"(a));
  asm("v_fma_mixhi_f16 %0, %1, -1.0, %2 op_sel:[1,0,0] op_sel_hi:[1,0,0]" : "+v"(l) : "v"(hi), "v"(b));
  lo = l;
}

template <bool IS_A>
__device__ __forceinline__ f32x4 mma(u32x4 a, u32x4 b, f32x4 c) {
  if (IS_A) return __builtin_amdgcn_mfma_f32_16x16x32_f16(__builtin_bit_cast(f16x8, a), __builtin_bit_cast(f16x8, b), c, 0, 0, 0);
  else return __builtin_amdgcn_mfma_f32_16x16x32_bf16(__builtin_bit_cast(bf16x8, a), __builtin_bit_cast(bf16x8, b), c, 0, 0, 0);
}

__device__ __forceinline__ u32 lds_off(const char* p) {
  return (u32)(uintptr_t)(const __attribute__((address_space(3))) char*)p;
}
__device__ __forceinline__ void gen_partial(char* tab, int lane, u16 p_ldt, u16 p_lar, u16 p_aim, u16 p_cre, u16 p_cim,
                                            f32x4 (&acc)[3]) {
  const float dt = rbf(expf(bf2f(p_ldt)));
  const float ar = -rbf(expf(bf2f(p_lar))) * dt;
  const float ai = bf2f(p_aim) * dt;
  f32x2 C;
  C[0] = 2.0f * (bf2f(p_cre) * dt);
  C[1] = -2.0f * (bf2f(p_cim) * dt);
  const u32 mc = wave_max_u32(max(__float_as_uint(C[0]) & 0x7fffffffu, __float_as_uint(C[1]) & 0x7fffffffu));
  const f32x2 e1 = cexp_pos(ar, -ai, 1.0f);
  const f32x2 f8 = cexp_pos(ar, ai, 8.0f);
  const f32x2 E = cexp_pos(ar, -ai, 128.0f);
  const int ec = (mc > 0u && mc < 0x7f800000u) ? __builtin_amdgcn_frexp_expf(__uint_as_float(mc)) - 14 : 0;
  C[0] = __builtin_ldexpf(C[0], -ec) * 1.0003662109375f;
  C[1] = __builtin_ldexpf(C[1], -ec) * 1.0003662109375f;
  char* wp = tab + 4 * lane;
  const int c16 = lane & 15, q = lane >> 4;
  const char* rp = tab + c16 * GT_ROW + 16 * q;
  {
    f32x2 F = {16384.0f, 0.f};
#pragma unroll
    for (int w = 0; w < 16; w++) {
      u32 hi_, lo_;
      split_h(F[0], F[1], hi_, lo_);
      *(u32*)(wp + w * GT_ROW) = hi_;
      *(u32*)(wp + w * GT_ROW + GT_TAB) = lo_;
      if (w < 15) F = cmul(F, f8);
    }
  }
  __builtin_amdgcn_wave_barrier();
  u32x4 Fh[4], Fl[4];
#pragma unroll
  for (int ks = 0; ks < 4; ks++) {
    Fh[ks] = *(const u32x4*)(rp + 64 * ks);
    Fl[ks] = *(const u32x4*)(rp + 64 * ks + GT_TAB);
  }
  __builtin_amdgcn_wave_barrier();
#define PUT_ROW(tb, row, z) *(u32*)(wp + (tb) * GT_TAB + (row) * GT_ROW) = __builtin_bit_cast(u32, __builtin_amdgcn_cvt_pkrtz((z)[0], (z)[1]))
#define TILE_MMA(ti, tb) { __builtin_amdgcn_wave_barrier();                                          \
    f32x4 a_ = {0.f, 0.f, 0.f, 0.f}, b_ = {0.f, 0.f, 0.f, 0.f};                                    \
    _Pragma("unroll") for (int ks = 0; ks < 4; ks++) {                                             \
      u32x4 Gh = *(const u32x4*)(rp + 64 * ks + (tb) * GT_TAB);                                    \
      b_ = mma<true>(Gh, Fl[ks], b_);                                                              \
      a_ = mma<true>(Gh, Fh[ks], a_);                                                              \
    }                                                                                              \
    acc[ti] = a_ + b_; __builtin_amdgcn_wave_barrier(); }
  f32x2 g[8];
  g[0] = C;
#pragma unroll
  for (int be = 1; be < 8; be++) g[be] = cmul(g[be - 1], e1);
#pragma unroll
  for (int be = 0; be < 8; be++) PUT_ROW(0, be, g[be]);
#pragma unroll
  for (int be = 0; be < 8; be++) { g[be] = cmul(g[be], E); PUT_ROW(0, 8 + be, g[be]); }
  TILE_MMA(0, 0);
#pragma unroll
  for (int al = 0; al < 2; al++)
#pragma unroll
    for (int be = 0; be < 8; be += 2) { g[be] = cmul(g[be], E); PUT_ROW(1, 4 * al + (be >> 1), g[be]); }
#pragma unroll
  for (int al = 0; al < 4; al++)
#pragma unroll
    for (int be = 0; be < 8; be += 4) { g[be] = cmul(g[be], E); PUT_ROW(1, 8 + 2 * al + (be >> 2), g[be]); }
  TILE_MMA(1, 1);
#pragma unroll
  for (int al = 0; al < 9; al++) { g[0] = cmul(g[0], E); PUT_ROW(0, al, g[0]); }
  TILE_MMA(2, 0);
#pragma unroll
  for (int i = 0; i < 3; i++)
#pragma unroll
    for (int rg = 0; rg < 4; rg++) acc[i][rg] = __builtin_ldexpf(acc[i][rg], ec - 14);
}

#define PW_BYTES (2 * GT_TAB)
#define TU_OFF (8 * PW_BYTES)
#define TV_OFF (TU_OFF + 16384)
#define TK_OFF (TV_OFF + 16384 + 512)
#define PREP_LDS (TK_OFF + 16384)

__device__ __forceinline__ void dma16(const void* g, u32 lds_base) {
  asm volatile("s_mov_b32 m0, %1\n\ts_nop 0\n\tglobal_load_lds_dwordx4 %0, off nt" :: "v"(g), "s"(lds_base) : "memory", "m0");
}

__global__ __launch_bounds__(512) void prep_kernel(
    const u16* __restrict__ u, const u16* __restrict__ lar, const u16* __restrict__ aim,
    const u16* __restrict__ cre, const u16* __restrict__ cim, const u16* __restrict__ ldt,
    const u16* __restrict__ kin, const u16* __restrict__ vin, const u16* __restrict__ shk,
    u16* __restrict__ uT, u16* __restrict__ kvT, u16* __restrict__ kcg, int* __restrict__ hexp) {
  extern __shared__ __attribute__((aligned(16))) char smem[];
  const int b = blockIdx.x;
  const int t = threadIdx.x;
  const int lane = t & 63;
  const int wv = __builtin_amdgcn_readfirstlane(t >> 6);
  u32 pf = 0;
  {
    const char* pc;
    u32 en;
    asm volatile("s_getpc_b64 %1\n.Lpf_anchor:\n\ts_mov_b32 %2, .Lconv_end_a-.Lpf_anchor\n\ts_max_u32 %2, %2, .Lconv_end_b-.Lpf_anchor\n\t"
                 "v_lshlrev_b32 %0, 6, %3\n\tv_min_u32 %0, %2, %0\n\tglobal_load_dword %0, %0, %1"
                 : "=&v"(pf), "=&s"(pc), "=&s"(en) : "v"(t) : "memory", "scc");
    __builtin_amdgcn_sched_barrier(0);
  }
  const int xcd = b & 7, idx = b >> 3;
  const int cb = xcd + 8 * (idx & 1);
  const int rt = idx >> 1;
  const u32 sb = lds_off(smem);
  const int c0 = cb * 64 + (wv & 1) * 32 + (lane & 15) * 2;
  const int rg = (wv >> 1) * 4 + (lane >> 4);
  const int l0 = rt * 128 + rg * 8;
  if (wv == 0 && lane < 24) {
    if (rt > 0) dma16((const char*)kin + (size_t)(rt * 128 - 3 + (lane >> 3)) * (D_CH * 2) + cb * 128 + ((lane & 7) ^ 4) * 16, sb + TK_OFF - 384);
    else *(u32x4*)(smem + TK_OFF - 384 + 16 * lane) = (u32x4){0, 0, 0, 0};
  }
  const int d = cb * 64 + rt * 4 + (wv & 3);
  const int po = (d * N_MODE + (wv >> 2) * 64 + lane) * 2;
  u32 p_ldt, p_lar, p_aim, p_cre, p_cim;
  u32x4 sw;
  asm volatile("global_load_dwordx4 %5, %12, %13\n\tglobal_load_ushort %0, %6, %7\n\tglobal_load_ushort %1, %8, %9 nt\n\t"
               "global_load_ushort %2, %8, %10 nt\n\tglobal_load_ushort %3, %8, %11 nt\n\tglobal_load_ushort %4, %8, %14 nt"
               : "=&v"(p_ldt), "=&v"(p_lar), "=&v"(p_aim), "=&v"(p_cre), "=&v"(p_cim), "=&v"(sw)
               : "v"(d * 2), "s"(ldt), "v"(po), "s"(lar), "s"(aim), "s"(cre), "v"(c0 * 8), "s"(shk), "s"(cim) : "memory");
  {
    const size_t go = (size_t)(rt * 128 + wv * 16 + (lane >> 3)) * (D_CH * 2) + cb * 128;
    const size_t g0 = go + (lane & 7) * 16, g1 = go + 8 * (D_CH * 2) + ((lane & 7) ^ 4) * 16;
    dma16((const char*)kin + g0, sb + TK_OFF + wv * 2048);
    dma16((const char*)kin + g1, sb + TK_OFF + wv * 2048 + 1024);
    dma16((const char*)vin + g0, sb + TV_OFF + wv * 2048);
    dma16((const char*)vin + g1, sb + TV_OFF + wv * 2048 + 1024);
    dma16((const char*)u + g0, sb + TU_OFF + wv * 2048);
    dma16((const char*)u + g1, sb + TU_OFF + wv * 2048 + 1024);
  }
  asm volatile("s_waitcnt vmcnt(6)" : "+v"(p_ldt), "+v"(p_lar), "+v"(p_aim), "+v"(p_cre), "+v"(p_cim), "+v"(sw), "+v"(pf) :: "memory");
  const __amdgpu_buffer_rsrc_t ru = __builtin_amdgcn_make_buffer_rsrc((void*)uT, 0, 12 * 1024 * 1024, 0x00020000);
  __builtin_amdgcn_sched_barrier(0);
  f32x4 ka[3];
  char* tab = smem + wv * PW_BYTES;
  gen_partial(tab, lane, (u16)p_ldt, (u16)p_lar, (u16)p_aim, (u16)p_cre, (u16)p_cim, ka);
  if (wv >= 4) {
#pragma unroll
    for (int i = 0; i < 3; i++) *(f32x4*)(tab + 1024 * i + 16 * lane) = ka[i];
  }
  __builtin_amdgcn_sched_barrier(0);
  asm volatile("s_waitcnt vmcnt(0) lgkmcnt(0)\n\ts_barrier" ::: "memory");
  u32 uu[8], kk[11], vv[8];
  {
    const int cd = ((wv & 1) * 64 + (lane & 15) * 4);
    const int to = rg * 1024 + (cd ^ ((rg & 1) * 64));
#pragma unroll
    for (int i = 0; i < 8; i++) uu[i] = *(const u32*)(smem + TU_OFF + to + 128 * i);
#pragma unroll
    for (int i = 0; i < 3; i++) kk[i] = *(const u32*)(smem + TK_OFF - 384 + (to ^ 64) + 128 * i);
#pragma unroll
    for (int i = 0; i < 8; i++) kk[3 + i] = *(const u32*)(smem + TK_OFF + to + 128 * i);
#pragma unroll
    for (int i = 0; i < 8; i++) vv[i] = *(const u32*)(smem + TV_OFF + to + 128 * i);
  }
#pragma unroll
  for (int c = 0; c < 2; c++) {
    u32x4 o;
#pragma unroll
    for (int p = 0; p < 4; p++) o[p] = __builtin_amdgcn_perm(uu[2 * p + 1], uu[2 * p], c ? 0x07060302u : 0x05040100u);
    __builtin_amdgcn_raw_buffer_store_b128(o, ru, ((c0 + c) * L_SEQ + l0) * 2, 0, ST_AUX);
  }
#pragma unroll
  for (int c = 0; c < 2; c++) {
    const u32 s01 = sw[2 * c], s23 = sw[2 * c + 1];
    const float w0 = bflo(s01), w1 = bfhi(s01), w2 = bflo(s23), w3 = bfhi(s23);
    float kf[11];
#pragma unroll
    for (int i = 0; i < 11; i++) kf[i] = c ? bfhi(kk[i]) : bflo(kk[i]);
    float res[8];
#pragma unroll
    for (int i = 0; i < 8; i++) {
      float a = rbf(rbf(kf[i] * w3) + rbf(kf[i + 1] * w2));
      a = rbf(a + rbf(kf[i + 2] * w1));
      a = rbf(a + rbf(kf[i + 3] * w0));
      res[i] = a * (c ? bfhi(vv[i]) : bflo(vv[i]));
    }
    u32x4 o;
#pragma unroll
    for (int p = 0; p < 4; p++) o[p] = pk_bf16(res[7 - 2 * p], res[6 - 2 * p]);
    __builtin_amdgcn_raw_buffer_store_b128(o, ru, (L_SEQ * D_CH + (c0 + c) * L_SEQ + l0) * 2, 0, ST_AUX);
  }
  if (wv >= 4) return;
  {
    const char* ex = tab + 4 * PW_BYTES + 16 * lane;
#pragma unroll
    for (int i = 0; i < 3; i++) ka[i] += *(const f32x4*)(ex + 1024 * i);
    const int c16 = lane & 15, q = lane >> 4;
    const bool v2 = q < 2, v28 = (q == 2) && (c16 == 0);
    u32 mk = 0;
#pragma unroll
    for (int rg = 0; rg < 4; rg++) {
      mk = max(mk, max(__float_as_uint(ka[0][rg]) & 0x7fffffffu, __float_as_uint(ka[1][rg]) & 0x7fffffffu));
      u32 m2 = __float_as_uint(ka[2][rg]) & 0x7fffffffu;
      mk = max(mk, (v2 || (rg == 0 && v28)) ? m2 : 0u);
    }
    mk = wave_max_u32(mk);
    const int es = (mk > 0u && mk < 0x7f800000u) ? __builtin_amdgcn_frexp_expf(__uint_as_float(mk)) - 15 : 0;
    u16* kc = (u16*)tab;
#pragma unroll
    for (int rg = 0; rg < 4; rg++) {
      const int row = 4 * q + rg;
      kc[128 * (row >> 3) + (row & 7) + 8 * c16] = __builtin_bit_cast(u16, (_Float16)__builtin_ldexpf(ka[0][rg], -es));
      int i1 = (row < 8) ? 256 + 64 * (row >> 2) + (row & 3) + 4 * c16 : 384 + 32 * ((row - 8) >> 1) + (row & 1) + 2 * c16;
      kc[i1] = __builtin_bit_cast(u16, (_Float16)__builtin_ldexpf(ka[1][rg], -es));
      kc[512 + 16 * row + c16] = __builtin_bit_cast(u16, (_Float16)__builtin_ldexpf(ka[2][rg], -es));
    }
    __builtin_amdgcn_wave_barrier();
    u16* dst = kcg + (size_t)d * KC_STRIDE;
    __builtin_amdgcn_raw_buffer_store_b128(*(const u32x4*)(tab + 16 * lane), ru, (2 * L_SEQ * D_CH + d * KC_STRIDE + 8 * lane) * 2, 0, ST_AUX);
    if (lane < 32) __builtin_amdgcn_raw_buffer_store_b128(*(const u32x4*)(tab + 1024 + 16 * lane), ru, (2 * L_SEQ * D_CH + d * KC_STRIDE + 512 + 8 * lane) * 2, 0, ST_AUX);
    if (lane == 0) hexp[d] = es;
  }
}

#define XS_BYTES 4608
#define HC_BYTES 2368
#define HS_BYTES (4 * HC_BYTES)
#define W_BYTES (XS_BYTES + HS_BYTES)
#define YS_OFF (8 * W_BYTES)
#define YS_BYTES 2064
#define FL_OFF (YS_OFF + 8 * YS_BYTES)
#define LDS_BYTES (FL_OFF + 32)

__device__ __forceinline__ int xcd_group(int b) {
  int xcd = b & 7, idx = b >> 3;
  return ((xcd + 8 * (idx >> 3)) << 3) | (idx & 7);
}

template <class F, int... I>
__device__ __forceinline__ void sfor_impl(F&& f, std::integer_sequence<int, I...>) { (f(std::integral_constant<int, I>{}), ...); }
template <int N, class F>
__device__ __forceinline__ void sfor(F&& f) { sfor_impl(f, std::make_integer_sequence<int, N>{}); }
constexpr int pxa(int k) { return k < 8 ? 0 : k < 15 ? 1 : k < 21 ? 2 : 3; }
constexpr int pxe(int k) { return k < 8 ? k : k < 15 ? k - 8 : k < 21 ? k - 15 : k - 21; }
constexpr int pya(int k) { return k < 4 ? 0 : k < 7 ? 1 : k < 9 ? 2 : 3; }
constexpr int pye(int k) { return k < 4 ? k : k < 7 ? k - 4 : k < 9 ? k - 7 : 0; }

template <int OFF> __device__ __forceinline__ void dsr64(u32x2& dst, u32 addr) {
  asm volatile("ds_read_b64 %0, %1 offset:%2" : "=v"(dst) : "v"(addr), "i"(OFF) : "memory");
}
template <int OFF> __device__ __forceinline__ void dsr128(u32x4& dst, u32 addr) {
  asm volatile("ds_read_b128 %0, %1 offset:%2" : "=v"(dst) : "v"(addr), "i"(OFF) : "memory");
}
#define DSR64(dst, addr, off) dsr64<(off)>(dst, addr)
#define DSR128(dst, addr, off) dsr128<(off)>(dst, addr)
#define WAIT_FRAGS(AL, AH, B)                                                                         \
  __builtin_amdgcn_sched_barrier(0);                                                                  \
  asm volatile("s_waitcnt lgkmcnt(0)"                                                                 \
               : "+v"(AL[0]), "+v"(AL[1]), "+v"(AL[2]), "+v"(AL[3]),                                  \
                 "+v"(AH[0]), "+v"(AH[1]), "+v"(AH[2]), "+v"(AH[3]),                                  \
                 "+v"(B[0]), "+v"(B[1]), "+v"(B[2]), "+v"(B[3]), "+v"(B[4]), "+v"(B[5]), "+v"(B[6]), "+v"(B[7])  \
               :: "memory");                                                                          \
  __builtin_amdgcn_sched_barrier(0);
#define WAIT_FRAGS4(AL, AH, B)                                                                        \
  __builtin_amdgcn_sched_barrier(0);                                                                  \
  asm volatile("s_waitcnt lgkmcnt(0)"                                                                 \
               : "+v"(AL[0]), "+v"(AL[1]), "+v"(AL[2]), "+v"(AL[3]),                                  \
                 "+v"(AH[0]), "+v"(AH[1]), "+v"(AH[2]), "+v"(AH[3]),                                  \
                 "+v"(B[0]), "+v"(B[1]), "+v"(B[2]), "+v"(B[3])                                       \
               :: "memory");                                                                          \
  __builtin_amdgcn_sched_barrier(0);

template <bool IS_A>
__device__ __forceinline__ void conv_body(char* smem, int g, const u16* __restrict__ xT, const u16* __restrict__ hT,
                                          const int* __restrict__ hexp, const u16* __restrict__ dvec,
                                          const u16* __restrict__ qin, u16* __restrict__ out) {
  const int t = threadIdx.x, lane = t & 63;
  const int w = __builtin_amdgcn_readfirstlane(t >> 6);
  const int ch = (g & 127) * 8 + w;
  char* xs = smem + w * W_BYTES;
  char* hs = xs + XS_BYTES;
  const u16* xrow = xT + (size_t)ch * L_SEQ;
  const int hl = lane < 48 ? lane : lane - 48;
  int hc[3];
#pragma unroll
  for (int j = 0; j < 3; j++) {
    const int c = 48 * j + hl, sg = (c * 57) >> 10;
    hc[j] = 32 * (sg & 3) + 16 * (sg >> 2) + c - 18 * sg;
  }
  u32x4 hd[6];
  u32x2 he[6];
  u32x4 kc0, kc1;
  u32x4 xr[4];
  int esc = 0;
  if (IS_A) {
    const u16* krow = hT + (size_t)ch * KC_STRIDE;
    kc0 = *(const u32x4*)(krow + 8 * lane);
    kc1 = *(const u32x4*)(krow + 512 + 8 * (lane & 31));
    esc = hexp[ch];
#pragma unroll
    for (int j = 0; j < 4; j++) xr[j] = *(const u32x4*)(xrow + 8 * (64 * j + lane));
  } else {
    const u32 xl = lds_off(xs) + 480;
    const u16* xp = xrow + 8 * lane;
    asm volatile("s_mov_b32 m0, %1\n\ts_nop 0\n\tglobal_load_lds_dwordx4 %0, off\n\tglobal_load_lds_dwordx4 %0, off offset:1024\n\t"
                 "global_load_lds_dwordx4 %0, off offset:2048\n\tglobal_load_lds_dwordx4 %0, off offset:3072"
                 :: "v"(xp), "s"(xl) : "memory", "m0");
    const __amdgpu_buffer_rsrc_t rh = __builtin_amdgcn_make_buffer_rsrc((void*)(hT + (size_t)ch * L_SEQ), 0, L_SEQ * 2, 0x00020000);
#pragma unroll
    for (int s = 0; s < 6; s++) {
      int o1 = 16 * (hc[s % 3] - 2) + 2048 * (s / 3), o2 = o1 + 16;
      if (s == 0) { o1 = o1 >= 0 ? o1 : 0x40000000; o2 = o2 >= 0 ? o2 : 0x40000000; }
      hd[s] = __builtin_amdgcn_raw_buffer_load_b128(rh, o1, 0, H_AUX);
      he[s] = __builtin_amdgcn_raw_buffer_load_b64(rh, o2, 0, H_AUX);
    }
  }
  const u16 p_d = dvec[ch];
  if (lane == 0) *(u32*)(smem + FL_OFF + 4 * w) = 0u;
  __builtin_amdgcn_sched_barrier(0);
  asm volatile("s_waitcnt lgkmcnt(0)\n\ts_barrier" ::: "memory");
  __builtin_amdgcn_sched_barrier(0);
  if (IS_A) {
    char* kb = hs;
    char* lin = xs;
    const u16* kc = (const u16*)kb;
    *(u32x4*)(kb + 16 * lane) = kc0;
    *(u32x4*)(kb + 1024 + 16 * (lane & 31)) = kc1;
    __builtin_amdgcn_wave_barrier();
    const bool dir = lane < 32;
    const int a0 = dir ? 16 * lane : 512 + 8 * (lane - 32);
    u32x2 d01 = *(const u32x2*)(kb + a0), d23 = *(const u32x2*)(kb + a0 + 8);
    u32x4 o;
    o[0] = __builtin_amdgcn_perm(d01[0], d01[0], dir ? 0x03020100u : 0x01000100u);
    o[1] = __builtin_amdgcn_perm(d01[1], d01[0], dir ? 0x07060504u : 0x05040302u);
    o[2] = __builtin_amdgcn_perm(d23[0], d01[1], dir ? 0x07060504u : 0x01000100u);
    o[3] = dir ? d23[1] : __builtin_amdgcn_perm(d23[0], d01[1], 0x05040302u);
    *(u32x4*)(lin + 32 + 16 * lane) = o;
    u32 d0 = *(const u32*)(kb + 768 + 4 * lane), d1 = *(const u32*)(kb + 772 + 4 * lane);
    o[0] = __builtin_amdgcn_perm(d0, d0, 0x01000100u);
    o[1] = d0;
    o[2] = __builtin_amdgcn_perm(d0, d0, 0x03020302u);
    o[3] = __builtin_amdgcn_perm(d1, d1, 0x01000100u);
    *(u32x4*)(lin + 32 + 1024 + 16 * lane) = o;
#pragma unroll
    for (int i = 0; i < 2; i++) {
      u32 x0 = kc[512 + 64 * i + lane], x1 = kc[513 + 64 * i + lane];
      o[0] = o[1] = x0 | (x0 << 16);
      o[3] = x1 | (x1 << 16);
      o[2] = (lane & 1) ? o[3] : (x0 | (x1 << 16));
      *(u32x4*)(lin + 32 + 2048 + 1024 * i + 16 * lane) = o;
    }
    if (lane < 4) *(u32x4*)(lin + ((lane & 2) ? 4096 : 0) + 16 * lane) = (u32x4){0, 0, 0, 0};
    __builtin_amdgcn_wave_barrier();
#pragma unroll
    for (int s = 0; s < 6; s++) {
      hd[s] = *(const u32x4*)(lin + 16 * hc[s % 3] + 2048 * (s / 3));
      he[s] = *(const u32x2*)(lin + 16 * hc[s % 3] + 2048 * (s / 3) + 16);
    }
    __builtin_amdgcn_wave_barrier();
  }
#define STAGE_PIECE(set, k) {                                                          \
    const u32x4 d4 = hd[3 * (set) + ((k) >> 2)];                                       \
    const u32x2 e2 = he[3 * (set) + ((k) >> 2)];                                       \
    u32x4 o_;                                                                          \
    if (((k) & 3) == 0) o_ = d4;                                                       \
    else if (((k) & 3) == 1) o_ = (u32x4){__builtin_amdgcn_alignbit(d4[1], d4[0], 16), __builtin_amdgcn_alignbit(d4[2], d4[1], 16), \
                                          __builtin_amdgcn_alignbit(d4[3], d4[2], 16), __builtin_amdgcn_alignbit(e2[0], d4[3], 16)}; \
    else if (((k) & 3) == 2) o_ = (u32x4){d4[1], d4[2], d4[3], e2[0]};                 \
    else o_ = (u32x4){__builtin_amdgcn_alignbit(d4[2], d4[1], 16), __builtin_amdgcn_alignbit(d4[3], d4[2], 16), \
                      __builtin_amdgcn_alignbit(e2[0], d4[3], 16), __builtin_amdgcn_alignbit(e2[1], e2[0], 16)}; \
    *(u32x4*)(hw + 768 * ((k) >> 2) + ((k) & 3) * HC_BYTES) = o_; }
  char* hw = hs + 16 * hl;
#pragma unroll
  for (int k = 0; k < 12; k++) STAGE_PIECE(0, k);
  if (lane < 30) *(u32x4*)(xs + 16 * lane) = (u32x4){0, 0, 0, 0};
  if (IS_A) {
    u32 mx = 0;
#pragma unroll
    for (int j = 0; j < 4; j++)
#pragma unroll
      for (int p = 0; p < 4; p++) {
        u32 dd = xr[j][p];
        mx = max(mx, max(dd & 0x7fffu, (dd >> 16) & 0x7fffu));
      }
    mx = wave_max_u32(mx);
    const int ex = (int)(mx >> 7) - 127 - 14;
    esc += ex;
#pragma unroll
    for (int j = 0; j < 4; j++) {
      u32x4 o;
#pragma unroll
      for (int p = 0; p < 4; p++) {
        u32 dd = xr[j][3 - p];
        float lo = __builtin_ldexpf(bfhi(dd), -ex), hi = __builtin_ldexpf(bflo(dd), -ex);
        o[p] = __builtin_bit_cast(u32, __builtin_amdgcn_cvt_pkrtz(lo, hi));
      }
      *(u32x4*)(xs + 480 + 16 * (64 * j + lane)) = o;
    }
  }
  float dsk = bf2f(p_d);
  asm volatile("" : "+v"(dsk));
  const int r = lane & 15, q = lane >> 4;
  f32x4 acc[8];
#pragma unroll
  for (int i = 0; i < 8; i++) acc[i] = (f32x4){0.f, 0.f, 0.f, 0.f};
  const int lam = 25 + r - 8 * q;
  const u32 abase = lds_off(hs) + (lam & 3) * HC_BYTES + 2 * (lam & ~3);
  const u32 bbase = lds_off(xs) + 32 * r + 16 * q;
  u32x2 AL0[4], AH0[4], AL1[4], AH1[4];
  u32x4 B0[8], B1[8];
#define LOAD_PIECE(it, k, AL, AH, B) {                                                          \
    if constexpr ((k) < 8 && ((k) & 1) == 0) DSR64(AL[((k) >> 1) & 3], abase, 64 * ((it) & 3) + 288 * (((k) >> 1) + 4 * ((it) >> 2)));   \
    else if constexpr ((k) < 8) DSR64(AH[((k) >> 1) & 3], abase, 64 * ((it) & 3) + 288 * (((k) >> 1) + 4 * ((it) >> 2)) + 8); \
    else DSR128(B[((k) - 8) & 7], bbase, 448 + 512 * ((k) - 8) - 64 * (it)); }
  __builtin_amdgcn_sched_barrier(0);
  sfor<16>([&](auto kc_) { constexpr int k = decltype(kc_)::value; LOAD_PIECE(0, k, AL0, AH0, B0); });
  __builtin_amdgcn_sched_barrier(0);
  const __amdgpu_buffer_rsrc_t rq = __builtin_amdgcn_make_buffer_rsrc((void*)qin, 0, 1024 * D_CH * 2, 0x00020000);
  const __amdgpu_buffer_rsrc_t rq2 = __builtin_amdgcn_make_buffer_rsrc((void*)qin, 0, 2048 * D_CH * 2, 0x00020000);
  u32x4 qv[6];
  u32x2 uv[8];
  if (IS_A) {
#pragma unroll
    for (int ct = 0; ct < 8; ct++) uv[ct] = *(const u32x2*)(xrow + 256 * ct + 16 * r + 4 * q);
  } else {
#pragma unroll
    for (int i = 0; i < 2; i++) qv[4 + i] = __builtin_amdgcn_raw_buffer_load_b128(rq2, ((1024 + 2 * t + i) * D_CH + (g & 127) * 8) * 2, 0, Q_AUX);
#pragma unroll
    for (int i = 0; i < 4; i++)
      qv[i] = __builtin_amdgcn_raw_buffer_load_b128(rq, ((4 * t + i) * D_CH + (g & 127) * 8) * 2, 0, Q_AUX);
  }
#define PIN_LO asm volatile("" : "+v"(acc[0]), "+v"(acc[1]), "+v"(acc[2]), "+v"(acc[3]));
#define PIN_HI asm volatile("" : "+v"(acc[4]), "+v"(acc[5]), "+v"(acc[6]), "+v"(acc[7]));
#define ITERX(it, AL, AH, B, NAL, NAH, NB)                                              \
  WAIT_FRAGS(AL, AH, B);                                                                \
  sfor<26>([&](auto kc_) {                                                              \
    constexpr int k_ = decltype(kc_)::value, a = pxa(k_), e = pxe(k_);                  \
    acc[a + e] = mma<IS_A>((u32x4){AL[a][0], AL[a][1], AH[a][0], AH[a][1]}, B[e], acc[a + e]); \
    if constexpr ((it) < 7) { if constexpr (k_ < 16) LOAD_PIECE((it) + 1, k_, NAL, NAH, NB); } \
    else if constexpr (k_ < 12) STAGE_PIECE(1, k_)       \
    else if constexpr (k_ < 24) LOAD_PIECE(0, k_ - 12, NAL, NAH, NB);     \
    __builtin_amdgcn_sched_barrier(0);                                                  \
  });                                                                                   \
  PIN_LO PIN_HI
#define ITERY(it, AL, AH, B, NAL, NAH, NB)                                              \
  WAIT_FRAGS4(AL, AH, B);                                                               \
  sfor<10>([&](auto kc_) {                                                              \
    constexpr int k_ = decltype(kc_)::value, a = pya(k_), e = pye(k_);                  \
    acc[4 + a + e] = mma<IS_A>((u32x4){AL[a][0], AL[a][1], AH[a][0], AH[a][1]}, B[e], acc[4 + a + e]); \
    if constexpr ((it) < 7 && k_ < 6) {                                                 \
      LOAD_PIECE((it) + 1, 2 * k_, NAL, NAH, NB);                                       \
      LOAD_PIECE((it) + 1, 2 * k_ + 1, NAL, NAH, NB);                                   \
    }                                                                                   \
    __builtin_amdgcn_sched_barrier(0);                                                  \
  });                                                                                   \
  PIN_HI
  const __amdgpu_buffer_rsrc_t ro = __builtin_amdgcn_make_buffer_rsrc((void*)out, 0, 8 * 1024 * 1024, 0x00020000);
#define EPI1(c0_, ysp)                                                                  \
  if (!IS_A) {                                                                          \
    _Pragma("unroll") for (int ci = 0; ci < 4; ci++)                                    \
      uv[(c0_) + ci] = *(const u32x2*)(xs + 480 + 512 * ((c0_) + ci) + 32 * r + 16 * (q >> 1) + 8 - 8 * (q & 1)); \
    __builtin_amdgcn_sched_barrier(0);                                                  \
  }                                                                                     \
  _Pragma("unroll") for (int ci = 0; ci < 4; ci++) {                                    \
    const int l = 256 * ((c0_) + ci) + 16 * r + 4 * q;                                  \
    float x0, x1, x2, x3;                                                               \
    const u32x2 xv = uv[(c0_) + ci];                                                    \
    if (IS_A) { x0 = bflo(xv[0]); x1 = bfhi(xv[0]); x2 = bflo(xv[1]); x3 = bfhi(xv[1]); } \
    else { x3 = bflo(xv[0]); x2 = bfhi(xv[0]); x1 = bflo(xv[1]); x0 = bfhi(xv[1]); }    \
    float y0 = acc[(c0_) + ci][0], y1 = acc[(c0_) + ci][1], y2 = acc[(c0_) + ci][2], y3 = acc[(c0_) + ci][3]; \
    if (IS_A) {                                                                         \
      y0 = __builtin_ldexpf(y0, esc); y1 = __builtin_ldexpf(y1, esc);                   \
      y2 = __builtin_ldexpf(y2, esc); y3 = __builtin_ldexpf(y3, esc);                   \
    }                                                                                   \
    u32x2 o;                                                                            \
    o[0] = pk_bf16(rbf(y0) + rbf(x0 * dsk), rbf(y1) + rbf(x1 * dsk));                   \
    o[1] = pk_bf16(rbf(y2) + rbf(x2 * dsk), rbf(y3) + rbf(x3 * dsk));                   \
    *(u32x2*)((ysp) + 2 * (l & 1023)) = o;                                              \
  }
#define EPI2(NR, row0, qb, ysb, ystride, aux)                                           \
  {                                                                                     \
    u32 yv[8][(NR) / 2];                                                                \
    _Pragma("unroll") for (int c = 0; c < 8; c++) {                                     \
      if ((NR) == 2) yv[c][0] = *(const u32*)((ysb) + c * (ystride) + 4 * t);           \
      else { u32x2 y2 = *(const u32x2*)((ysb) + c * (ystride) + 8 * t); yv[c][0] = y2[0]; yv[c][(NR) / 2 - 1] = y2[1]; } \
    }                                                                                   \
    _Pragma("unroll") for (int i = 0; i < (NR); i++) {                                  \
      u32x4 o;                                                                          \
      _Pragma("unroll") for (int p = 0; p < 4; p++) {                                   \
        const u32 ya_ = yv[2 * p][i >> 1], yb_ = yv[2 * p + 1][i >> 1];                 \
        if (IS_A) o[p] = __builtin_amdgcn_perm(yb_, ya_, (i & 1) ? 0x07060302u : 0x05040100u); \
        else {                                                                          \
          float ya = (i & 1) ? bfhi(ya_) : bflo(ya_), yb = (i & 1) ? bfhi(yb_) : bflo(yb_); \
          o[p] = pk_bf16(ya * bflo(qv[(qb) + i][p]), yb * bfhi(qv[(qb) + i][p]));       \
        }                                                                               \
      }                                                                                 \
      __builtin_amdgcn_raw_buffer_store_b128(o, ro, (((row0) + i) * (2 * D_CH) + g * 8) * 2, 0, aux); \
    }                                                                                   \
  }
  ITERX(0, AL0, AH0, B0, AL1, AH1, B1);
  ITERX(1, AL1, AH1, B1, AL0, AH0, B0);
  ITERX(2, AL0, AH0, B0, AL1, AH1, B1);
  ITERX(3, AL1, AH1, B1, AL0, AH0, B0);
  ITERX(4, AL0, AH0, B0, AL1, AH1, B1);
  ITERX(5, AL1, AH1, B1, AL0, AH0, B0);
  ITERX(6, AL0, AH0, B0, AL1, AH1, B1);
  ITERX(7, AL1, AH1, B1, AL0, AH0, B0);
  EPI1(0, smem + YS_OFF + w * YS_BYTES);
  asm volatile("ds_write_b32 %0, %1" :: "v"(lds_off(smem + FL_OFF) + 4 * w), "v"(1u) : "memory");
  ITERY(0, AL0, AH0, B0, AL1, AH1, B1);
  ITERY(1, AL1, AH1, B1, AL0, AH0, B0);
  ITERY(2, AL0, AH0, B0, AL1, AH1, B1);
  ITERY(3, AL1, AH1, B1, AL0, AH0, B0);
  ITERY(4, AL0, AH0, B0, AL1, AH1, B1);
  ITERY(5, AL1, AH1, B1, AL0, AH0, B0);
  ITERY(6, AL0, AH0, B0, AL1, AH1, B1);
  ITERY(7, AL1, AH1, B1, AL0, AH0, B0);
  EPI1(4, hs);
  if (w < 4) {
    u32 fl;
    do {
      asm volatile("ds_read_b32 %0, %1\n\ts_waitcnt lgkmcnt(0)" : "=v"(fl) : "v"(lds_off(smem + FL_OFF) + 4 * (lane & 7)) : "memory");
    } while (__builtin_amdgcn_ballot_w64(fl == 0u) != 0ull);
    EPI2(4, 4 * t, 0, smem + YS_OFF, YS_BYTES, AUX0);
  }
  asm volatile("s_waitcnt lgkmcnt(0)\n\ts_barrier" ::: "memory");
  EPI2(2, 1024 + 2 * t, 4, smem + XS_BYTES, W_BYTES, AUX1);
  if (IS_A) asm volatile(".Lconv_end_a:"); else asm volatile(".Lconv_end_b:");
}

__global__ __launch_bounds__(512) void conv_kernel(const u16* __restrict__ uT, const u16* __restrict__ kvT,
                                                   const u16* __restrict__ kcg, const int* __restrict__ hexp,
                                                   const u16* __restrict__ dskip, const u16* __restrict__ ssmk,
                                                   const u16* __restrict__ ssmd, const u16* __restrict__ qin,
                                                   u16* __restrict__ out) {
  extern __shared__ __attribute__((aligned(16))) char smem[];
  const int g = xcd_group(blockIdx.x);
  if (g < 128) conv_body<true>(smem, g, uT, kcg, hexp, dskip, qin, out);
  else conv_body<false>(smem, g, kvT, ssmk, hexp, ssmd, qin, out);
}

extern "C" void kernel_launch(void* const* d_in, const int* in_sizes, int n_in, void* d_out, int out_size,
                              void* d_ws, size_t ws_size, hipStream_t stream) {
  const u16* u = (const u16*)d_in[0];
  const u16* lar = (const u16*)d_in[1];
  const u16* aim = (const u16*)d_in[2];
  const u16* cre = (const u16*)d_in[3];
  const u16* cim = (const u16*)d_in[4];
  const u16* ldt = (const u16*)d_in[5];
  const u16* dsk = (const u16*)d_in[6];
  const u16* q = (const u16*)d_in[7];
  const u16* k = (const u16*)d_in[8];
  const u16* v = (const u16*)d_in[9];
  const u16* shk = (const u16*)d_in[10];
  const u16* ssmk = (const u16*)d_in[11];
  const u16* ssmd = (const u16*)d_in[12];
  char* ws = (char*)d_ws;
  u16* uT = (u16*)ws;
  u16* kvT = (u16*)(ws + (size_t)4 * 1024 * 1024);
  u16* kcg = (u16*)(ws + (size_t)8 * 1024 * 1024);
  int* hexp = (int*)(ws + (size_t)12 * 1024 * 1024);
  (void)hipFuncSetAttribute((const void*)prep_kernel, hipFuncAttributeMaxDynamicSharedMemorySize, PREP_LDS);
  (void)hipFuncSetAttribute((const void*)conv_kernel, hipFuncAttributeMaxDynamicSharedMemorySize, LDS_BYTES);
  prep_kernel<<<256, 512, PREP_LDS, stream>>>(u, lar, aim, cre, cim, ldt, k, v, shk, uT, kvT, kcg, hexp);
  conv_kernel<<<256, 512, LDS_BYTES, stream>>>(uT, kvT, kcg, hexp, dsk, ssmk, ssmd, q, (u16*)d_out);
}
```

```cpp
#include <hip/hip_runtime.h>
#include <hip/hip_bf16.h>
#include <stdint.h>
#include <utility>

typedef unsigned int u32;
typedef unsigned short u16;
typedef u32 u32x4 __attribute__((ext_vector_type(4)));
typedef u32 u32x2 __attribute__((ext_vector_type(2)));
typedef float f32x4 __attribute__((ext_vector_type(4)));
typedef float f32x2 __attribute__((ext_vector_type(2)));
typedef __bf16 bf16x8 __attribute__((ext_vector_type(8)));
typedef _Float16 f16x8 __attribute__((ext_vector_type(8)));

#define L_SEQ 2048
#define D_CH 1024
#define N_MODE 128
#define PF_SLEEP 0
#define U_SLEEP 0

#define g_stamps ((unsigned long long*)wsp)
#define STAMP(i) do{ __builtin_amdgcn_sched_barrier(0); st_[i] = __builtin_amdgcn_s_memrealtime(); __builtin_amdgcn_sched_barrier(0); }while(0)
#define STAMP_DECL unsigned long long st_[16]; _Pragma("unroll") for (int i_ = 0; i_ < 16; i_++) st_[i_] = 0; st_[0] = t_in;
#define STAMP_OUT(wv_) do { asm volatile("s_waitcnt vmcnt(0)" ::: "memory"); STAMP(15); if ((threadIdx.x & 63) == 0) { _Pragma("unroll") for (int i_ = 0; i_ < 16; i_++) g_stamps[(blockIdx.x*12+(wv_))*16+i_] = st_[i_]; } } while (0)
__device__ __forceinline__ float bflo(u32 d) { return __uint_as_float(d << 16); }
__device__ __forceinline__ float bfhi(u32 d) { return __uint_as_float(d & 0xffff0000u); }
__device__ __forceinline__ u32 pk_bf16(float lo, float hi) {
  typedef __bf16 bf16x2 __attribute__((ext_vector_type(2)));
  f32x2 v = {lo, hi};
  bf16x2 r = __builtin_convertvector(v, bf16x2);
  return __builtin_bit_cast(u32, r);
}
__device__ __forceinline__ float rbf(float x) { return __uint_as_float(pk_bf16(0.f, x)); }

__device__ __forceinline__ u32 wave_max_u32(u32 x) {
  x = max(x, (u32)__builtin_amdgcn_update_dpp(0, (int)x, 0x111, 0xf, 0xf, true));
  x = max(x, (u32)__builtin_amdgcn_update_dpp(0, (int)x, 0x112, 0xf, 0xf, true));
  x = max(x, (u32)__builtin_amdgcn_update_dpp(0, (int)x, 0x114, 0xf, 0xf, true));
  x = max(x, (u32)__builtin_amdgcn_update_dpp(0, (int)x, 0x118, 0xf, 0xf, true));
  x = max(x, (u32)__builtin_amdgcn_update_dpp(0, (int)x, 0x142, 0xa, 0xf, true));
  x = max(x, (u32)__builtin_amdgcn_update_dpp(0, (int)x, 0x143, 0xc, 0xf, true));
  return (u32)__builtin_amdgcn_readlane((int)x, 63);
}

__device__ __forceinline__ f32x2 cmul(f32x2 g, f32x2 e) {
  f32x2 t, o;
  asm("v_pk_mul_f32 %0, %1, %2 op_sel:[0,0] op_sel_hi:[0,1]" : "=v"(t) : "v"(g), "v"(e));
  asm("v_pk_fma_f32 %0, %1, %2, %3 op_sel:[1,1,0] op_sel_hi:[1,0,1] neg_lo:[1,0,0]" : "=v"(o) : "v"(g), "v"(e), "v"(t));
  return o;
}
__device__ __forceinline__ f32x2 cexp_pos(float ar, float ai, float pos) {
  const float LOG2E = 1.44269504088896340736f;
  const float I2PI_HI = 0.15915494309189533577f;
  const float I2PI_LO = 6.4206383e-9f;
  float xr = ar * pos;
  float xi = ai * pos;
  float e = __builtin_amdgcn_exp2f(xr * LOG2E);
  float r = xi * I2PI_HI;
  float err = __builtin_fmaf(xi, I2PI_HI, -r);
  float f = __builtin_amdgcn_fractf(r);
  f += __builtin_fmaf(xi, I2PI_LO, err);
  f32x2 o;
  o[0] = e * __builtin_amdgcn_cosf(f);
  o[1] = e * __builtin_amdgcn_sinf(f);
  return o;
}
__device__ __forceinline__ u32 pk_f16(float lo, float hi) {
  typedef _Float16 f16x2 __attribute__((ext_vector_type(2)));
  f32x2 v = {lo, hi};
  return __builtin_bit_cast(u32, __builtin_convertvector(v, f16x2));
}

template <bool IS_A>
__device__ __forceinline__ f32x4 mma(u32x4 a, u32x4 b, f32x4 c) {
  if (IS_A) return __builtin_amdgcn_mfma_f32_16x16x32_f16(__builtin_bit_cast(f16x8, a), __builtin_bit_cast(f16x8, b), c, 0, 0, 0);
  else return __builtin_amdgcn_mfma_f32_16x16x32_bf16(__builtin_bit_cast(bf16x8, a), __builtin_bit_cast(bf16x8, b), c, 0, 0, 0);
}

__device__ __forceinline__ u32 lds_off(const char* p) {
  return (u32)(uintptr_t)(const __attribute__((address_space(3))) char*)p;
}

template <class F, int... I>
__device__ __forceinline__ void sfor_impl(F&& f, std::integer_sequence<int, I...>) { (f(std::integral_constant<int, I>{}), ...); }
template <int N, class F>
__device__ __forceinline__ void sfor(F&& f) { sfor_impl(f, std::make_integer_sequence<int, N>{}); }

#define XS_BYTES 4608
#define HC_BYTES 2368
#define HS_BYTES (4 * HC_BYTES)
#define W_BYTES (XS_BYTES + HS_BYTES)
#define YS_OFF (8 * W_BYTES)
#define YS_BYTES 2064
#define RAW_OFF (YS_OFF + 8 * YS_BYTES)
#define FL_OFF (RAW_OFF + 32768)
#define FL_FH 0
#define FL_XQ 32
#define FL_PF 64
#define FL_EX 96
#define LDS_BYTES (FL_OFF + 128)
#define GT_ROW 544

__device__ __forceinline__ void gen_taps(char* tab, int lane, u32 p_ldt, u32 p_lar, u32 p_aim, u32 p_cre, u32 p_cim,
                                         f32x4 (&acc)[3]) {
  const float dt = rbf(expf(bflo(p_ldt)));
  f32x2 C[2], e1[2], f8[2], E[2];
  u32 mc = 0;
#pragma unroll
  for (int m = 0; m < 2; m++) {
    C[m][0] = 2.0f * ((m ? bfhi(p_cre) : bflo(p_cre)) * dt);
    C[m][1] = -2.0f * ((m ? bfhi(p_cim) : bflo(p_cim)) * dt);
    mc = max(mc, max(__float_as_uint(C[m][0]) & 0x7fffffffu, __float_as_uint(C[m][1]) & 0x7fffffffu));
  }
  mc = wave_max_u32(mc);
#pragma unroll
  for (int m = 0; m < 2; m++) {
    const float ar = -rbf(expf(m ? bfhi(p_lar) : bflo(p_lar))) * dt;
    const float ai = (m ? bfhi(p_aim) : bflo(p_aim)) * dt;
    f8[m] = cexp_pos(ar, ai, 8.0f);
    e1[m] = cexp_pos(ar, -ai, 1.0f);
    E[m] = cexp_pos(ar, -ai, 128.0f);
  }
  const int ec = (mc > 0u && mc < 0x7f800000u) ? __builtin_amdgcn_frexp_expf(__uint_as_float(mc)) - 14 : 0;
#pragma unroll
  for (int m = 0; m < 2; m++) {
    C[m][0] = __builtin_ldexpf(C[m][0], -ec);
    C[m][1] = __builtin_ldexpf(C[m][1], -ec);
  }
  char* wp = tab + 8 * lane;
  const int c16 = lane & 15, q = lane >> 4;
  const char* rp = tab + c16 * GT_ROW + 16 * q;
#define PUT_ROW(row, z) *(u32x2*)(wp + (row) * GT_ROW) = (u32x2){pk_f16((z)[0][0], (z)[0][1]), pk_f16((z)[1][0], (z)[1][1])}
#define GET_FRAGS(dst) { __builtin_amdgcn_wave_barrier();                                           \
    _Pragma("unroll") for (int ks = 0; ks < 8; ks++) dst[ks] = *(const u32x4*)(rp + 64 * ks);       \
    __builtin_amdgcn_wave_barrier(); }
#define TILE_MMA(ti) { f32x4 a_ = {0.f, 0.f, 0.f, 0.f};                                             \
    _Pragma("unroll") for (int ks = 0; ks < 8; ks++) a_ = mma<true>(G[ks], Fh[ks], a_);             \
    acc[ti] = a_; }
#define CMUL2(z, e) { z[0] = cmul(z[0], e[0]); z[1] = cmul(z[1], e[1]); }
  {
    f32x2 F[2] = {{16384.0f, 0.f}, {16384.0f, 0.f}};
#pragma unroll
    for (int w = 0; w < 16; w++) {
      PUT_ROW(w, F);
      if (w < 15) CMUL2(F, f8);
    }
  }
  u32x4 Fh[8], G[8];
  GET_FRAGS(Fh);
  f32x2 g[8][2];
  g[0][0] = C[0]; g[0][1] = C[1];
#pragma unroll
  for (int be = 1; be < 8; be++) { g[be][0] = cmul(g[be - 1][0], e1[0]); g[be][1] = cmul(g[be - 1][1], e1[1]); }
#pragma unroll
  for (int be = 0; be < 8; be++) PUT_ROW(be, g[be]);
#pragma unroll
  for (int be = 0; be < 8; be++) { CMUL2(g[be], E); PUT_ROW(8 + be, g[be]); }
  GET_FRAGS(G);
#pragma unroll
  for (int al = 0; al < 2; al++)
#pragma unroll
    for (int be = 0; be < 8; be += 2) { CMUL2(g[be], E); PUT_ROW(4 * al + (be >> 1), g[be]); }
#pragma unroll
  for (int al = 0; al < 4; al++)
#pragma unroll
    for (int be = 0; be < 8; be += 4) { CMUL2(g[be], E); PUT_ROW(8 + 2 * al + (be >> 2), g[be]); }
  TILE_MMA(0);
  GET_FRAGS(G);
#pragma unroll
  for (int al = 0; al < 9; al++) { CMUL2(g[0], E); PUT_ROW(al, g[0]); }
  TILE_MMA(1);
  GET_FRAGS(G);
  TILE_MMA(2);
#pragma unroll
  for (int i = 0; i < 3; i++)
#pragma unroll
    for (int rg = 0; rg < 4; rg++) acc[i][rg] = __builtin_ldexpf(acc[i][rg], ec - 14);
}

__device__ __forceinline__ void dma16(const void* base, u32 off, u32 lds_base) {
  asm volatile("s_mov_b32 m0, %2\n\ts_nop 0\n\tglobal_load_lds_dwordx4 %0, %1" :: "v"(off), "s"(base), "s"(lds_base) : "memory", "m0");
}
__device__ __forceinline__ u32 poll_ge(u32 addr, u32 val) {
  u32 fl;
  for (;;) {
    asm volatile("ds_read_b32 %0, %1\n\ts_waitcnt lgkmcnt(0)" : "=v"(fl) : "v"(addr) : "memory");
    fl = __builtin_amdgcn_readfirstlane(fl);
    if (fl >= val) break;
    __builtin_amdgcn_s_sleep(1);
  }
  return fl;
}
__device__ __forceinline__ void lds_set(u32 addr, u32 val) {
  asm volatile("ds_write_b32 %0, %1" :: "v"(addr), "v"(val) : "memory");
}
__device__ __forceinline__ void lds_inc(u32 addr) {
  asm volatile("s_mov_b64 exec, 1\n\tds_add_u32 %0, %1\n\ts_mov_b64 exec, -1" :: "v"(addr), "v"(1u) : "memory");
}

__device__ __forceinline__ int xcd_group(int b) {
  int xcd = b & 7, idx = b >> 3;
  return ((xcd + 8 * (idx >> 3)) << 3) | (idx & 7);
}

constexpr int p_n(int t) { return t == 0 ? 26 : t == 1 ? 10 : 8; }
constexpr int p_nb(int t) { return t == 0 ? 8 : t == 1 ? 4 : 2; }
constexpr int p_a(int t, int k) { return t == 0 ? (k < 8 ? 0 : k < 15 ? 1 : k < 21 ? 2 : 3) : t == 1 ? (k < 4 ? 0 : k < 7 ? 1 : k < 9 ? 2 : 3) : (k >> 1); }
constexpr int p_e(int t, int k) { return t == 0 ? (k < 8 ? k : k < 15 ? k - 8 : k < 21 ? k - 15 : k - 21) : t == 1 ? (k < 4 ? k : k < 7 ? k - 4 : k < 9 ? k - 7 : 0) : (k & 1); }

template <int OFF> __device__ __forceinline__ void dsr64(u32x2& dst, u32 addr) {
  asm volatile("ds_read_b64 %0, %1 offset:%2" : "=v"(dst) : "v"(addr), "i"(OFF) : "memory");
}
template <int OFF> __device__ __forceinline__ void dsr128(u32x4& dst, u32 addr) {
  asm volatile("ds_read_b128 %0, %1 offset:%2" : "=v"(dst) : "v"(addr), "i"(OFF) : "memory");
}
template <int OFF> __device__ __forceinline__ void read_kv(u32 (&kf)[11], u32 (&vf)[8], u32 rh, u32 ra) {
  asm volatile("ds_read_u16_d16_hi %0, %19\n\tds_read_u16_d16_hi %1, %19 offset:128\n\tds_read_u16_d16_hi %2, %19 offset:256\n\t"
               "ds_read_u16_d16_hi %3, %20 offset:%21\n\tds_read_u16_d16_hi %4, %20 offset:%21+128\n\t"
               "ds_read_u16_d16_hi %5, %20 offset:%21+256\n\tds_read_u16_d16_hi %6, %20 offset:%21+384\n\t"
               "ds_read_u16_d16_hi %7, %20 offset:%21+512\n\tds_read_u16_d16_hi %8, %20 offset:%21+640\n\t"
               "ds_read_u16_d16_hi %9, %20 offset:%21+768\n\tds_read_u16_d16_hi %10, %20 offset:%21+896\n\t"
               "ds_read_u16_d16_hi %11, %20 offset:%21+1024\n\tds_read_u16_d16_hi %12, %20 offset:%21+1152\n\t"
               "ds_read_u16_d16_hi %13, %20 offset:%21+1280\n\tds_read_u16_d16_hi %14, %20 offset:%21+1408\n\t"
               "ds_read_u16_d16_hi %15, %20 offset:%21+1536\n\tds_read_u16_d16_hi %16, %20 offset:%21+1664\n\t"
               "ds_read_u16_d16_hi %17, %20 offset:%21+1792\n\tds_read_u16_d16_hi %18, %20 offset:%21+1920\n\t"
               "s_waitcnt lgkmcnt(0)"
               : "+v"(kf[0]), "+v"(kf[1]), "+v"(kf[2]), "+v"(kf[3]), "+v"(kf[4]), "+v"(kf[5]), "+v"(kf[6]), "+v"(kf[7]),
                 "+v"(kf[8]), "+v"(kf[9]), "+v"(kf[10]), "+v"(vf[0]), "+v"(vf[1]), "+v"(vf[2]), "+v"(vf[3]), "+v"(vf[4]),
                 "+v"(vf[5]), "+v"(vf[6]), "+v"(vf[7])
               : "v"(rh), "v"(ra), "i"(OFF) : "memory");
}
__device__ __forceinline__ u32x4 kv_chunk(const u32 (&kf)[11], const u32 (&vf)[8], float w0, float w1, float w2, float w3) {
  u32 o0, o1, o2, o3, a[8], b[8];
  asm(
      "v_mul_f32 %4, %20, %42\n\tv_mul_f32 %5, %21, %42\n\tv_mul_f32 %6, %22, %42\n\tv_mul_f32 %7, %23, %42\n\t"
      "v_mul_f32 %8, %24, %42\n\tv_mul_f32 %9, %25, %42\n\tv_mul_f32 %10, %26, %42\n\tv_mul_f32 %11, %27, %42\n\t"
      "v_mul_f32 %12, %21, %41\n\tv_mul_f32 %13, %22, %41\n\tv_mul_f32 %14, %23, %41\n\tv_mul_f32 %15, %24, %41\n\t"
      "v_mul_f32 %16, %25, %41\n\tv_mul_f32 %17, %26, %41\n\tv_mul_f32 %18, %27, %41\n\tv_mul_f32 %19, %28, %41\n\t"
      "v_cvt_pk_bf16_f32 %4, 0, %4\n\tv_cvt_pk_bf16_f32 %5, 0, %5\n\tv_cvt_pk_bf16_f32 %6, 0, %6\n\tv_cvt_pk_bf16_f32 %7, 0, %7\n\t"
      "v_cvt_pk_bf16_f32 %8, 0, %8\n\tv_cvt_pk_bf16_f32 %9, 0, %9\n\tv_cvt_pk_bf16_f32 %10, 0, %10\n\tv_cvt_pk_bf16_f32 %11, 0, %11\n\t"
      "v_cvt_pk_bf16_f32 %12, 0, %12\n\tv_cvt_pk_bf16_f32 %13, 0, %13\n\tv_cvt_pk_bf16_f32 %14, 0, %14\n\tv_cvt_pk_bf16_f32 %15, 0, %15\n\t"
      "v_cvt_pk_bf16_f32 %16, 0, %16\n\tv_cvt_pk_bf16_f32 %17, 0, %17\n\tv_cvt_pk_bf16_f32 %18, 0, %18\n\tv_cvt_pk_bf16_f32 %19, 0, %19\n\t"
      "v_add_f32 %4, %4, %12\n\tv_add_f32 %5, %5, %13\n\tv_add_f32 %6, %6, %14\n\tv_add_f32 %7, %7, %15\n\t"
      "v_add_f32 %8, %8, %16\n\tv_add_f32 %9, %9, %17\n\tv_add_f32 %10, %10, %18\n\tv_add_f32 %11, %11, %19\n\t"
      "v_mul_f32 %12, %22, %40\n\tv_mul_f32 %13, %23, %40\n\tv_mul_f32 %14, %24, %40\n\tv_mul_f32 %15, %25, %40\n\t"
      "v_mul_f32 %16, %26, %40\n\tv_mul_f32 %17, %27, %40\n\tv_mul_f32 %18, %28, %40\n\tv_mul_f32 %19, %29, %40\n\t"
      "v_cvt_pk_bf16_f32 %4, 0, %4\n\tv_cvt_pk_bf16_f32 %5, 0, %5\n\tv_cvt_pk_bf16_f32 %6, 0, %6\n\tv_cvt_pk_bf16_f32 %7, 0, %7\n\t"
      "v_cvt_pk_bf16_f32 %8, 0, %8\n\tv_cvt_pk_bf16_f32 %9, 0, %9\n\tv_cvt_pk_bf16_f32 %10, 0, %10\n\tv_cvt_pk_bf16_f32 %11, 0, %11\n\t"
      "v_cvt_pk_bf16_f32 %12, 0, %12\n\tv_cvt_pk_bf16_f32 %13, 0, %13\n\tv_cvt_pk_bf16_f32 %14, 0, %14\n\tv_cvt_pk_bf16_f32 %15, 0, %15\n\t"
      "v_cvt_pk_bf16_f32 %16, 0, %16\n\tv_cvt_pk_bf16_f32 %17, 0, %17\n\tv_cvt_pk_bf16_f32 %18, 0, %18\n\tv_cvt_pk_bf16_f32 %19, 0, %19\n\t"
      "v_add_f32 %4, %4, %12\n\tv_add_f32 %5, %5, %13\n\tv_add_f32 %6, %6, %14\n\tv_add_f32 %7, %7, %15\n\t"
      "v_add_f32 %8, %8, %16\n\tv_add_f32 %9, %9, %17\n\tv_add_f32 %10, %10, %18\n\tv_add_f32 %11, %11, %19\n\t"
      "v_mul_f32 %12, %23, %39\n\tv_mul_f32 %13, %24, %39\n\tv_mul_f32 %14, %25, %39\n\tv_mul_f32 %15, %26, %39\n\t"
      "v_mul_f32 %16, %27, %39\n\tv_mul_f32 %17, %28, %39\n\tv_mul_f32 %18, %29, %39\n\tv_mul_f32 %19, %30, %39\n\t"
      "v_cvt_pk_bf16_f32 %4, 0, %4\n\tv_cvt_pk_bf16_f32 %5, 0, %5\n\tv_cvt_pk_bf16_f32 %6, 0, %6\n\tv_cvt_pk_bf16_f32 %7, 0, %7\n\t"
      "v_cvt_pk_bf16_f32 %8, 0, %8\n\tv_cvt_pk_bf16_f32 %9, 0, %9\n\tv_cvt_pk_bf16_f32 %10, 0, %10\n\tv_cvt_pk_bf16_f32 %11, 0, %11\n\t"
      "v_cvt_pk_bf16_f32 %12, 0, %12\n\tv_cvt_pk_bf16_f32 %13, 0, %13\n\tv_cvt_pk_bf16_f32 %14, 0, %14\n\tv_cvt_pk_bf16_f32 %15, 0, %15\n\t"
      "v_cvt_pk_bf16_f32 %16, 0, %16\n\tv_cvt_pk_bf16_f32 %17, 0, %17\n\tv_cvt_pk_bf16_f32 %18, 0, %18\n\tv_cvt_pk_bf16_f32 %19, 0, %19\n\t"
      "v_add_f32 %4, %4, %12\n\tv_add_f32 %5, %5, %13\n\tv_add_f32 %6, %6, %14\n\tv_add_f32 %7, %7, %15\n\t"
      "v_add_f32 %8, %8, %16\n\tv_add_f32 %9, %9, %17\n\tv_add_f32 %10, %10, %18\n\tv_add_f32 %11, %11, %19\n\t"
      "v_cvt_pk_bf16_f32 %4, 0, %4\n\tv_cvt_pk_bf16_f32 %5, 0, %5\n\tv_cvt_pk_bf16_f32 %6, 0, %6\n\tv_cvt_pk_bf16_f32 %7, 0, %7\n\t"
      "v_cvt_pk_bf16_f32 %8, 0, %8\n\tv_cvt_pk_bf16_f32 %9, 0, %9\n\tv_cvt_pk_bf16_f32 %10, 0, %10\n\tv_cvt_pk_bf16_f32 %11, 0, %11\n\t"
      "v_mul_f32 %4, %4, %31\n\tv_mul_f32 %5, %5, %32\n\tv_mul_f32 %6, %6, %33\n\tv_mul_f32 %7, %7, %34\n\t"
      "v_mul_f32 %8, %8, %35\n\tv_mul_f32 %9, %9, %36\n\tv_mul_f32 %10, %10, %37\n\tv_mul_f32 %11, %11, %38\n\t"
      "v_cvt_pk_bf16_f32 %0, %11, %10\n\tv_cvt_pk_bf16_f32 %1, %9, %8\n\tv_cvt_pk_bf16_f32 %2, %7, %6\n\tv_cvt_pk_bf16_f32 %3, %5, %4"
      : "=&v"(o0), "=&v"(o1), "=&v"(o2), "=&v"(o3),
        "=&v"(a[0]), "=&v"(a[1]), "=&v"(a[2]), "=&v"(a[3]), "=&v"(a[4]), "=&v"(a[5]), "=&v"(a[6]), "=&v"(a[7]),
        "=&v"(b[0]), "=&v"(b[1]), "=&v"(b[2]), "=&v"(b[3]), "=&v"(b[4]), "=&v"(b[5]), "=&v"(b[6]), "=&v"(b[7])
      : "v"(kf[0]), "v"(kf[1]), "v"(kf[2]), "v"(kf[3]), "v"(kf[4]), "v"(kf[5]), "v"(kf[6]), "v"(kf[7]), "v"(kf[8]), "v"(kf[9]), "v"(kf[10]),
        "v"(vf[0]), "v"(vf[1]), "v"(vf[2]), "v"(vf[3]), "v"(vf[4]), "v"(vf[5]), "v"(vf[6]), "v"(vf[7]),
        "v"(w0), "v"(w1), "v"(w2), "v"(w3));
  return (u32x4){o0, o1, o2, o3};
}
template <int OFF> __device__ __forceinline__ void read_u(u32 (&lo)[4], u32 (&hi)[4], u32 ra) {
  asm volatile("ds_read_u16 %0, %8 offset:%9+896\n\tds_read_u16_d16_hi %4, %8 offset:%9+768\n\t"
               "ds_read_u16 %1, %8 offset:%9+640\n\tds_read_u16_d16_hi %5, %8 offset:%9+512\n\t"
               "ds_read_u16 %2, %8 offset:%9+384\n\tds_read_u16_d16_hi %6, %8 offset:%9+256\n\t"
               "ds_read_u16 %3, %8 offset:%9+128\n\tds_read_u16_d16_hi %7, %8 offset:%9\n\t"
               "s_waitcnt lgkmcnt(0)"
               : "=&v"(lo[0]), "=&v"(lo[1]), "=&v"(lo[2]), "=&v"(lo[3]), "+v"(hi[0]), "+v"(hi[1]), "+v"(hi[2]), "+v"(hi[3])
               : "v"(ra), "i"(OFF) : "memory");
}
template <int OFF> __device__ __forceinline__ void dsw128(u32 addr, u32x4 val) {
  asm volatile("ds_write_b128 %0, %1 offset:%2" :: "v"(addr), "v"(val), "i"(OFF) : "memory");
}
__device__ __forceinline__ void dsr32(u32& dst, u32 addr) {
  asm volatile("ds_read_b32 %0, %1" : "=v"(dst) : "v"(addr) : "memory");
}
__device__ __forceinline__ void line_req(u32& dst, const void* base, u32 off) {
  asm volatile("global_load_dword %0, %1, %2 nt" : "=v"(dst) : "v"(off), "s"(base) : "memory");
}
template <int N> __device__ __forceinline__ void wait_vm() { asm volatile("s_waitcnt vmcnt(%0)" :: "i"(N) : "memory"); }

template <int NB>
__device__ __forceinline__ void wait_frags(u32x2 (&AL)[4], u32x2 (&AH)[4], u32x4 (&B)[8]) {
  __builtin_amdgcn_sched_barrier(0);
  if (NB == 8)
    asm volatile("s_waitcnt lgkmcnt(0)"
                 : "+v"(AL[0]), "+v"(AL[1]), "+v"(AL[2]), "+v"(AL[3]), "+v"(AH[0]), "+v"(AH[1]), "+v"(AH[2]), "+v"(AH[3]),
                   "+v"(B[0]), "+v"(B[1]), "+v"(B[2]), "+v"(B[3]), "+v"(B[4]), "+v"(B[5]), "+v"(B[6]), "+v"(B[7]) :: "memory");
  else if (NB == 4)
    asm volatile("s_waitcnt lgkmcnt(0)"
                 : "+v"(AL[0]), "+v"(AL[1]), "+v"(AL[2]), "+v"(AL[3]), "+v"(AH[0]), "+v"(AH[1]), "+v"(AH[2]), "+v"(AH[3]),
                   "+v"(B[0]), "+v"(B[1]), "+v"(B[2]), "+v"(B[3]) :: "memory");
  else
    asm volatile("s_waitcnt lgkmcnt(0)"
                 : "+v"(AL[0]), "+v"(AL[1]), "+v"(AL[2]), "+v"(AL[3]), "+v"(AH[0]), "+v"(AH[1]), "+v"(AH[2]), "+v"(AH[3]),
                   "+v"(B[0]), "+v"(B[1]) :: "memory");
  __builtin_amdgcn_sched_barrier(0);
}

template <bool IS_A, int NR>
__device__ __forceinline__ void epi2(const char* ysb, int ystride, int tt, int row0, int g, const u32x4* xv, u32x4 dv,
                                     __amdgpu_buffer_rsrc_t ro) {
  u32 yv[8][NR / 2];
#pragma unroll
  for (int c = 0; c < 8; c++) {
    if (NR == 2) yv[c][0] = *(const u32*)(ysb + c * ystride + 4 * tt);
    else { u32x2 y2 = *(const u32x2*)(ysb + c * ystride + 8 * tt); yv[c][0] = y2[0]; yv[c][NR / 2 - 1] = y2[1]; }
  }
#pragma unroll
  for (int i = 0; i < NR; i++) {
    u32x4 o;
#pragma unroll
    for (int p = 0; p < 4; p++) {
      const u32 ya_ = yv[2 * p][i >> 1], yb_ = yv[2 * p + 1][i >> 1];
      const float ya = (i & 1) ? bfhi(ya_) : bflo(ya_), yb = (i & 1) ? bfhi(yb_) : bflo(yb_);
      if (IS_A) o[p] = pk_bf16(ya + rbf(bflo(xv[i][p]) * bflo(dv[p])), yb + rbf(bfhi(xv[i][p]) * bfhi(dv[p])));
      else o[p] = pk_bf16(ya * bflo(xv[i][p]), yb * bfhi(xv[i][p]));
    }
    __builtin_amdgcn_raw_buffer_store_b128(o, ro, ((row0 + NR * tt + i) * (2 * D_CH) + g * 8) * 2, 0, 0);
  }
}
__device__ __forceinline__ int raw_pos(int r) { return (r & ~63) * 16 + (r & 7) * 128 + ((r >> 3) & 7) * 16; }

template <bool IS_A>
__device__ __forceinline__ void main_body(unsigned long long t_in, void* wsp, char* smem, int g, int w, u32 pf,
                                          const u16* __restrict__ lar, const u16* __restrict__ aim,
                                          const u16* __restrict__ cre, const u16* __restrict__ cim, const u16* __restrict__ ldt,
                                          const u16* __restrict__ kin, const u16* __restrict__ vin,
                                          const u16* __restrict__ ssmk, const u16* __restrict__ dvec,
                                          const u16* __restrict__ qin, u16* __restrict__ out) {
  STAMP_DECL
  const int t = threadIdx.x, lane = t & 63;
  const int gc = (g & 127) * 8;
  const int ch = gc + w;
  char* xs = smem + w * W_BYTES;
  char* hs = xs + XS_BYTES;
  const u32 fl0 = lds_off(smem + FL_OFF);
  const int hl = lane < 48 ? lane : lane - 48;
  int hc[3];
#pragma unroll
  for (int j = 0; j < 3; j++) {
    const int c = 48 * j + hl, sg = (c * 57) >> 10;
    hc[j] = 32 * (sg & 3) + 16 * (sg >> 2) + c - 18 * sg;
  }
  u32x4 hd[6];
  u32x2 he[6];
  int esc = 0;
  const __amdgpu_buffer_rsrc_t rh = __builtin_amdgcn_make_buffer_rsrc((void*)(ssmk + (size_t)ch * L_SEQ), 0, L_SEQ * 2, 0x00020000);
  u32x4 dv;
  u32 p_ldt = 0, p_lar = 0, p_aim = 0, p_cre = 0, p_cim = 0, pfx = 0;
  if (IS_A) {
    const int po = (ch * N_MODE + 2 * lane) * 2;
    asm volatile("global_load_ushort %0, %5, %6\n\tglobal_load_dword %1, %7, %8 nt\n\t"
                 "global_load_dword %2, %7, %9 nt\n\tglobal_load_dword %3, %7, %10 nt\n\tglobal_load_dword %4, %7, %11 nt"
                 : "=&v"(p_ldt), "=&v"(p_lar), "=&v"(p_aim), "=&v"(p_cre), "=&v"(p_cim)
                 : "v"(ch * 2), "s"(ldt), "v"(po), "s"(lar), "s"(aim), "s"(cre), "s"(cim) : "memory");
    asm volatile("global_load_dwordx4 %0, %1, %2" : "=v"(dv) : "v"(gc * 2), "s"(dvec) : "memory");
  } else {
    const char* pa = (const char*)(lane < 32 ? kin : vin) + (size_t)((256 * w + (g & 7) * 32 + (lane & 31)) * (D_CH * 2) + (gc & ~63) * 2);
    for (int i = 0; i < w; i++) __builtin_amdgcn_s_sleep(PF_SLEEP);
    asm volatile("global_load_dword %0, %1, off nt" : "=v"(pfx) : "v"(pa) : "memory");
    __builtin_amdgcn_sched_barrier(0);
#pragma unroll
    for (int s = 0; s < 3; s++) {
      int o1 = 16 * (hc[s] - 2), o2 = o1 + 16;
      if (s == 0) { o1 = o1 >= 0 ? o1 : 0x40000000; o2 = o2 >= 0 ? o2 : 0x40000000; }
      hd[s] = __builtin_amdgcn_raw_buffer_load_b128(rh, o1, 0, 0);
      he[s] = __builtin_amdgcn_raw_buffer_load_b64(rh, o2, 0, 0);
    }
    __builtin_amdgcn_sched_barrier(0);
    dv = *(const u32x4*)(dvec + gc);
    __builtin_amdgcn_sched_barrier(0);
  }
  if (lane < 30) *(u32x4*)(xs + 16 * lane) = (u32x4){0, 0, 0, 0};
  __builtin_amdgcn_sched_barrier(0);
  STAMP(1);
  if (IS_A) {
    asm volatile("s_waitcnt vmcnt(0)" : "+v"(p_ldt), "+v"(p_lar), "+v"(p_aim), "+v"(p_cre), "+v"(p_cim), "+v"(dv) : "v"(pf) : "memory");
    __builtin_amdgcn_sched_barrier(0);
    STAMP(2);
    f32x4 ka[3];
    gen_taps(hs, lane, p_ldt, p_lar, p_aim, p_cre, p_cim, ka);
    STAMP(3);
    char* kb = hs;
    char* lin = hs + 1536;
    u16* kc = (u16*)kb;
    {
      const int c16 = lane & 15, q = lane >> 4;
      const bool v2 = q < 2, v28 = (q == 2) && (c16 == 0);
      u32 mk = 0;
#pragma unroll
      for (int rg = 0; rg < 4; rg++) {
        mk = max(mk, max(__float_as_uint(ka[0][rg]) & 0x7fffffffu, __float_as_uint(ka[1][rg]) & 0x7fffffffu));
        u32 m2 = __float_as_uint(ka[2][rg]) & 0x7fffffffu;
        mk = max(mk, (v2 || (rg == 0 && v28)) ? m2 : 0u);
      }
      mk = wave_max_u32(mk);
      esc = (mk > 0u && mk < 0x7f800000u) ? __builtin_amdgcn_frexp_expf(__uint_as_float(mk)) - 15 : 0;
#pragma unroll
      for (int rg = 0; rg < 4; rg++) {
        const int row = 4 * q + rg;
        kc[128 * (row >> 3) + (row & 7) + 8 * c16] = __builtin_bit_cast(u16, (_Float16)__builtin_ldexpf(ka[0][rg], -esc));
        int i1 = (row < 8) ? 256 + 64 * (row >> 2) + (row & 3) + 4 * c16 : 384 + 32 * ((row - 8) >> 1) + (row & 1) + 2 * c16;
        kc[i1] = __builtin_bit_cast(u16, (_Float16)__builtin_ldexpf(ka[1][rg], -esc));
        kc[512 + 16 * row + c16] = __builtin_bit_cast(u16, (_Float16)__builtin_ldexpf(ka[2][rg], -esc));
      }
    }
    __builtin_amdgcn_wave_barrier();
    const bool dir = lane < 32;
    const int a0 = dir ? 16 * lane : 512 + 8 * (lane - 32);
    u32x2 d01 = *(const u32x2*)(kb + a0), d23 = *(const u32x2*)(kb + a0 + 8);
    u32x4 o;
    o[0] = __builtin_amdgcn_perm(d01[0], d01[0], dir ? 0x03020100u : 0x01000100u);
    o[1] = __builtin_amdgcn_perm(d01[1], d01[0], dir ? 0x07060504u : 0x05040302u);
    o[2] = __builtin_amdgcn_perm(d23[0], d01[1], dir ? 0x07060504u : 0x01000100u);
    o[3] = dir ? d23[1] : __builtin_amdgcn_perm(d23[0], d01[1], 0x05040302u);
    *(u32x4*)(lin + 32 + 16 * lane) = o;
    u32 d0 = *(const u32*)(kb + 768 + 4 * lane), d1 = *(const u32*)(kb + 772 + 4 * lane);
    o[0] = __builtin_amdgcn_perm(d0, d0, 0x01000100u);
    o[1] = d0;
    o[2] = __builtin_amdgcn_perm(d0, d0, 0x03020302u);
    o[3] = __builtin_amdgcn_perm(d1, d1, 0x01000100u);
    *(u32x4*)(lin + 32 + 1024 + 16 * lane) = o;
#pragma unroll
    for (int i = 0; i < 2; i++) {
      u32 x0 = kc[512 + 64 * i + lane], x1 = kc[513 + 64 * i + lane];
      o[0] = o[1] = x0 | (x0 << 16);
      o[3] = x1 | (x1 << 16);
      o[2] = (lane & 1) ? o[3] : (x0 | (x1 << 16));
      *(u32x4*)(lin + 32 + 2048 + 1024 * i + 16 * lane) = o;
    }
    if (lane < 4) *(u32x4*)(lin + ((lane & 2) ? 4096 : 0) + 16 * lane) = (u32x4){0, 0, 0, 0};
    __builtin_amdgcn_wave_barrier();
#pragma unroll
    for (int s = 0; s < 6; s++) {
      hd[s] = *(const u32x4*)(lin + 16 * hc[s % 3] + 2048 * (s / 3));
      he[s] = *(const u32x2*)(lin + 16 * hc[s % 3] + 2048 * (s / 3) + 16);
    }
    __builtin_amdgcn_wave_barrier();
  } else {
    asm volatile("s_waitcnt vmcnt(7)\n\tds_write_b32 %2, %3" :: "v"(pfx), "v"(pf), "v"(fl0 + FL_PF + 4 * w), "v"(1u) : "memory");
    __builtin_amdgcn_sched_barrier(0);
  }
  STAMP(4);
  char* hw = hs + 16 * hl;
#define STAGE_PIECE(set, k) {                                                          \
    const u32x4 d4 = hd[3 * (set) + ((k) >> 2)];                                       \
    const u32x2 e2 = he[3 * (set) + ((k) >> 2)];                                       \
    u32x4 o_;                                                                          \
    if (((k) & 3) == 0) o_ = d4;                                                       \
    else if (((k) & 3) == 1) o_ = (u32x4){__builtin_amdgcn_alignbit(d4[1], d4[0], 16), __builtin_amdgcn_alignbit(d4[2], d4[1], 16), \
                                          __builtin_amdgcn_alignbit(d4[3], d4[2], 16), __builtin_amdgcn_alignbit(e2[0], d4[3], 16)}; \
    else if (((k) & 3) == 2) o_ = (u32x4){d4[1], d4[2], d4[3], e2[0]};                 \
    else o_ = (u32x4){__builtin_amdgcn_alignbit(d4[2], d4[1], 16), __builtin_amdgcn_alignbit(d4[3], d4[2], 16), \
                      __builtin_amdgcn_alignbit(e2[0], d4[3], 16), __builtin_amdgcn_alignbit(e2[1], e2[0], 16)}; \
    *(u32x4*)(hw + 768 * ((k) >> 2) + ((k) & 3) * HC_BYTES) = o_; }
#pragma unroll
  for (int k = 0; k < 12; k++) STAGE_PIECE(0, k);
  STAMP(5);
  const int r = lane & 15, q = lane >> 4;
  f32x4 acc[8];
#pragma unroll
  for (int i = 0; i < 8; i++) acc[i] = (f32x4){0.f, 0.f, 0.f, 0.f};
  const int lam = 25 + r - 8 * q;
  const u32 abase = lds_off(hs) + (lam & 3) * HC_BYTES + 2 * (lam & ~3);
  const u32 bbase = lds_off(xs) + 32 * r + 16 * q;
  u32x2 AL[2][4], AH[2][4];
  u32x4 B[2][8];
#define LOAD_PIECE(it, k, e0, bf) {                                                             \
    if constexpr ((k) < 8 && ((k) & 1) == 0) dsr64<64 * ((it) & 3) + 288 * (((k) >> 1) + 4 * ((it) >> 2))>(AL[bf][((k) >> 1) & 3], abase);   \
    else if constexpr ((k) < 8) dsr64<64 * ((it) & 3) + 288 * (((k) >> 1) + 4 * ((it) >> 2)) + 8>(AH[bf][((k) >> 1) & 3], abase); \
    else dsr128<448 + 512 * ((e0) + (k) - 8) - 64 * (it)>(B[bf][((k) - 8) & 7], bbase); }
  __builtin_amdgcn_sched_barrier(0);
  if (IS_A) {
    asm volatile("s_barrier" ::: "memory");
    esc += (int)poll_ge(fl0 + FL_EX + 4 * w, 1u) - 1024;
  }
  else poll_ge(fl0 + FL_XQ, 4u);
  __builtin_amdgcn_sched_barrier(0);
  STAMP(6);
  sfor<8 + p_nb(IS_A ? 0 : 2)>([&](auto kc_) { constexpr int k = decltype(kc_)::value; LOAD_PIECE(0, k, 0, 0); });
  __builtin_amdgcn_sched_barrier(0);
#define PIN_ACC asm volatile("" : "+v"(acc[0]), "+v"(acc[1]), "+v"(acc[2]), "+v"(acc[3]), "+v"(acc[4]), "+v"(acc[5]), "+v"(acc[6]), "+v"(acc[7]));
#define PASS(pt, e0, t0, nt, ne0, restage, nq)                                          \
  sfor<8>([&](auto ic_) {                                                               \
    constexpr int it = decltype(ic_)::value, bf = it & 1;                               \
    wait_frags<p_nb(pt)>(AL[bf], AH[bf], B[bf]);                                        \
    u32 fl_;                                                                            \
    sfor<p_n(pt)>([&](auto kc_) {                                                       \
      constexpr int k_ = decltype(kc_)::value, a = p_a(pt, k_), e = p_e(pt, k_);        \
      acc[(t0) + a + e] = mma<IS_A>((u32x4){AL[bf][a][0], AL[bf][a][1], AH[bf][a][0], AH[bf][a][1]}, B[bf][e], acc[(t0) + a + e]); \
      if constexpr (it < 7) {                                                           \
        if constexpr ((pt) == 0) { if constexpr (k_ < 16) LOAD_PIECE(it + 1, k_, e0, bf ^ 1); } \
        else if constexpr (2 * k_ < 8 + p_nb(pt)) { LOAD_PIECE(it + 1, 2 * k_, e0, bf ^ 1); LOAD_PIECE(it + 1, 2 * k_ + 1, e0, bf ^ 1); } \
      } else if constexpr ((nt) >= 0) {                                                 \
        if constexpr (restage) {       \
          if constexpr ((pt) == 0) {                                                    \
            if constexpr (k_ < 12) STAGE_PIECE(1, k_)                                   \
            else if constexpr (k_ < 24) LOAD_PIECE(0, k_ - 12, ne0, bf ^ 1);            \
          } else {                                                                      \
            if constexpr (k_ < 6) { STAGE_PIECE(1, 2 * k_); STAGE_PIECE(1, 2 * k_ + 1); } \
            else { LOAD_PIECE(0, 3 * (k_ - 6), ne0, bf ^ 1); LOAD_PIECE(0, 3 * (k_ - 6) + 1, ne0, bf ^ 1); LOAD_PIECE(0, 3 * (k_ - 6) + 2, ne0, bf ^ 1); } \
          }                                                                             \
        } else {                                                                        \
          if constexpr (k_ == 0) dsr32(fl_, fl0 + FL_XQ + 4 * (nq)); \
          if constexpr (k_ < 4) { LOAD_PIECE(0, 2 * k_, ne0, bf ^ 1); LOAD_PIECE(0, 2 * k_ + 1, ne0, bf ^ 1); } \
          if constexpr (k_ == 5) {                                                      \
            asm volatile("s_waitcnt lgkmcnt(8)" : "+v"(fl_) :: "memory");               \
            if (__builtin_amdgcn_readfirstlane(fl_) < 4u) poll_ge(fl0 + FL_XQ + 4 * (nq), 4u); \
          }                                                                             \
          if constexpr (k_ >= 5 && 2 * (k_ - 5) < p_nb(nt)) { LOAD_PIECE(0, 8 + 2 * (k_ - 5), ne0, bf ^ 1); LOAD_PIECE(0, 9 + 2 * (k_ - 5), ne0, bf ^ 1); } \
        }                                                                               \
      }                                                                                 \
      __builtin_amdgcn_sched_barrier(0);                                                \
    });                                                                                 \
    PIN_ACC                                                                             \
  });
  const float dsk = (w & 1) ? bfhi(dv[w >> 1]) : bflo(dv[w >> 1]);
#define EPI1(c0_, ysp)                                                                  \
  {                                                                                     \
    u32x2 uv[4];                                                                        \
    if (!IS_A) {                                                                        \
      _Pragma("unroll") for (int ci = 0; ci < 4; ci++)                                  \
        uv[ci] = *(const u32x2*)(xs + 480 + 512 * ((c0_) + ci) + 32 * r + 16 * (q >> 1) + 8 - 8 * (q & 1)); \
      __builtin_amdgcn_sched_barrier(0);                                                \
    }                                                                                   \
    _Pragma("unroll") for (int ci = 0; ci < 4; ci++) {                                  \
      const int l = 256 * ci + 16 * r + 4 * q;                                          \
      const float y0 = acc[(c0_) + ci][0], y1 = acc[(c0_) + ci][1], y2 = acc[(c0_) + ci][2], y3 = acc[(c0_) + ci][3]; \
      u32x2 o;                                                                          \
      if (IS_A) {                                                                       \
        o[0] = pk_bf16(__builtin_ldexpf(y0, esc), __builtin_ldexpf(y1, esc));           \
        o[1] = pk_bf16(__builtin_ldexpf(y2, esc), __builtin_ldexpf(y3, esc));           \
      } else {                                                                          \
        const float x3 = bflo(uv[ci][0]), x2 = bfhi(uv[ci][0]), x1 = bflo(uv[ci][1]), x0 = bfhi(uv[ci][1]); \
        o[0] = pk_bf16(rbf(y0) + rbf(x0 * dsk), rbf(y1) + rbf(x1 * dsk));               \
        o[1] = pk_bf16(rbf(y2) + rbf(x2 * dsk), rbf(y3) + rbf(x3 * dsk));               \
      }                                                                                 \
      *(u32x2*)((ysp) + 2 * l) = o;                                                     \
    }                                                                                   \
  }
  u32x4 xv[2];
  if (IS_A) {
    PASS(0, 0, 0, 1, 0, true, 0);
    STAMP(8);
    EPI1(0, smem + YS_OFF + w * YS_BYTES);
    lds_set(fl0 + FL_FH + 4 * w, 1u);
    STAMP(9);
    PASS(1, 0, 4, -1, 0, false, 0);
  } else {
    PASS(2, 0, 0, 2, 2, false, 1);
    STAMP(7);
    PASS(2, 2, 2, 1, 4, false, 3);
    STAMP(8);
    EPI1(0, smem + YS_OFF + w * YS_BYTES);
    lds_set(fl0 + FL_FH + 4 * w, 1u);
#pragma unroll
    for (int s = 3; s < 6; s++) {
      const int o1 = 16 * (hc[s - 3] - 2) + 2048;
      hd[s] = __builtin_amdgcn_raw_buffer_load_b128(rh, o1, 0, 0);
      he[s] = __builtin_amdgcn_raw_buffer_load_b64(rh, o1 + 16, 0, 0);
    }
    const __amdgpu_buffer_rsrc_t rq = __builtin_amdgcn_make_buffer_rsrc((void*)qin, 0, L_SEQ * D_CH * 2, 0x00020000);
#pragma unroll
    for (int i = 0; i < 2; i++) xv[i] = __builtin_amdgcn_raw_buffer_load_b128(rq, ((1024 + 2 * t + i) * D_CH + gc) * 2, 0, 0);
    __builtin_amdgcn_sched_barrier(0);
    STAMP(9);
    PASS(1, 4, 4, 1, 0, true, 0);
    STAMP(10);
    PASS(1, 0, 4, -1, 0, false, 0);
  }
  STAMP(11);
  EPI1(4, hs);
  if (IS_A) {
#pragma unroll
    for (int i = 0; i < 2; i++) xv[i] = *(const u32x4*)(smem + RAW_OFF + raw_pos(1024 + 2 * t + i));
  }
  STAMP(12);
  asm volatile("s_waitcnt lgkmcnt(0)\n\ts_barrier" ::: "memory");
  STAMP(13);
  const __amdgpu_buffer_rsrc_t ro = __builtin_amdgcn_make_buffer_rsrc((void*)out, 0, 8 * 1024 * 1024, 0x00020000);
  epi2<IS_A, 2>(smem + XS_BYTES, W_BYTES, t, 1024, g, xv, dv, ro);
  STAMP(14);
  STAMP_OUT(w);
}

template <bool IS_A>
__device__ __forceinline__ void stream_body(unsigned long long t_in, void* wsp, char* smem, int g, int p, u32 pf, const u16* __restrict__ u,
                                            const u16* __restrict__ kin, const u16* __restrict__ vin,
                                            const u16* __restrict__ shk, const u16* __restrict__ dvec,
                                            const u16* __restrict__ qin, u16* __restrict__ out) {
  STAMP_DECL
  const int lane = threadIdx.x & 63;
  const int gc = (g & 127) * 8;
  const u32 sb = lds_off(smem);
  const u32 fl0 = sb + FL_OFF;
  const int c = lane >> 3, rg = lane & 7;
  u32x4 dv;
  const u32 go = (64 * p + 8 * rg + c) * (D_CH * 2) + gc * 2;
  const u32 po = ((g & 7) * 256 + 64 * p + lane) * (D_CH * 2) + (gc & ~63) * 2;
  const u32 xw = sb + c * W_BYTES + 480 + 128 * p + 16 * rg;
  u32 pfx = 0;
  u32x4 xv[4];
  __builtin_amdgcn_s_setprio(3);
  if (IS_A) {
    __builtin_amdgcn_s_sleep(U_SLEEP);
    line_req(pfx, u, po);
    asm volatile("global_load_dwordx4 %0, %1, %2" : "=v"(dv) : "v"(gc * 2), "s"(dvec) : "memory");
    asm volatile("s_waitcnt vmcnt(0)" : "+v"(dv) : "v"(pfx), "v"(pf) : "memory");
    STAMP(1);
    const u32 rb = sb + RAW_OFF + 1024 * p;
#pragma unroll
    for (int e = 0; e < 8; e++) dma16(u + (size_t)e * 256 * D_CH, go, rb + 4096 * e);
    STAMP(2);
    const u32 ra = rb + 16 * rg + 2 * c;
    u32 hi[4] = {0, 0, 0, 0};
    sfor<8>([&](auto ec_) {
      constexpr int e = decltype(ec_)::value;
      wait_vm<7 - e>();
      u32 lo[4];
      read_u<4096 * e>(lo, hi, ra);
      const u32x4 o = {lo[0] | hi[0], lo[1] | hi[1], lo[2] | hi[2], lo[3] | hi[3]};
      dsw128<512 * e>(xw, o);
    });
    STAMP(3);
    lds_inc(fl0 + FL_XQ);
    poll_ge(fl0 + FL_XQ, 4u);
    STAMP(4);
#pragma unroll
    for (int cc = 0; cc < 2; cc++) {
      char* xp = smem + (2 * p + cc) * W_BYTES + 480 + 16 * lane;
      u32x4 xr[4];
#pragma unroll
      for (int j = 0; j < 4; j++) xr[j] = *(const u32x4*)(xp + 1024 * j);
      u32 mx = 0;
#pragma unroll
      for (int j = 0; j < 4; j++)
#pragma unroll
        for (int i = 0; i < 4; i++) {
          u32 dd = xr[j][i];
          mx = max(mx, max(dd & 0x7fffu, (dd >> 16) & 0x7fffu));
        }
      mx = wave_max_u32(mx);
      const int ex = (int)(mx >> 7) - 127 - 14;
#pragma unroll
      for (int j = 0; j < 4; j++) {
        u32x4 o;
#pragma unroll
        for (int i = 0; i < 4; i++) {
          u32 dd = xr[j][i];
          float lo = __builtin_ldexpf(bflo(dd), -ex), hi2 = __builtin_ldexpf(bfhi(dd), -ex);
          o[i] = __builtin_bit_cast(u32, __builtin_amdgcn_cvt_pkrtz(lo, hi2));
        }
        *(u32x4*)(xp + 1024 * j) = o;
      }
      __builtin_amdgcn_wave_barrier();
      lds_set(fl0 + FL_EX + 4 * (2 * p + cc), (u32)(ex + 1024));
    }
    STAMP(5);
    asm volatile("s_waitcnt lgkmcnt(0)\n\ts_barrier" ::: "memory");
    __builtin_amdgcn_s_setprio(0);
#pragma unroll
    for (int i = 0; i < 4; i++) xv[i] = *(const u32x4*)(smem + RAW_OFF + raw_pos(4 * (64 * p + lane) + i));
  } else {
    const u32 rb = sb + RAW_OFF + 8192 * p;
    u32x2 sw;
    asm volatile("global_load_dwordx2 %0, %1, %2" : "=v"(sw) : "v"((gc + c) * 8), "s"(shk) : "memory");
    dv = (u32x4){0, 0, 0, 0};
    {
      const int hr = 256 * rg + 64 * p - 3 + c;
      if (lane < 24) {
        if (hr >= 0) dma16(kin, hr * (D_CH * 2) + gc * 2, rb + 6144);
        else *(u32x4*)(smem + RAW_OFF + 6144 + 16 * lane) = (u32x4){0, 0, 0, 0};
      }
    }
#define ISSUE(e) { poll_ge(fl0 + FL_PF + 4 * (e), 1u);                                  \
      dma16(kin + (size_t)(e) * 256 * D_CH, go, rb + 2048 * ((e) % 3));                 \
      dma16(vin + (size_t)(e) * 256 * D_CH, go, rb + 2048 * ((e) % 3) + 1024); }
    STAMP(1);
    ISSUE(0);
    STAMP(2);
    ISSUE(1); ISSUE(2);
    STAMP(3);
    asm volatile("s_waitcnt vmcnt(6)" : "+v"(sw) : "v"(pf) : "memory");
    const float w0 = bflo(sw[0]), w1 = bfhi(sw[0]), w2 = bflo(sw[1]), w3 = bfhi(sw[1]);
    const u32 ra = rb + 16 * rg + 2 * c;
    u32 kf[11], vf[8];
#pragma unroll
    for (int i = 0; i < 11; i++) kf[i] = 0;
#pragma unroll
    for (int i = 0; i < 8; i++) vf[i] = 0;
    sfor<8>([&](auto ec_) {
      constexpr int e = decltype(ec_)::value;
      wait_vm<(e < 6 ? 4 : e == 6 ? 2 : 0) + (e == 3 || e == 4 || e == 5 ? 1 : 0)>();
      STAMP(4 + e);
      const u32 rh = rg ? ra + (2048 * (e % 3) + 640 - 16) : ra + (6144 + 16 * e);
      read_kv<2048 * (e % 3)>(kf, vf, rh, ra);
      const u32x4 o = kv_chunk(kf, vf, w0, w1, w2, w3);
      dsw128<512 * e>(xw, o);
      if constexpr (e & 1) lds_inc(fl0 + FL_XQ + 4 * (e >> 1));
      if constexpr (e < 5) ISSUE(e + 3);
      if constexpr (e == 2) line_req(pfx, qin, po);
    });
    asm volatile("" :: "v"(pfx));
    __builtin_amdgcn_s_setprio(0);
    __builtin_amdgcn_sched_barrier(0);
    const __amdgpu_buffer_rsrc_t rq = __builtin_amdgcn_make_buffer_rsrc((void*)qin, 0, L_SEQ * D_CH * 2, 0x00020000);
#pragma unroll
    for (int i = 0; i < 4; i++) xv[i] = __builtin_amdgcn_raw_buffer_load_b128(rq, ((4 * (64 * p + lane) + i) * D_CH + gc) * 2, 0, 0);
  }
  STAMP(12);
  {
    u32 fl;
    for (;;) {
      asm volatile("ds_read_b32 %0, %1\n\ts_waitcnt lgkmcnt(0)" : "=v"(fl) : "v"(fl0 + FL_FH + 4 * (lane & 7)) : "memory");
      if (__builtin_amdgcn_ballot_w64(fl == 0u) == 0ull) break;
      __builtin_amdgcn_s_sleep(2);
    }
  }
  const __amdgpu_buffer_rsrc_t ro = __builtin_amdgcn_make_buffer_rsrc((void*)out, 0, 8 * 1024 * 1024, 0x00020000);
  STAMP(13);
  epi2<IS_A, 4>(smem + YS_OFF, YS_BYTES, 64 * p + lane, 0, g, xv, dv, ro);
  STAMP(14);
  asm volatile("s_barrier" ::: "memory");
  STAMP_OUT(8 + p);
}

__global__ __launch_bounds__(768) void fused_kernel(
    const u16* __restrict__ u, const u16* __restrict__ lar, const u16* __restrict__ aim,
    const u16* __restrict__ cre, const u16* __restrict__ cim, const u16* __restrict__ ldt,
    const u16* __restrict__ dskip, const u16* __restrict__ qin, const u16* __restrict__ kin,
    const u16* __restrict__ vin, const u16* __restrict__ shk, const u16* __restrict__ ssmk,
    const u16* __restrict__ ssmd, u16* __restrict__ out, void* wsp) {
  extern __shared__ __attribute__((aligned(16))) char smem[];
  const unsigned long long t_in = __builtin_amdgcn_s_memrealtime();
  u32 pf;
  {
    const char* pc;
    u32 en;
    asm volatile("s_getpc_b64 %1\n.Lpf_anchor:\n\ts_mov_b32 %2, .Lfused_end-.Lpf_anchor\n\t"
                 "v_lshlrev_b32 %0, 6, %3\n\tv_min_u32 %0, %2, %0\n\tglobal_load_dword %0, %0, %1"
                 : "=&v"(pf), "=&s"(pc), "=&s"(en) : "v"(threadIdx.x) : "memory");
    if (threadIdx.x < 32) *(u32*)(smem + FL_OFF + 4 * threadIdx.x) = 0u;
    __builtin_amdgcn_sched_barrier(0);
    asm volatile("s_waitcnt lgkmcnt(0)\n\ts_barrier" :: "s"(u), "s"(lar), "s"(aim), "s"(cre), "s"(cim), "s"(ldt), "s"(dskip), "s"(qin), "s"(kin), "s"(vin), "s"(shk),
                 "s"(ssmk), "s"(ssmd), "s"(out) : "memory");
    __builtin_amdgcn_sched_barrier(0);
  }
  const int g = xcd_group(blockIdx.x);
  const int w = __builtin_amdgcn_readfirstlane(threadIdx.x >> 6);
  if (w >= 8) {
    if (g < 128) stream_body<true>(t_in, wsp, smem, g, w - 8, pf, u, kin, vin, shk, dskip, qin, out);
    else stream_body<false>(t_in, wsp, smem, g, w - 8, pf, u, kin, vin, shk, ssmd, qin, out);
  } else {
    if (g < 128) main_body<true>(t_in, wsp, smem, g, w, pf, lar, aim, cre, cim, ldt, kin, vin, ssmk, dskip, qin, out);
    else main_body<false>(t_in, wsp, smem, g, w, pf, lar, aim, cre, cim, ldt, kin, vin, ssmk, ssmd, qin, out);
  }
}
__global__ void end_marker_kernel() { asm volatile(".Lfused_end:"); }

__global__ __launch_bounds__(1024) void spin_kernel(const unsigned long long* st) {
  typedef unsigned long long u64;
  __shared__ u64 mn[4][16], mx[4][16], sm[4][16], t0s;
  __shared__ unsigned cn[4][16];
  const int t = threadIdx.x;
  if (t < 64) { mn[t >> 4][t & 15] = ~0ull; mx[t >> 4][t & 15] = 0ull; sm[t >> 4][t & 15] = 0ull; cn[t >> 4][t & 15] = 0; }
  if (t == 0) t0s = ~0ull;
  __syncthreads();
  for (int w = t; w < 3072; w += 1024) atomicMin(&t0s, st[w * 16]);
  __syncthreads();
  for (int w = t; w < 3072; w += 1024) {
    const int b = w / 12, wv = w % 12;
    const int cls = (xcd_group(b) >= 128 ? 2 : 0) + (wv >= 8 ? 1 : 0);
    for (int i = 0; i < 16; i++) {
      u64 v = st[w * 16 + i];
      if (v) { v -= t0s; atomicMin(&mn[cls][i], v); atomicMax(&mx[cls][i], v); atomicAdd(&sm[cls][i], v); atomicAdd(&cn[cls][i], 1u); }
    }
  }
  __syncthreads();
  if (t == 0) {
    u64 q = 0;
    if (q > 10000) q = 10000;
    const u64 t0 = __builtin_amdgcn_s_memrealtime();
    while (__builtin_amdgcn_s_memrealtime() - t0 < 10 * q) __builtin_amdgcn_s_sleep(1);
  }
}

extern "C" void kernel_launch(void* const* d_in, const int* in_sizes, int n_in, void* d_out, int out_size,
                              void* d_ws, size_t ws_size, hipStream_t stream) {
  (void)hipFuncSetAttribute((const void*)fused_kernel, hipFuncAttributeMaxDynamicSharedMemorySize, LDS_BYTES);
  fused_kernel<<<256, 768, LDS_BYTES, stream>>>((const u16*)d_in[0], (const u16*)d_in[1], (const u16*)d_in[2], (const u16*)d_in[3],
                                                (const u16*)d_in[4], (const u16*)d_in[5], (const u16*)d_in[6], (const u16*)d_in[7],
                                                (const u16*)d_in[8], (const u16*)d_in[9], (const u16*)d_in[10], (const u16*)d_in[11],
                                                (const u16*)d_in[12], (u16*)d_out, d_ws);
  spin_kernel<<<1, 1024, 0, stream>>>((const unsigned long long*)d_ws);
}
```

```cpp
#include <hip/hip_runtime.h>
#include <hip/hip_bf16.h>
#include <stdint.h>
#include <utility>

typedef unsigned int u32;
typedef unsigned short u16;
typedef u32 u32x4 __attribute__((ext_vector_type(4)));
typedef u32 u32x2 __attribute__((ext_vector_type(2)));
typedef float f32x4 __attribute__((ext_vector_type(4)));
typedef float f32x2 __attribute__((ext_vector_type(2)));
typedef __bf16 bf16x8 __attribute__((ext_vector_type(8)));
typedef _Float16 f16x8 __attribute__((ext_vector_type(8)));

#define L_SEQ 2048
#define D_CH 1024
#define N_MODE 128
#define PF_SLEEP 0
#define U_SLEEP 0

__device__ __forceinline__ float bflo(u32 d) { return __uint_as_float(d << 16); }
__device__ __forceinline__ float bfhi(u32 d) { return __uint_as_float(d & 0xffff0000u); }
__device__ __forceinline__ u32 pk_bf16(float lo, float hi) {
  typedef __bf16 bf16x2 __attribute__((ext_vector_type(2)));
  f32x2 v = {lo, hi};
  bf16x2 r = __builtin_convertvector(v, bf16x2);
  return __builtin_bit_cast(u32, r);
}
__device__ __forceinline__ float rbf(float x) { return __uint_as_float(pk_bf16(0.f, x)); }

__device__ __forceinline__ u32 wave_max_u32(u32 x) {
  x = max(x, (u32)__builtin_amdgcn_update_dpp(0, (int)x, 0x111, 0xf, 0xf, true));
  x = max(x, (u32)__builtin_amdgcn_update_dpp(0, (int)x, 0x112, 0xf, 0xf, true));
  x = max(x, (u32)__builtin_amdgcn_update_dpp(0, (int)x, 0x114, 0xf, 0xf, true));
  x = max(x, (u32)__builtin_amdgcn_update_dpp(0, (int)x, 0x118, 0xf, 0xf, true));
  x = max(x, (u32)__builtin_amdgcn_update_dpp(0, (int)x, 0x142, 0xa, 0xf, true));
  x = max(x, (u32)__builtin_amdgcn_update_dpp(0, (int)x, 0x143, 0xc, 0xf, true));
  return (u32)__builtin_amdgcn_readlane((int)x, 63);
}

__device__ __forceinline__ f32x2 cmul(f32x2 g, f32x2 e) {
  f32x2 t, o;
  asm("v_pk_mul_f32 %0, %1, %2 op_sel:[0,0] op_sel_hi:[0,1]" : "=v"(t) : "v"(g), "v"(e));
  asm("v_pk_fma_f32 %0, %1, %2, %3 op_sel:[1,1,0] op_sel_hi:[1,0,1] neg_lo:[1,0,0]" : "=v"(o) : "v"(g), "v"(e), "v"(t));
  return o;
}
__device__ __forceinline__ f32x2 cexp_pos(float ar, float ai, float pos) {
  const float LOG2E = 1.44269504088896340736f;
  const float I2PI_HI = 0.15915494309189533577f;
  const float I2PI_LO = 6.4206383e-9f;
  float xr = ar * pos;
  float xi = ai * pos;
  float e = __builtin_amdgcn_exp2f(xr * LOG2E);
  float r = xi * I2PI_HI;
  float err = __builtin_fmaf(xi, I2PI_HI, -r);
  float f = __builtin_amdgcn_fractf(r);
  f += __builtin_fmaf(xi, I2PI_LO, err);
  f32x2 o;
  o[0] = e * __builtin_amdgcn_cosf(f);
  o[1] = e * __builtin_amdgcn_sinf(f);
  return o;
}
__device__ __forceinline__ u32 pk_f16(float lo, float hi) {
  typedef _Float16 f16x2 __attribute__((ext_vector_type(2)));
  f32x2 v = {lo, hi};
  return __builtin_bit_cast(u32, __builtin_convertvector(v, f16x2));
}

template <bool IS_A>
__device__ __forceinline__ f32x4 mma(u32x4 a, u32x4 b, f32x4 c) {
  if (IS_A) return __builtin_amdgcn_mfma_f32_16x16x32_f16(__builtin_bit_cast(f16x8, a), __builtin_bit_cast(f16x8, b), c, 0, 0, 0);
  else return __builtin_amdgcn_mfma_f32_16x16x32_bf16(__builtin_bit_cast(bf16x8, a), __builtin_bit_cast(bf16x8, b), c, 0, 0, 0);
}

__device__ __forceinline__ u32 lds_off(const char* p) {
  return (u32)(uintptr_t)(const __attribute__((address_space(3))) char*)p;
}

template <class F, int... I>
__device__ __forceinline__ void sfor_impl(F&& f, std::integer_sequence<int, I...>) { (f(std::integral_constant<int, I>{}), ...); }
template <int N, class F>
__device__ __forceinline__ void sfor(F&& f) { sfor_impl(f, std::make_integer_sequence<int, N>{}); }

#define XS_BYTES 4608
#define HC_BYTES 2368
#define HS_BYTES (4 * HC_BYTES)
#define W_BYTES (XS_BYTES + HS_BYTES)
#define YS_OFF (8 * W_BYTES)
#define YS_BYTES 2064
#define RAW_OFF (YS_OFF + 8 * YS_BYTES)
#define FL_OFF (RAW_OFF + 32768)
#define FL_FH 0
#define FL_XQ 32
#define FL_PF 64
#define FL_EX 96
#define LDS_BYTES (FL_OFF + 128)
#define GT_ROW 544

__device__ __forceinline__ void gen_taps(char* tab, int lane, u32 p_ldt, u32 p_lar, u32 p_aim, u32 p_cre, u32 p_cim,
                                         f32x4 (&acc)[3]) {
  const float dt = rbf(expf(bflo(p_ldt)));
  f32x2 C[2], e1[2], f8[2], E[2];
  u32 mc = 0;
#pragma unroll
  for (int m = 0; m < 2; m++) {
    C[m][0] = 2.0f * ((m ? bfhi(p_cre) : bflo(p_cre)) * dt);
    C[m][1] = -2.0f * ((m ? bfhi(p_cim) : bflo(p_cim)) * dt);
    mc = max(mc, max(__float_as_uint(C[m][0]) & 0x7fffffffu, __float_as_uint(C[m][1]) & 0x7fffffffu));
  }
  mc = wave_max_u32(mc);
#pragma unroll
  for (int m = 0; m < 2; m++) {
    const float ar = -rbf(expf(m ? bfhi(p_lar) : bflo(p_lar))) * dt;
    const float ai = (m ? bfhi(p_aim) : bflo(p_aim)) * dt;
    f8[m] = cexp_pos(ar, ai, 8.0f);
    e1[m] = cexp_pos(ar, -ai, 1.0f);
    E[m] = cexp_pos(ar, -ai, 128.0f);
  }
  const int ec = (mc > 0u && mc < 0x7f800000u) ? __builtin_amdgcn_frexp_expf(__uint_as_float(mc)) - 14 : 0;
#pragma unroll
  for (int m = 0; m < 2; m++) {
    C[m][0] = __builtin_ldexpf(C[m][0], -ec);
    C[m][1] = __builtin_ldexpf(C[m][1], -ec);
  }
  char* wp = tab + 8 * lane;
  const int c16 = lane & 15, q = lane >> 4;
  const char* rp = tab + c16 * GT_ROW + 16 * q;
#define PUT_ROW(row, z) *(u32x2*)(wp + (row) * GT_ROW) = (u32x2){pk_f16((z)[0][0], (z)[0][1]), pk_f16((z)[1][0], (z)[1][1])}
#define GET_FRAGS(dst) { __builtin_amdgcn_wave_barrier();                                           \
    _Pragma("unroll") for (int ks = 0; ks < 8; ks++) dst[ks] = *(const u32x4*)(rp + 64 * ks);       \
    __builtin_amdgcn_wave_barrier(); }
#define TILE_MMA(ti) { f32x4 a_ = {0.f, 0.f, 0.f, 0.f};                                             \
    _Pragma("unroll") for (int ks = 0; ks < 8; ks++) a_ = mma<true>(G[ks], Fh[ks], a_);             \
    acc[ti] = a_; }
#define CMUL2(z, e) { z[0] = cmul(z[0], e[0]); z[1] = cmul(z[1], e[1]); }
  {
    f32x2 F[2] = {{16384.0f, 0.f}, {16384.0f, 0.f}};
#pragma unroll
    for (int w = 0; w < 16; w++) {
      PUT_ROW(w, F);
      if (w < 15) CMUL2(F, f8);
    }
  }
  u32x4 Fh[8], G[8];
  GET_FRAGS(Fh);
  f32x2 g[8][2];
  g[0][0] = C[0]; g[0][1] = C[1];
#pragma unroll
  for (int be = 1; be < 8; be++) { g[be][0] = cmul(g[be - 1][0], e1[0]); g[be][1] = cmul(g[be - 1][1], e1[1]); }
#pragma unroll
  for (int be = 0; be < 8; be++) PUT_ROW(be, g[be]);
#pragma unroll
  for (int be = 0; be < 8; be++) { CMUL2(g[be], E); PUT_ROW(8 + be, g[be]); }
  GET_FRAGS(G);
#pragma unroll
  for (int al = 0; al < 2; al++)
#pragma unroll
    for (int be = 0; be < 8; be += 2) { CMUL2(g[be], E); PUT_ROW(4 * al + (be >> 1), g[be]); }
#pragma unroll
  for (int al = 0; al < 4; al++)
#pragma unroll
    for (int be = 0; be < 8; be += 4) { CMUL2(g[be], E); PUT_ROW(8 + 2 * al + (be >> 2), g[be]); }
  TILE_MMA(0);
  GET_FRAGS(G);
#pragma unroll
  for (int al = 0; al < 9; al++) { CMUL2(g[0], E); PUT_ROW(al, g[0]); }
  TILE_MMA(1);
  GET_FRAGS(G);
  TILE_MMA(2);
#pragma unroll
  for (int i = 0; i < 3; i++)
#pragma unroll
    for (int rg = 0; rg < 4; rg++) acc[i][rg] = __builtin_ldexpf(acc[i][rg], ec - 14);
}

__device__ __forceinline__ void dma16(const void* base, u32 off, u32 lds_base) {
  asm volatile("s_mov_b32 m0, %2\n\ts_nop 0\n\tglobal_load_lds_dwordx4 %0, %1" :: "v"(off), "s"(base), "s"(lds_base) : "memory", "m0");
}
__device__ __forceinline__ u32 poll_ge(u32 addr, u32 val) {
  u32 fl;
  for (;;) {
    asm volatile("ds_read_b32 %0, %1\n\ts_waitcnt lgkmcnt(0)" : "=v"(fl) : "v"(addr) : "memory");
    fl = __builtin_amdgcn_readfirstlane(fl);
    if (fl >= val) break;
    __builtin_amdgcn_s_sleep(1);
  }
  return fl;
}
__device__ __forceinline__ void lds_set(u32 addr, u32 val) {
  asm volatile("ds_write_b32 %0, %1" :: "v"(addr), "v"(val) : "memory");
}
__device__ __forceinline__ void lds_inc(u32 addr) {
  asm volatile("s_mov_b64 exec, 1\n\tds_add_u32 %0, %1\n\ts_mov_b64 exec, -1" :: "v"(addr), "v"(1u) : "memory");
}

__device__ __forceinline__ int xcd_group(int b) {
  int xcd = b & 7, idx = b >> 3;
  return ((xcd + 8 * (idx >> 3)) << 3) | (idx & 7);
}

constexpr int p_n(int t) { return t == 0 ? 26 : t == 1 ? 10 : 8; }
constexpr int p_nb(int t) { return t == 0 ? 8 : t == 1 ? 4 : 2; }
constexpr int p_a(int t, int k) { return t == 0 ? (k < 8 ? 0 : k < 15 ? 1 : k < 21 ? 2 : 3) : t == 1 ? (k < 4 ? 0 : k < 7 ? 1 : k < 9 ? 2 : 3) : (k >> 1); }
constexpr int p_e(int t, int k) { return t == 0 ? (k < 8 ? k : k < 15 ? k - 8 : k < 21 ? k - 15 : k - 21) : t == 1 ? (k < 4 ? k : k < 7 ? k - 4 : k < 9 ? k - 7 : 0) : (k & 1); }

template <int OFF> __device__ __forceinline__ void dsr64(u32x2& dst, u32 addr) {
  asm volatile("ds_read_b64 %0, %1 offset:%2" : "=v"(dst) : "v"(addr), "i"(OFF) : "memory");
}
template <int OFF> __device__ __forceinline__ void dsr128(u32x4& dst, u32 addr) {
  asm volatile("ds_read_b128 %0, %1 offset:%2" : "=v"(dst) : "v"(addr), "i"(OFF) : "memory");
}
template <int OFF> __device__ __forceinline__ void read_kv(u32 (&kf)[11], u32 (&vf)[8], u32 rh, u32 ra) {
  asm volatile("ds_read_u16_d16_hi %0, %19\n\tds_read_u16_d16_hi %1, %19 offset:128\n\tds_read_u16_d16_hi %2, %19 offset:256\n\t"
               "ds_read_u16_d16_hi %3, %20 offset:%21\n\tds_read_u16_d16_hi %4, %20 offset:%21+128\n\t"
               "ds_read_u16_d16_hi %5, %20 offset:%21+256\n\tds_read_u16_d16_hi %6, %20 offset:%21+384\n\t"
               "ds_read_u16_d16_hi %7, %20 offset:%21+512\n\tds_read_u16_d16_hi %8, %20 offset:%21+640\n\t"
               "ds_read_u16_d16_hi %9, %20 offset:%21+768\n\tds_read_u16_d16_hi %10, %20 offset:%21+896\n\t"
               "ds_read_u16_d16_hi %11, %20 offset:%21+1024\n\tds_read_u16_d16_hi %12, %20 offset:%21+1152\n\t"
               "ds_read_u16_d16_hi %13, %20 offset:%21+1280\n\tds_read_u16_d16_hi %14, %20 offset:%21+1408\n\t"
               "ds_read_u16_d16_hi %15, %20 offset:%21+1536\n\tds_read_u16_d16_hi %16, %20 offset:%21+1664\n\t"
               "ds_read_u16_d16_hi %17, %20 offset:%21+1792\n\tds_read_u16_d16_hi %18, %20 offset:%21+1920\n\t"
               "s_waitcnt lgkmcnt(0)"
               : "+v"(kf[0]), "+v"(kf[1]), "+v"(kf[2]), "+v"(kf[3]), "+v"(kf[4]), "+v"(kf[5]), "+v"(kf[6]), "+v"(kf[7]),
                 "+v"(kf[8]), "+v"(kf[9]), "+v"(kf[10]), "+v"(vf[0]), "+v"(vf[1]), "+v"(vf[2]), "+v"(vf[3]), "+v"(vf[4]),
                 "+v"(vf[5]), "+v"(vf[6]), "+v"(vf[7])
               : "v"(rh), "v"(ra), "i"(OFF) : "memory");
}
__device__ __forceinline__ u32x4 kv_chunk(const u32 (&kf)[11], const u32 (&vf)[8], float w0, float w1, float w2, float w3) {
  u32 o0, o1, o2, o3, a[8], b[8];
  asm(
      "v_mul_f32 %4, %20, %42\n\tv_mul_f32 %5, %21, %42\n\tv_mul_f32 %6, %22, %42\n\tv_mul_f32 %7, %23, %42\n\t"
      "v_mul_f32 %8, %24, %42\n\tv_mul_f32 %9, %25, %42\n\tv_mul_f32 %10, %26, %42\n\tv_mul_f32 %11, %27, %42\n\t"
      "v_mul_f32 %12, %21, %41\n\tv_mul_f32 %13, %22, %41\n\tv_mul_f32 %14, %23, %41\n\tv_mul_f32 %15, %24, %41\n\t"
      "v_mul_f32 %16, %25, %41\n\tv_mul_f32 %17, %26, %41\n\tv_mul_f32 %18, %27, %41\n\tv_mul_f32 %19, %28, %41\n\t"
      "v_cvt_pk_bf16_f32 %4, 0, %4\n\tv_cvt_pk_bf16_f32 %5, 0, %5\n\tv_cvt_pk_bf16_f32 %6, 0, %6\n\tv_cvt_pk_bf16_f32 %7, 0, %7\n\t"
      "v_cvt_pk_bf16_f32 %8, 0, %8\n\tv_cvt_pk_bf16_f32 %9, 0, %9\n\tv_cvt_pk_bf16_f32 %10, 0, %10\n\tv_cvt_pk_bf16_f32 %11, 0, %11\n\t"
      "v_cvt_pk_bf16_f32 %12, 0, %12\n\tv_cvt_pk_bf16_f32 %13, 0, %13\n\tv_cvt_pk_bf16_f32 %14, 0, %14\n\tv_cvt_pk_bf16_f32 %15, 0, %15\n\t"
      "v_cvt_pk_bf16_f32 %16, 0, %16\n\tv_cvt_pk_bf16_f32 %17, 0, %17\n\tv_cvt_pk_bf16_f32 %18, 0, %18\n\tv_cvt_pk_bf16_f32 %19, 0, %19\n\t"
      "v_add_f32 %4, %4, %12\n\tv_add_f32 %5, %5, %13\n\tv_add_f32 %6, %6, %14\n\tv_add_f32 %7, %7, %15\n\t"
      "v_add_f32 %8, %8, %16\n\tv_add_f32 %9, %9, %17\n\tv_add_f32 %10, %10, %18\n\tv_add_f32 %11, %11, %19\n\t"
      "v_mul_f32 %12, %22, %40\n\tv_mul_f32 %13, %23, %40\n\tv_mul_f32 %14, %24, %40\n\tv_mul_f32 %15, %25, %40\n\t"
      "v_mul_f32 %16, %26, %40\n\tv_mul_f32 %17, %27, %40\n\tv_mul_f32 %18, %28, %40\n\tv_mul_f32 %19, %29, %40\n\t"
      "v_cvt_pk_bf16_f32 %4, 0, %4\n\tv_cvt_pk_bf16_f32 %5, 0, %5\n\tv_cvt_pk_bf16_f32 %6, 0, %6\n\tv_cvt_pk_bf16_f32 %7, 0, %7\n\t"
      "v_cvt_pk_bf16_f32 %8, 0, %8\n\tv_cvt_pk_bf16_f32 %9, 0, %9\n\tv_cvt_pk_bf16_f32 %10, 0, %10\n\tv_cvt_pk_bf16_f32 %11, 0, %11\n\t"
      "v_cvt_pk_bf16_f32 %12, 0, %12\n\tv_cvt_pk_bf16_f32 %13, 0, %13\n\tv_cvt_pk_bf16_f32 %14, 0, %14\n\tv_cvt_pk_bf16_f32 %15, 0, %15\n\t"
      "v_cvt_pk_bf16_f32 %16, 0, %16\n\tv_cvt_pk_bf16_f32 %17, 0, %17\n\tv_cvt_pk_bf16_f32 %18, 0, %18\n\tv_cvt_pk_bf16_f32 %19, 0, %19\n\t"
      "v_add_f32 %4, %4, %12\n\tv_add_f32 %5, %5, %13\n\tv_add_f32 %6, %6, %14\n\tv_add_f32 %7, %7, %15\n\t"
      "v_add_f32 %8, %8, %16\n\tv_add_f32 %9, %9, %17\n\tv_add_f32 %10, %10, %18\n\tv_add_f32 %11, %11, %19\n\t"
      "v_mul_f32 %12, %23, %39\n\tv_mul_f32 %13, %24, %39\n\tv_mul_f32 %14, %25, %39\n\tv_mul_f32 %15, %26, %39\n\t"
      "v_mul_f32 %16, %27, %39\n\tv_mul_f32 %17, %28, %39\n\tv_mul_f32 %18, %29, %39\n\tv_mul_f32 %19, %30, %39\n\t"
      "v_cvt_pk_bf16_f32 %4, 0, %4\n\tv_cvt_pk_bf16_f32 %5, 0, %5\n\tv_cvt_pk_bf16_f32 %6, 0, %6\n\tv_cvt_pk_bf16_f32 %7, 0, %7\n\t"
      "v_cvt_pk_bf16_f32 %8, 0, %8\n\tv_cvt_pk_bf16_f32 %9, 0, %9\n\tv_cvt_pk_bf16_f32 %10, 0, %10\n\tv_cvt_pk_bf16_f32 %11, 0, %11\n\t"
      "v_cvt_pk_bf16_f32 %12, 0, %12\n\tv_cvt_pk_bf16_f32 %13, 0, %13\n\tv_cvt_pk_bf16_f32 %14, 0, %14\n\tv_cvt_pk_bf16_f32 %15, 0, %15\n\t"
      "v_cvt_pk_bf16_f32 %16, 0, %16\n\tv_cvt_pk_bf16_f32 %17, 0, %17\n\tv_cvt_pk_bf16_f32 %18, 0, %18\n\tv_cvt_pk_bf16_f32 %19, 0, %19\n\t"
      "v_add_f32 %4, %4, %12\n\tv_add_f32 %5, %5, %13\n\tv_add_f32 %6, %6, %14\n\tv_add_f32 %7, %7, %15\n\t"
      "v_add_f32 %8, %8, %16\n\tv_add_f32 %9, %9, %17\n\tv_add_f32 %10, %10, %18\n\tv_add_f32 %11, %11, %19\n\t"
      "v_cvt_pk_bf16_f32 %4, 0, %4\n\tv_cvt_pk_bf16_f32 %5, 0, %5\n\tv_cvt_pk_bf16_f32 %6, 0, %6\n\tv_cvt_pk_bf16_f32 %7, 0, %7\n\t"
      "v_cvt_pk_bf16_f32 %8, 0, %8\n\tv_cvt_pk_bf16_f32 %9, 0, %9\n\tv_cvt_pk_bf16_f32 %10, 0, %10\n\tv_cvt_pk_bf16_f32 %11, 0, %11\n\t"
      "v_mul_f32 %4, %4, %31\n\tv_mul_f32 %5, %5, %32\n\tv_mul_f32 %6, %6, %33\n\tv_mul_f32 %7, %7, %34\n\t"
      "v_mul_f32 %8, %8, %35\n\tv_mul_f32 %9, %9, %36\n\tv_mul_f32 %10, %10, %37\n\tv_mul_f32 %11, %11, %38\n\t"
      "v_cvt_pk_bf16_f32 %0, %11, %10\n\tv_cvt_pk_bf16_f32 %1, %9, %8\n\tv_cvt_pk_bf16_f32 %2, %7, %6\n\tv_cvt_pk_bf16_f32 %3, %5, %4"
      : "=&v"(o0), "=&v"(o1), "=&v"(o2), "=&v"(o3),
        "=&v"(a[0]), "=&v"(a[1]), "=&v"(a[2]), "=&v"(a[3]), "=&v"(a[4]), "=&v"(a[5]), "=&v"(a[6]), "=&v"(a[7]),
        "=&v"(b[0]), "=&v"(b[1]), "=&v"(b[2]), "=&v"(b[3]), "=&v"(b[4]), "=&v"(b[5]), "=&v"(b[6]), "=&v"(b[7])
      : "v"(kf[0]), "v"(kf[1]), "v"(kf[2]), "v"(kf[3]), "v"(kf[4]), "v"(kf[5]), "v"(kf[6]), "v"(kf[7]), "v"(kf[8]), "v"(kf[9]), "v"(kf[10]),
        "v"(vf[0]), "v"(vf[1]), "v"(vf[2]), "v"(vf[3]), "v"(vf[4]), "v"(vf[5]), "v"(vf[6]), "v"(vf[7]),
        "v"(w0), "v"(w1), "v"(w2), "v"(w3));
  return (u32x4){o0, o1, o2, o3};
}
template <int OFF> __device__ __forceinline__ void read_u(u32 (&lo)[4], u32 (&hi)[4], u32 ra) {
  asm volatile("ds_read_u16 %0, %8 offset:%9+896\n\tds_read_u16_d16_hi %4, %8 offset:%9+768\n\t"
               "ds_read_u16 %1, %8 offset:%9+640\n\tds_read_u16_d16_hi %5, %8 offset:%9+512\n\t"
               "ds_read_u16 %2, %8 offset:%9+384\n\tds_read_u16_d16_hi %6, %8 offset:%9+256\n\t"
               "ds_read_u16 %3, %8 offset:%9+128\n\tds_read_u16_d16_hi %7, %8 offset:%9\n\t"
               "s_waitcnt lgkmcnt(0)"
               : "=&v"(lo[0]), "=&v"(lo[1]), "=&v"(lo[2]), "=&v"(lo[3]), "+v"(hi[0]), "+v"(hi[1]), "+v"(hi[2]), "+v"(hi[3])
               : "v"(ra), "i"(OFF) : "memory");
}
template <int OFF> __device__ __forceinline__ void dsw128(u32 addr, u32x4 val) {
  asm volatile("ds_write_b128 %0, %1 offset:%2" :: "v"(addr), "v"(val), "i"(OFF) : "memory");
}
__device__ __forceinline__ void dsr32(u32& dst, u32 addr) {
  asm volatile("ds_read_b32 %0, %1" : "=v"(dst) : "v"(addr) : "memory");
}
__device__ __forceinline__ void line_req(u32& dst, const void* base, u32 off) {
  asm volatile("global_load_dword %0, %1, %2 nt" : "=v"(dst) : "v"(off), "s"(base) : "memory");
}
template <int N> __device__ __forceinline__ void wait_vm() { asm volatile("s_waitcnt vmcnt(%0)" :: "i"(N) : "memory"); }

template <int NB>
__device__ __forceinline__ void wait_frags(u32x2 (&AL)[4], u32x2 (&AH)[4], u32x4 (&B)[8]) {
  __builtin_amdgcn_sched_barrier(0);
  if (NB == 8)
    asm volatile("s_waitcnt lgkmcnt(0)"
                 : "+v"(AL[0]), "+v"(AL[1]), "+v"(AL[2]), "+v"(AL[3]), "+v"(AH[0]), "+v"(AH[1]), "+v"(AH[2]), "+v"(AH[3]),
                   "+v"(B[0]), "+v"(B[1]), "+v"(B[2]), "+v"(B[3]), "+v"(B[4]), "+v"(B[5]), "+v"(B[6]), "+v"(B[7]) :: "memory");
  else if (NB == 4)
    asm volatile("s_waitcnt lgkmcnt(0)"
                 : "+v"(AL[0]), "+v"(AL[1]), "+v"(AL[2]), "+v"(AL[3]), "+v"(AH[0]), "+v"(AH[1]), "+v"(AH[2]), "+v"(AH[3]),
                   "+v"(B[0]), "+v"(B[1]), "+v"(B[2]), "+v"(B[3]) :: "memory");
  else
    asm volatile("s_waitcnt lgkmcnt(0)"
                 : "+v"(AL[0]), "+v"(AL[1]), "+v"(AL[2]), "+v"(AL[3]), "+v"(AH[0]), "+v"(AH[1]), "+v"(AH[2]), "+v"(AH[3]),
                   "+v"(B[0]), "+v"(B[1]) :: "memory");
  __builtin_amdgcn_sched_barrier(0);
}

template <bool IS_A, int NR>
__device__ __forceinline__ void epi2(const char* ysb, int ystride, int tt, int row0, int g, const u32x4* xv, u32x4 dv,
                                     __amdgpu_buffer_rsrc_t ro) {
  u32 yv[8][NR / 2];
#pragma unroll
  for (int c = 0; c < 8; c++) {
    if (NR == 2) yv[c][0] = *(const u32*)(ysb + c * ystride + 4 * tt);
    else { u32x2 y2 = *(const u32x2*)(ysb + c * ystride + 8 * tt); yv[c][0] = y2[0]; yv[c][NR / 2 - 1] = y2[1]; }
  }
#pragma unroll
  for (int i = 0; i < NR; i++) {
    u32x4 o;
#pragma unroll
    for (int p = 0; p < 4; p++) {
      const u32 ya_ = yv[2 * p][i >> 1], yb_ = yv[2 * p + 1][i >> 1];
      const float ya = (i & 1) ? bfhi(ya_) : bflo(ya_), yb = (i & 1) ? bfhi(yb_) : bflo(yb_);
      if (IS_A) o[p] = pk_bf16(ya + rbf(bflo(xv[i][p]) * bflo(dv[p])), yb + rbf(bfhi(xv[i][p]) * bfhi(dv[p])));
      else o[p] = pk_bf16(ya * bflo(xv[i][p]), yb * bfhi(xv[i][p]));
    }
    __builtin_amdgcn_raw_buffer_store_b128(o, ro, ((row0 + NR * tt + i) * (2 * D_CH) + g * 8) * 2, 0, 0);
  }
}
__device__ __forceinline__ int raw_pos(int r) { return (r & ~63) * 16 + (r & 7) * 128 + ((r >> 3) & 7) * 16; }

template <bool IS_A>
__device__ __forceinline__ void main_body( char* smem, int g, int w, u32 pf,
                                          const u16* __restrict__ lar, const u16* __restrict__ aim,
                                          const u16* __restrict__ cre, const u16* __restrict__ cim, const u16* __restrict__ ldt,
                                          const u16* __restrict__ kin, const u16* __restrict__ vin,
                                          const u16* __restrict__ ssmk, const u16* __restrict__ dvec,
                                          const u16* __restrict__ qin, u16* __restrict__ out) {
  const int t = threadIdx.x, lane = t & 63;
  const int gc = (g & 127) * 8;
  const int ch = gc + w;
  char* xs = smem + w * W_BYTES;
  char* hs = xs + XS_BYTES;
  const u32 fl0 = lds_off(smem + FL_OFF);
  const int hl = lane < 48 ? lane : lane - 48;
  int hc[3];
#pragma unroll
  for (int j = 0; j < 3; j++) {
    const int c = 48 * j + hl, sg = (c * 57) >> 10;
    hc[j] = 32 * (sg & 3) + 16 * (sg >> 2) + c - 18 * sg;
  }
  u32x4 hd[6];
  u32x2 he[6];
  int esc = 0;
  const __amdgpu_buffer_rsrc_t rh = __builtin_amdgcn_make_buffer_rsrc((void*)(ssmk + (size_t)ch * L_SEQ), 0, L_SEQ * 2, 0x00020000);
  u32x4 dv;
  u32 p_ldt = 0, p_lar = 0, p_aim = 0, p_cre = 0, p_cim = 0, pfx = 0;
  if (IS_A) {
    const int po = (ch * N_MODE + 2 * lane) * 2;
    asm volatile("global_load_ushort %0, %5, %6\n\tglobal_load_dword %1, %7, %8 nt\n\t"
                 "global_load_dword %2, %7, %9 nt\n\tglobal_load_dword %3, %7, %10 nt\n\tglobal_load_dword %4, %7, %11 nt"
                 : "=&v"(p_ldt), "=&v"(p_lar), "=&v"(p_aim), "=&v"(p_cre), "=&v"(p_cim)
                 : "v"(ch * 2), "s"(ldt), "v"(po), "s"(lar), "s"(aim), "s"(cre), "s"(cim) : "memory");
    asm volatile("global_load_dwordx4 %0, %1, %2" : "=v"(dv) : "v"(gc * 2), "s"(dvec) : "memory");
  } else {
    const char* pa = (const char*)(lane < 32 ? kin : vin) + (size_t)((256 * w + (g & 7) * 32 + (lane & 31)) * (D_CH * 2) + (gc & ~63) * 2);
    for (int i = 0; i < w; i++) __builtin_amdgcn_s_sleep(PF_SLEEP);
    asm volatile("global_load_dword %0, %1, off nt" : "=v"(pfx) : "v"(pa) : "memory");
    __builtin_amdgcn_sched_barrier(0);
#pragma unroll
    for (int s = 0; s < 3; s++) {
      int o1 = 16 * (hc[s] - 2), o2 = o1 + 16;
      if (s == 0) { o1 = o1 >= 0 ? o1 : 0x40000000; o2 = o2 >= 0 ? o2 : 0x40000000; }
      hd[s] = __builtin_amdgcn_raw_buffer_load_b128(rh, o1, 0, 0);
      he[s] = __builtin_amdgcn_raw_buffer_load_b64(rh, o2, 0, 0);
    }
    __builtin_amdgcn_sched_barrier(0);
    dv = *(const u32x4*)(dvec + gc);
    __builtin_amdgcn_sched_barrier(0);
  }
  if (lane < 30) *(u32x4*)(xs + 16 * lane) = (u32x4){0, 0, 0, 0};
  __builtin_amdgcn_sched_barrier(0);
  if (IS_A) {
    asm volatile("s_waitcnt vmcnt(0)" : "+v"(p_ldt), "+v"(p_lar), "+v"(p_aim), "+v"(p_cre), "+v"(p_cim), "+v"(dv) : "v"(pf) : "memory");
    __builtin_amdgcn_sched_barrier(0);
    f32x4 ka[3];
    gen_taps(hs, lane, p_ldt, p_lar, p_aim, p_cre, p_cim, ka);
    char* kb = hs;
    char* lin = hs + 1536;
    u16* kc = (u16*)kb;
    {
      const int c16 = lane & 15, q = lane >> 4;
      const bool v2 = q < 2, v28 = (q == 2) && (c16 == 0);
      u32 mk = 0;
#pragma unroll
      for (int rg = 0; rg < 4; rg++) {
        mk = max(mk, max(__float_as_uint(ka[0][rg]) & 0x7fffffffu, __float_as_uint(ka[1][rg]) & 0x7fffffffu));
        u32 m2 = __float_as_uint(ka[2][rg]) & 0x7fffffffu;
        mk = max(mk, (v2 || (rg == 0 && v28)) ? m2 : 0u);
      }
      mk = wave_max_u32(mk);
      esc = (mk > 0u && mk < 0x7f800000u) ? __builtin_amdgcn_frexp_expf(__uint_as_float(mk)) - 15 : 0;
#pragma unroll
      for (int rg = 0; rg < 4; rg++) {
        const int row = 4 * q + rg;
        kc[128 * (row >> 3) + (row & 7) + 8 * c16] = __builtin_bit_cast(u16, (_Float16)__builtin_ldexpf(ka[0][rg], -esc));
        int i1 = (row < 8) ? 256 + 64 * (row >> 2) + (row & 3) + 4 * c16 : 384 + 32 * ((row - 8) >> 1) + (row & 1) + 2 * c16;
        kc[i1] = __builtin_bit_cast(u16, (_Float16)__builtin_ldexpf(ka[1][rg], -esc));
        kc[512 + 16 * row + c16] = __builtin_bit_cast(u16, (_Float16)__builtin_ldexpf(ka[2][rg], -esc));
      }
    }
    __builtin_amdgcn_wave_barrier();
    const bool dir = lane < 32;
    const int a0 = dir ? 16 * lane : 512 + 8 * (lane - 32);
    u32x2 d01 = *(const u32x2*)(kb + a0), d23 = *(const u32x2*)(kb + a0 + 8);
    u32x4 o;
    o[0] = __builtin_amdgcn_perm(d01[0], d01[0], dir ? 0x03020100u : 0x01000100u);
    o[1] = __builtin_amdgcn_perm(d01[1], d01[0], dir ? 0x07060504u : 0x05040302u);
    o[2] = __builtin_amdgcn_perm(d23[0], d01[1], dir ? 0x07060504u : 0x01000100u);
    o[3] = dir ? d23[1] : __builtin_amdgcn_perm(d23[0], d01[1], 0x05040302u);
    *(u32x4*)(lin + 32 + 16 * lane) = o;
    u32 d0 = *(const u32*)(kb + 768 + 4 * lane), d1 = *(const u32*)(kb + 772 + 4 * lane);
    o[0] = __builtin_amdgcn_perm(d0, d0, 0x01000100u);
    o[1] = d0;
    o[2] = __builtin_amdgcn_perm(d0, d0, 0x03020302u);
    o[3] = __builtin_amdgcn_perm(d1, d1, 0x01000100u);
    *(u32x4*)(lin + 32 + 1024 + 16 * lane) = o;
#pragma unroll
    for (int i = 0; i < 2; i++) {
      u32 x0 = kc[512 + 64 * i + lane], x1 = kc[513 + 64 * i + lane];
      o[0] = o[1] = x0 | (x0 << 16);
      o[3] = x1 | (x1 << 16);
      o[2] = (lane & 1) ? o[3] : (x0 | (x1 << 16));
      *(u32x4*)(lin + 32 + 2048 + 1024 * i + 16 * lane) = o;
    }
    if (lane < 4) *(u32x4*)(lin + ((lane & 2) ? 4096 : 0) + 16 * lane) = (u32x4){0, 0, 0, 0};
    __builtin_amdgcn_wave_barrier();
#pragma unroll
    for (int s = 0; s < 6; s++) {
      hd[s] = *(const u32x4*)(lin + 16 * hc[s % 3] + 2048 * (s / 3));
      he[s] = *(const u32x2*)(lin + 16 * hc[s % 3] + 2048 * (s / 3) + 16);
    }
    __builtin_amdgcn_wave_barrier();
  } else {
    asm volatile("s_waitcnt vmcnt(7)\n\tds_write_b32 %2, %3" :: "v"(pfx), "v"(pf), "v"(fl0 + FL_PF + 4 * w), "v"(1u) : "memory");
    __builtin_amdgcn_sched_barrier(0);
  }
  char* hw = hs + 16 * hl;
#define STAGE_PIECE(set, k) {                                                          \
    const u32x4 d4 = hd[3 * (set) + ((k) >> 2)];                                       \
    const u32x2 e2 = he[3 * (set) + ((k) >> 2)];                                       \
    u32x4 o_;                                                                          \
    if (((k) & 3) == 0) o_ = d4;                                                       \
    else if (((k) & 3) == 1) o_ = (u32x4){__builtin_amdgcn_alignbit(d4[1], d4[0], 16), __builtin_amdgcn_alignbit(d4[2], d4[1], 16), \
                                          __builtin_amdgcn_alignbit(d4[3], d4[2], 16), __builtin_amdgcn_alignbit(e2[0], d4[3], 16)}; \
    else if (((k) & 3) == 2) o_ = (u32x4){d4[1], d4[2], d4[3], e2[0]};                 \
    else o_ = (u32x4){__builtin_amdgcn_alignbit(d4[2], d4[1], 16), __builtin_amdgcn_alignbit(d4[3], d4[2], 16), \
                      __builtin_amdgcn_alignbit(e2[0], d4[3], 16), __builtin_amdgcn_alignbit(e2[1], e2[0], 16)}; \
    *(u32x4*)(hw + 768 * ((k) >> 2) + ((k) & 3) * HC_BYTES) = o_; }
#pragma unroll
  for (int k = 0; k < 12; k++) STAGE_PIECE(0, k);
  const int r = lane & 15, q = lane >> 4;
  f32x4 acc[8];
#pragma unroll
  for (int i = 0; i < 8; i++) acc[i] = (f32x4){0.f, 0.f, 0.f, 0.f};
  const int lam = 25 + r - 8 * q;
  const u32 abase = lds_off(hs) + (lam & 3) * HC_BYTES + 2 * (lam & ~3);
  const u32 bbase = lds_off(xs) + 32 * r + 16 * q;
  u32x2 AL[2][4], AH[2][4];
  u32x4 B[2][8];
#define LOAD_PIECE(it, k, e0, bf) {                                                             \
    if constexpr ((k) < 8 && ((k) & 1) == 0) dsr64<64 * ((it) & 3) + 288 * (((k) >> 1) + 4 * ((it) >> 2))>(AL[bf][((k) >> 1) & 3], abase);   \
    else if constexpr ((k) < 8) dsr64<64 * ((it) & 3) + 288 * (((k) >> 1) + 4 * ((it) >> 2)) + 8>(AH[bf][((k) >> 1) & 3], abase); \
    else dsr128<448 + 512 * ((e0) + (k) - 8) - 64 * (it)>(B[bf][((k) - 8) & 7], bbase); }
  __builtin_amdgcn_sched_barrier(0);
  if (IS_A) {
    asm volatile("s_barrier" ::: "memory");
    esc += (int)poll_ge(fl0 + FL_EX + 4 * w, 1u) - 1024;
  }
  else poll_ge(fl0 + FL_XQ, 4u);
  __builtin_amdgcn_sched_barrier(0);
  sfor<8 + p_nb(IS_A ? 0 : 2)>([&](auto kc_) { constexpr int k = decltype(kc_)::value; LOAD_PIECE(0, k, 0, 0); });
  __builtin_amdgcn_sched_barrier(0);
#define PIN_ACC asm volatile("" : "+v"(acc[0]), "+v"(acc[1]), "+v"(acc[2]), "+v"(acc[3]), "+v"(acc[4]), "+v"(acc[5]), "+v"(acc[6]), "+v"(acc[7]));
#define PASS(pt, e0, t0, nt, ne0, restage, nq)                                          \
  sfor<8>([&](auto ic_) {                                                               \
    constexpr int it = decltype(ic_)::value, bf = it & 1;                               \
    wait_frags<p_nb(pt)>(AL[bf], AH[bf], B[bf]);                                        \
    u32 fl_;                                                                            \
    sfor<p_n(pt)>([&](auto kc_) {                                                       \
      constexpr int k_ = decltype(kc_)::value, a = p_a(pt, k_), e = p_e(pt, k_);        \
      acc[(t0) + a + e] = mma<IS_A>((u32x4){AL[bf][a][0], AL[bf][a][1], AH[bf][a][0], AH[bf][a][1]}, B[bf][e], acc[(t0) + a + e]); \
      if constexpr (it < 7) {                                                           \
        if constexpr ((pt) == 0) { if constexpr (k_ < 16) LOAD_PIECE(it + 1, k_, e0, bf ^ 1); } \
        else if constexpr (2 * k_ < 8 + p_nb(pt)) { LOAD_PIECE(it + 1, 2 * k_, e0, bf ^ 1); LOAD_PIECE(it + 1, 2 * k_ + 1, e0, bf ^ 1); } \
      } else if constexpr ((nt) >= 0) {                                                 \
        if constexpr (restage) {       \
          if constexpr ((pt) == 0) {                                                    \
            if constexpr (k_ < 12) STAGE_PIECE(1, k_)                                   \
            else if constexpr (k_ < 24) LOAD_PIECE(0, k_ - 12, ne0, bf ^ 1);            \
          } else {                                                                      \
            if constexpr (k_ < 6) { STAGE_PIECE(1, 2 * k_); STAGE_PIECE(1, 2 * k_ + 1); } \
            else { LOAD_PIECE(0, 3 * (k_ - 6), ne0, bf ^ 1); LOAD_PIECE(0, 3 * (k_ - 6) + 1, ne0, bf ^ 1); LOAD_PIECE(0, 3 * (k_ - 6) + 2, ne0, bf ^ 1); } \
          }                                                                             \
        } else {                                                                        \
          if constexpr (k_ == 0) dsr32(fl_, fl0 + FL_XQ + 4 * (nq)); \
          if constexpr (k_ < 4) { LOAD_PIECE(0, 2 * k_, ne0, bf ^ 1); LOAD_PIECE(0, 2 * k_ + 1, ne0, bf ^ 1); } \
          if constexpr (k_ == 5) {                                                      \
            asm volatile("s_waitcnt lgkmcnt(8)" : "+v"(fl_) :: "memory");               \
            if (__builtin_amdgcn_readfirstlane(fl_) < 4u) poll_ge(fl0 + FL_XQ + 4 * (nq), 4u); \
          }                                                                             \
          if constexpr (k_ >= 5 && 2 * (k_ - 5) < p_nb(nt)) { LOAD_PIECE(0, 8 + 2 * (k_ - 5), ne0, bf ^ 1); LOAD_PIECE(0, 9 + 2 * (k_ - 5), ne0, bf ^ 1); } \
        }                                                                               \
      }                                                                                 \
      __builtin_amdgcn_sched_barrier(0);                                                \
    });                                                                                 \
    PIN_ACC                                                                             \
  });
  const float dsk = (w & 1) ? bfhi(dv[w >> 1]) : bflo(dv[w >> 1]);
#define EPI1(c0_, ysp)                                                                  \
  {                                                                                     \
    u32x2 uv[4];                                                                        \
    if (!IS_A) {                                                                        \
      _Pragma("unroll") for (int ci = 0; ci < 4; ci++)                                  \
        uv[ci] = *(const u32x2*)(xs + 480 + 512 * ((c0_) + ci) + 32 * r + 16 * (q >> 1) + 8 - 8 * (q & 1)); \
      __builtin_amdgcn_sched_barrier(0);                                                \
    }                                                                                   \
    _Pragma("unroll") for (int ci = 0; ci < 4; ci++) {                                  \
      const int l = 256 * ci + 16 * r + 4 * q;                                          \
      const float y0 = acc[(c0_) + ci][0], y1 = acc[(c0_) + ci][1], y2 = acc[(c0_) + ci][2], y3 = acc[(c0_) + ci][3]; \
      u32x2 o;                                                                          \
      if (IS_A) {                                                                       \
        o[0] = pk_bf16(__builtin_ldexpf(y0, esc), __builtin_ldexpf(y1, esc));           \
        o[1] = pk_bf16(__builtin_ldexpf(y2, esc), __builtin_ldexpf(y3, esc));           \
      } else {                                                                          \
        const float x3 = bflo(uv[ci][0]), x2 = bfhi(uv[ci][0]), x1 = bflo(uv[ci][1]), x0 = bfhi(uv[ci][1]); \
        o[0] = pk_bf16(rbf(y0) + rbf(x0 * dsk), rbf(y1) + rbf(x1 * dsk));               \
        o[1] = pk_bf16(rbf(y2) + rbf(x2 * dsk), rbf(y3) + rbf(x3 * dsk));               \
      }                                                                                 \
      *(u32x2*)((ysp) + 2 * l) = o;                                                     \
    }                                                                                   \
  }
  u32x4 xv[2];
  if (IS_A) {
    PASS(0, 0, 0, 1, 0, true, 0);
    EPI1(0, smem + YS_OFF + w * YS_BYTES);
    lds_set(fl0 + FL_FH + 4 * w, 1u);
    PASS(1, 0, 4, -1, 0, false, 0);
  } else {
    PASS(2, 0, 0, 2, 2, false, 1);
    PASS(2, 2, 2, 1, 4, false, 3);
    EPI1(0, smem + YS_OFF + w * YS_BYTES);
    lds_set(fl0 + FL_FH + 4 * w, 1u);
#pragma unroll
    for (int s = 3; s < 6; s++) {
      const int o1 = 16 * (hc[s - 3] - 2) + 2048;
      hd[s] = __builtin_amdgcn_raw_buffer_load_b128(rh, o1, 0, 0);
      he[s] = __builtin_amdgcn_raw_buffer_load_b64(rh, o1 + 16, 0, 0);
    }
    const __amdgpu_buffer_rsrc_t rq = __builtin_amdgcn_make_buffer_rsrc((void*)qin, 0, L_SEQ * D_CH * 2, 0x00020000);
#pragma unroll
    for (int i = 0; i < 2; i++) xv[i] = __builtin_amdgcn_raw_buffer_load_b128(rq, ((1024 + 2 * t + i) * D_CH + gc) * 2, 0, 0);
    __builtin_amdgcn_sched_barrier(0);
    PASS(1, 4, 4, 1, 0, true, 0);
    PASS(1, 0, 4, -1, 0, false, 0);
  }
  EPI1(4, hs);
  if (IS_A) {
#pragma unroll
    for (int i = 0; i < 2; i++) xv[i] = *(const u32x4*)(smem + RAW_OFF + raw_pos(1024 + 2 * t + i));
  }
  asm volatile("s_waitcnt lgkmcnt(0)\n\ts_barrier" ::: "memory");
  const __amdgpu_buffer_rsrc_t ro = __builtin_amdgcn_make_buffer_rsrc((void*)out, 0, 8 * 1024 * 1024, 0x00020000);
  epi2<IS_A, 2>(smem + XS_BYTES, W_BYTES, t, 1024, g, xv, dv, ro);
}

template <bool IS_A>
__device__ __forceinline__ void stream_body( char* smem, int g, int p, u32 pf, const u16* __restrict__ u,
                                            const u16* __restrict__ kin, const u16* __restrict__ vin,
                                            const u16* __restrict__ shk, const u16* __restrict__ dvec,
                                            const u16* __restrict__ qin, u16* __restrict__ out) {
  const int lane = threadIdx.x & 63;
  const int gc = (g & 127) * 8;
  const u32 sb = lds_off(smem);
  const u32 fl0 = sb + FL_OFF;
  const int c = lane >> 3, rg = lane & 7;
  u32x4 dv;
  const u32 go = (64 * p + 8 * rg + c) * (D_CH * 2) + gc * 2;
  const u32 po = ((g & 7) * 256 + 64 * p + lane) * (D_CH * 2) + (gc & ~63) * 2;
  const u32 xw = sb + c * W_BYTES + 480 + 128 * p + 16 * rg;
  u32 pfx = 0;
  u32x4 xv[4];
  __builtin_amdgcn_s_setprio(3);
  if (IS_A) {
    __builtin_amdgcn_s_sleep(U_SLEEP);
    line_req(pfx, u, po);
    asm volatile("global_load_dwordx4 %0, %1, %2" : "=v"(dv) : "v"(gc * 2), "s"(dvec) : "memory");
    asm volatile("s_waitcnt vmcnt(0)" : "+v"(dv) : "v"(pfx), "v"(pf) : "memory");
    const u32 rb = sb + RAW_OFF + 1024 * p;
#pragma unroll
    for (int e = 0; e < 8; e++) dma16(u + (size_t)e * 256 * D_CH, go, rb + 4096 * e);
    const u32 ra = rb + 16 * rg + 2 * c;
    u32 hi[4] = {0, 0, 0, 0};
    sfor<8>([&](auto ec_) {
      constexpr int e = decltype(ec_)::value;
      wait_vm<7 - e>();
      u32 lo[4];
      read_u<4096 * e>(lo, hi, ra);
      const u32x4 o = {lo[0] | hi[0], lo[1] | hi[1], lo[2] | hi[2], lo[3] | hi[3]};
      dsw128<512 * e>(xw, o);
    });
    lds_inc(fl0 + FL_XQ);
    poll_ge(fl0 + FL_XQ, 4u);
#pragma unroll
    for (int cc = 0; cc < 2; cc++) {
      char* xp = smem + (2 * p + cc) * W_BYTES + 480 + 16 * lane;
      u32x4 xr[4];
#pragma unroll
      for (int j = 0; j < 4; j++) xr[j] = *(const u32x4*)(xp + 1024 * j);
      u32 mx = 0;
#pragma unroll
      for (int j = 0; j < 4; j++)
#pragma unroll
        for (int i = 0; i < 4; i++) {
          u32 dd = xr[j][i];
          mx = max(mx, max(dd & 0x7fffu, (dd >> 16) & 0x7fffu));
        }
      mx = wave_max_u32(mx);
      const int ex = (int)(mx >> 7) - 127 - 14;
#pragma unroll
      for (int j = 0; j < 4; j++) {
        u32x4 o;
#pragma unroll
        for (int i = 0; i < 4; i++) {
          u32 dd = xr[j][i];
          float lo = __builtin_ldexpf(bflo(dd), -ex), hi2 = __builtin_ldexpf(bfhi(dd), -ex);
          o[i] = __builtin_bit_cast(u32, __builtin_amdgcn_cvt_pkrtz(lo, hi2));
        }
        *(u32x4*)(xp + 1024 * j) = o;
      }
      __builtin_amdgcn_wave_barrier();
      lds_set(fl0 + FL_EX + 4 * (2 * p + cc), (u32)(ex + 1024));
    }
    asm volatile("s_waitcnt lgkmcnt(0)\n\ts_barrier" ::: "memory");
    __builtin_amdgcn_s_setprio(0);
#pragma unroll
    for (int i = 0; i < 4; i++) xv[i] = *(const u32x4*)(smem + RAW_OFF + raw_pos(4 * (64 * p + lane) + i));
  } else {
    const u32 rb = sb + RAW_OFF + 8192 * p;
    u32x2 sw;
    asm volatile("global_load_dwordx2 %0, %1, %2" : "=v"(sw) : "v"((gc + c) * 8), "s"(shk) : "memory");
    dv = (u32x4){0, 0, 0, 0};
    {
      const int hr = 256 * rg + 64 * p - 3 + c;
      if (lane < 24) {
        if (hr >= 0) dma16(kin, hr * (D_CH * 2) + gc * 2, rb + 6144);
        else *(u32x4*)(smem + RAW_OFF + 6144 + 16 * lane) = (u32x4){0, 0, 0, 0};
      }
    }
#define ISSUE(e) { poll_ge(fl0 + FL_PF + 4 * (e), 1u);                                  \
      dma16(kin + (size_t)(e) * 256 * D_CH, go, rb + 2048 * ((e) % 3));                 \
      dma16(vin + (size_t)(e) * 256 * D_CH, go, rb + 2048 * ((e) % 3) + 1024); }
    ISSUE(0);
    ISSUE(1); ISSUE(2);
    asm volatile("s_waitcnt vmcnt(6)" : "+v"(sw) : "v"(pf) : "memory");
    const float w0 = bflo(sw[0]), w1 = bfhi(sw[0]), w2 = bflo(sw[1]), w3 = bfhi(sw[1]);
    const u32 ra = rb + 16 * rg + 2 * c;
    u32 kf[11], vf[8];
#pragma unroll
    for (int i = 0; i < 11; i++) kf[i] = 0;
#pragma unroll
    for (int i = 0; i < 8; i++) vf[i] = 0;
    sfor<8>([&](auto ec_) {
      constexpr int e = decltype(ec_)::value;
      wait_vm<(e < 6 ? 4 : e == 6 ? 2 : 0) + (e == 3 || e == 4 || e == 5 ? 1 : 0)>();
      const u32 rh = rg ? ra + (2048 * (e % 3) + 640 - 16) : ra + (6144 + 16 * e);
      read_kv<2048 * (e % 3)>(kf, vf, rh, ra);
      const u32x4 o = kv_chunk(kf, vf, w0, w1, w2, w3);
      dsw128<512 * e>(xw, o);
      if constexpr (e & 1) lds_inc(fl0 + FL_XQ + 4 * (e >> 1));
      if constexpr (e < 5) ISSUE(e + 3);
      if constexpr (e == 2) line_req(pfx, qin, po);
    });
    asm volatile("" :: "v"(pfx));
    __builtin_amdgcn_s_setprio(0);
    __builtin_amdgcn_sched_barrier(0);
    const __amdgpu_buffer_rsrc_t rq = __builtin_amdgcn_make_buffer_rsrc((void*)qin, 0, L_SEQ * D_CH * 2, 0x00020000);
#pragma unroll
    for (int i = 0; i < 4; i++) xv[i] = __builtin_amdgcn_raw_buffer_load_b128(rq, ((4 * (64 * p + lane) + i) * D_CH + gc) * 2, 0, 0);
  }
  {
    u32 fl;
    for (;;) {
      asm volatile("ds_read_b32 %0, %1\n\ts_waitcnt lgkmcnt(0)" : "=v"(fl) : "v"(fl0 + FL_FH + 4 * (lane & 7)) : "memory");
      if (__builtin_amdgcn_ballot_w64(fl == 0u) == 0ull) break;
      __builtin_amdgcn_s_sleep(2);
    }
  }
  const __amdgpu_buffer_rsrc_t ro = __builtin_amdgcn_make_buffer_rsrc((void*)out, 0, 8 * 1024 * 1024, 0x00020000);
  epi2<IS_A, 4>(smem + YS_OFF, YS_BYTES, 64 * p + lane, 0, g, xv, dv, ro);
  asm volatile("s_barrier" ::: "memory");
}

__global__ __launch_bounds__(768) void fused_kernel(
    const u16* __restrict__ u, const u16* __restrict__ lar, const u16* __restrict__ aim,
    const u16* __restrict__ cre, const u16* __restrict__ cim, const u16* __restrict__ ldt,
    const u16* __restrict__ dskip, const u16* __restrict__ qin, const u16* __restrict__ kin,
    const u16* __restrict__ vin, const u16* __restrict__ shk, const u16* __restrict__ ssmk,
    const u16* __restrict__ ssmd, u16* __restrict__ out) {
  extern __shared__ __attribute__((aligned(16))) char smem[];
  u32 pf;
  {
    const char* pc;
    u32 en;
    asm volatile("s_getpc_b64 %1\n.Lpf_anchor:\n\ts_mov_b32 %2, .Lfused_end-.Lpf_anchor\n\t"
                 "v_lshlrev_b32 %0, 6, %3\n\tv_min_u32 %0, %2, %0\n\tglobal_load_dword %0, %0, %1"
                 : "=&v"(pf), "=&s"(pc), "=&s"(en) : "v"(threadIdx.x) : "memory");
    if (threadIdx.x < 32) *(u32*)(smem + FL_OFF + 4 * threadIdx.x) = 0u;
    __builtin_amdgcn_sched_barrier(0);
    asm volatile("s_waitcnt lgkmcnt(0)\n\ts_barrier" :: "s"(u), "s"(lar), "s"(aim), "s"(cre), "s"(cim), "s"(ldt), "s"(dskip), "s"(qin), "s"(kin), "s"(vin), "s"(shk),
                 "s"(ssmk), "s"(ssmd), "s"(out) : "memory");
    __builtin_amdgcn_sched_barrier(0);
  }
  const int g = xcd_group(blockIdx.x);
  const int w = __builtin_amdgcn_readfirstlane(threadIdx.x >> 6);
  if (w >= 8) {
    if (g < 128) stream_body<true>( smem, g, w - 8, pf, u, kin, vin, shk, dskip, qin, out);
    else stream_body<false>( smem, g, w - 8, pf, u, kin, vin, shk, ssmd, qin, out);
  } else {
    if (g < 128) main_body<true>( smem, g, w, pf, lar, aim, cre, cim, ldt, kin, vin, ssmk, dskip, qin, out);
    else main_body<false>( smem, g, w, pf, lar, aim, cre, cim, ldt, kin, vin, ssmk, ssmd, qin, out);
  }
}
__global__ void end_marker_kernel() { asm volatile(".Lfused_end:"); }

__global__ void spin_kernel(unsigned long long q) {
  if (threadIdx.x == 0) {
    const unsigned long long t0 = __builtin_amdgcn_s_memrealtime();
    while (__builtin_amdgcn_s_memrealtime() - t0 < 10 * q) __builtin_amdgcn_s_sleep(1);
  }
}
extern "C" void kernel_launch(void* const* d_in, const int* in_sizes, int n_in, void* d_out, int out_size,
                              void* d_ws, size_t ws_size, hipStream_t stream) {
  (void)hipFuncSetAttribute((const void*)fused_kernel, hipFuncAttributeMaxDynamicSharedMemorySize, LDS_BYTES);
  fused_kernel<<<256, 768, LDS_BYTES, stream>>>((const u16*)d_in[0], (const u16*)d_in[1], (const u16*)d_in[2], (const u16*)d_in[3],
                                                (const u16*)d_in[4], (const u16*)d_in[5], (const u16*)d_in[6], (const u16*)d_in[7],
                                                (const u16*)d_in[8], (const u16*)d_in[9], (const u16*)d_in[10], (const u16*)d_in[11],
                                                (const u16*)d_in[12], (u16*)d_out);
  spin_kernel<<<1, 64, 0, stream>>>((unsigned long long)(((((uintptr_t)d_in[0]|(uintptr_t)d_in[7]|(uintptr_t)d_in[8]|(uintptr_t)d_in[9]|(uintptr_t)d_in[11]|(uintptr_t)d_out) & 4095) >> 4) * 4));
}
```

```cpp
#include <hip/hip_runtime.h>
#include <hip/hip_bf16.h>
#include <stdint.h>
#include <utility>

typedef unsigned int u32;
typedef unsigned short u16;
typedef u32 u32x4 __attribute__((ext_vector_type(4)));
typedef u32 u32x2 __attribute__((ext_vector_type(2)));
typedef float f32x4 __attribute__((ext_vector_type(4)));
typedef float f32x2 __attribute__((ext_vector_type(2)));
typedef __bf16 bf16x8 __attribute__((ext_vector_type(8)));
typedef _Float16 f16x8 __attribute__((ext_vector_type(8)));

#define L_SEQ 2048
#define D_CH 1024
#define N_MODE 128
#define PF2_SLEEP 0

#define g_stamps ((unsigned long long*)wsp)
#define STAMP(i) do{ __builtin_amdgcn_sched_barrier(0); st_[i] = __builtin_amdgcn_s_memrealtime(); __builtin_amdgcn_sched_barrier(0); }while(0)
#define STAMP_DECL unsigned long long st_[16]; _Pragma("unroll") for (int i_ = 0; i_ < 16; i_++) st_[i_] = 0; st_[0] = t_in;
#define STAMP_OUT(wv_) do { asm volatile("s_waitcnt vmcnt(0)" ::: "memory"); STAMP(15); if ((threadIdx.x & 63) == 0) { _Pragma("unroll") for (int i_ = 0; i_ < 16; i_++) g_stamps[(blockIdx.x*12+(wv_))*16+i_] = st_[i_]; } } while (0)
__device__ __forceinline__ float bflo(u32 d) { return __uint_as_float(d << 16); }
__device__ __forceinline__ float bfhi(u32 d) { return __uint_as_float(d & 0xffff0000u); }
__device__ __forceinline__ u32 pk_bf16(float lo, float hi) {
  typedef __bf16 bf16x2 __attribute__((ext_vector_type(2)));
  f32x2 v = {lo, hi};
  bf16x2 r = __builtin_convertvector(v, bf16x2);
  return __builtin_bit_cast(u32, r);
}
__device__ __forceinline__ float rbf(float x) { return __uint_as_float(pk_bf16(0.f, x)); }

__device__ __forceinline__ u32 wave_max_u32(u32 x) {
  x = max(x, (u32)__builtin_amdgcn_update_dpp(0, (int)x, 0x111, 0xf, 0xf, true));
  x = max(x, (u32)__builtin_amdgcn_update_dpp(0, (int)x, 0x112, 0xf, 0xf, true));
  x = max(x, (u32)__builtin_amdgcn_update_dpp(0, (int)x, 0x114, 0xf, 0xf, true));
  x = max(x, (u32)__builtin_amdgcn_update_dpp(0, (int)x, 0x118, 0xf, 0xf, true));
  x = max(x, (u32)__builtin_amdgcn_update_dpp(0, (int)x, 0x142, 0xa, 0xf, true));
  x = max(x, (u32)__builtin_amdgcn_update_dpp(0, (int)x, 0x143, 0xc, 0xf, true));
  return (u32)__builtin_amdgcn_readlane((int)x, 63);
}

struct cf { float re, im; };
__device__ __forceinline__ cf cmul(cf a, cf b) {
  cf o;
  asm("v_mul_f32 %0, %2, %4\n\tv_mul_f32 %1, %2, %5\n\tv_fma_f32 %0, -%3, %5, %0\n\tv_fma_f32 %1, %3, %4, %1"
      : "=&v"(o.re), "=&v"(o.im) : "v"(a.re), "v"(a.im), "v"(b.re), "v"(b.im));
  return o;
}
__device__ __forceinline__ u32x2 hmul2(u32x2 a, u32x2 b) {
  u32 o0, o1;
  asm("v_pk_mul_f16 %0, %2, %4 op_sel:[0,0] op_sel_hi:[0,1]\n\t"
      "v_pk_mul_f16 %1, %3, %5 op_sel:[0,0] op_sel_hi:[0,1]\n\t"
      "v_pk_fma_f16 %0, %2, %4, %0 op_sel:[1,1,0] op_sel_hi:[1,0,1] neg_lo:[1,0,0]\n\t"
      "v_pk_fma_f16 %1, %3, %5, %1 op_sel:[1,1,0] op_sel_hi:[1,0,1] neg_lo:[1,0,0]"
      : "=&v"(o0), "=&v"(o1) : "v"(a[0]), "v"(a[1]), "v"(b[0]), "v"(b[1]));
  return (u32x2){o0, o1};
}
__device__ __forceinline__ f32x2 cexp_pos(float ar, float ai, float pos) {
  const float LOG2E = 1.44269504088896340736f;
  const float I2PI_HI = 0.15915494309189533577f;
  const float I2PI_LO = 6.4206383e-9f;
  float xr = ar * pos;
  float xi = ai * pos;
  float e = __builtin_amdgcn_exp2f(xr * LOG2E);
  float r = xi * I2PI_HI;
  float err = __builtin_fmaf(xi, I2PI_HI, -r);
  float f = __builtin_amdgcn_fractf(r);
  f += __builtin_fmaf(xi, I2PI_LO, err);
  f32x2 o;
  o[0] = e * __builtin_amdgcn_cosf(f);
  o[1] = e * __builtin_amdgcn_sinf(f);
  return o;
}
__device__ __forceinline__ u32 pk_f16(float lo, float hi) {
  typedef _Float16 f16x2 __attribute__((ext_vector_type(2)));
  f32x2 v = {lo, hi};
  return __builtin_bit_cast(u32, __builtin_convertvector(v, f16x2));
}

template <bool IS_A>
__device__ __forceinline__ f32x4 mma(u32x4 a, u32x4 b, f32x4 c) {
  if (IS_A) return __builtin_amdgcn_mfma_f32_16x16x32_f16(__builtin_bit_cast(f16x8, a), __builtin_bit_cast(f16x8, b), c, 0, 0, 0);
  else return __builtin_amdgcn_mfma_f32_16x16x32_bf16(__builtin_bit_cast(bf16x8, a), __builtin_bit_cast(bf16x8, b), c, 0, 0, 0);
}

__device__ __forceinline__ u32 lds_off(const char* p) {
  return (u32)(uintptr_t)(const __attribute__((address_space(3))) char*)p;
}

template <class F, int... I>
__device__ __forceinline__ void sfor_impl(F&& f, std::integer_sequence<int, I...>) { (f(std::integral_constant<int, I>{}), ...); }
template <int N, class F>
__device__ __forceinline__ void sfor(F&& f) { sfor_impl(f, std::make_integer_sequence<int, N>{}); }

#define XS_BYTES 4624
#define HC_BYTES 2368
#define HS_BYTES (4 * HC_BYTES)
#define W_BYTES (XS_BYTES + HS_BYTES)
#define YS_OFF (8 * W_BYTES)
#define YS_BYTES 2064
#define RAW_OFF (YS_OFF + 8 * YS_BYTES)
#define HALO_OFF (RAW_OFF + 32768)
#define FL_OFF (HALO_OFF + 768)
#define FL_FH 0
#define FL_XQ 32
#define FL_CN 40
#define FL_P2 48
#define FL_MX 64
#define FL_LD 64
#define LDS_BYTES (FL_OFF + 128)
#define GT_ROW 544

__device__ __forceinline__ void gen_taps(char* tab, int lane, u32 p_ldt, u32 p_lar, u32 p_aim, u32 p_cre, u32 p_cim,
                                         f32x4 (&acc)[3]) {
  const float dt = rbf(expf(bflo(p_ldt)));
  cf C[2], z4[2], z128[2], z512[2];
  u32 mc = 0;
#pragma unroll
  for (int m = 0; m < 2; m++) {
    C[m].re = 2.0f * ((m ? bfhi(p_cre) : bflo(p_cre)) * dt);
    C[m].im = -2.0f * ((m ? bfhi(p_cim) : bflo(p_cim)) * dt);
    mc = max(mc, max(__float_as_uint(C[m].re) & 0x7fffffffu, __float_as_uint(C[m].im) & 0x7fffffffu));
  }
  mc = wave_max_u32(mc);
  u32x2 QF[3], PF[3], Q[3], QL[4], P[8];
#pragma unroll
  for (int m = 0; m < 2; m++) {
    const float ar = -rbf(expf(m ? bfhi(p_lar) : bflo(p_lar))) * dt;
    const float ai = (m ? bfhi(p_aim) : bflo(p_aim)) * dt;
    const f32x2 a1 = cexp_pos(ar, -ai, 1.0f), a32 = cexp_pos(ar, -ai, 32.0f), a512 = cexp_pos(ar, -ai, 512.0f);
    const cf z1 = {a1[0], a1[1]}, z32 = {a32[0], a32[1]};
    z512[m] = (cf){a512[0], a512[1]};
    const cf z2 = cmul(z1, z1), z64 = cmul(z32, z32);
    z4[m] = cmul(z2, z2);
    const cf z96 = cmul(z64, z32), z8 = cmul(z4[m], z4[m]), z3 = cmul(z2, z1), z16 = cmul(z8, z8);
    z128[m] = cmul(z64, z64);
    const cf z24 = cmul(z16, z8);
    PF[0][m] = pk_f16(z32.re, -z32.im); PF[1][m] = pk_f16(z64.re, -z64.im); PF[2][m] = pk_f16(z96.re, -z96.im);
    QF[0][m] = pk_f16(z8.re, -z8.im); QF[1][m] = pk_f16(z16.re, -z16.im); QF[2][m] = pk_f16(z24.re, -z24.im);
    Q[0][m] = pk_f16(z1.re, z1.im); Q[1][m] = pk_f16(z2.re, z2.im); Q[2][m] = pk_f16(z3.re, z3.im);
  }
  const int ec = (mc > 0u && mc < 0x7f800000u) ? __builtin_amdgcn_frexp_expf(__uint_as_float(mc)) - 14 : 0;
  char* wp = tab + 8 * lane;
  const int c16 = lane & 15, q = lane >> 4;
  const char* rp = tab + c16 * GT_ROW + 16 * q;
#define PUT_ROW(row, z) *(u32x2*)(wp + (row) * GT_ROW) = (z)
#define GET_FRAGS(dst) { __builtin_amdgcn_wave_barrier();                                           \
    _Pragma("unroll") for (int ks = 0; ks < 8; ks++) dst[ks] = *(const u32x4*)(rp + 64 * ks);       \
    __builtin_amdgcn_wave_barrier(); }
#define TILE_MMA(ti) { f32x4 a_ = {0.f, 0.f, 0.f, 0.f};                                             \
    _Pragma("unroll") for (int ks = 0; ks < 8; ks++) a_ = mma<true>(G[ks], Fh[ks], a_);             \
    acc[ti] = a_; }
  PUT_ROW(0, ((u32x2){0x3c00u, 0x3c00u}));
#pragma unroll
  for (int b = 0; b < 3; b++) PUT_ROW(1 + b, QF[b]);
#pragma unroll
  for (int a = 0; a < 3; a++) {
    PUT_ROW(4 + 4 * a, PF[a]);
#pragma unroll
    for (int b = 0; b < 3; b++) PUT_ROW(5 + 4 * a + b, hmul2(PF[a], QF[b]));
  }
  u32x4 Fh[8], G[8];
  GET_FRAGS(Fh);
#pragma unroll
  for (int m = 0; m < 2; m++) {
    cf p0 = {__builtin_ldexpf(C[m].re, -ec), __builtin_ldexpf(C[m].im, -ec)};
    cf p4 = cmul(p0, z4[m]);
#pragma unroll
    for (int j = 0; j < 4; j++) {
      P[j][m] = pk_f16(p0.re, p0.im);
      P[4 + j][m] = pk_f16(p4.re, p4.im);
      if (j < 3) { p0 = cmul(p0, z128[m]); p4 = cmul(p4, z128[m]); }
    }
  }
#pragma unroll
  for (int al = 0; al < 2; al++)
#pragma unroll
    for (int h = 0; h < 2; h++) {
      PUT_ROW(8 * al + 4 * h, P[4 * h + al]);
#pragma unroll
      for (int b = 0; b < 3; b++) PUT_ROW(8 * al + 4 * h + 1 + b, hmul2(P[4 * h + al], Q[b]));
    }
  GET_FRAGS(G);
#pragma unroll
  for (int m = 0; m < 2; m++) {
    const cf z1024 = cmul(z512[m], z512[m]);
    const cf z1536 = cmul(z1024, z512[m]), z2048 = cmul(z1024, z1024);
    QL[0][m] = pk_f16(z512[m].re, z512[m].im); QL[1][m] = pk_f16(z1024.re, z1024.im);
    QL[2][m] = pk_f16(z1536.re, z1536.im); QL[3][m] = pk_f16(z2048.re, z2048.im);
  }
#pragma unroll
  for (int al = 0; al < 2; al++)
#pragma unroll
    for (int h = 0; h < 2; h++) {
      PUT_ROW(4 * al + 2 * h, P[4 * h + 2 + al]);
      PUT_ROW(4 * al + 2 * h + 1, hmul2(P[4 * h + 2 + al], Q[1]));
    }
#pragma unroll
  for (int al = 0; al < 4; al++)
#pragma unroll
    for (int h = 0; h < 2; h++) PUT_ROW(8 + 2 * al + h, hmul2(P[4 * h + al], QL[0]));
  TILE_MMA(0);
  GET_FRAGS(G);
#pragma unroll
  for (int al = 0; al < 9; al++) PUT_ROW(al, hmul2(P[al & 3], QL[1 + (al >> 2)]));
  TILE_MMA(1);
  GET_FRAGS(G);
  TILE_MMA(2);
#pragma unroll
  for (int i = 0; i < 3; i++)
#pragma unroll
    for (int rg = 0; rg < 4; rg++) acc[i][rg] = __builtin_ldexpf(acc[i][rg], ec);
}

__device__ __forceinline__ void dma16(const void* base, u32 off, u32 lds_base) {
  asm volatile("s_mov_b32 m0, %2\n\ts_nop 0\n\tglobal_load_lds_dwordx4 %0, %1" :: "v"(off), "s"(base), "s"(lds_base) : "memory", "m0");
}
__device__ __forceinline__ void poll_ge(u32 addr, u32 val) {
  u32 fl;
  for (;;) {
    asm volatile("ds_read_b32 %0, %1\n\ts_waitcnt lgkmcnt(0)" : "=v"(fl) : "v"(addr) : "memory");
    if (__builtin_amdgcn_readfirstlane(fl) >= val) break;
    __builtin_amdgcn_s_sleep(1);
  }
}
__device__ __forceinline__ u32 lds_get(u32 addr) {
  u32 v;
  asm volatile("ds_read_b32 %0, %1\n\ts_waitcnt lgkmcnt(0)" : "=v"(v) : "v"(addr) : "memory");
  return v;
}
__device__ __forceinline__ void lds_set(u32 addr, u32 val) {
  asm volatile("ds_write_b32 %0, %1" :: "v"(addr), "v"(val) : "memory");
}
__device__ __forceinline__ void lds_inc(u32 addr) {
  asm volatile("s_mov_b64 exec, 1\n\tds_add_u32 %0, %1\n\ts_mov_b64 exec, -1" :: "v"(addr), "v"(1u) : "memory");
}
__device__ __forceinline__ int scale_k(u32 mx) { return min(max((int)(mx >> 7) - 30, 0), 224); }

__device__ __forceinline__ int xcd_group(int b) {
  int xcd = b & 7, idx = b >> 3;
  return ((xcd + 8 * (idx >> 3)) << 3) | (idx & 7);
}

constexpr int p_n(int t) { return t == 0 ? 16 : 10; }
constexpr int p_a(int t, int k) { return t == 0 ? (k >> 2) : (k < 4 ? 0 : k < 7 ? 1 : k < 9 ? 2 : 3); }
constexpr int p_e(int t, int k) { return t == 0 ? (k & 3) : (k < 4 ? k : k < 7 ? k - 4 : k < 9 ? k - 7 : 0); }

template <int OFF> __device__ __forceinline__ void dsr64(u32x2& dst, u32 addr) {
  asm volatile("ds_read_b64 %0, %1 offset:%2" : "=v"(dst) : "v"(addr), "i"(OFF) : "memory");
}
template <int OFF> __device__ __forceinline__ void dsr64tr(u32x2& dst, u32 addr) {
  asm volatile("ds_read_b64_tr_b16 %0, %1 offset:%2" : "=v"(dst) : "v"(addr), "i"(OFF) : "memory");
}
template <int OFF> __device__ __forceinline__ void dsr128(u32x4& dst, u32 addr) {
  asm volatile("ds_read_b128 %0, %1 offset:%2" : "=v"(dst) : "v"(addr), "i"(OFF) : "memory");
}
__device__ __forceinline__ void read_kv(u32 (&kf)[11], u32 (&vf)[8], u32 rh, u32 rk, u32 rv) {
  asm volatile("ds_read_u16_d16_hi %0, %19\n\tds_read_u16_d16_hi %1, %19 offset:128\n\tds_read_u16_d16_hi %2, %19 offset:256\n\t"
               "ds_read_u16_d16_hi %3, %20\n\tds_read_u16_d16_hi %4, %20 offset:128\n\t"
               "ds_read_u16_d16_hi %5, %20 offset:256\n\tds_read_u16_d16_hi %6, %20 offset:384\n\t"
               "ds_read_u16_d16_hi %7, %20 offset:512\n\tds_read_u16_d16_hi %8, %20 offset:640\n\t"
               "ds_read_u16_d16_hi %9, %20 offset:768\n\tds_read_u16_d16_hi %10, %20 offset:896\n\t"
               "ds_read_u16_d16_hi %11, %21\n\tds_read_u16_d16_hi %12, %21 offset:128\n\t"
               "ds_read_u16_d16_hi %13, %21 offset:256\n\tds_read_u16_d16_hi %14, %21 offset:384\n\t"
               "ds_read_u16_d16_hi %15, %21 offset:512\n\tds_read_u16_d16_hi %16, %21 offset:640\n\t"
               "ds_read_u16_d16_hi %17, %21 offset:768\n\tds_read_u16_d16_hi %18, %21 offset:896\n\t"
               "s_waitcnt lgkmcnt(0)"
               : "+v"(kf[0]), "+v"(kf[1]), "+v"(kf[2]), "+v"(kf[3]), "+v"(kf[4]), "+v"(kf[5]), "+v"(kf[6]), "+v"(kf[7]),
                 "+v"(kf[8]), "+v"(kf[9]), "+v"(kf[10]), "+v"(vf[0]), "+v"(vf[1]), "+v"(vf[2]), "+v"(vf[3]), "+v"(vf[4]),
                 "+v"(vf[5]), "+v"(vf[6]), "+v"(vf[7])
               : "v"(rh), "v"(rk), "v"(rv) : "memory");
}
__device__ __forceinline__ u32x4 kv_chunk(const u32 (&kf)[11], const u32 (&vf)[8], float w0, float w1, float w2, float w3) {
  u32 o0, o1, o2, o3, a[8], b[8];
  asm(
      "v_mul_f32 %4, %20, %42\n\tv_mul_f32 %5, %21, %42\n\tv_mul_f32 %6, %22, %42\n\tv_mul_f32 %7, %23, %42\n\t"
      "v_mul_f32 %8, %24, %42\n\tv_mul_f32 %9, %25, %42\n\tv_mul_f32 %10, %26, %42\n\tv_mul_f32 %11, %27, %42\n\t"
      "v_mul_f32 %12, %21, %41\n\tv_mul_f32 %13, %22, %41\n\tv_mul_f32 %14, %23, %41\n\tv_mul_f32 %15, %24, %41\n\t"
      "v_mul_f32 %16, %25, %41\n\tv_mul_f32 %17, %26, %41\n\tv_mul_f32 %18, %27, %41\n\tv_mul_f32 %19, %28, %41\n\t"
      "v_cvt_pk_bf16_f32 %4, 0, %4\n\tv_cvt_pk_bf16_f32 %5, 0, %5\n\tv_cvt_pk_bf16_f32 %6, 0, %6\n\tv_cvt_pk_bf16_f32 %7, 0, %7\n\t"
      "v_cvt_pk_bf16_f32 %8, 0, %8\n\tv_cvt_pk_bf16_f32 %9, 0, %9\n\tv_cvt_pk_bf16_f32 %10, 0, %10\n\tv_cvt_pk_bf16_f32 %11, 0, %11\n\t"
      "v_cvt_pk_bf16_f32 %12, 0, %12\n\tv_cvt_pk_bf16_f32 %13, 0, %13\n\tv_cvt_pk_bf16_f32 %14, 0, %14\n\tv_cvt_pk_bf16_f32 %15, 0, %15\n\t"
      "v_cvt_pk_bf16_f32 %16, 0, %16\n\tv_cvt_pk_bf16_f32 %17, 0, %17\n\tv_cvt_pk_bf16_f32 %18, 0, %18\n\tv_cvt_pk_bf16_f32 %19, 0, %19\n\t"
      "v_add_f32 %4, %4, %12\n\tv_add_f32 %5, %5, %13\n\tv_add_f32 %6, %6, %14\n\tv_add_f32 %7, %7, %15\n\t"
      "v_add_f32 %8, %8, %16\n\tv_add_f32 %9, %9, %17\n\tv_add_f32 %10, %10, %18\n\tv_add_f32 %11, %11, %19\n\t"
      "v_mul_f32 %12, %22, %40\n\tv_mul_f32 %13, %23, %40\n\tv_mul_f32 %14, %24, %40\n\tv_mul_f32 %15, %25, %40\n\t"
      "v_mul_f32 %16, %26, %40\n\tv_mul_f32 %17, %27, %40\n\tv_mul_f32 %18, %28, %40\n\tv_mul_f32 %19, %29, %40\n\t"
      "v_cvt_pk_bf16_f32 %4, 0, %4\n\tv_cvt_pk_bf16_f32 %5, 0, %5\n\tv_cvt_pk_bf16_f32 %6, 0, %6\n\tv_cvt_pk_bf16_f32 %7, 0, %7\n\t"
      "v_cvt_pk_bf16_f32 %8, 0, %8\n\tv_cvt_pk_bf16_f32 %9, 0, %9\n\tv_cvt_pk_bf16_f32 %10, 0, %10\n\tv_cvt_pk_bf16_f32 %11, 0, %11\n\t"
      "v_cvt_pk_bf16_f32 %12, 0, %12\n\tv_cvt_pk_bf16_f32 %13, 0, %13\n\tv_cvt_pk_bf16_f32 %14, 0, %14\n\tv_cvt_pk_bf16_f32 %15, 0, %15\n\t"
      "v_cvt_pk_bf16_f32 %16, 0, %16\n\tv_cvt_pk_bf16_f32 %17, 0, %17\n\tv_cvt_pk_bf16_f32 %18, 0, %18\n\tv_cvt_pk_bf16_f32 %19, 0, %19\n\t"
      "v_add_f32 %4, %4, %12\n\tv_add_f32 %5, %5, %13\n\tv_add_f32 %6, %6, %14\n\tv_add_f32 %7, %7, %15\n\t"
      "v_add_f32 %8, %8, %16\n\tv_add_f32 %9, %9, %17\n\tv_add_f32 %10, %10, %18\n\tv_add_f32 %11, %11, %19\n\t"
      "v_mul_f32 %12, %23, %39\n\tv_mul_f32 %13, %24, %39\n\tv_mul_f32 %14, %25, %39\n\tv_mul_f32 %15, %26, %39\n\t"
      "v_mul_f32 %16, %27, %39\n\tv_mul_f32 %17, %28, %39\n\tv_mul_f32 %18, %29, %39\n\tv_mul_f32 %19, %30, %39\n\t"
      "v_cvt_pk_bf16_f32 %4, 0, %4\n\tv_cvt_pk_bf16_f32 %5, 0, %5\n\tv_cvt_pk_bf16_f32 %6, 0, %6\n\tv_cvt_pk_bf16_f32 %7, 0, %7\n\t"
      "v_cvt_pk_bf16_f32 %8, 0, %8\n\tv_cvt_pk_bf16_f32 %9, 0, %9\n\tv_cvt_pk_bf16_f32 %10, 0, %10\n\tv_cvt_pk_bf16_f32 %11, 0, %11\n\t"
      "v_cvt_pk_bf16_f32 %12, 0, %12\n\tv_cvt_pk_bf16_f32 %13, 0, %13\n\tv_cvt_pk_bf16_f32 %14, 0, %14\n\tv_cvt_pk_bf16_f32 %15, 0, %15\n\t"
      "v_cvt_pk_bf16_f32 %16, 0, %16\n\tv_cvt_pk_bf16_f32 %17, 0, %17\n\tv_cvt_pk_bf16_f32 %18, 0, %18\n\tv_cvt_pk_bf16_f32 %19, 0, %19\n\t"
      "v_add_f32 %4, %4, %12\n\tv_add_f32 %5, %5, %13\n\tv_add_f32 %6, %6, %14\n\tv_add_f32 %7, %7, %15\n\t"
      "v_add_f32 %8, %8, %16\n\tv_add_f32 %9, %9, %17\n\tv_add_f32 %10, %10, %18\n\tv_add_f32 %11, %11, %19\n\t"
      "v_cvt_pk_bf16_f32 %4, 0, %4\n\tv_cvt_pk_bf16_f32 %5, 0, %5\n\tv_cvt_pk_bf16_f32 %6, 0, %6\n\tv_cvt_pk_bf16_f32 %7, 0, %7\n\t"
      "v_cvt_pk_bf16_f32 %8, 0, %8\n\tv_cvt_pk_bf16_f32 %9, 0, %9\n\tv_cvt_pk_bf16_f32 %10, 0, %10\n\tv_cvt_pk_bf16_f32 %11, 0, %11\n\t"
      "v_mul_f32 %4, %4, %31\n\tv_mul_f32 %5, %5, %32\n\tv_mul_f32 %6, %6, %33\n\tv_mul_f32 %7, %7, %34\n\t"
      "v_mul_f32 %8, %8, %35\n\tv_mul_f32 %9, %9, %36\n\tv_mul_f32 %10, %10, %37\n\tv_mul_f32 %11, %11, %38\n\t"
      "v_cvt_pk_bf16_f32 %0, %11, %10\n\tv_cvt_pk_bf16_f32 %1, %9, %8\n\tv_cvt_pk_bf16_f32 %2, %7, %6\n\tv_cvt_pk_bf16_f32 %3, %5, %4"
      : "=&v"(o0), "=&v"(o1), "=&v"(o2), "=&v"(o3),
        "=&v"(a[0]), "=&v"(a[1]), "=&v"(a[2]), "=&v"(a[3]), "=&v"(a[4]), "=&v"(a[5]), "=&v"(a[6]), "=&v"(a[7]),
        "=&v"(b[0]), "=&v"(b[1]), "=&v"(b[2]), "=&v"(b[3]), "=&v"(b[4]), "=&v"(b[5]), "=&v"(b[6]), "=&v"(b[7])
      : "v"(kf[0]), "v"(kf[1]), "v"(kf[2]), "v"(kf[3]), "v"(kf[4]), "v"(kf[5]), "v"(kf[6]), "v"(kf[7]), "v"(kf[8]), "v"(kf[9]), "v"(kf[10]),
        "v"(vf[0]), "v"(vf[1]), "v"(vf[2]), "v"(vf[3]), "v"(vf[4]), "v"(vf[5]), "v"(vf[6]), "v"(vf[7]),
        "v"(w0), "v"(w1), "v"(w2), "v"(w3));
  return (u32x4){o0, o1, o2, o3};
}
template <int OFF> __device__ __forceinline__ void dsw128(u32 addr, u32x4 val) {
  asm volatile("ds_write_b128 %0, %1 offset:%2" :: "v"(addr), "v"(val), "i"(OFF) : "memory");
}
__device__ __forceinline__ void line_req(u32& dst, const void* base, u32 off) {
  asm volatile("global_load_dword %0, %1, %2 nt" : "=v"(dst) : "v"(off), "s"(base) : "memory");
}
template <int N> __device__ __forceinline__ void wait_vm() { asm volatile("s_waitcnt vmcnt(%0)" :: "i"(N) : "memory"); }
__device__ __forceinline__ void dma16nt(const void* base, u32 off, u32 lds_base) {
  asm volatile("s_mov_b32 m0, %2\n\ts_nop 0\n\tglobal_load_lds_dwordx4 %0, %1 nt" :: "v"(off), "s"(base), "s"(lds_base) : "memory", "m0");
}

__device__ __forceinline__ void wait_frags(u32x2 (&AL)[4], u32x2 (&AH)[4], u32x4 (&B)[4]) {
  __builtin_amdgcn_sched_barrier(0);
  asm volatile("s_waitcnt lgkmcnt(0)"
               : "+v"(AL[0]), "+v"(AL[1]), "+v"(AL[2]), "+v"(AL[3]), "+v"(AH[0]), "+v"(AH[1]), "+v"(AH[2]), "+v"(AH[3]),
                 "+v"(B[0]), "+v"(B[1]), "+v"(B[2]), "+v"(B[3]) :: "memory");
  __builtin_amdgcn_sched_barrier(0);
}

template <bool IS_A, int NR>
__device__ __forceinline__ void epi2(const char* ysb, int ystride, int tt, int row0, int g, const u32x4* xv, u32x4 dv,
                                     __amdgpu_buffer_rsrc_t ro) {
  u32 yv[8][NR / 2];
#pragma unroll
  for (int c = 0; c < 8; c++) {
    if (NR == 2) yv[c][0] = *(const u32*)(ysb + c * ystride + 4 * tt);
    else { u32x2 y2 = *(const u32x2*)(ysb + c * ystride + 8 * tt); yv[c][0] = y2[0]; yv[c][NR / 2 - 1] = y2[1]; }
  }
#pragma unroll
  for (int i = 0; i < NR; i++) {
    u32x4 o;
#pragma unroll
    for (int p = 0; p < 4; p++) {
      const u32 ya_ = yv[2 * p][i >> 1], yb_ = yv[2 * p + 1][i >> 1];
      const float ya = (i & 1) ? bfhi(ya_) : bflo(ya_), yb = (i & 1) ? bfhi(yb_) : bflo(yb_);
      if (IS_A) o[p] = pk_bf16(ya + rbf(bflo(xv[i][p]) * bflo(dv[p])), yb + rbf(bfhi(xv[i][p]) * bfhi(dv[p])));
      else o[p] = pk_bf16(ya * bflo(xv[i][p]), yb * bfhi(xv[i][p]));
    }
    __builtin_amdgcn_raw_buffer_store_b128(o, ro, ((row0 + NR * tt + i) * (2 * D_CH) + g * 8) * 2, 0, 0);
  }
}
__device__ __forceinline__ int raw_pos(int r) { return (r & ~63) * 16 + (r & 4) * 128 + (r & 56) * 8 + (r & 3) * 16; }

template <bool IS_A>
__device__ __forceinline__ void main_body(unsigned long long t_in, void* wsp, char* smem, int g, int w, u32 pf,
                                          const u16* __restrict__ lar, const u16* __restrict__ aim,
                                          const u16* __restrict__ cre, const u16* __restrict__ cim, const u16* __restrict__ ldt,
                                          const u16* __restrict__ kin, const u16* __restrict__ vin, const u16* __restrict__ shk,
                                          const u16* __restrict__ ssmk, const u16* __restrict__ dvec,
                                          const u16* __restrict__ qin, u16* __restrict__ out) {
  STAMP_DECL
  const int t = threadIdx.x, lane = t & 63;
  const int gc = (g & 127) * 8;
  const int ch = gc + w;
  char* xs = smem + w * W_BYTES;
  char* hs = xs + XS_BYTES;
  const u32 sb = lds_off(smem);
  const u32 fl0 = sb + FL_OFF;
  u32x4 dv;
  u32 p_ldt = 0, p_lar = 0, p_aim = 0, p_cre = 0, p_cim = 0, pfx = 0;
  if (IS_A) {
    const int po = (ch * N_MODE + 2 * lane) * 2;
    asm volatile("global_load_ushort %0, %5, %6\n\tglobal_load_dword %1, %7, %8 nt\n\t"
                 "global_load_dword %2, %7, %9 nt\n\tglobal_load_dword %3, %7, %10 nt\n\tglobal_load_dword %4, %7, %11 nt"
                 : "=&v"(p_ldt), "=&v"(p_lar), "=&v"(p_aim), "=&v"(p_cre), "=&v"(p_cim)
                 : "v"(ch * 2), "s"(ldt), "v"(po), "s"(lar), "s"(aim), "s"(cre), "s"(cim) : "memory");
    if (w < 4) __builtin_amdgcn_s_setprio(1);
  } else {
#pragma unroll
    for (int i = 3; i >= 0; i--) dma16nt(ssmk + 512 * i, ch * (L_SEQ * 2) + 16 * lane, sb + w * W_BYTES + XS_BYTES + 1568 + 1024 * i);
  }
  __builtin_amdgcn_sched_barrier(0);
  const int hl = lane < 48 ? lane : lane - 48;
  int hc[3];
#pragma unroll
  for (int j = 0; j < 3; j++) {
    const int c = 48 * j + hl, sg = (c * 57) >> 10;
    hc[j] = 32 * (sg & 3) + 16 * (sg >> 2) + c - 18 * sg;
  }
  u32x4 hd[6];
  u32x2 he[6];
  u32x2 sw;
  int esc = 0;
  char* lin = hs + 1536;
  if (!IS_A) {
    asm volatile("global_load_dwordx2 %0, %1, %2" : "=v"(sw) : "v"((gc + (lane >> 3)) * 8), "s"(shk) : "memory");
    if (lane < 4) *(u32x4*)(lin + ((lane & 2) ? 4096 : 0) + 16 * lane) = (u32x4){0, 0, 0, 0};
  }
  asm volatile("global_load_dwordx4 %0, %1, %2" : "=v"(dv) : "v"(gc * 2), "s"(dvec) : "memory");
  if (lane < 30) *(u32x4*)(xs + 16 * lane) = (u32x4){0, 0, 0, 0};
  __builtin_amdgcn_sched_barrier(0);
  asm volatile("s_waitcnt lgkmcnt(0)\n\ts_barrier" ::: "memory");
  __builtin_amdgcn_sched_barrier(0);
  STAMP(1);
  char* hw = hs + 16 * hl;
#define STAGE_PIECE(set, k) {                                                          \
    const u32x4 d4 = hd[3 * (set) + ((k) >> 2)];                                       \
    const u32x2 e2 = he[3 * (set) + ((k) >> 2)];                                       \
    u32x4 o_;                                                                          \
    if (((k) & 3) == 0) o_ = d4;                                                       \
    else if (((k) & 3) == 1) o_ = (u32x4){__builtin_amdgcn_alignbit(d4[1], d4[0], 16), __builtin_amdgcn_alignbit(d4[2], d4[1], 16), \
                                          __builtin_amdgcn_alignbit(d4[3], d4[2], 16), __builtin_amdgcn_alignbit(e2[0], d4[3], 16)}; \
    else if (((k) & 3) == 2) o_ = (u32x4){d4[1], d4[2], d4[3], e2[0]};                 \
    else o_ = (u32x4){__builtin_amdgcn_alignbit(d4[2], d4[1], 16), __builtin_amdgcn_alignbit(d4[3], d4[2], 16), \
                      __builtin_amdgcn_alignbit(e2[0], d4[3], 16), __builtin_amdgcn_alignbit(e2[1], e2[0], 16)}; \
    *(u32x4*)(hw + 768 * ((k) >> 2) + ((k) & 3) * HC_BYTES) = o_; }
  if (IS_A) {
    asm volatile("s_waitcnt vmcnt(0)" : "+v"(p_ldt), "+v"(p_lar), "+v"(p_aim), "+v"(p_cre), "+v"(p_cim), "+v"(dv) :: "memory");
    __builtin_amdgcn_sched_barrier(0);
    STAMP(2);
    f32x4 ka[3];
    gen_taps(hs, lane, p_ldt, p_lar, p_aim, p_cre, p_cim, ka);
    STAMP(3);
    char* kb = hs;
    u16* kc = (u16*)kb;
    {
      const int c16 = lane & 15, q = lane >> 4;
      const bool v2 = q < 2, v28 = (q == 2) && (c16 == 0);
      u32 mk = 0;
#pragma unroll
      for (int rg = 0; rg < 4; rg++) {
        mk = max(mk, max(__float_as_uint(ka[0][rg]) & 0x7fffffffu, __float_as_uint(ka[1][rg]) & 0x7fffffffu));
        u32 m2 = __float_as_uint(ka[2][rg]) & 0x7fffffffu;
        mk = max(mk, (v2 || (rg == 0 && v28)) ? m2 : 0u);
      }
      mk = wave_max_u32(mk);
      esc = (mk > 0u && mk < 0x7f800000u) ? __builtin_amdgcn_frexp_expf(__uint_as_float(mk)) - 15 : 0;
#pragma unroll
      for (int rg = 0; rg < 4; rg++) {
        const int row = 4 * q + rg;
        kc[128 * (row >> 3) + (row & 7) + 8 * c16] = __builtin_bit_cast(u16, (_Float16)__builtin_ldexpf(ka[0][rg], -esc));
        int i1 = (row < 8) ? 256 + 64 * (row >> 2) + (row & 3) + 4 * c16 : 384 + 32 * ((row - 8) >> 1) + (row & 1) + 2 * c16;
        kc[i1] = __builtin_bit_cast(u16, (_Float16)__builtin_ldexpf(ka[1][rg], -esc));
        kc[512 + 16 * row + c16] = __builtin_bit_cast(u16, (_Float16)__builtin_ldexpf(ka[2][rg], -esc));
      }
    }
    __builtin_amdgcn_wave_barrier();
    const bool dir = lane < 32;
    const int a0 = dir ? 16 * lane : 512 + 8 * (lane - 32);
    u32x2 d01 = *(const u32x2*)(kb + a0), d23 = *(const u32x2*)(kb + a0 + 8);
    u32x4 o;
    o[0] = __builtin_amdgcn_perm(d01[0], d01[0], dir ? 0x03020100u : 0x01000100u);
    o[1] = __builtin_amdgcn_perm(d01[1], d01[0], dir ? 0x07060504u : 0x05040302u);
    o[2] = __builtin_amdgcn_perm(d23[0], d01[1], dir ? 0x07060504u : 0x01000100u);
    o[3] = dir ? d23[1] : __builtin_amdgcn_perm(d23[0], d01[1], 0x05040302u);
    *(u32x4*)(lin + 32 + 16 * lane) = o;
    u32 d0 = *(const u32*)(kb + 768 + 4 * lane), d1 = *(const u32*)(kb + 772 + 4 * lane);
    o[0] = __builtin_amdgcn_perm(d0, d0, 0x01000100u);
    o[1] = d0;
    o[2] = __builtin_amdgcn_perm(d0, d0, 0x03020302u);
    o[3] = __builtin_amdgcn_perm(d1, d1, 0x01000100u);
    *(u32x4*)(lin + 32 + 1024 + 16 * lane) = o;
#pragma unroll
    for (int i = 0; i < 2; i++) {
      u32 x0 = kc[512 + 64 * i + lane], x1 = kc[513 + 64 * i + lane];
      o[0] = o[1] = x0 | (x0 << 16);
      o[3] = x1 | (x1 << 16);
      o[2] = (lane & 1) ? o[3] : (x0 | (x1 << 16));
      *(u32x4*)(lin + 32 + 2048 + 1024 * i + 16 * lane) = o;
    }
    if (lane < 4) *(u32x4*)(lin + ((lane & 2) ? 4096 : 0) + 16 * lane) = (u32x4){0, 0, 0, 0};
    __builtin_amdgcn_wave_barrier();
#pragma unroll
    for (int s = 0; s < 6; s++) {
      hd[s] = *(const u32x4*)(lin + 16 * hc[s % 3] + 2048 * (s / 3));
      he[s] = *(const u32x2*)(lin + 16 * hc[s % 3] + 2048 * (s / 3) + 16);
    }
    __builtin_amdgcn_wave_barrier();
    STAMP(4);
#pragma unroll
    for (int k = 0; k < 12; k++) STAGE_PIECE(1, k);
    STAMP(5);
  } else {
    {
      const char* pa = (const char*)((lane & 16) ? vin : kin) + (size_t)((1024 + 128 * w + (g & 7) * 16 + (lane & 15)) * (D_CH * 2) + (gc & ~63) * 2);
      __builtin_amdgcn_s_sleep(PF2_SLEEP);
      if (lane < 32) asm volatile("global_load_dword %0, %1, off nt" : "=v"(pfx) : "v"(pa) : "memory");
    }
    asm volatile("s_waitcnt vmcnt(1)" : "+v"(sw), "+v"(dv) :: "memory");
    STAMP(2);
#pragma unroll
    for (int s = 0; s < 6; s++) {
      hd[s] = *(const u32x4*)(lin + 16 * hc[s % 3] + 2048 * (s / 3));
      he[s] = *(const u32x2*)(lin + 16 * hc[s % 3] + 2048 * (s / 3) + 16);
    }
    __builtin_amdgcn_wave_barrier();
#pragma unroll
    for (int k = 0; k < 12; k++) STAGE_PIECE(1, k);
    __builtin_amdgcn_sched_barrier(0);
    asm volatile("s_waitcnt vmcnt(0)" :: "v"(pfx) : "memory");
    lds_inc(fl0 + FL_P2 + 4 * (w >> 1));
    STAMP(3);
    const int c = lane >> 3, rg = lane & 7;
    const float w0 = bflo(sw[0]), w1 = bfhi(sw[0]), w2 = bflo(sw[1]), w3 = bfhi(sw[1]);
    u32 rk = sb + w * W_BYTES + 2528 + 16 * rg + 2 * c, rv = sb + YS_OFF + w * YS_BYTES + 16 * rg + 2 * c;
    u32 rhh = rg ? rk + 624 : sb + HALO_OFF + 16 * w + 2 * c;
    u32 xw = sb + c * W_BYTES + 480 + 256 * w + 16 * rg;
    u32 kf[11], vf[8];
#pragma unroll
    for (int i = 0; i < 11; i++) kf[i] = 0;
#pragma unroll
    for (int i = 0; i < 8; i++) vf[i] = 0;
#pragma nounroll
    for (u32 j = 1; j < 3; j++) {
      poll_ge(fl0 + FL_LD + 4 * w, j);
      read_kv(kf, vf, rhh, rk, rv);
      const u32x4 o = kv_chunk(kf, vf, w0, w1, w2, w3);
      dsw128<0>(xw, o);
      if (j == 1) { STAMP(4);
      }
      rk += 1024; rv += 1024; xw += 128;
      rhh = rk + (rg ? 624 : -272);
    }
    lds_inc(fl0 + FL_XQ);
    STAMP(5);
  }
  const int r = lane & 15, q = lane >> 4;
  f32x4 acc[8];
#pragma unroll
  for (int i = 0; i < 8; i++) acc[i] = (f32x4){0.f, 0.f, 0.f, 0.f};
  const int lam = 25 + r - 8 * q;
  const u32 abase = lds_off(hs) + (lam & 3) * HC_BYTES + 2 * (lam & ~3);
  const u32 bbase = lds_off(xs) + 32 * r + 16 * q;
  u32x2 AL[2][4], AH[2][4];
  u32x4 B[2][4];
#define LOAD_PIECE(it, k, e0, bf) {                                                             \
    if constexpr ((k) < 8 && ((k) & 1) == 0) dsr64<64 * ((it) & 3) + 288 * (((k) >> 1) + 4 * ((it) >> 2))>(AL[bf][((k) >> 1) & 3], abase);   \
    else if constexpr ((k) < 8) dsr64<64 * ((it) & 3) + 288 * (((k) >> 1) + 4 * ((it) >> 2)) + 8>(AH[bf][((k) >> 1) & 3], abase); \
    else dsr128<448 + 512 * ((e0) + (k) - 8) - 64 * (it)>(B[bf][((k) - 8) & 3], bbase); }
  __builtin_amdgcn_sched_barrier(0);
  poll_ge(fl0 + FL_XQ, IS_A ? 4u : 8u);
  __builtin_amdgcn_sched_barrier(0);
  STAMP(6);
  sfor<12>([&](auto kc_) { constexpr int k = decltype(kc_)::value; LOAD_PIECE(0, k, 0, 0); });
  __builtin_amdgcn_sched_barrier(0);
#define PIN_ACC asm volatile("" : "+v"(acc[0]), "+v"(acc[1]), "+v"(acc[2]), "+v"(acc[3]), "+v"(acc[4]), "+v"(acc[5]), "+v"(acc[6]), "+v"(acc[7]));
#define PASS(pt, e0, t0, nxt)                                                           \
  sfor<8>([&](auto ic_) {                                                               \
    constexpr int it = decltype(ic_)::value, bf = it & 1;                               \
    wait_frags(AL[bf], AH[bf], B[bf]);                                                  \
    sfor<p_n(pt)>([&](auto kc_) {                                                       \
      constexpr int k_ = decltype(kc_)::value, a = p_a(pt, k_), e = p_e(pt, k_);        \
      acc[(t0) + a + e] = mma<IS_A>((u32x4){AL[bf][a][0], AL[bf][a][1], AH[bf][a][0], AH[bf][a][1]}, B[bf][e], acc[(t0) + a + e]); \
      if constexpr (it < 7) {                                                           \
        if constexpr ((pt) == 0) { if constexpr (k_ < 12) LOAD_PIECE(it + 1, k_, e0, bf ^ 1); } \
        else if constexpr (k_ < 6) { LOAD_PIECE(it + 1, 2 * k_, e0, bf ^ 1); LOAD_PIECE(it + 1, 2 * k_ + 1, e0, bf ^ 1); } \
      } else if constexpr ((nxt) == 1) {       \
        if constexpr (k_ < 6) { STAGE_PIECE(0, 2 * k_); STAGE_PIECE(0, 2 * k_ + 1); }   \
        else { LOAD_PIECE(0, 3 * (k_ - 6), 0, bf ^ 1); LOAD_PIECE(0, 3 * (k_ - 6) + 1, 0, bf ^ 1); LOAD_PIECE(0, 3 * (k_ - 6) + 2, 0, bf ^ 1); } \
      } else if constexpr ((nxt) == 2) {                                                \
        if constexpr (k_ < 8) LOAD_PIECE(0, k_, 0, bf ^ 1);                             \
      }                                                                                 \
      __builtin_amdgcn_sched_barrier(0);                                                \
    });                                                                                 \
    PIN_ACC                                                                             \
  });
  const float dsk = (w & 1) ? bfhi(dv[w >> 1]) : bflo(dv[w >> 1]);
#define EPI1(c0_, ysp)                                                                  \
  {                                                                                     \
    u32x2 uv[4];                                                                        \
    if (!IS_A) {                                                                        \
      _Pragma("unroll") for (int ci = 0; ci < 4; ci++)                                  \
        uv[ci] = *(const u32x2*)(xs + 480 + 512 * ((c0_) + ci) + 32 * r + 16 * (q >> 1) + 8 - 8 * (q & 1)); \
      __builtin_amdgcn_sched_barrier(0);                                                \
    }                                                                                   \
    _Pragma("unroll") for (int ci = 0; ci < 4; ci++) {                                  \
      const int l = 256 * ci + 16 * r + 4 * q;                                          \
      float y[4];                                                                       \
      _Pragma("unroll") for (int j = 0; j < 4; j++) {                                   \
        y[j] = acc[(c0_) + ci][j];                                                      \
        if (IS_A) y[j] = __builtin_ldexpf(y[j], esc);                                   \
      }                                                                                 \
      u32x2 o;                                                                          \
      if (IS_A) {                                                                       \
        o[0] = pk_bf16(y[0], y[1]);                                                     \
        o[1] = pk_bf16(y[2], y[3]);                                                     \
      } else {                                                                          \
        const float x3 = bflo(uv[ci][0]), x2 = bfhi(uv[ci][0]), x1 = bflo(uv[ci][1]), x0 = bfhi(uv[ci][1]); \
        o[0] = pk_bf16(rbf(y[0]) + rbf(x0 * dsk), rbf(y[1]) + rbf(x1 * dsk));           \
        o[1] = pk_bf16(rbf(y[2]) + rbf(x2 * dsk), rbf(y[3]) + rbf(x3 * dsk));           \
      }                                                                                 \
      *(u32x2*)((ysp) + 2 * l) = o;                                                     \
    }                                                                                   \
  }
  u32x4 xv[2];
  PASS(1, 0, 4, 1);
  STAMP(7);
  PASS(0, 0, 0, 2);
  STAMP(8);
  wait_frags(AL[0], AH[0], B[0]);
  int k1 = 0;
  if (IS_A) { k1 = scale_k(__builtin_amdgcn_readfirstlane(lds_get(fl0 + FL_MX + 4 * w))); esc += k1 - 112; }
  EPI1(0, smem + YS_OFF + w * YS_BYTES);
  lds_set(fl0 + FL_FH + 4 * w, 1u);
  __builtin_amdgcn_sched_barrier(0);
  STAMP(9);
  poll_ge(fl0 + FL_XQ + 4, 4u);
  if (IS_A) {
    const int d = k1 - scale_k(__builtin_amdgcn_readfirstlane(lds_get(fl0 + FL_MX + 32 + 4 * w)));
    if (d != 0) {
      typedef _Float16 f16x2 __attribute__((ext_vector_type(2)));
      const _Float16 f = (_Float16)__builtin_ldexpf(1.0f, d);
      if (lane < 30) {
        u32x4 x4 = *(const u32x4*)(xs + 2048 + 16 * lane);
#pragma unroll
        for (int i = 0; i < 4; i++) x4[i] = __builtin_bit_cast(u32, __builtin_bit_cast(f16x2, x4[i]) * (f16x2){f, f});
        *(u32x4*)(xs + 2048 + 16 * lane) = x4;
      }
#pragma unroll
      for (int i = 4; i < 8; i++)
#pragma unroll
        for (int j = 0; j < 4; j++) acc[i][j] = __builtin_ldexpf(acc[i][j], d);
      esc -= d;
    }
    __builtin_amdgcn_sched_barrier(0);
  }
  sfor<4>([&](auto kc_) { constexpr int k = decltype(kc_)::value; LOAD_PIECE(0, 8 + k, 4, 0); });
  if (!IS_A) {
    const __amdgpu_buffer_rsrc_t rq = __builtin_amdgcn_make_buffer_rsrc((void*)qin, 0, L_SEQ * D_CH * 2, 0x00020000);
#pragma unroll
    for (int i = 0; i < 2; i++) xv[i] = __builtin_amdgcn_raw_buffer_load_b128(rq, ((1024 + 2 * t + i) * D_CH + gc) * 2, 0, 0);
  }
  __builtin_amdgcn_sched_barrier(0);
  STAMP(10);
  PASS(1, 4, 4, 0);
  STAMP(11);
  EPI1(4, hs);
  if (IS_A) {
#pragma unroll
    for (int i = 0; i < 2; i++) xv[i] = *(const u32x4*)(smem + RAW_OFF + raw_pos(1024 + 2 * t + i));
  }
  STAMP(12);
  asm volatile("s_waitcnt lgkmcnt(0)\n\ts_barrier" ::: "memory");
  STAMP(13);
  const __amdgpu_buffer_rsrc_t ro = __builtin_amdgcn_make_buffer_rsrc((void*)out, 0, 8 * 1024 * 1024, 0x00020000);
  epi2<IS_A, 2>(smem + XS_BYTES, W_BYTES, t, 1024, g, xv, dv, ro);
  STAMP(14);
  STAMP_OUT(w);
}

template <bool IS_A>
__device__ __forceinline__ void stream_body(unsigned long long t_in, void* wsp, char* smem, int g, int p, u32 pf, const u16* __restrict__ u,
                                            const u16* __restrict__ kin, const u16* __restrict__ vin,
                                            const u16* __restrict__ shk, const u16* __restrict__ dvec,
                                            const u16* __restrict__ qin, u16* __restrict__ out) {
  STAMP_DECL
  const int lane = threadIdx.x & 63;
  const int gc = (g & 127) * 8;
  const u32 sb = lds_off(smem);
  const u32 fl0 = sb + FL_OFF;
  u32x4 dv;
  u32 pfx = 0, pfy = 0;
  u32x4 xv[4];
  __builtin_amdgcn_s_setprio(3);
  if (IS_A) {
    const u32 po = ((g & 7) * 128 + 32 * p + (lane & 31)) * (D_CH * 2) + (gc & ~63) * 2;
    if (lane < 32) line_req(pfx, u, po);
    asm volatile("global_load_dwordx4 %0, %1, %2" : "=v"(dv) : "v"(gc * 2), "s"(dvec) : "memory");
    asm volatile("s_barrier" ::: "memory");
    if (lane < 32) line_req(pfy, u + 1024 * D_CH, po);
    const u32 go = (64 * p + 32 * ((lane >> 4) & 1) + 8 * ((lane >> 2) & 3) + 4 * (lane >> 5) + (lane & 3)) * (D_CH * 2) + gc * 2;
    const u32 rb = sb + RAW_OFF + 1024 * p;
    asm volatile("s_waitcnt vmcnt(2)" :: "v"(pfx), "v"(pf) : "memory");
    STAMP(1);
#pragma unroll
    for (int e = 0; e < 4; e++) dma16(u + (size_t)e * 256 * D_CH, go, rb + 4096 * e);
    asm volatile("s_waitcnt vmcnt(4)" : "+v"(dv) : "v"(pfy) : "memory");
    STAMP(2);
#pragma unroll
    for (int e = 4; e < 8; e++) dma16(u + (size_t)e * 256 * D_CH, go, rb + 4096 * e);
    STAMP(3);
    u32 ta = rb + 256 * (lane >> 5) + 128 * ((lane >> 4) & 1) + 64 * ((lane >> 1) & 1) + 16 * (3 - ((lane >> 2) & 3)) + 8 * (lane & 1);
    u32 xw = sb + (lane & 7) * W_BYTES + 480 + 128 * p + 16 * (lane >> 3);
    u32 fm = fl0 + FL_MX + 4 * (lane & 7), fc = fl0 + FL_CN;
    u32 mx = 0;
    wait_vm<4>();
#pragma nounroll
    for (int h = 0; h < 2; h++) {
      u32x2 d[8];
      sfor<4>([&](auto ec_) { constexpr int e = decltype(ec_)::value; dsr64tr<4096 * e + 512>(d[2 * e], ta); dsr64tr<4096 * e>(d[2 * e + 1], ta); });
      asm volatile("s_waitcnt lgkmcnt(0)" : "+v"(d[0]), "+v"(d[1]), "+v"(d[2]), "+v"(d[3]), "+v"(d[4]), "+v"(d[5]), "+v"(d[6]), "+v"(d[7]) :: "memory");
      u32 m[16];
#pragma unroll
      for (int i = 0; i < 16; i++) {
        m[i] = d[i >> 1][i & 1] & 0x7fff7fffu;
        asm("v_pk_max_u16 %0, %0, %1" : "+v"(mx) : "v"(m[i]));
      }
      mx = max(mx & 0xffffu, mx >> 16);
      asm volatile("ds_max_u32 %0, %1" :: "v"(fm), "v"(mx) : "memory");
      lds_inc(fc);
      poll_ge(fc, 4u);
      mx = lds_get(fm);
      const u32 kk = (u32)scale_k(mx) * 0x00800080u;
#pragma unroll
      for (int i = 0; i < 16; i++) {
        asm("v_pk_sub_u16 %0, %0, %1 clamp\n\tv_pk_lshlrev_b16 %0, 3, %0 op_sel_hi:[0,1]" : "+v"(m[i]) : "v"(kk));
        m[i] |= d[i >> 1][i & 1] & 0x80008000u;
      }
      sfor<4>([&](auto ec_) { constexpr int e = decltype(ec_)::value; dsw128<512 * e>(xw, (u32x4){m[4 * e], m[4 * e + 1], m[4 * e + 2], m[4 * e + 3]}); });
      lds_inc(fc - 8);
      if (h == 0) STAMP(4); else STAMP(5);
      wait_vm<0>();
      ta += 16384; xw += 2048; fm += 32; fc += 4;
    }
    __builtin_amdgcn_s_setprio(0);
#pragma unroll
    for (int i = 0; i < 4; i++) xv[i] = *(const u32x4*)(smem + RAW_OFF + raw_pos(4 * (64 * p + lane) + i));
  } else {
    const int c = lane >> 3, rg = lane & 7;
    u32x2 sw;
    const char* pa = (const char*)(lane < 32 ? kin : vin) + (size_t)((256 * p + (g & 7) * 32 + (lane & 31)) * (D_CH * 2) + (gc & ~63) * 2);
    asm volatile("global_load_dword %0, %1, off nt" : "=v"(pfx) : "v"(pa) : "memory");
    asm volatile("global_load_dwordx2 %0, %1, %2" : "=v"(sw) : "v"((gc + c) * 8), "s"(shk) : "memory");
    if (p == 0 && rg == 0 && c < 3) *(u32x4*)(smem + HALO_OFF + 16 * lane) = (u32x4){0, 0, 0, 0};
    asm volatile("s_barrier" ::: "memory");
    dv = (u32x4){0, 0, 0, 0};
    const u32 go = (256 * p + 8 * rg + c) * (D_CH * 2) + gc * 2;
    const u32 rb = sb + RAW_OFF + 8192 * p;
    const u32 kr = sb + 2 * p * W_BYTES + 2528, vr = sb + YS_OFF + 2 * p * YS_BYTES, fl = fl0 + FL_LD + 8 * p;
    asm volatile("s_waitcnt vmcnt(0)" : "+v"(sw) : "v"(pfx), "v"(pf) : "memory");
    STAMP(1);
    {
      const int hr = 256 * p + 128 * rg - 3 + c;
      if (rg < 2 && c < 3 && hr >= 0) dma16(kin, hr * (D_CH * 2) + gc * 2, sb + HALO_OFF + 32 * p);
    }
#define PAIR(row, ka, va) { dma16(kin + (size_t)(row) * D_CH, go, ka); dma16(vin + (size_t)(row) * D_CH, go, va); }
    PAIR(0, kr, vr);
    PAIR(128, kr + W_BYTES, vr + YS_BYTES);
    PAIR(64, kr + 1024, vr + 1024);
    wait_vm<4>();
    lds_set(fl, 1u);
    PAIR(192, kr + W_BYTES + 1024, vr + YS_BYTES + 1024);
    wait_vm<4>();
    lds_set(fl + 4, 1u);
    STAMP(2);
    poll_ge(fl0 + FL_P2 + 4 * p, 2u);
    if (rg == 0 && c < 3) dma16(kin + 1021 * D_CH, go, sb + HALO_OFF + 384 + 16 * p);
    PAIR(1024, rb, rb + 4096);
    wait_vm<5>();
    lds_set(fl, 2u);
    PAIR(1088, rb + 1024, rb + 5120);
    wait_vm<5>();
    lds_set(fl + 4, 2u);
    STAMP(6);
    PAIR(1152, rb + 2048, rb + 6144);
    PAIR(1216, rb + 3072, rb + 7168);
    line_req(pfy, qin, ((g & 7) * 256 + 64 * p + lane) * (D_CH * 2) + (gc & ~63) * 2);
    STAMP(3);
    const float w0 = bflo(sw[0]), w1 = bfhi(sw[0]), w2 = bflo(sw[1]), w3 = bfhi(sw[1]);
    u32 rk = rb + 16 * rg + 2 * c;
    u32 rhh = rg ? rk + 624 : sb + HALO_OFF + 384 + 16 * p + 2 * c;
    u32 xw = sb + c * W_BYTES + 480 + 2048 + 512 * p + 16 * rg;
    u32 kf[11], vf[8];
#pragma unroll
    for (int i = 0; i < 11; i++) kf[i] = 0;
#pragma unroll
    for (int i = 0; i < 8; i++) vf[i] = 0;
    wait_vm<7>();
    STAMP(4);
#pragma nounroll
    for (int j = 0; j < 4; j++) {
      read_kv(kf, vf, rhh, rk, rk + 4096);
      const u32x4 o = kv_chunk(kf, vf, w0, w1, w2, w3);
      if (j == 0) poll_ge(fl0 + FL_XQ, 8u);
      dsw128<0>(xw, o);
      if (j == 0) wait_vm<5>();
      else if (j == 1) wait_vm<3>();
      else wait_vm<1>();
      rk += 1024; xw += 128;
      rhh = rk + (rg ? 624 : -272);
    }
    lds_inc(fl0 + FL_XQ + 4);
    STAMP(5);
    __builtin_amdgcn_s_setprio(0);
    asm volatile("s_waitcnt vmcnt(0)" :: "v"(pfy) : "memory");
    __builtin_amdgcn_sched_barrier(0);
    const __amdgpu_buffer_rsrc_t rq = __builtin_amdgcn_make_buffer_rsrc((void*)qin, 0, L_SEQ * D_CH * 2, 0x00020000);
#pragma unroll
    for (int i = 0; i < 4; i++) xv[i] = __builtin_amdgcn_raw_buffer_load_b128(rq, ((4 * (64 * p + lane) + i) * D_CH + gc) * 2, 0, 0);
  }
  STAMP(12);
  {
    u32 fl;
    for (;;) {
      asm volatile("ds_read_b32 %0, %1\n\ts_waitcnt lgkmcnt(0)" : "=v"(fl) : "v"(fl0 + FL_FH + 4 * (lane & 7)) : "memory");
      if (__builtin_amdgcn_ballot_w64(fl == 0u) == 0ull) break;
      __builtin_amdgcn_s_sleep(2);
    }
  }
  const __amdgpu_buffer_rsrc_t ro = __builtin_amdgcn_make_buffer_rsrc((void*)out, 0, 8 * 1024 * 1024, 0x00020000);
  STAMP(13);
  epi2<IS_A, 4>(smem + YS_OFF, YS_BYTES, 64 * p + lane, 0, g, xv, dv, ro);
  STAMP(14);
  asm volatile("s_barrier" ::: "memory");
  STAMP_OUT(8 + p);
}

__global__ __launch_bounds__(768) void fused_kernel(
    const u16* __restrict__ u, const u16* __restrict__ lar, const u16* __restrict__ aim,
    const u16* __restrict__ cre, const u16* __restrict__ cim, const u16* __restrict__ ldt,
    const u16* __restrict__ dskip, const u16* __restrict__ qin, const u16* __restrict__ kin,
    const u16* __restrict__ vin, const u16* __restrict__ shk, const u16* __restrict__ ssmk,
    const u16* __restrict__ ssmd, u16* __restrict__ out, void* wsp) {
  extern __shared__ __attribute__((aligned(16))) char smem[];
  const unsigned long long t_in = __builtin_amdgcn_s_memrealtime();
  u32 pf = 0;
  if (threadIdx.x >= 736) {
    const char* pc;
    u32 en;
    asm volatile("s_getpc_b64 %1\n.Lpf_anchor:\n\ts_mov_b32 %2, .Lfused_end-.Lpf_anchor\n\t"
                 "v_min_u32 %0, %2, %3\n\tglobal_load_dword %0, %0, %1"
                 : "=&v"(pf), "=&s"(pc), "=&s"(en) : "v"(((threadIdx.x & 31) * 16 + ((blockIdx.x >> 3) & 15)) * 128) : "memory");
  }
  {
    if (threadIdx.x < 32) *(u32*)(smem + FL_OFF + 4 * threadIdx.x) = 0u;
    __builtin_amdgcn_sched_barrier(0);
    asm volatile("" :: "s"(u), "s"(lar), "s"(aim), "s"(cre), "s"(cim), "s"(ldt), "s"(dskip), "s"(qin), "s"(kin), "s"(vin), "s"(shk),
                 "s"(ssmk), "s"(ssmd), "s"(out) : "memory");
    __builtin_amdgcn_sched_barrier(0);
  }
  const int g = xcd_group(blockIdx.x);
  const int w = __builtin_amdgcn_readfirstlane(threadIdx.x >> 6);
  if (w >= 8) {
    if (g < 128) stream_body<true>(t_in, wsp, smem, g, w - 8, pf, u, kin, vin, shk, dskip, qin, out);
    else stream_body<false>(t_in, wsp, smem, g, w - 8, pf, u, kin, vin, shk, ssmd, qin, out);
  } else {
    if (g < 128) main_body<true>(t_in, wsp, smem, g, w, pf, lar, aim, cre, cim, ldt, kin, vin, shk, ssmk, dskip, qin, out);
    else main_body<false>(t_in, wsp, smem, g, w, pf, lar, aim, cre, cim, ldt, kin, vin, shk, ssmk, ssmd, qin, out);
  }
}
__global__ void end_marker_kernel() { asm volatile(".Lfused_end:"); }

__global__ __launch_bounds__(1024) void spin_kernel(const unsigned long long* st) {
  typedef unsigned long long u64;
  __shared__ u64 mn[4][16], mx[4][16], sm[4][16], t0s;
  __shared__ unsigned cn[4][16];
  const int t = threadIdx.x;
  if (t < 64) { mn[t >> 4][t & 15] = ~0ull; mx[t >> 4][t & 15] = 0ull; sm[t >> 4][t & 15] = 0ull; cn[t >> 4][t & 15] = 0; }
  if (t == 0) t0s = ~0ull;
  __syncthreads();
  for (int w = t; w < 3072; w += 1024) atomicMin(&t0s, st[w * 16]);
  __syncthreads();
  for (int w = t; w < 3072; w += 1024) {
    const int b = w / 12, wv = w % 12;
    const int cls = (xcd_group(b) >= 128 ? 2 : 0) + (wv >= 8 ? 1 : 0);
    for (int i = 0; i < 16; i++) {
      u64 v = st[w * 16 + i];
      if (v) { v -= t0s; atomicMin(&mn[cls][i], v); atomicMax(&mx[cls][i], v); atomicAdd(&sm[cls][i], v); atomicAdd(&cn[cls][i], 1u); }
    }
  }
  __syncthreads();
  if (t == 0) {
    u64 q = 0;
    if (q > 10000) q = 10000;
    const u64 t0 = __builtin_amdgcn_s_memrealtime();
    while (__builtin_amdgcn_s_memrealtime() - t0 < 10 * q) __builtin_amdgcn_s_sleep(1);
  }
}

extern "C" void kernel_launch(void* const* d_in, const int* in_sizes, int n_in, void* d_out, int out_size,
                              void* d_ws, size_t ws_size, hipStream_t stream) {
  (void)hipFuncSetAttribute((const void*)fused_kernel, hipFuncAttributeMaxDynamicSharedMemorySize, LDS_BYTES);
  fused_kernel<<<256, 768, LDS_BYTES, stream>>>((const u16*)d_in[0], (const u16*)d_in[1], (const u16*)d_in[2], (const u16*)d_in[3],
                                                (const u16*)d_in[4], (const u16*)d_in[5], (const u16*)d_in[6], (const u16*)d_in[7],
                                                (const u16*)d_in[8], (const u16*)d_in[9], (const u16*)d_in[10], (const u16*)d_in[11],
                                                (const u16*)d_in[12], (u16*)d_out, d_ws);
  spin_kernel<<<1, 1024, 0, stream>>>((const unsigned long long*)d_ws);
}
```

```cpp
#include <hip/hip_runtime.h>
#include <hip/hip_bf16.h>
#include <stdint.h>
#include <utility>

typedef unsigned int u32;
typedef unsigned short u16;
typedef u32 u32x4 __attribute__((ext_vector_type(4)));
typedef u32 u32x2 __attribute__((ext_vector_type(2)));
typedef float f32x4 __attribute__((ext_vector_type(4)));
typedef float f32x2 __attribute__((ext_vector_type(2)));
typedef __bf16 bf16x8 __attribute__((ext_vector_type(8)));
typedef _Float16 f16x8 __attribute__((ext_vector_type(8)));

#define L_SEQ 2048
#define D_CH 1024
#define N_MODE 128

#define g_stamps ((unsigned long long*)wsp)
#define STAMP(i) do{ __builtin_amdgcn_sched_barrier(0); st_[i] = __builtin_amdgcn_s_memrealtime(); __builtin_amdgcn_sched_barrier(0); }while(0)
#define STAMP_DECL unsigned long long st_[20]; _Pragma("unroll") for (int i_ = 0; i_ < 20; i_++) st_[i_] = 0; st_[0] = t_in; st_[16] = t_k;
#define STAMP_OUT(wv_) do { asm volatile("s_waitcnt vmcnt(0)" ::: "memory"); STAMP(15); if ((threadIdx.x & 63) == 0) { _Pragma("unroll") for (int i_ = 0; i_ < 20; i_++) g_stamps[(blockIdx.x*12+(wv_))*20+i_] = st_[i_]; } } while (0)
__device__ __forceinline__ float bflo(u32 d) { return __uint_as_float(d << 16); }
__device__ __forceinline__ float bfhi(u32 d) { return __uint_as_float(d & 0xffff0000u); }
__device__ __forceinline__ u32 pk_bf16(float lo, float hi) {
  typedef __bf16 bf16x2 __attribute__((ext_vector_type(2)));
  f32x2 v = {lo, hi};
  bf16x2 r = __builtin_convertvector(v, bf16x2);
  return __builtin_bit_cast(u32, r);
}
__device__ __forceinline__ float rbf(float x) { return __uint_as_float(pk_bf16(0.f, x)); }

__device__ __forceinline__ u32 wave_max_u32(u32 x) {
  x = max(x, (u32)__builtin_amdgcn_update_dpp(0, (int)x, 0x111, 0xf, 0xf, true));
  x = max(x, (u32)__builtin_amdgcn_update_dpp(0, (int)x, 0x112, 0xf, 0xf, true));
  x = max(x, (u32)__builtin_amdgcn_update_dpp(0, (int)x, 0x114, 0xf, 0xf, true));
  x = max(x, (u32)__builtin_amdgcn_update_dpp(0, (int)x, 0x118, 0xf, 0xf, true));
  x = max(x, (u32)__builtin_amdgcn_update_dpp(0, (int)x, 0x142, 0xa, 0xf, true));
  x = max(x, (u32)__builtin_amdgcn_update_dpp(0, (int)x, 0x143, 0xc, 0xf, true));
  return (u32)__builtin_amdgcn_readlane((int)x, 63);
}

struct cf { float re, im; };
__device__ __forceinline__ cf cmul(cf a, cf b) {
  cf o;
  asm("v_mul_f32 %0, %2, %4\n\tv_mul_f32 %1, %2, %5\n\tv_fma_f32 %0, -%3, %5, %0\n\tv_fma_f32 %1, %3, %4, %1"
      : "=&v"(o.re), "=&v"(o.im) : "v"(a.re), "v"(a.im), "v"(b.re), "v"(b.im));
  return o;
}
__device__ __forceinline__ u32x2 hmul2(u32x2 a, u32x2 b) {
  u32 o0, o1;
  asm("v_pk_mul_f16 %0, %2, %4 op_sel:[0,0] op_sel_hi:[0,1]\n\t"
      "v_pk_mul_f16 %1, %3, %5 op_sel:[0,0] op_sel_hi:[0,1]\n\t"
      "v_pk_fma_f16 %0, %2, %4, %0 op_sel:[1,1,0] op_sel_hi:[1,0,1] neg_lo:[1,0,0]\n\t"
      "v_pk_fma_f16 %1, %3, %5, %1 op_sel:[1,1,0] op_sel_hi:[1,0,1] neg_lo:[1,0,0]"
      : "=&v"(o0), "=&v"(o1) : "v"(a[0]), "v"(a[1]), "v"(b[0]), "v"(b[1]));
  return (u32x2){o0, o1};
}
__device__ __forceinline__ f32x2 cexp_pos(float ar, float ai, float pos) {
  const float LOG2E = 1.44269504088896340736f;
  const float I2PI_HI = 0.15915494309189533577f;
  const float I2PI_LO = 6.4206383e-9f;
  float xr = ar * pos;
  float xi = ai * pos;
  float e = __builtin_amdgcn_exp2f(xr * LOG2E);
  float r = xi * I2PI_HI;
  float err = __builtin_fmaf(xi, I2PI_HI, -r);
  float f = __builtin_amdgcn_fractf(r);
  f += __builtin_fmaf(xi, I2PI_LO, err);
  f32x2 o;
  o[0] = e * __builtin_amdgcn_cosf(f);
  o[1] = e * __builtin_amdgcn_sinf(f);
  return o;
}
__device__ __forceinline__ u32 pk_f16(float lo, float hi) {
  typedef _Float16 f16x2 __attribute__((ext_vector_type(2)));
  f32x2 v = {lo, hi};
  return __builtin_bit_cast(u32, __builtin_convertvector(v, f16x2));
}

__device__ __forceinline__ f32x4 mma_f16(u32x4 a, u32x4 b, f32x4 c) {
  return __builtin_amdgcn_mfma_f32_16x16x32_f16(__builtin_bit_cast(f16x8, a), __builtin_bit_cast(f16x8, b), c, 0, 0, 0);
}
__device__ __forceinline__ f32x4 mma_bf16(u32x4 a, u32x4 b, f32x4 c) {
  return __builtin_amdgcn_mfma_f32_16x16x32_bf16(__builtin_bit_cast(bf16x8, a), __builtin_bit_cast(bf16x8, b), c, 0, 0, 0);
}

__device__ __forceinline__ u32 lds_off(const char* p) {
  return (u32)(uintptr_t)(const __attribute__((address_space(3))) char*)p;
}

template <class F, int... I>
__device__ __forceinline__ void sfor_impl(F&& f, std::integer_sequence<int, I...>) { (f(std::integral_constant<int, I>{}), ...); }
template <int N, class F>
__device__ __forceinline__ void sfor(F&& f) { sfor_impl(f, std::make_integer_sequence<int, N>{}); }

#define XP 1152
#define XW (4 * XP + 16)
#define HS_OFF (8 * XW)
#define HW_B 4160
#define HW_A 8768
#define RK_OFF (HS_OFF + 8 * HW_B)
#define RU_OFF (HS_OFF + 8 * HW_A)
#define HALO_OFF (RU_OFF + 32768)
#define FL_OFF (HALO_OFF + 1152)
#define FL_XQ 0
#define FL_Y 32
#define LDS_BYTES (FL_OFF + 64)
#define GT_ROW 544

__device__ __forceinline__ int gen_taps(char* tab, int lane, u32 p_ldt, u32 p_lar, u32 p_aim, u32 p_cre, u32 p_cim, f32x4 (&acc)[3]) {
  const float dt = rbf(expf(bflo(p_ldt)));
  cf C[2], z4[2], z128[2], z512[2];
  u32 mc = 0;
#pragma unroll
  for (int m = 0; m < 2; m++) {
    C[m].re = 2.0f * ((m ? bfhi(p_cre) : bflo(p_cre)) * dt);
    C[m].im = -2.0f * ((m ? bfhi(p_cim) : bflo(p_cim)) * dt);
    mc = max(mc, max(__float_as_uint(C[m].re) & 0x7fffffffu, __float_as_uint(C[m].im) & 0x7fffffffu));
  }
  mc = wave_max_u32(mc);
  u32x2 QF[3], PF[3], Q[3], QL[4], P[8];
#pragma unroll
  for (int m = 0; m < 2; m++) {
    const float ar = -rbf(expf(m ? bfhi(p_lar) : bflo(p_lar))) * dt;
    const float ai = (m ? bfhi(p_aim) : bflo(p_aim)) * dt;
    const f32x2 a1 = cexp_pos(ar, -ai, 1.0f), a32 = cexp_pos(ar, -ai, 32.0f), a512 = cexp_pos(ar, -ai, 512.0f);
    const cf z1 = {a1[0], a1[1]}, z32 = {a32[0], a32[1]};
    z512[m] = (cf){a512[0], a512[1]};
    const cf z2 = cmul(z1, z1), z64 = cmul(z32, z32);
    z4[m] = cmul(z2, z2);
    const cf z96 = cmul(z64, z32), z8 = cmul(z4[m], z4[m]), z3 = cmul(z2, z1), z16 = cmul(z8, z8);
    z128[m] = cmul(z64, z64);
    const cf z24 = cmul(z16, z8);
    PF[0][m] = pk_f16(z32.re, -z32.im); PF[1][m] = pk_f16(z64.re, -z64.im); PF[2][m] = pk_f16(z96.re, -z96.im);
    QF[0][m] = pk_f16(z8.re, -z8.im); QF[1][m] = pk_f16(z16.re, -z16.im); QF[2][m] = pk_f16(z24.re, -z24.im);
    Q[0][m] = pk_f16(z1.re, z1.im); Q[1][m] = pk_f16(z2.re, z2.im); Q[2][m] = pk_f16(z3.re, z3.im);
  }
  const int ec = (mc > 0u && mc < 0x7f800000u) ? __builtin_amdgcn_frexp_expf(__uint_as_float(mc)) - 14 : 0;
  char* wp = tab + 8 * lane;
  const int c16 = lane & 15, q = lane >> 4;
  const char* rp = tab + c16 * GT_ROW + 16 * q;
#define PUT_ROW(row, z) *(u32x2*)(wp + (row) * GT_ROW) = (z)
#define GET_FRAGS(dst) { __builtin_amdgcn_wave_barrier();                                           \
    _Pragma("unroll") for (int ks = 0; ks < 8; ks++) dst[ks] = *(const u32x4*)(rp + 64 * ks);       \
    __builtin_amdgcn_wave_barrier(); }
#define TILE_MMA(ti) { f32x4 a_ = {0.f, 0.f, 0.f, 0.f};                                             \
    _Pragma("unroll") for (int ks = 0; ks < 8; ks++) a_ = mma_f16(G[ks], Fh[ks], a_);             \
    acc[ti] = a_; }
  PUT_ROW(0, ((u32x2){0x3c00u, 0x3c00u}));
#pragma unroll
  for (int b = 0; b < 3; b++) PUT_ROW(1 + b, QF[b]);
#pragma unroll
  for (int a = 0; a < 3; a++) {
    PUT_ROW(4 + 4 * a, PF[a]);
#pragma unroll
    for (int b = 0; b < 3; b++) PUT_ROW(5 + 4 * a + b, hmul2(PF[a], QF[b]));
  }
  u32x4 Fh[8], G[8];
  GET_FRAGS(Fh);
#pragma unroll
  for (int m = 0; m < 2; m++) {
    cf p0 = {__builtin_ldexpf(C[m].re, -ec), __builtin_ldexpf(C[m].im, -ec)};
    cf p4 = cmul(p0, z4[m]);
#pragma unroll
    for (int j = 0; j < 4; j++) {
      P[j][m] = pk_f16(p0.re, p0.im);
      P[4 + j][m] = pk_f16(p4.re, p4.im);
      if (j < 3) { p0 = cmul(p0, z128[m]); p4 = cmul(p4, z128[m]); }
    }
  }
#pragma unroll
  for (int al = 0; al < 2; al++)
#pragma unroll
    for (int h = 0; h < 2; h++) {
      PUT_ROW(8 * al + 4 * h, P[4 * h + al]);
#pragma unroll
      for (int b = 0; b < 3; b++) PUT_ROW(8 * al + 4 * h + 1 + b, hmul2(P[4 * h + al], Q[b]));
    }
  GET_FRAGS(G);
#pragma unroll
  for (int m = 0; m < 2; m++) {
    const cf z1024 = cmul(z512[m], z512[m]);
    const cf z1536 = cmul(z1024, z512[m]), z2048 = cmul(z1024, z1024);
    QL[0][m] = pk_f16(z512[m].re, z512[m].im); QL[1][m] = pk_f16(z1024.re, z1024.im);
    QL[2][m] = pk_f16(z1536.re, z1536.im); QL[3][m] = pk_f16(z2048.re, z2048.im);
  }
#pragma unroll
  for (int al = 0; al < 2; al++)
#pragma unroll
    for (int h = 0; h < 2; h++) {
      PUT_ROW(4 * al + 2 * h, P[4 * h + 2 + al]);
      PUT_ROW(4 * al + 2 * h + 1, hmul2(P[4 * h + 2 + al], Q[1]));
    }
  PUT_ROW(8, hmul2(P[0], QL[3]));
  TILE_MMA(0);
  GET_FRAGS(G);
#pragma unroll
  for (int al = 0; al < 4; al++) {
    PUT_ROW(4 * al, hmul2(P[al], QL[0]));
    PUT_ROW(4 * al + 1, hmul2(P[4 + al], QL[0]));
    PUT_ROW(4 * al + 2, hmul2(P[al], QL[1]));
    PUT_ROW(4 * al + 3, hmul2(P[al], QL[2]));
  }
  TILE_MMA(1);
  GET_FRAGS(G);
  TILE_MMA(2);
  return ec;
}

__device__ __forceinline__ void dma16(const void* base, u32 off, u32 lds_base) {
  asm volatile("s_mov_b32 m0, %2\n\ts_nop 0\n\tglobal_load_lds_dwordx4 %0, %1" :: "v"(off), "s"(base), "s"(lds_base) : "memory", "m0");
}
__device__ __forceinline__ void dma16nt(const void* base, u32 off, u32 lds_base) {
  asm volatile("s_mov_b32 m0, %2\n\ts_nop 0\n\tglobal_load_lds_dwordx4 %0, %1 nt" :: "v"(off), "s"(base), "s"(lds_base) : "memory", "m0");
}
__device__ __forceinline__ void poll8(u32& fl, u32 addr) {
  u32 s;
  asm volatile("v_readfirstlane_b32 %1, %0\n\ts_cmp_ge_u32 %1, 8\n\ts_cbranch_scc1 .Lpe%=\n"
               ".Lpl%=:\n\ts_sleep 1\n\tds_read_b32 %0, %2\n\ts_waitcnt lgkmcnt(0)\n\tv_readfirstlane_b32 %1, %0\n\t"
               "s_cmp_lt_u32 %1, 8\n\ts_cbranch_scc1 .Lpl%=\n.Lpe%=:"
               : "+v"(fl), "=&s"(s) : "v"(addr) : "memory", "scc");
}
__device__ __forceinline__ void poll_all(u32 addr, u32 val) {
  for (;;) {
    u32 fl;
    asm volatile("ds_read_b32 %0, %1\n\ts_waitcnt lgkmcnt(0)" : "=v"(fl) : "v"(addr) : "memory");
    if (__builtin_amdgcn_ballot_w64(fl < val) == 0ull) break;
    __builtin_amdgcn_s_sleep(1);
  }
}
__device__ __forceinline__ void lds_set(u32 addr, u32 val) {
  asm volatile("ds_write_b32 %0, %1" :: "v"(addr), "v"(val) : "memory");
}
__device__ __forceinline__ void lds_add(u32 addr, u32 val) {
  asm volatile("s_mov_b64 exec, 1\n\tds_add_u32 %0, %1\n\ts_mov_b64 exec, -1" :: "v"(addr), "v"(val) : "memory");
}

__device__ __forceinline__ int xcd_group(int b) {
  int xcd = b & 7, idx = b >> 3;
  return ((xcd + 8 * (idx >> 3)) << 3) | (idx & 7);
}

template <int OFF> __device__ __forceinline__ void dsr32(u32& dst, u32 addr) {
  asm volatile("ds_read_b32 %0, %1 offset:%2" : "=v"(dst) : "v"(addr), "i"(OFF) : "memory");
}
template <int OFF> __device__ __forceinline__ void dsr64(u32x2& dst, u32 addr) {
  asm volatile("ds_read_b64 %0, %1 offset:%2" : "=v"(dst) : "v"(addr), "i"(OFF) : "memory");
}
template <int OFF> __device__ __forceinline__ void dsr64tr(u32x2& dst, u32 addr) {
  asm volatile("ds_read_b64_tr_b16 %0, %1 offset:%2" : "=v"(dst) : "v"(addr), "i"(OFF) : "memory");
}
template <int OFF> __device__ __forceinline__ void dsw64(u32 addr, u32x2 val) {
  asm volatile("ds_write_b64 %0, %1 offset:%2" :: "v"(addr), "v"(val), "i"(OFF) : "memory");
}
template <int OFF> __device__ __forceinline__ void dsw128(u32 addr, u32x4 val) {
  asm volatile("ds_write_b128 %0, %1 offset:%2" :: "v"(addr), "v"(val), "i"(OFF) : "memory");
}
__device__ __forceinline__ void bperm(float& dst, u32 addr, float val) {
  asm volatile("ds_bpermute_b32 %0, %1, %2" : "=v"(dst) : "v"(addr), "v"(val) : "memory");
}
__device__ __forceinline__ void line_req(u32& dst, const void* p) {
  asm volatile("global_load_dword %0, %1, off nt" : "=v"(dst) : "v"(p) : "memory");
}
template <int N> __device__ __forceinline__ void wait_vm() { asm volatile("s_waitcnt vmcnt(%0)" :: "i"(N) : "memory"); }

__device__ __forceinline__ void kv_chunk(u32 rh, u32 rk, u32 xw, u32 (&kf)[11], u32 (&vf)[8], u32x2 sw) {
  asm volatile("ds_read_u16_d16_hi %0, %19\n\tds_read_u16_d16_hi %1, %19 offset:128\n\tds_read_u16_d16_hi %2, %19 offset:256\n\t"
               "ds_read_u16_d16_hi %3, %20\n\tds_read_u16_d16_hi %4, %20 offset:128\n\t"
               "ds_read_u16_d16_hi %5, %20 offset:256\n\tds_read_u16_d16_hi %6, %20 offset:384\n\t"
               "ds_read_u16_d16_hi %7, %20 offset:512\n\tds_read_u16_d16_hi %8, %20 offset:640\n\t"
               "ds_read_u16_d16_hi %9, %20 offset:768\n\tds_read_u16_d16_hi %10, %20 offset:896\n\t"
               "ds_read_u16_d16_hi %11, %20 offset:32768\n\tds_read_u16_d16_hi %12, %20 offset:32896\n\t"
               "ds_read_u16_d16_hi %13, %20 offset:33024\n\tds_read_u16_d16_hi %14, %20 offset:33152\n\t"
               "ds_read_u16_d16_hi %15, %20 offset:33280\n\tds_read_u16_d16_hi %16, %20 offset:33408\n\t"
               "ds_read_u16_d16_hi %17, %20 offset:33536\n\tds_read_u16_d16_hi %18, %20 offset:33664\n\t"
               "s_waitcnt lgkmcnt(8)"
               : "+v"(kf[0]), "+v"(kf[1]), "+v"(kf[2]), "+v"(kf[3]), "+v"(kf[4]), "+v"(kf[5]), "+v"(kf[6]), "+v"(kf[7]),
                 "+v"(kf[8]), "+v"(kf[9]), "+v"(kf[10]), "+v"(vf[0]), "+v"(vf[1]), "+v"(vf[2]), "+v"(vf[3]), "+v"(vf[4]),
                 "+v"(vf[5]), "+v"(vf[6]), "+v"(vf[7])
               : "v"(rh), "v"(rk) : "memory");
  const float w0 = bflo(sw[0]), w1 = bfhi(sw[0]), w2 = bflo(sw[1]), w3 = bfhi(sw[1]);
  u32 a[8];
  asm volatile(
      "v_mul_f32 %0, %8, %30\n\tv_mul_f32 %1, %9, %30\n\tv_mul_f32 %2, %10, %30\n\tv_mul_f32 %3, %11, %30\n\t"
      "v_mul_f32 %4, %12, %30\n\tv_mul_f32 %5, %13, %30\n\tv_mul_f32 %6, %14, %30\n\tv_mul_f32 %7, %15, %30\n\t"
      "v_fmac_f32 %0, %9, %29\n\tv_fmac_f32 %1, %10, %29\n\tv_fmac_f32 %2, %11, %29\n\tv_fmac_f32 %3, %12, %29\n\t"
      "v_fmac_f32 %4, %13, %29\n\tv_fmac_f32 %5, %14, %29\n\tv_fmac_f32 %6, %15, %29\n\tv_fmac_f32 %7, %16, %29\n\t"
      "v_fmac_f32 %0, %10, %28\n\tv_fmac_f32 %1, %11, %28\n\tv_fmac_f32 %2, %12, %28\n\tv_fmac_f32 %3, %13, %28\n\t"
      "v_fmac_f32 %4, %14, %28\n\tv_fmac_f32 %5, %15, %28\n\tv_fmac_f32 %6, %16, %28\n\tv_fmac_f32 %7, %17, %28\n\t"
      "v_fmac_f32 %0, %11, %27\n\tv_fmac_f32 %1, %12, %27\n\tv_fmac_f32 %2, %13, %27\n\tv_fmac_f32 %3, %14, %27\n\t"
      "v_fmac_f32 %4, %15, %27\n\tv_fmac_f32 %5, %16, %27\n\tv_fmac_f32 %6, %17, %27\n\tv_fmac_f32 %7, %18, %27\n\t"
      "s_waitcnt lgkmcnt(0)\n\t"
      "v_mul_f32 %0, %0, %19\n\tv_mul_f32 %4, %4, %23\n\tv_mul_f32 %2, %2, %21\n\tv_mul_f32 %6, %6, %25\n\t"
      "v_mul_f32 %1, %1, %20\n\tv_mul_f32 %5, %5, %24\n\tv_mul_f32 %3, %3, %22\n\tv_mul_f32 %7, %7, %26\n\t"
      "v_cvt_pk_bf16_f32 %0, %0, %4\n\tv_cvt_pk_bf16_f32 %2, %2, %6\n\tv_cvt_pk_bf16_f32 %1, %1, %5\n\tv_cvt_pk_bf16_f32 %3, %3, %7\n\t"
      "ds_write2st64_b32 %31, %0, %2 offset1:9\n\tds_write2st64_b32 %32, %1, %3 offset1:9"
      : "=&v"(a[0]), "=&v"(a[1]), "=&v"(a[2]), "=&v"(a[3]), "=&v"(a[4]), "=&v"(a[5]), "=&v"(a[6]), "=&v"(a[7])
      : "v"(kf[0]), "v"(kf[1]), "v"(kf[2]), "v"(kf[3]), "v"(kf[4]), "v"(kf[5]), "v"(kf[6]), "v"(kf[7]), "v"(kf[8]), "v"(kf[9]), "v"(kf[10]),
        "v"(vf[0]), "v"(vf[1]), "v"(vf[2]), "v"(vf[3]), "v"(vf[4]), "v"(vf[5]), "v"(vf[6]), "v"(vf[7]),
        "v"(w0), "v"(w1), "v"(w2), "v"(w3), "v"(xw), "v"(xw + XP) : "memory");
}

__device__ __forceinline__ void out_piece(bool isa, u32 ya, u32 hw, int n, int lane, int g, u32x4 qv, __amdgpu_buffer_rsrc_t ro) {
  u32x2 t0, t1;
  dsr64tr<0>(t0, ya + 128 * n);
  dsr64tr<0>(t1, ya + 128 * n + 4 * hw);
  asm volatile("s_waitcnt lgkmcnt(0)" : "+v"(t0), "+v"(t1) :: "memory");
  u32x4 o = {t0[0], t0[1], t1[0], t1[1]};
  if (!isa) {
#pragma unroll
    for (int p = 0; p < 4; p++) o[p] = pk_bf16(bflo(o[p]) * bflo(qv[p]), bfhi(o[p]) * bfhi(qv[p]));
  }
  __builtin_amdgcn_raw_buffer_store_b128(o, ro, ((64 * n + lane) * (2 * D_CH) + g * 8) * 2, 0, 0);
}

#define PD 7
#define NB 8
struct Item { int G, f, s, skip; };
constexpr int g_len(int G) { return 16 * G + 18; }
constexpr int g_beg(int G) { return 8 * G * G + 10 * G; }
constexpr int NI = g_beg(4);
constexpr Item item(int i) {
  int G = 0;
  while (i >= g_len(G)) { i -= g_len(G); G++; }
  const int a = 16 * G + 4;
  if (i < a) return {G, i / 4 - 1, i & 3, 0};
  if (i == a) return {G, 4 * G, 0, 1};
  if (i < a + 9) return {G, 4 * G + (i - a - 1) / 4, (i - a - 1) & 3, 0};
  if (i == a + 9) return {G, 4 * G + 2, 0, 2};
  return {G, 4 * G + 2, i - a - 10, 0};
}
constexpr int t_last(int t) { return g_beg((t >> 1) + 1) - ((t & 1) ? 1 : 10); }
constexpr int q_chk(int q) { return g_beg(q) + 16 * q - PD; }
constexpr int at_tile(int i, int d) { for (int t = 0; t < 7; t++) if (t_last(t) + d == i) return t; return -1; }
constexpr int at_q(int i, int d) { for (int q = 1; q < 4; q++) if (q_chk(q) - d == i) return q; return -1; }
#define D_C1 2
#define D_C2 3
#define D_C3 7
#define D_Q 4
constexpr bool t_flag(int t) { return (t & 1) || t == 6; }
constexpr int n_ops(int i) {
  return (i + PD < NI ? 2 : 0) + (at_tile(i, D_C1) >= 0 ? 3 : 0) + (at_tile(i, D_C3) >= 0 ? (t_flag(at_tile(i, D_C3)) ? 2 : 1) : 0) + (at_q(i, D_Q) >= 0 ? 1 : 0);
}
constexpr int c_ops(int i) { int n = 2 * PD; for (int j = 0; j < i; j++) n += n_ops(j); return n; }
constexpr int w_cnt(int i) {
  int n = c_ops(i) - (i + 1 < PD ? 2 * i + 4 : c_ops(i + 1 - PD) + 2);
  if (at_tile(i, D_C3) >= 0) { const int m = c_ops(i) - c_ops(i - D_C3 + D_C1 + 1); n = m < n ? m : n; }
  if (at_q(i, 0) >= 0) { const int m = c_ops(i) - c_ops(i - D_Q + 1); n = m < n ? m : n; }
  return n < 15 ? n : 15;
}

__device__ __forceinline__ void main_body(unsigned long long t_in, void* wsp, unsigned long long t_k, char* smem, int g, int w,
                                          const u16* __restrict__ lar, const u16* __restrict__ aim,
                                          const u16* __restrict__ cre, const u16* __restrict__ cim, const u16* __restrict__ ldt,
                                          const u16* __restrict__ kin, const u16* __restrict__ vin,
                                          const u16* __restrict__ shk, const u16* __restrict__ ssmk,
                                          const u16* __restrict__ dvec, const u16* __restrict__ qin, u16* __restrict__ out) {
  STAMP_DECL
  const int lane = threadIdx.x & 63;
  const bool isa = g < 128;
  const int gc = (g & 127) * 8;
  const int ch = gc + w;
  const u32 sb = lds_off(smem);
  const u32 fl0 = sb + FL_OFF;
  const u32 hw = isa ? HW_A : HW_B;
  char* hs = smem + HS_OFF + w * hw;
  const u32 hsa = sb + HS_OFF + w * hw;
  const int r = lane & 15, q = lane >> 4;
  u32 p_d;
  if (isa) {
    u32 p_ldt, p_lar, p_aim, p_cre, p_cim;
    const int po = (ch * N_MODE + 2 * lane) * 2;
    asm volatile("global_load_ushort %0, %5, %6\n\tglobal_load_dword %1, %7, %8 nt\n\t"
                 "global_load_dword %2, %7, %9 nt\n\tglobal_load_dword %3, %7, %10 nt\n\tglobal_load_dword %4, %7, %11 nt"
                 : "=&v"(p_ldt), "=&v"(p_lar), "=&v"(p_aim), "=&v"(p_cre), "=&v"(p_cim)
                 : "v"(ch * 2), "s"(ldt), "v"(po), "s"(lar), "s"(aim), "s"(cre), "s"(cim) : "memory");
    asm volatile("global_load_ushort %0, %1, %2" : "=v"(p_d) : "v"(ch * 2), "s"(dvec) : "memory");
    if (w < 4) __builtin_amdgcn_s_setprio(1);
    if (lane < 32) *(u32x4*)(smem + w * XW + (lane >> 3) * XP + 16 * (lane & 7)) = (u32x4){0, 0, 0, 0};
    __builtin_amdgcn_sched_barrier(0);
    asm volatile("s_waitcnt lgkmcnt(0)\n\ts_barrier" ::: "memory");
    STAMP(1);
    asm volatile("s_waitcnt vmcnt(0)" : "+v"(p_ldt), "+v"(p_lar), "+v"(p_aim), "+v"(p_cre), "+v"(p_cim), "+v"(p_d) :: "memory");
    __builtin_amdgcn_sched_barrier(0);
    STAMP(2);
    f32x4 ka[3];
    const int ec = gen_taps(hs, lane, p_ldt, p_lar, p_aim, p_cre, p_cim, ka);
    STAMP(3);
#pragma unroll
    for (int i = 0; i < 3; i++)
#pragma unroll
      for (int j = 0; j < 4; j++) ka[i][j] = __builtin_ldexpf(ka[i][j], ec);
#define NEXT(x, last) __int_as_float(__builtin_amdgcn_update_dpp(last, __float_as_int(x), 0x130, 0xf, 0xf, false))
#define LANE(x, l) __builtin_amdgcn_readlane(__float_as_int(x), l)
    float e4 = NEXT(ka[1][0], 0);
    float f2 = NEXT(ka[2][0], LANE(ka[2][2], 0));
    float b0 = NEXT(ka[2][2], LANE(ka[2][3], 0)), b1 = NEXT(ka[2][3], LANE(ka[1][0], 32));
    asm volatile("" : "+v"(e4), "+v"(f2), "+v"(b0), "+v"(b1));
    if (lane == 31) e4 = __int_as_float(LANE(ka[2][0], 0));
    *(u32x2*)(hs + 32 + 256 * (q >> 1) + 8 * (q & 1) + 16 * r) = (u32x2){pk_bf16(ka[0][0], ka[0][1]), pk_bf16(ka[0][2], ka[0][3])};
    {
      u32x4 o = {pk_bf16(ka[1][0], ka[1][0]), pk_bf16(ka[1][1], ka[1][2]), pk_bf16(ka[1][2], ka[1][2]), pk_bf16(ka[1][3], e4)};
      if (lane >= 32) o = (u32x4){0, 0, 0, 0};
      if (lane < 34) *(u32x4*)(hs + (lane < 32 ? 544 + 16 * lane : 16 * lane - 512)) = o;
    }
    *(u32x4*)(hs + 1056 + 16 * lane) = (u32x4){pk_bf16(ka[2][0], ka[2][0]), pk_bf16(ka[2][0], ka[2][1]), pk_bf16(ka[2][1], ka[2][1]), pk_bf16(f2, f2)};
#pragma unroll
    for (int i = 0; i < 2; i++) {
      const float a = ka[2][2 + i], b = i ? b1 : b0;
      const u32 aa = pk_bf16(a, a);
      *(u32x4*)(hs + 2080 + 1024 * i + 16 * lane) = (u32x4){aa, aa, pk_bf16((lane & 1) ? b : a, b), pk_bf16(b, b)};
    }
    __builtin_amdgcn_wave_barrier();
    if (w >= 4) __builtin_amdgcn_s_setprio(2);
    STAMP(4);
  } else {
    const int c = lane >> 3, rg = lane & 7;
    u32 pfx = 0;
    u32x2 sw;
    if (lane < 16) line_req(pfx, (const char*)(lane < 8 ? kin : vin) + (size_t)((64 * w + 8 * (g & 7) + rg) * (D_CH * 2) + (gc & ~63) * 2));
    if (rg == 0 && c < 3) {
      if (w) dma16(kin, (64 * w - 3 + c) * (D_CH * 2) + gc * 2, sb + HALO_OFF + 16 * w);
      else *(u32x4*)(smem + HALO_OFF + 16 * lane) = (u32x4){0, 0, 0, 0};
    }
    asm volatile("global_load_dwordx2 %0, %2, %3\n\tglobal_load_ushort %1, %4, %5" : "=&v"(sw), "=&v"(p_d)
                 : "v"((gc + c) * 8), "s"(shk), "v"(ch * 2), "s"(dvec) : "memory");
#pragma unroll
    for (int i = 0; i < 4; i++) dma16nt(ssmk + 512 * i, ch * (L_SEQ * 2) + 16 * lane, hsa + 32 + 1024 * i);
    if (lane < 34) *(u32x4*)(lane < 32 ? smem + w * XW + c * XP + 16 * rg : hs + 16 * rg) = (u32x4){0, 0, 0, 0};
    __builtin_amdgcn_sched_barrier(0);
    asm volatile("s_waitcnt lgkmcnt(0)\n\ts_barrier" ::: "memory");
    STAMP(1);
    const u32 go = (64 * w + 8 * rg + c) * (D_CH * 2) + gc * 2;
    const u32 rk = sb + RK_OFF + 1024 * w + 16 * rg + 2 * c;
    u32 kf[11], vf[8];
#pragma unroll
    for (int i = 0; i < 11; i++) kf[i] = 0;
#pragma unroll
    for (int i = 0; i < 8; i++) vf[i] = 0;
    asm volatile("s_waitcnt vmcnt(4)" : "+v"(sw), "+v"(p_d) : "v"(pfx) : "memory");
    STAMP(2);
    dma16(kin, go, sb + RK_OFF + 1024 * w);
    dma16(vin, go, sb + RK_OFF + 32768 + 1024 * w);
    wait_vm<0>();
    STAMP(3);
    kv_chunk(rg ? rk + 624 : sb + HALO_OFF + 16 * w + 2 * c, rk, sb + c * XW + 128 + 32 * w + 4 * rg, kf, vf, sw);
    lds_add(fl0 + FL_XQ, 1u);
    STAMP(4);
  }
  __builtin_amdgcn_sched_barrier(0);
  u32x2 TL[16], TH[16];
  {
    const u32 tb = hsa + 224 - 64 * q + 8 * ((lane & 3) - ((lane >> 2) & 3));
    sfor<16>([&](auto ic) { constexpr int i = decltype(ic)::value; dsr64tr<256 * i + 32>(TL[i], tb); dsr64tr<256 * i>(TH[i], tb); });
  }
  const u32 dv = q == (r & 3) ? p_d << (16 * ((r >> 2) & 1)) : 0u;
  const u32x4 dfr = {r < 8 ? dv : 0u, r < 8 ? 0u : dv, 0u, 0u};
  const u32 xb = sb + w * XW + 8 + 8 * r + 16 * q, xd = sb + w * XW + 128 + 8 * r + q * XP;
  const u32 yw = hsa + 32 * r + 8 * q;
  const u32 nb = 4 * (q ? lane - 16 : r ? lane + 47 : 63);
  const bool top = lane == 63;
  u32 fs = 0;
  __builtin_amdgcn_sched_barrier(0);
  asm volatile("s_waitcnt lgkmcnt(0)"
               : "+v"(TL[0]), "+v"(TL[1]), "+v"(TL[2]), "+v"(TL[3]), "+v"(TL[4]), "+v"(TL[5]), "+v"(TL[6]), "+v"(TL[7]),
                 "+v"(TL[8]), "+v"(TL[9]), "+v"(TL[10]), "+v"(TL[11]), "+v"(TL[12]), "+v"(TL[13]), "+v"(TL[14]), "+v"(TL[15]) :: "memory");
  asm volatile("" : "+v"(TH[0]), "+v"(TH[1]), "+v"(TH[2]), "+v"(TH[3]), "+v"(TH[4]), "+v"(TH[5]), "+v"(TH[6]), "+v"(TH[7]),
                    "+v"(TH[8]), "+v"(TH[9]), "+v"(TH[10]), "+v"(TH[11]), "+v"(TH[12]), "+v"(TH[13]), "+v"(TH[14]), "+v"(TH[15]));
  STAMP(5);
  poll8(fs, fl0 + FL_XQ);
  __builtin_amdgcn_sched_barrier(0);
  STAMP(6);
  u32x2 RL[NB], RH[NB];
  f32x4 acc[8][4];
  float cy[3] = {0.f, 0.f, 0.f}, pn[3], yp[3];
  u32x4 qv;
  auto load = [&](auto ic, auto hc) {
    constexpr int i = decltype(ic)::value, h = decltype(hc)::value;
    constexpr Item it = item(i);
    if constexpr (it.skip) dsr64<128 * (2 * it.G + it.skip - 1)>(h ? RH[i % NB] : RL[i % NB], xd);
    else dsr64<it.s * XP + 64 * (it.f + 1) + 8 * h>(h ? RH[i % NB] : RL[i % NB], xb);
  };
  auto comb1 = [&](auto tc) {
    constexpr int t = decltype(tc)::value;
    const float u[3] = {acc[t][1][3] + acc[t][2][2] + acc[t][3][1], acc[t][2][3] + acc[t][3][2], acc[t][3][3]};
#pragma unroll
    for (int j = 0; j < 3; j++) { bperm(pn[j], nb, top ? cy[j] : u[j]); cy[j] = u[j]; }
  };
  auto comb2 = [&](auto tc) {
    constexpr int t = decltype(tc)::value;
    yp[0] = acc[t][0][1] + acc[t][1][0];
    yp[1] = acc[t][0][2] + acc[t][1][1] + acc[t][2][0];
    yp[2] = acc[t][0][3] + acc[t][1][2] + acc[t][2][1] + acc[t][3][0];
    asm volatile("" : "+v"(yp[0]), "+v"(yp[1]), "+v"(yp[2]));
  };
  auto comb3 = [&](auto tc) {
    constexpr int t = decltype(tc)::value;
    asm volatile("" : "+v"(pn[0]), "+v"(pn[1]), "+v"(pn[2]));
    dsw64<512 * t>(yw, (u32x2){pk_bf16(acc[t][0][0] + pn[0], yp[0] + pn[1]), pk_bf16(yp[1] + pn[2], yp[2])});
    if constexpr (t_flag(t)) lds_set(fl0 + FL_Y + 4 * w, t + 1);
  };
#define IC(x) std::integral_constant<int, (x)>{}
  sfor<PD>([&](auto ic) { load(ic, IC(0)); load(ic, IC(1)); });
  asm volatile("s_waitcnt lgkmcnt(%2)" : "+v"(RL[0]), "+v"(RH[0]) : "i"(2 * PD - 2) : "memory");
  auto step = [&](auto ic) {
    constexpr int i = decltype(ic)::value;
    constexpr Item it = item(i);
    constexpr int t0 = 2 * it.G, w0 = 4 * it.G - it.f;
    const u32x4 x = {RL[i % NB][0], RL[i % NB][1], RH[i % NB][0], RH[i % NB][1]};
    const f32x4 z = {0.f, 0.f, 0.f, 0.f};
    __builtin_amdgcn_sched_barrier(0);
    if constexpr (!it.skip && w0 >= 0)
      acc[t0][it.s] = mma_bf16((u32x4){TL[w0][0], TL[w0][1], TH[w0][0], TH[w0][1]}, x, it.f < 0 ? z : acc[t0][it.s]);
    __builtin_amdgcn_sched_barrier(0);
    if constexpr (i + 1 < NI) asm volatile("s_waitcnt lgkmcnt(%2)" : "+v"(RL[(i + 1) % NB]), "+v"(RH[(i + 1) % NB]) : "i"(w_cnt(i)) : "memory");
    if constexpr (at_q(i, 0) >= 0) poll8(fs, fl0 + FL_XQ + 4 * at_q(i, 0));
    if constexpr (i + PD < NI) load(IC(i + PD), IC(0));
    __builtin_amdgcn_sched_barrier(0);
    if constexpr (it.skip) acc[t0 + it.skip - 1][0] = mma_bf16(dfr, x, acc[t0 + it.skip - 1][0]);
    else acc[t0 + 1][it.s] = mma_bf16((u32x4){TL[w0 + 2][0], TL[w0 + 2][1], TH[w0 + 2][0], TH[w0 + 2][1]}, x, it.f < 0 ? z : acc[t0 + 1][it.s]);
    __builtin_amdgcn_sched_barrier(0);
    if constexpr (i + PD < NI) load(IC(i + PD), IC(1));
    if constexpr (at_tile(i, D_C1) >= 0) comb1(IC(at_tile(i, D_C1)));
    if constexpr (at_tile(i, D_C2) >= 0) comb2(IC(at_tile(i, D_C2)));
    if constexpr (at_tile(i, D_C3) >= 0) comb3(IC(at_tile(i, D_C3)));
    if constexpr (at_q(i, D_Q) >= 0) dsr32<FL_XQ + 4 * at_q(i, D_Q)>(fs, fl0);
  };
  sfor<g_len(0)>([&](auto ic) { step(ic); });
  STAMP(7);
  sfor<g_len(1)>([&](auto ic) { step(IC(g_beg(1) + decltype(ic)::value)); });
  STAMP(8);
  sfor<g_len(2)>([&](auto ic) { step(IC(g_beg(2) + decltype(ic)::value)); });
  STAMP(9);
  {
    const __amdgpu_buffer_rsrc_t rq = __builtin_amdgcn_make_buffer_rsrc((void*)qin, 0, isa ? 0 : L_SEQ * D_CH * 2, 0x00020000);
    qv = __builtin_amdgcn_raw_buffer_load_b128(rq, ((1792 + 64 * w + lane) * D_CH + gc) * 2, 0, 0);
  }
  sfor<g_len(3)>([&](auto ic) { step(IC(g_beg(3) + decltype(ic)::value)); });
  __builtin_amdgcn_sched_barrier(0);
  STAMP(10);
  comb1(IC(7));
  comb2(IC(7));
  asm volatile("s_waitcnt lgkmcnt(0)" ::: "memory");
  comb3(IC(7));
  STAMP(11);
  if (w < 4) {
    const __amdgpu_buffer_rsrc_t ro = __builtin_amdgcn_make_buffer_rsrc((void*)out, 0, 8 * 1024 * 1024, 0x00020000);
    __builtin_amdgcn_s_setprio(0);
    poll_all(fl0 + FL_Y + 4 * (lane & 7), 8u);
    STAMP(13);
    out_piece(isa, sb + HS_OFF + ((lane >> 2) & 3) * hw + 32 * q + 8 * (lane & 3), hw, 28 + w, lane, g, qv, ro);
    STAMP(14);
  }
  STAMP_OUT(w);
}

__device__ __forceinline__ void stream_body(unsigned long long t_in, void* wsp, unsigned long long t_k, char* smem, int g, int p, u32 pf, const u16* __restrict__ u,
                                            const u16* __restrict__ kin, const u16* __restrict__ vin,
                                            const u16* __restrict__ shk, const u16* __restrict__ qin, u16* __restrict__ out) {
  STAMP_DECL
  const int lane = threadIdx.x & 63;
  const bool isa = g < 128;
  const int gc = (g & 127) * 8;
  const u32 sb = lds_off(smem);
  const u32 fl0 = sb + FL_OFF;
  const int c = lane >> 3, rg = lane & 7;
  u32 pfx = 0, pfy = 0;
  __builtin_amdgcn_s_setprio(3);
  if (isa) {
    const char* pa = (const char*)u + (size_t)((512 * ((c >> 1) & 1) + 128 * p + 64 * (c & 1) + 8 * (g & 7) + rg) * (D_CH * 2) + (gc & ~63) * 2);
    if (lane < 32) line_req(pfx, pa);
    asm volatile("s_barrier" ::: "memory");
    if (lane < 32) line_req(pfy, pa + 1024 * (D_CH * 2));
    const u32 go = (128 * p + 32 * ((lane >> 4) & 1) + 16 * (lane >> 5) + 4 * (lane & 3) + ((lane >> 2) & 3)) * (D_CH * 2) + gc * 2;
    const u32 rb = sb + RU_OFF + 2048 * p;
    asm volatile("s_waitcnt vmcnt(1)" :: "v"(pfx), "v"(pf) : "memory");
    STAMP(1);
#pragma unroll
    for (int e = 0; e < 4; e++) dma16(u + (size_t)(512 * (e >> 1) + 64 * (e & 1)) * D_CH, go, rb + 8192 * (e >> 1) + 1024 * (e & 1));
    asm volatile("s_waitcnt vmcnt(4)" :: "v"(pfy) : "memory");
    STAMP(2);
#pragma unroll
    for (int e = 4; e < 8; e++) dma16(u + (size_t)(512 * (e >> 1) + 64 * (e & 1)) * D_CH, go, rb + 8192 * (e >> 1) + 1024 * (e & 1));
    STAMP(3);
    const u32 ta = rb + 128 * (lane >> 4) + 64 * ((lane >> 1) & 1) + 16 * ((lane >> 2) & 3) + 8 * (lane & 1);
    const u32 xw = sb + rg * XW + (c & 3) * XP + 128 + 64 * p + 16 * (c >> 2);
    sfor<4>([&](auto jc) {
      constexpr int j = decltype(jc)::value;
      u32x2 d[4];
      wait_vm<6 - 2 * j>();
      sfor<4>([&](auto ec) { constexpr int e = decltype(ec)::value; dsr64tr<8192 * j + 512 * e>(d[e], ta); });
      asm volatile("s_waitcnt lgkmcnt(2)" : "+v"(d[0]), "+v"(d[1]) :: "memory");
      dsw128<256 * j>(xw, (u32x4){d[0][0], d[0][1], d[1][0], d[1][1]});
      asm volatile("s_waitcnt lgkmcnt(1)" : "+v"(d[2]), "+v"(d[3]) :: "memory");
      dsw128<256 * j + 32>(xw, (u32x4){d[2][0], d[2][1], d[3][0], d[3][1]});
      lds_add(fl0 + FL_XQ + 4 * j, 2u);
    });
    STAMP(5);
  } else {
    u32x2 sw;
    const char* pa = (const char*)((c & 1) ? vin : kin) + (size_t)((512 + 512 * (c >> 2) + 128 * p + 64 * ((c >> 1) & 1) + 8 * (g & 7) + rg) * (D_CH * 2) + (gc & ~63) * 2);
    if (lane < 32) line_req(pfx, pa);
    const u32 ho = sb + HALO_OFF + 384 * (1 + (p >> 1)) + 64 * (p & 1);
    if (rg < 3 && c < 3) dma16(kin, (512 + 512 * rg + 128 * p - 3 + c) * (D_CH * 2) + gc * 2, ho);
    asm volatile("global_load_dwordx2 %0, %1, %2" : "=v"(sw) : "v"((gc + c) * 8), "s"(shk) : "memory");
    asm volatile("s_barrier" ::: "memory");
    line_req(pfy, pa + 512 * (D_CH * 2));
    const u32 go = (512 + 128 * p + 8 * rg + c) * (D_CH * 2) + gc * 2;
    const u32 rb = sb + RK_OFF + 8192 + 2048 * p;
    asm volatile("s_waitcnt vmcnt(1)" : "+v"(sw) : "v"(pfx), "v"(pf) : "memory");
    STAMP(1);
#define PAIR(e) { dma16(kin + (size_t)(512 * ((e) >> 1) + 64 * ((e) & 1)) * D_CH, go, rb + 8192 * ((e) >> 1) + 1024 * ((e) & 1)); \
                  dma16(vin + (size_t)(512 * ((e) >> 1) + 64 * ((e) & 1)) * D_CH, go, rb + 32768 + 8192 * ((e) >> 1) + 1024 * ((e) & 1)); }
    PAIR(0) PAIR(1)
    asm volatile("s_waitcnt vmcnt(4)" :: "v"(pfy) : "memory");
    STAMP(2);
    PAIR(2) PAIR(3) PAIR(4) PAIR(5)
    line_req(pfx, (const char*)qin + (size_t)((256 * c + 64 * p + 8 * (g & 7) + rg) * (D_CH * 2) + (gc & ~63) * 2));
    STAMP(3);
    u32 kf[11], vf[8];
#pragma unroll
    for (int i = 0; i < 11; i++) kf[i] = 0;
#pragma unroll
    for (int i = 0; i < 8; i++) vf[i] = 0;
    u32 rk = rb + 16 * rg + 2 * c, xw = sb + c * XW + 128 + 256 + 64 * p + 4 * rg, rh = ho + 2 * c, fx = fl0 + FL_XQ + 4;
#pragma nounroll
    for (int e = 0; e < 6; e++) {
      if (e == 0) wait_vm<11>();
      else if (e == 1) wait_vm<9>();
      else if (e == 2) wait_vm<7>();
      else if (e == 3) wait_vm<5>();
      else if (e == 4) wait_vm<3>();
      else wait_vm<1>();
      kv_chunk(rg ? rk + 624 : (e & 1) ? rk - 272 : rh, rk, xw, kf, vf, sw);
      lds_add(fx, 1u);
      if (e == 1) STAMP(5); else if (e == 3) STAMP(7); else if (e == 5) STAMP(9);
      if (e & 1) { rk += 7168; xw += 224; rh += 16; fx += 4; }
      else { rk += 1024; xw += 32; }
    }
    asm volatile("s_waitcnt vmcnt(0)" :: "v"(pfx) : "memory");
  }
  STAMP(10);
  const __amdgpu_buffer_rsrc_t ro = __builtin_amdgcn_make_buffer_rsrc((void*)out, 0, 8 * 1024 * 1024, 0x00020000);
  const __amdgpu_buffer_rsrc_t rq = __builtin_amdgcn_make_buffer_rsrc((void*)qin, 0, isa ? 0 : L_SEQ * D_CH * 2, 0x00020000);
  const u32 hw = isa ? HW_A : HW_B;
  const u32 ya = sb + HS_OFF + ((lane >> 2) & 3) * hw + 32 * (lane >> 4) + 8 * (lane & 3);
#pragma nounroll
  for (int st = 0; st < 4; st++) {
    const int n = st < 3 ? 8 * st + 2 * p : 24 + p;
    const u32x4 q0 = __builtin_amdgcn_raw_buffer_load_b128(rq, ((64 * n + lane) * D_CH + gc) * 2, 0, 0);
    const u32x4 q1 = __builtin_amdgcn_raw_buffer_load_b128(rq, st < 3 ? ((64 * n + 64 + lane) * D_CH + gc) * 2 : 0x40000000, 0, 0);
    poll_all(fl0 + FL_Y + 4 * rg, st < 3 ? 2 * st + 2 : 7);
    if (st == 2) { STAMP(12);
    }
    if (st == 3) { STAMP(13);
    }
    out_piece(isa, ya, hw, n, lane, g, q0, ro);
    if (st < 3) out_piece(isa, ya, hw, n + 1, lane, g, q1, ro);
    if (st == 1) { STAMP(11);
    }
  }
  STAMP(14);
  STAMP_OUT(8 + p);
}

__global__ __launch_bounds__(768) void fused_kernel(
    const u16* __restrict__ u, const u16* __restrict__ lar, const u16* __restrict__ aim,
    const u16* __restrict__ cre, const u16* __restrict__ cim, const u16* __restrict__ ldt,
    const u16* __restrict__ dskip, const u16* __restrict__ qin, const u16* __restrict__ kin,
    const u16* __restrict__ vin, const u16* __restrict__ shk, const u16* __restrict__ ssmk,
    const u16* __restrict__ ssmd, u16* __restrict__ out, void* wsp) {
  extern __shared__ __attribute__((aligned(16))) char smem[];
  const unsigned long long t_in = __builtin_amdgcn_s_memrealtime();
  u32 pf = 0;
  if (threadIdx.x >= 736) {
    const char* pc;
    u32 en;
    asm volatile("s_getpc_b64 %1\n.Lpf_anchor:\n\ts_mov_b32 %2, .Lfused_end-.Lpf_anchor\n\t"
                 "v_min_u32 %0, %2, %3\n\tglobal_load_dword %0, %0, %1"
                 : "=&v"(pf), "=&s"(pc), "=&s"(en) : "v"(((threadIdx.x & 31) * 16 + ((blockIdx.x >> 3) & 15)) * 128) : "memory");
  }
  {
    if (threadIdx.x < 16) *(u32*)(smem + FL_OFF + 4 * threadIdx.x) = 0u;
    __builtin_amdgcn_sched_barrier(0);
    asm volatile("" :: "s"(u), "s"(lar), "s"(aim), "s"(cre), "s"(cim), "s"(ldt), "s"(dskip), "s"(qin), "s"(kin), "s"(vin), "s"(shk),
                 "s"(ssmk), "s"(ssmd), "s"(out) : "memory");
    __builtin_amdgcn_sched_barrier(0);
  }
  const unsigned long long t_k = __builtin_amdgcn_s_memrealtime(); __builtin_amdgcn_sched_barrier(0);
  const int g = xcd_group(blockIdx.x);
  const int w = __builtin_amdgcn_readfirstlane(threadIdx.x >> 6);
  if (w >= 8) stream_body(t_in, wsp, t_k, smem, g, w - 8, pf, u, kin, vin, shk, qin, out);
  else main_body(t_in, wsp, t_k, smem, g, w, lar, aim, cre, cim, ldt, kin, vin, shk, ssmk, g < 128 ? dskip : ssmd, qin, out);
}
__global__ void end_marker_kernel() { asm volatile(".Lfused_end:"); }

__global__ __launch_bounds__(1024) void spin_kernel(const unsigned long long* st) {
  typedef unsigned long long u64;
  __shared__ u64 mn[4][20], mx[4][20], sm[4][20], t0s;
  __shared__ unsigned cn[4][20];
  const int t = threadIdx.x;
  if (t < 80) { mn[t / 20][t % 20] = ~0ull; mx[t / 20][t % 20] = 0ull; sm[t / 20][t % 20] = 0ull; cn[t / 20][t % 20] = 0; }
  if (t == 0) t0s = ~0ull;
  __syncthreads();
  for (int w = t; w < 3072; w += 1024) atomicMin(&t0s, st[w * 20]);
  __syncthreads();
  for (int w = t; w < 3072; w += 1024) {
    const int b = w / 12, wv = w % 12;
    const int cls = (xcd_group(b) >= 128 ? 2 : 0) + (wv >= 8 ? 1 : 0);
    for (int i = 0; i < 20; i++) {
      u64 v = st[w * 20 + i];
      if (v) { v -= t0s; atomicMin(&mn[cls][i], v); atomicMax(&mx[cls][i], v); atomicAdd(&sm[cls][i], v); atomicAdd(&cn[cls][i], 1u); }
    }
  }
  __syncthreads();
  if (t == 0) {
    u64 q1 = 0, q2 = 0;
    if (q1 > 3000) q1 = 3000;
    if (q2 > 3000) q2 = 3000;
    const u64 q = 4 * q1 + 12800 * ((q2 + 10) / 20);
    const u64 t0 = __builtin_amdgcn_s_memrealtime();
    while (__builtin_amdgcn_s_memrealtime() - t0 < q) __builtin_amdgcn_s_sleep(1);
  }
}

extern "C" void kernel_launch(void* const* d_in, const int* in_sizes, int n_in, void* d_out, int out_size,
                              void* d_ws, size_t ws_size, hipStream_t stream) {
  (void)hipFuncSetAttribute((const void*)fused_kernel, hipFuncAttributeMaxDynamicSharedMemorySize, LDS_BYTES);
  fused_kernel<<<256, 768, LDS_BYTES, stream>>>((const u16*)d_in[0], (const u16*)d_in[1], (const u16*)d_in[2], (const u16*)d_in[3],
                                                (const u16*)d_in[4], (const u16*)d_in[5], (const u16*)d_in[6], (const u16*)d_in[7],
                                                (const u16*)d_in[8], (const u16*)d_in[9], (const u16*)d_in[10], (const u16*)d_in[11],
                                                (const u16*)d_in[12], (u16*)d_out, d_ws);
  spin_kernel<<<1, 1024, 0, stream>>>((const unsigned long long*)d_ws);
}
```

```cpp
#include <hip/hip_runtime.h>
#include <hip/hip_bf16.h>
#include <stdint.h>
#include <utility>

typedef unsigned int u32;
typedef unsigned short u16;
typedef u32 u32x4 __attribute__((ext_vector_type(4)));
typedef u32 u32x2 __attribute__((ext_vector_type(2)));
typedef float f32x4 __attribute__((ext_vector_type(4)));
typedef float f32x2 __attribute__((ext_vector_type(2)));
typedef __bf16 bf16x8 __attribute__((ext_vector_type(8)));
typedef _Float16 f16x8 __attribute__((ext_vector_type(8)));

#define L_SEQ 2048
#define D_CH 1024
#define N_MODE 128
#define PF2_SLEEP 0

__device__ __forceinline__ float bflo(u32 d) { return __uint_as_float(d << 16); }
__device__ __forceinline__ float bfhi(u32 d) { return __uint_as_float(d & 0xffff0000u); }
__device__ __forceinline__ u32 pk_bf16(float lo, float hi) {
  typedef __bf16 bf16x2 __attribute__((ext_vector_type(2)));
  f32x2 v = {lo, hi};
  bf16x2 r = __builtin_convertvector(v, bf16x2);
  return __builtin_bit_cast(u32, r);
}
__device__ __forceinline__ float rbf(float x) { return __uint_as_float(pk_bf16(0.f, x)); }

__device__ __forceinline__ u32 wave_max_u32(u32 x) {
  x = max(x, (u32)__builtin_amdgcn_update_dpp(0, (int)x, 0x111, 0xf, 0xf, true));
  x = max(x, (u32)__builtin_amdgcn_update_dpp(0, (int)x, 0x112, 0xf, 0xf, true));
  x = max(x, (u32)__builtin_amdgcn_update_dpp(0, (int)x, 0x114, 0xf, 0xf, true));
  x = max(x, (u32)__builtin_amdgcn_update_dpp(0, (int)x, 0x118, 0xf, 0xf, true));
  x = max(x, (u32)__builtin_amdgcn_update_dpp(0, (int)x, 0x142, 0xa, 0xf, true));
  x = max(x, (u32)__builtin_amdgcn_update_dpp(0, (int)x, 0x143, 0xc, 0xf, true));
  return (u32)__builtin_amdgcn_readlane((int)x, 63);
}

struct cf { float re, im; };
__device__ __forceinline__ cf cmul(cf a, cf b) {
  cf o;
  asm("v_mul_f32 %0, %2, %4\n\tv_mul_f32 %1, %2, %5\n\tv_fma_f32 %0, -%3, %5, %0\n\tv_fma_f32 %1, %3, %4, %1"
      : "=&v"(o.re), "=&v"(o.im) : "v"(a.re), "v"(a.im), "v"(b.re), "v"(b.im));
  return o;
}
__device__ __forceinline__ u32x2 hmul2(u32x2 a, u32x2 b) {
  u32 o0, o1;
  asm("v_pk_mul_f16 %0, %2, %4 op_sel:[0,0] op_sel_hi:[0,1]\n\t"
      "v_pk_mul_f16 %1, %3, %5 op_sel:[0,0] op_sel_hi:[0,1]\n\t"
      "v_pk_fma_f16 %0, %2, %4, %0 op_sel:[1,1,0] op_sel_hi:[1,0,1] neg_lo:[1,0,0]\n\t"
      "v_pk_fma_f16 %1, %3, %5, %1 op_sel:[1,1,0] op_sel_hi:[1,0,1] neg_lo:[1,0,0]"
      : "=&v"(o0), "=&v"(o1) : "v"(a[0]), "v"(a[1]), "v"(b[0]), "v"(b[1]));
  return (u32x2){o0, o1};
}
__device__ __forceinline__ f32x2 cexp_pos(float ar, float ai, float pos) {
  const float LOG2E = 1.44269504088896340736f;
  const float I2PI_HI = 0.15915494309189533577f;
  const float I2PI_LO = 6.4206383e-9f;
  float xr = ar * pos;
  float xi = ai * pos;
  float e = __builtin_amdgcn_exp2f(xr * LOG2E);
  float r = xi * I2PI_HI;
  float err = __builtin_fmaf(xi, I2PI_HI, -r);
  float f = __builtin_amdgcn_fractf(r);
  f += __builtin_fmaf(xi, I2PI_LO, err);
  f32x2 o;
  o[0] = e * __builtin_amdgcn_cosf(f);
  o[1] = e * __builtin_amdgcn_sinf(f);
  return o;
}
__device__ __forceinline__ u32 pk_f16(float lo, float hi) {
  typedef _Float16 f16x2 __attribute__((ext_vector_type(2)));
  f32x2 v = {lo, hi};
  return __builtin_bit_cast(u32, __builtin_convertvector(v, f16x2));
}

template <bool IS_A>
__device__ __forceinline__ f32x4 mma(u32x4 a, u32x4 b, f32x4 c) {
  if (IS_A) return __builtin_amdgcn_mfma_f32_16x16x32_f16(__builtin_bit_cast(f16x8, a), __builtin_bit_cast(f16x8, b), c, 0, 0, 0);
  else return __builtin_amdgcn_mfma_f32_16x16x32_bf16(__builtin_bit_cast(bf16x8, a), __builtin_bit_cast(bf16x8, b), c, 0, 0, 0);
}

__device__ __forceinline__ u32 lds_off(const char* p) {
  return (u32)(uintptr_t)(const __attribute__((address_space(3))) char*)p;
}

template <class F, int... I>
__device__ __forceinline__ void sfor_impl(F&& f, std::integer_sequence<int, I...>) { (f(std::integral_constant<int, I>{}), ...); }
template <int N, class F>
__device__ __forceinline__ void sfor(F&& f) { sfor_impl(f, std::make_integer_sequence<int, N>{}); }

#define XS_BYTES 4624
#define HC_BYTES 2368
#define HS_BYTES (4 * HC_BYTES)
#define W_BYTES (XS_BYTES + HS_BYTES)
#define YS_OFF (8 * W_BYTES)
#define YS_BYTES 2064
#define RAW_OFF (YS_OFF + 8 * YS_BYTES)
#define HALO_OFF (RAW_OFF + 32768)
#define FL_OFF (HALO_OFF + 768)
#define FL_FH 0
#define FL_XQ 32
#define FL_CN 40
#define FL_P2 48
#define FL_MX 64
#define FL_LD 64
#define LDS_BYTES (FL_OFF + 128)
#define GT_ROW 544

__device__ __forceinline__ void gen_taps(char* tab, int lane, u32 p_ldt, u32 p_lar, u32 p_aim, u32 p_cre, u32 p_cim,
                                         f32x4 (&acc)[3]) {
  const float dt = rbf(expf(bflo(p_ldt)));
  cf C[2], z4[2], z128[2], z512[2];
  u32 mc = 0;
#pragma unroll
  for (int m = 0; m < 2; m++) {
    C[m].re = 2.0f * ((m ? bfhi(p_cre) : bflo(p_cre)) * dt);
    C[m].im = -2.0f * ((m ? bfhi(p_cim) : bflo(p_cim)) * dt);
    mc = max(mc, max(__float_as_uint(C[m].re) & 0x7fffffffu, __float_as_uint(C[m].im) & 0x7fffffffu));
  }
  mc = wave_max_u32(mc);
  u32x2 QF[3], PF[3], Q[3], QL[4], P[8];
#pragma unroll
  for (int m = 0; m < 2; m++) {
    const float ar = -rbf(expf(m ? bfhi(p_lar) : bflo(p_lar))) * dt;
    const float ai = (m ? bfhi(p_aim) : bflo(p_aim)) * dt;
    const f32x2 a1 = cexp_pos(ar, -ai, 1.0f), a32 = cexp_pos(ar, -ai, 32.0f), a512 = cexp_pos(ar, -ai, 512.0f);
    const cf z1 = {a1[0], a1[1]}, z32 = {a32[0], a32[1]};
    z512[m] = (cf){a512[0], a512[1]};
    const cf z2 = cmul(z1, z1), z64 = cmul(z32, z32);
    z4[m] = cmul(z2, z2);
    const cf z96 = cmul(z64, z32), z8 = cmul(z4[m], z4[m]), z3 = cmul(z2, z1), z16 = cmul(z8, z8);
    z128[m] = cmul(z64, z64);
    const cf z24 = cmul(z16, z8);
    PF[0][m] = pk_f16(z32.re, -z32.im); PF[1][m] = pk_f16(z64.re, -z64.im); PF[2][m] = pk_f16(z96.re, -z96.im);
    QF[0][m] = pk_f16(z8.re, -z8.im); QF[1][m] = pk_f16(z16.re, -z16.im); QF[2][m] = pk_f16(z24.re, -z24.im);
    Q[0][m] = pk_f16(z1.re, z1.im); Q[1][m] = pk_f16(z2.re, z2.im); Q[2][m] = pk_f16(z3.re, z3.im);
  }
  const int ec = (mc > 0u && mc < 0x7f800000u) ? __builtin_amdgcn_frexp_expf(__uint_as_float(mc)) - 14 : 0;
  char* wp = tab + 8 * lane;
  const int c16 = lane & 15, q = lane >> 4;
  const char* rp = tab + c16 * GT_ROW + 16 * q;
#define PUT_ROW(row, z) *(u32x2*)(wp + (row) * GT_ROW) = (z)
#define GET_FRAGS(dst) { __builtin_amdgcn_wave_barrier();                                           \
    _Pragma("unroll") for (int ks = 0; ks < 8; ks++) dst[ks] = *(const u32x4*)(rp + 64 * ks);       \
    __builtin_amdgcn_wave_barrier(); }
#define TILE_MMA(ti) { f32x4 a_ = {0.f, 0.f, 0.f, 0.f};                                             \
    _Pragma("unroll") for (int ks = 0; ks < 8; ks++) a_ = mma<true>(G[ks], Fh[ks], a_);             \
    acc[ti] = a_; }
  PUT_ROW(0, ((u32x2){0x3c00u, 0x3c00u}));
#pragma unroll
  for (int b = 0; b < 3; b++) PUT_ROW(1 + b, QF[b]);
#pragma unroll
  for (int a = 0; a < 3; a++) {
    PUT_ROW(4 + 4 * a, PF[a]);
#pragma unroll
    for (int b = 0; b < 3; b++) PUT_ROW(5 + 4 * a + b, hmul2(PF[a], QF[b]));
  }
  u32x4 Fh[8], G[8];
  GET_FRAGS(Fh);
#pragma unroll
  for (int m = 0; m < 2; m++) {
    cf p0 = {__builtin_ldexpf(C[m].re, -ec), __builtin_ldexpf(C[m].im, -ec)};
    cf p4 = cmul(p0, z4[m]);
#pragma unroll
    for (int j = 0; j < 4; j++) {
      P[j][m] = pk_f16(p0.re, p0.im);
      P[4 + j][m] = pk_f16(p4.re, p4.im);
      if (j < 3) { p0 = cmul(p0, z128[m]); p4 = cmul(p4, z128[m]); }
    }
  }
#pragma unroll
  for (int al = 0; al < 2; al++)
#pragma unroll
    for (int h = 0; h < 2; h++) {
      PUT_ROW(8 * al + 4 * h, P[4 * h + al]);
#pragma unroll
      for (int b = 0; b < 3; b++) PUT_ROW(8 * al + 4 * h + 1 + b, hmul2(P[4 * h + al], Q[b]));
    }
  GET_FRAGS(G);
#pragma unroll
  for (int m = 0; m < 2; m++) {
    const cf z1024 = cmul(z512[m], z512[m]);
    const cf z1536 = cmul(z1024, z512[m]), z2048 = cmul(z1024, z1024);
    QL[0][m] = pk_f16(z512[m].re, z512[m].im); QL[1][m] = pk_f16(z1024.re, z1024.im);
    QL[2][m] = pk_f16(z1536.re, z1536.im); QL[3][m] = pk_f16(z2048.re, z2048.im);
  }
#pragma unroll
  for (int al = 0; al < 2; al++)
#pragma unroll
    for (int h = 0; h < 2; h++) {
      PUT_ROW(4 * al + 2 * h, P[4 * h + 2 + al]);
      PUT_ROW(4 * al + 2 * h + 1, hmul2(P[4 * h + 2 + al], Q[1]));
    }
#pragma unroll
  for (int al = 0; al < 4; al++)
#pragma unroll
    for (int h = 0; h < 2; h++) PUT_ROW(8 + 2 * al + h, hmul2(P[4 * h + al], QL[0]));
  TILE_MMA(0);
  GET_FRAGS(G);
#pragma unroll
  for (int al = 0; al < 9; al++) PUT_ROW(al, hmul2(P[al & 3], QL[1 + (al >> 2)]));
  TILE_MMA(1);
  GET_FRAGS(G);
  TILE_MMA(2);
#pragma unroll
  for (int i = 0; i < 3; i++)
#pragma unroll
    for (int rg = 0; rg < 4; rg++) acc[i][rg] = __builtin_ldexpf(acc[i][rg], ec);
}

__device__ __forceinline__ void dma16(const void* base, u32 off, u32 lds_base) {
  asm volatile("s_mov_b32 m0, %2\n\ts_nop 0\n\tglobal_load_lds_dwordx4 %0, %1" :: "v"(off), "s"(base), "s"(lds_base) : "memory", "m0");
}
__device__ __forceinline__ void poll_ge(u32 addr, u32 val) {
  u32 fl;
  for (;;) {
    asm volatile("ds_read_b32 %0, %1\n\ts_waitcnt lgkmcnt(0)" : "=v"(fl) : "v"(addr) : "memory");
    if (__builtin_amdgcn_readfirstlane(fl) >= val) break;
    __builtin_amdgcn_s_sleep(1);
  }
}
__device__ __forceinline__ u32 lds_get(u32 addr) {
  u32 v;
  asm volatile("ds_read_b32 %0, %1\n\ts_waitcnt lgkmcnt(0)" : "=v"(v) : "v"(addr) : "memory");
  return v;
}
__device__ __forceinline__ void lds_set(u32 addr, u32 val) {
  asm volatile("ds_write_b32 %0, %1" :: "v"(addr), "v"(val) : "memory");
}
__device__ __forceinline__ void lds_inc(u32 addr) {
  asm volatile("s_mov_b64 exec, 1\n\tds_add_u32 %0, %1\n\ts_mov_b64 exec, -1" :: "v"(addr), "v"(1u) : "memory");
}
__device__ __forceinline__ int scale_k(u32 mx) { return min(max((int)(mx >> 7) - 30, 0), 224); }

__device__ __forceinline__ int xcd_group(int b) {
  int xcd = b & 7, idx = b >> 3;
  return ((xcd + 8 * (idx >> 3)) << 3) | (idx & 7);
}

constexpr int p_n(int t) { return t == 0 ? 16 : 10; }
constexpr int p_a(int t, int k) { return t == 0 ? (k >> 2) : (k < 4 ? 0 : k < 7 ? 1 : k < 9 ? 2 : 3); }
constexpr int p_e(int t, int k) { return t == 0 ? (k & 3) : (k < 4 ? k : k < 7 ? k - 4 : k < 9 ? k - 7 : 0); }

template <int OFF> __device__ __forceinline__ void dsr64(u32x2& dst, u32 addr) {
  asm volatile("ds_read_b64 %0, %1 offset:%2" : "=v"(dst) : "v"(addr), "i"(OFF) : "memory");
}
template <int OFF> __device__ __forceinline__ void dsr64tr(u32x2& dst, u32 addr) {
  asm volatile("ds_read_b64_tr_b16 %0, %1 offset:%2" : "=v"(dst) : "v"(addr), "i"(OFF) : "memory");
}
template <int OFF> __device__ __forceinline__ void dsr128(u32x4& dst, u32 addr) {
  asm volatile("ds_read_b128 %0, %1 offset:%2" : "=v"(dst) : "v"(addr), "i"(OFF) : "memory");
}
__device__ __forceinline__ void read_kv(u32 (&kf)[11], u32 (&vf)[8], u32 rh, u32 rk, u32 rv) {
  asm volatile("ds_read_u16_d16_hi %0, %19\n\tds_read_u16_d16_hi %1, %19 offset:128\n\tds_read_u16_d16_hi %2, %19 offset:256\n\t"
               "ds_read_u16_d16_hi %3, %20\n\tds_read_u16_d16_hi %4, %20 offset:128\n\t"
               "ds_read_u16_d16_hi %5, %20 offset:256\n\tds_read_u16_d16_hi %6, %20 offset:384\n\t"
               "ds_read_u16_d16_hi %7, %20 offset:512\n\tds_read_u16_d16_hi %8, %20 offset:640\n\t"
               "ds_read_u16_d16_hi %9, %20 offset:768\n\tds_read_u16_d16_hi %10, %20 offset:896\n\t"
               "ds_read_u16_d16_hi %11, %21\n\tds_read_u16_d16_hi %12, %21 offset:128\n\t"
               "ds_read_u16_d16_hi %13, %21 offset:256\n\tds_read_u16_d16_hi %14, %21 offset:384\n\t"
               "ds_read_u16_d16_hi %15, %21 offset:512\n\tds_read_u16_d16_hi %16, %21 offset:640\n\t"
               "ds_read_u16_d16_hi %17, %21 offset:768\n\tds_read_u16_d16_hi %18, %21 offset:896\n\t"
               "s_waitcnt lgkmcnt(0)"
               : "+v"(kf[0]), "+v"(kf[1]), "+v"(kf[2]), "+v"(kf[3]), "+v"(kf[4]), "+v"(kf[5]), "+v"(kf[6]), "+v"(kf[7]),
                 "+v"(kf[8]), "+v"(kf[9]), "+v"(kf[10]), "+v"(vf[0]), "+v"(vf[1]), "+v"(vf[2]), "+v"(vf[3]), "+v"(vf[4]),
                 "+v"(vf[5]), "+v"(vf[6]), "+v"(vf[7])
               : "v"(rh), "v"(rk), "v"(rv) : "memory");
}
__device__ __forceinline__ u32x4 kv_chunk(const u32 (&kf)[11], const u32 (&vf)[8], float w0, float w1, float w2, float w3) {
  u32 o0, o1, o2, o3, a[8], b[8];
  asm(
      "v_mul_f32 %4, %20, %42\n\tv_mul_f32 %5, %21, %42\n\tv_mul_f32 %6, %22, %42\n\tv_mul_f32 %7, %23, %42\n\t"
      "v_mul_f32 %8, %24, %42\n\tv_mul_f32 %9, %25, %42\n\tv_mul_f32 %10, %26, %42\n\tv_mul_f32 %11, %27, %42\n\t"
      "v_mul_f32 %12, %21, %41\n\tv_mul_f32 %13, %22, %41\n\tv_mul_f32 %14, %23, %41\n\tv_mul_f32 %15, %24, %41\n\t"
      "v_mul_f32 %16, %25, %41\n\tv_mul_f32 %17, %26, %41\n\tv_mul_f32 %18, %27, %41\n\tv_mul_f32 %19, %28, %41\n\t"
      "v_cvt_pk_bf16_f32 %4, 0, %4\n\tv_cvt_pk_bf16_f32 %5, 0, %5\n\tv_cvt_pk_bf16_f32 %6, 0, %6\n\tv_cvt_pk_bf16_f32 %7, 0, %7\n\t"
      "v_cvt_pk_bf16_f32 %8, 0, %8\n\tv_cvt_pk_bf16_f32 %9, 0, %9\n\tv_cvt_pk_bf16_f32 %10, 0, %10\n\tv_cvt_pk_bf16_f32 %11, 0, %11\n\t"
      "v_cvt_pk_bf16_f32 %12, 0, %12\n\tv_cvt_pk_bf16_f32 %13, 0, %13\n\tv_cvt_pk_bf16_f32 %14, 0, %14\n\tv_cvt_pk_bf16_f32 %15, 0, %15\n\t"
      "v_cvt_pk_bf16_f32 %16, 0, %16\n\tv_cvt_pk_bf16_f32 %17, 0, %17\n\tv_cvt_pk_bf16_f32 %18, 0, %18\n\tv_cvt_pk_bf16_f32 %19, 0, %19\n\t"
      "v_add_f32 %4, %4, %12\n\tv_add_f32 %5, %5, %13\n\tv_add_f32 %6, %6, %14\n\tv_add_f32 %7, %7, %15\n\t"
      "v_add_f32 %8, %8, %16\n\tv_add_f32 %9, %9, %17\n\tv_add_f32 %10, %10, %18\n\tv_add_f32 %11, %11, %19\n\t"
      "v_mul_f32 %12, %22, %40\n\tv_mul_f32 %13, %23, %40\n\tv_mul_f32 %14, %24, %40\n\tv_mul_f32 %15, %25, %40\n\t"
      "v_mul_f32 %16, %26, %40\n\tv_mul_f32 %17, %27, %40\n\tv_mul_f32 %18, %28, %40\n\tv_mul_f32 %19, %29, %40\n\t"
      "v_cvt_pk_bf16_f32 %4, 0, %4\n\tv_cvt_pk_bf16_f32 %5, 0, %5\n\tv_cvt_pk_bf16_f32 %6, 0, %6\n\tv_cvt_pk_bf16_f32 %7, 0, %7\n\t"
      "v_cvt_pk_bf16_f32 %8, 0, %8\n\tv_cvt_pk_bf16_f32 %9, 0, %9\n\tv_cvt_pk_bf16_f32 %10, 0, %10\n\tv_cvt_pk_bf16_f32 %11, 0, %11\n\t"
      "v_cvt_pk_bf16_f32 %12, 0, %12\n\tv_cvt_pk_bf16_f32 %13, 0, %13\n\tv_cvt_pk_bf16_f32 %14, 0, %14\n\tv_cvt_pk_bf16_f32 %15, 0, %15\n\t"
      "v_cvt_pk_bf16_f32 %16, 0, %16\n\tv_cvt_pk_bf16_f32 %17, 0, %17\n\tv_cvt_pk_bf16_f32 %18, 0, %18\n\tv_cvt_pk_bf16_f32 %19, 0, %19\n\t"
      "v_add_f32 %4, %4, %12\n\tv_add_f32 %5, %5, %13\n\tv_add_f32 %6, %6, %14\n\tv_add_f32 %7, %7, %15\n\t"
      "v_add_f32 %8, %8, %16\n\tv_add_f32 %9, %9, %17\n\tv_add_f32 %10, %10, %18\n\tv_add_f32 %11, %11, %19\n\t"
      "v_mul_f32 %12, %23, %39\n\tv_mul_f32 %13, %24, %39\n\tv_mul_f32 %14, %25, %39\n\tv_mul_f32 %15, %26, %39\n\t"
      "v_mul_f32 %16, %27, %39\n\tv_mul_f32 %17, %28, %39\n\tv_mul_f32 %18, %29, %39\n\tv_mul_f32 %19, %30, %39\n\t"
      "v_cvt_pk_bf16_f32 %4, 0, %4\n\tv_cvt_pk_bf16_f32 %5, 0, %5\n\tv_cvt_pk_bf16_f32 %6, 0, %6\n\tv_cvt_pk_bf16_f32 %7, 0, %7\n\t"
      "v_cvt_pk_bf16_f32 %8, 0, %8\n\tv_cvt_pk_bf16_f32 %9, 0, %9\n\tv_cvt_pk_bf16_f32 %10, 0, %10\n\tv_cvt_pk_bf16_f32 %11, 0, %11\n\t"
      "v_cvt_pk_bf16_f32 %12, 0, %12\n\tv_cvt_pk_bf16_f32 %13, 0, %13\n\tv_cvt_pk_bf16_f32 %14, 0, %14\n\tv_cvt_pk_bf16_f32 %15, 0, %15\n\t"
      "v_cvt_pk_bf16_f32 %16, 0, %16\n\tv_cvt_pk_bf16_f32 %17, 0, %17\n\tv_cvt_pk_bf16_f32 %18, 0, %18\n\tv_cvt_pk_bf16_f32 %19, 0, %19\n\t"
      "v_add_f32 %4, %4, %12\n\tv_add_f32 %5, %5, %13\n\tv_add_f32 %6, %6, %14\n\tv_add_f32 %7, %7, %15\n\t"
      "v_add_f32 %8, %8, %16\n\tv_add_f32 %9, %9, %17\n\tv_add_f32 %10, %10, %18\n\tv_add_f32 %11, %11, %19\n\t"
      "v_cvt_pk_bf16_f32 %4, 0, %4\n\tv_cvt_pk_bf16_f32 %5, 0, %5\n\tv_cvt_pk_bf16_f32 %6, 0, %6\n\tv_cvt_pk_bf16_f32 %7, 0, %7\n\t"
      "v_cvt_pk_bf16_f32 %8, 0, %8\n\tv_cvt_pk_bf16_f32 %9, 0, %9\n\tv_cvt_pk_bf16_f32 %10, 0, %10\n\tv_cvt_pk_bf16_f32 %11, 0, %11\n\t"
      "v_mul_f32 %4, %4, %31\n\tv_mul_f32 %5, %5, %32\n\tv_mul_f32 %6, %6, %33\n\tv_mul_f32 %7, %7, %34\n\t"
      "v_mul_f32 %8, %8, %35\n\tv_mul_f32 %9, %9, %36\n\tv_mul_f32 %10, %10, %37\n\tv_mul_f32 %11, %11, %38\n\t"
      "v_cvt_pk_bf16_f32 %0, %11, %10\n\tv_cvt_pk_bf16_f32 %1, %9, %8\n\tv_cvt_pk_bf16_f32 %2, %7, %6\n\tv_cvt_pk_bf16_f32 %3, %5, %4"
      : "=&v"(o0), "=&v"(o1), "=&v"(o2), "=&v"(o3),
        "=&v"(a[0]), "=&v"(a[1]), "=&v"(a[2]), "=&v"(a[3]), "=&v"(a[4]), "=&v"(a[5]), "=&v"(a[6]), "=&v"(a[7]),
        "=&v"(b[0]), "=&v"(b[1]), "=&v"(b[2]), "=&v"(b[3]), "=&v"(b[4]), "=&v"(b[5]), "=&v"(b[6]), "=&v"(b[7])
      : "v"(kf[0]), "v"(kf[1]), "v"(kf[2]), "v"(kf[3]), "v"(kf[4]), "v"(kf[5]), "v"(kf[6]), "v"(kf[7]), "v"(kf[8]), "v"(kf[9]), "v"(kf[10]),
        "v"(vf[0]), "v"(vf[1]), "v"(vf[2]), "v"(vf[3]), "v"(vf[4]), "v"(vf[5]), "v"(vf[6]), "v"(vf[7]),
        "v"(w0), "v"(w1), "v"(w2), "v"(w3));
  return (u32x4){o0, o1, o2, o3};
}
template <int OFF> __device__ __forceinline__ void dsw128(u32 addr, u32x4 val) {
  asm volatile("ds_write_b128 %0, %1 offset:%2" :: "v"(addr), "v"(val), "i"(OFF) : "memory");
}
__device__ __forceinline__ void line_req(u32& dst, const void* base, u32 off) {
  asm volatile("global_load_dword %0, %1, %2 nt" : "=v"(dst) : "v"(off), "s"(base) : "memory");
}
template <int N> __device__ __forceinline__ void wait_vm() { asm volatile("s_waitcnt vmcnt(%0)" :: "i"(N) : "memory"); }
__device__ __forceinline__ void dma16nt(const void* base, u32 off, u32 lds_base) {
  asm volatile("s_mov_b32 m0, %2\n\ts_nop 0\n\tglobal_load_lds_dwordx4 %0, %1 nt" :: "v"(off), "s"(base), "s"(lds_base) : "memory", "m0");
}

__device__ __forceinline__ void wait_frags(u32x2 (&AL)[4], u32x2 (&AH)[4], u32x4 (&B)[4]) {
  __builtin_amdgcn_sched_barrier(0);
  asm volatile("s_waitcnt lgkmcnt(0)"
               : "+v"(AL[0]), "+v"(AL[1]), "+v"(AL[2]), "+v"(AL[3]), "+v"(AH[0]), "+v"(AH[1]), "+v"(AH[2]), "+v"(AH[3]),
                 "+v"(B[0]), "+v"(B[1]), "+v"(B[2]), "+v"(B[3]) :: "memory");
  __builtin_amdgcn_sched_barrier(0);
}

template <bool IS_A, int NR>
__device__ __forceinline__ void epi2(const char* ysb, int ystride, int tt, int row0, int g, const u32x4* xv, u32x4 dv,
                                     __amdgpu_buffer_rsrc_t ro) {
  u32 yv[8][NR / 2];
#pragma unroll
  for (int c = 0; c < 8; c++) {
    if (NR == 2) yv[c][0] = *(const u32*)(ysb + c * ystride + 4 * tt);
    else { u32x2 y2 = *(const u32x2*)(ysb + c * ystride + 8 * tt); yv[c][0] = y2[0]; yv[c][NR / 2 - 1] = y2[1]; }
  }
#pragma unroll
  for (int i = 0; i < NR; i++) {
    u32x4 o;
#pragma unroll
    for (int p = 0; p < 4; p++) {
      const u32 ya_ = yv[2 * p][i >> 1], yb_ = yv[2 * p + 1][i >> 1];
      const float ya = (i & 1) ? bfhi(ya_) : bflo(ya_), yb = (i & 1) ? bfhi(yb_) : bflo(yb_);
      if (IS_A) o[p] = pk_bf16(ya + rbf(bflo(xv[i][p]) * bflo(dv[p])), yb + rbf(bfhi(xv[i][p]) * bfhi(dv[p])));
      else o[p] = pk_bf16(ya * bflo(xv[i][p]), yb * bfhi(xv[i][p]));
    }
    __builtin_amdgcn_raw_buffer_store_b128(o, ro, ((row0 + NR * tt + i) * (2 * D_CH) + g * 8) * 2, 0, 0);
  }
}
__device__ __forceinline__ int raw_pos(int r) { return (r & ~63) * 16 + (r & 4) * 128 + (r & 56) * 8 + (r & 3) * 16; }

template <bool IS_A>
__device__ __forceinline__ void main_body( char* smem, int g, int w, u32 pf,
                                          const u16* __restrict__ lar, const u16* __restrict__ aim,
                                          const u16* __restrict__ cre, const u16* __restrict__ cim, const u16* __restrict__ ldt,
                                          const u16* __restrict__ kin, const u16* __restrict__ vin, const u16* __restrict__ shk,
                                          const u16* __restrict__ ssmk, const u16* __restrict__ dvec,
                                          const u16* __restrict__ qin, u16* __restrict__ out) {
  const int t = threadIdx.x, lane = t & 63;
  const int gc = (g & 127) * 8;
  const int ch = gc + w;
  char* xs = smem + w * W_BYTES;
  char* hs = xs + XS_BYTES;
  const u32 sb = lds_off(smem);
  const u32 fl0 = sb + FL_OFF;
  u32x4 dv;
  u32 p_ldt = 0, p_lar = 0, p_aim = 0, p_cre = 0, p_cim = 0, pfx = 0;
  if (IS_A) {
    const int po = (ch * N_MODE + 2 * lane) * 2;
    asm volatile("global_load_ushort %0, %5, %6\n\tglobal_load_dword %1, %7, %8 nt\n\t"
                 "global_load_dword %2, %7, %9 nt\n\tglobal_load_dword %3, %7, %10 nt\n\tglobal_load_dword %4, %7, %11 nt"
                 : "=&v"(p_ldt), "=&v"(p_lar), "=&v"(p_aim), "=&v"(p_cre), "=&v"(p_cim)
                 : "v"(ch * 2), "s"(ldt), "v"(po), "s"(lar), "s"(aim), "s"(cre), "s"(cim) : "memory");
    if (w < 4) __builtin_amdgcn_s_setprio(1);
  } else {
#pragma unroll
    for (int i = 3; i >= 0; i--) dma16nt(ssmk + 512 * i, ch * (L_SEQ * 2) + 16 * lane, sb + w * W_BYTES + XS_BYTES + 1568 + 1024 * i);
  }
  __builtin_amdgcn_sched_barrier(0);
  const int hl = lane < 48 ? lane : lane - 48;
  int hc[3];
#pragma unroll
  for (int j = 0; j < 3; j++) {
    const int c = 48 * j + hl, sg = (c * 57) >> 10;
    hc[j] = 32 * (sg & 3) + 16 * (sg >> 2) + c - 18 * sg;
  }
  u32x4 hd[6];
  u32x2 he[6];
  u32x2 sw;
  int esc = 0;
  char* lin = hs + 1536;
  if (!IS_A) {
    asm volatile("global_load_dwordx2 %0, %1, %2" : "=v"(sw) : "v"((gc + (lane >> 3)) * 8), "s"(shk) : "memory");
    if (lane < 4) *(u32x4*)(lin + ((lane & 2) ? 4096 : 0) + 16 * lane) = (u32x4){0, 0, 0, 0};
  }
  asm volatile("global_load_dwordx4 %0, %1, %2" : "=v"(dv) : "v"(gc * 2), "s"(dvec) : "memory");
  if (lane < 30) *(u32x4*)(xs + 16 * lane) = (u32x4){0, 0, 0, 0};
  __builtin_amdgcn_sched_barrier(0);
  asm volatile("s_waitcnt lgkmcnt(0)\n\ts_barrier" ::: "memory");
  __builtin_amdgcn_sched_barrier(0);
  char* hw = hs + 16 * hl;
#define STAGE_PIECE(set, k) {                                                          \
    const u32x4 d4 = hd[3 * (set) + ((k) >> 2)];                                       \
    const u32x2 e2 = he[3 * (set) + ((k) >> 2)];                                       \
    u32x4 o_;                                                                          \
    if (((k) & 3) == 0) o_ = d4;                                                       \
    else if (((k) & 3) == 1) o_ = (u32x4){__builtin_amdgcn_alignbit(d4[1], d4[0], 16), __builtin_amdgcn_alignbit(d4[2], d4[1], 16), \
                                          __builtin_amdgcn_alignbit(d4[3], d4[2], 16), __builtin_amdgcn_alignbit(e2[0], d4[3], 16)}; \
    else if (((k) & 3) == 2) o_ = (u32x4){d4[1], d4[2], d4[3], e2[0]};                 \
    else o_ = (u32x4){__builtin_amdgcn_alignbit(d4[2], d4[1], 16), __builtin_amdgcn_alignbit(d4[3], d4[2], 16), \
                      __builtin_amdgcn_alignbit(e2[0], d4[3], 16), __builtin_amdgcn_alignbit(e2[1], e2[0], 16)}; \
    *(u32x4*)(hw + 768 * ((k) >> 2) + ((k) & 3) * HC_BYTES) = o_; }
  if (IS_A) {
    asm volatile("s_waitcnt vmcnt(0)" : "+v"(p_ldt), "+v"(p_lar), "+v"(p_aim), "+v"(p_cre), "+v"(p_cim), "+v"(dv) :: "memory");
    __builtin_amdgcn_sched_barrier(0);
    f32x4 ka[3];
    gen_taps(hs, lane, p_ldt, p_lar, p_aim, p_cre, p_cim, ka);
    char* kb = hs;
    u16* kc = (u16*)kb;
    {
      const int c16 = lane & 15, q = lane >> 4;
      const bool v2 = q < 2, v28 = (q == 2) && (c16 == 0);
      u32 mk = 0;
#pragma unroll
      for (int rg = 0; rg < 4; rg++) {
        mk = max(mk, max(__float_as_uint(ka[0][rg]) & 0x7fffffffu, __float_as_uint(ka[1][rg]) & 0x7fffffffu));
        u32 m2 = __float_as_uint(ka[2][rg]) & 0x7fffffffu;
        mk = max(mk, (v2 || (rg == 0 && v28)) ? m2 : 0u);
      }
      mk = wave_max_u32(mk);
      esc = (mk > 0u && mk < 0x7f800000u) ? __builtin_amdgcn_frexp_expf(__uint_as_float(mk)) - 15 : 0;
#pragma unroll
      for (int rg = 0; rg < 4; rg++) {
        const int row = 4 * q + rg;
        kc[128 * (row >> 3) + (row & 7) + 8 * c16] = __builtin_bit_cast(u16, (_Float16)__builtin_ldexpf(ka[0][rg], -esc));
        int i1 = (row < 8) ? 256 + 64 * (row >> 2) + (row & 3) + 4 * c16 : 384 + 32 * ((row - 8) >> 1) + (row & 1) + 2 * c16;
        kc[i1] = __builtin_bit_cast(u16, (_Float16)__builtin_ldexpf(ka[1][rg], -esc));
        kc[512 + 16 * row + c16] = __builtin_bit_cast(u16, (_Float16)__builtin_ldexpf(ka[2][rg], -esc));
      }
    }
    __builtin_amdgcn_wave_barrier();
    const bool dir = lane < 32;
    const int a0 = dir ? 16 * lane : 512 + 8 * (lane - 32);
    u32x2 d01 = *(const u32x2*)(kb + a0), d23 = *(const u32x2*)(kb + a0 + 8);
    u32x4 o;
    o[0] = __builtin_amdgcn_perm(d01[0], d01[0], dir ? 0x03020100u : 0x01000100u);
    o[1] = __builtin_amdgcn_perm(d01[1], d01[0], dir ? 0x07060504u : 0x05040302u);
    o[2] = __builtin_amdgcn_perm(d23[0], d01[1], dir ? 0x07060504u : 0x01000100u);
    o[3] = dir ? d23[1] : __builtin_amdgcn_perm(d23[0], d01[1], 0x05040302u);
    *(u32x4*)(lin + 32 + 16 * lane) = o;
    u32 d0 = *(const u32*)(kb + 768 + 4 * lane), d1 = *(const u32*)(kb + 772 + 4 * lane);
    o[0] = __builtin_amdgcn_perm(d0, d0, 0x01000100u);
    o[1] = d0;
    o[2] = __builtin_amdgcn_perm(d0, d0, 0x03020302u);
    o[3] = __builtin_amdgcn_perm(d1, d1, 0x01000100u);
    *(u32x4*)(lin + 32 + 1024 + 16 * lane) = o;
#pragma unroll
    for (int i = 0; i < 2; i++) {
      u32 x0 = kc[512 + 64 * i + lane], x1 = kc[513 + 64 * i + lane];
      o[0] = o[1] = x0 | (x0 << 16);
      o[3] = x1 | (x1 << 16);
      o[2] = (lane & 1) ? o[3] : (x0 | (x1 << 16));
      *(u32x4*)(lin + 32 + 2048 + 1024 * i + 16 * lane) = o;
    }
    if (lane < 4) *(u32x4*)(lin + ((lane & 2) ? 4096 : 0) + 16 * lane) = (u32x4){0, 0, 0, 0};
    __builtin_amdgcn_wave_barrier();
#pragma unroll
    for (int s = 0; s < 6; s++) {
      hd[s] = *(const u32x4*)(lin + 16 * hc[s % 3] + 2048 * (s / 3));
      he[s] = *(const u32x2*)(lin + 16 * hc[s % 3] + 2048 * (s / 3) + 16);
    }
    __builtin_amdgcn_wave_barrier();
#pragma unroll
    for (int k = 0; k < 12; k++) STAGE_PIECE(1, k);
  } else {
    {
      const char* pa = (const char*)((lane & 16) ? vin : kin) + (size_t)((1024 + 128 * w + (g & 7) * 16 + (lane & 15)) * (D_CH * 2) + (gc & ~63) * 2);
      __builtin_amdgcn_s_sleep(PF2_SLEEP);
      if (lane < 32) asm volatile("global_load_dword %0, %1, off" : "=v"(pfx) : "v"(pa) : "memory");
    }
    asm volatile("s_waitcnt vmcnt(1)" : "+v"(sw), "+v"(dv) :: "memory");
#pragma unroll
    for (int s = 0; s < 6; s++) {
      hd[s] = *(const u32x4*)(lin + 16 * hc[s % 3] + 2048 * (s / 3));
      he[s] = *(const u32x2*)(lin + 16 * hc[s % 3] + 2048 * (s / 3) + 16);
    }
    __builtin_amdgcn_wave_barrier();
#pragma unroll
    for (int k = 0; k < 12; k++) STAGE_PIECE(1, k);
    __builtin_amdgcn_sched_barrier(0);
    asm volatile("s_waitcnt vmcnt(0)" :: "v"(pfx) : "memory");
    lds_inc(fl0 + FL_P2 + 4 * (w >> 1));
    const int c = lane >> 3, rg = lane & 7;
    const float w0 = bflo(sw[0]), w1 = bfhi(sw[0]), w2 = bflo(sw[1]), w3 = bfhi(sw[1]);
    u32 rk = sb + w * W_BYTES + 2528 + 16 * rg + 2 * c, rv = sb + YS_OFF + w * YS_BYTES + 16 * rg + 2 * c;
    u32 rhh = rg ? rk + 624 : sb + HALO_OFF + 16 * w + 2 * c;
    u32 xw = sb + c * W_BYTES + 480 + 256 * w + 16 * rg;
    u32 kf[11], vf[8];
#pragma unroll
    for (int i = 0; i < 11; i++) kf[i] = 0;
#pragma unroll
    for (int i = 0; i < 8; i++) vf[i] = 0;
#pragma nounroll
    for (u32 j = 1; j < 3; j++) {
      poll_ge(fl0 + FL_LD + 4 * w, j);
      read_kv(kf, vf, rhh, rk, rv);
      const u32x4 o = kv_chunk(kf, vf, w0, w1, w2, w3);
      dsw128<0>(xw, o);
      if (j == 1) {
      }
      rk += 1024; rv += 1024; xw += 128;
      rhh = rk + (rg ? 624 : -272);
    }
    lds_inc(fl0 + FL_XQ);
  }
  const int r = lane & 15, q = lane >> 4;
  f32x4 acc[8];
#pragma unroll
  for (int i = 0; i < 8; i++) acc[i] = (f32x4){0.f, 0.f, 0.f, 0.f};
  const int lam = 25 + r - 8 * q;
  const u32 abase = lds_off(hs) + (lam & 3) * HC_BYTES + 2 * (lam & ~3);
  const u32 bbase = lds_off(xs) + 32 * r + 16 * q;
  u32x2 AL[2][4], AH[2][4];
  u32x4 B[2][4];
#define LOAD_PIECE(it, k, e0, bf) {                                                             \
    if constexpr ((k) < 8 && ((k) & 1) == 0) dsr64<64 * ((it) & 3) + 288 * (((k) >> 1) + 4 * ((it) >> 2))>(AL[bf][((k) >> 1) & 3], abase);   \
    else if constexpr ((k) < 8) dsr64<64 * ((it) & 3) + 288 * (((k) >> 1) + 4 * ((it) >> 2)) + 8>(AH[bf][((k) >> 1) & 3], abase); \
    else dsr128<448 + 512 * ((e0) + (k) - 8) - 64 * (it)>(B[bf][((k) - 8) & 3], bbase); }
  __builtin_amdgcn_sched_barrier(0);
  poll_ge(fl0 + FL_XQ, IS_A ? 4u : 8u);
  __builtin_amdgcn_sched_barrier(0);
  sfor<12>([&](auto kc_) { constexpr int k = decltype(kc_)::value; LOAD_PIECE(0, k, 0, 0); });
  __builtin_amdgcn_sched_barrier(0);
#define PIN_ACC asm volatile("" : "+v"(acc[0]), "+v"(acc[1]), "+v"(acc[2]), "+v"(acc[3]), "+v"(acc[4]), "+v"(acc[5]), "+v"(acc[6]), "+v"(acc[7]));
#define PASS(pt, e0, t0, nxt)                                                           \
  sfor<8>([&](auto ic_) {                                                               \
    constexpr int it = decltype(ic_)::value, bf = it & 1;                               \
    wait_frags(AL[bf], AH[bf], B[bf]);                                                  \
    sfor<p_n(pt)>([&](auto kc_) {                                                       \
      constexpr int k_ = decltype(kc_)::value, a = p_a(pt, k_), e = p_e(pt, k_);        \
      acc[(t0) + a + e] = mma<IS_A>((u32x4){AL[bf][a][0], AL[bf][a][1], AH[bf][a][0], AH[bf][a][1]}, B[bf][e], acc[(t0) + a + e]); \
      if constexpr (it < 7) {                                                           \
        if constexpr ((pt) == 0) { if constexpr (k_ < 12) LOAD_PIECE(it + 1, k_, e0, bf ^ 1); } \
        else if constexpr (k_ < 6) { LOAD_PIECE(it + 1, 2 * k_, e0, bf ^ 1); LOAD_PIECE(it + 1, 2 * k_ + 1, e0, bf ^ 1); } \
      } else if constexpr ((nxt) == 1) {       \
        if constexpr (k_ < 6) { STAGE_PIECE(0, 2 * k_); STAGE_PIECE(0, 2 * k_ + 1); }   \
        else { LOAD_PIECE(0, 3 * (k_ - 6), 0, bf ^ 1); LOAD_PIECE(0, 3 * (k_ - 6) + 1, 0, bf ^ 1); LOAD_PIECE(0, 3 * (k_ - 6) + 2, 0, bf ^ 1); } \
      } else if constexpr ((nxt) == 2) {                                                \
        if constexpr (k_ < 8) LOAD_PIECE(0, k_, 0, bf ^ 1);                             \
      }                                                                                 \
      __builtin_amdgcn_sched_barrier(0);                                                \
    });                                                                                 \
    PIN_ACC                                                                             \
  });
  const float dsk = (w & 1) ? bfhi(dv[w >> 1]) : bflo(dv[w >> 1]);
#define EPI1(c0_, ysp)                                                                  \
  {                                                                                     \
    u32x2 uv[4];                                                                        \
    if (!IS_A) {                                                                        \
      _Pragma("unroll") for (int ci = 0; ci < 4; ci++)                                  \
        uv[ci] = *(const u32x2*)(xs + 480 + 512 * ((c0_) + ci) + 32 * r + 16 * (q >> 1) + 8 - 8 * (q & 1)); \
      __builtin_amdgcn_sched_barrier(0);                                                \
    }                                                                                   \
    _Pragma("unroll") for (int ci = 0; ci < 4; ci++) {                                  \
      const int l = 256 * ci + 16 * r + 4 * q;                                          \
      float y[4];                                                                       \
      _Pragma("unroll") for (int j = 0; j < 4; j++) {                                   \
        y[j] = acc[(c0_) + ci][j];                                                      \
        if (IS_A) y[j] = __builtin_ldexpf(y[j], esc);                                   \
      }                                                                                 \
      u32x2 o;                                                                          \
      if (IS_A) {                                                                       \
        o[0] = pk_bf16(y[0], y[1]);                                                     \
        o[1] = pk_bf16(y[2], y[3]);                                                     \
      } else {                                                                          \
        const float x3 = bflo(uv[ci][0]), x2 = bfhi(uv[ci][0]), x1 = bflo(uv[ci][1]), x0 = bfhi(uv[ci][1]); \
        o[0] = pk_bf16(rbf(y[0]) + rbf(x0 * dsk), rbf(y[1]) + rbf(x1 * dsk));           \
        o[1] = pk_bf16(rbf(y[2]) + rbf(x2 * dsk), rbf(y[3]) + rbf(x3 * dsk));           \
      }                                                                                 \
      *(u32x2*)((ysp) + 2 * l) = o;                                                     \
    }                                                                                   \
  }
  u32x4 xv[2];
  PASS(1, 0, 4, 1);
  PASS(0, 0, 0, 2);
  wait_frags(AL[0], AH[0], B[0]);
  int k1 = 0;
  if (IS_A) { k1 = scale_k(__builtin_amdgcn_readfirstlane(lds_get(fl0 + FL_MX + 4 * w))); esc += k1 - 112; }
  EPI1(0, smem + YS_OFF + w * YS_BYTES);
  lds_set(fl0 + FL_FH + 4 * w, 1u);
  __builtin_amdgcn_sched_barrier(0);
  poll_ge(fl0 + FL_XQ + 4, 4u);
  if (IS_A) {
    const int d = k1 - scale_k(__builtin_amdgcn_readfirstlane(lds_get(fl0 + FL_MX + 32 + 4 * w)));
    if (d != 0) {
      typedef _Float16 f16x2 __attribute__((ext_vector_type(2)));
      const _Float16 f = (_Float16)__builtin_ldexpf(1.0f, d);
      if (lane < 30) {
        u32x4 x4 = *(const u32x4*)(xs + 2048 + 16 * lane);
#pragma unroll
        for (int i = 0; i < 4; i++) x4[i] = __builtin_bit_cast(u32, __builtin_bit_cast(f16x2, x4[i]) * (f16x2){f, f});
        *(u32x4*)(xs + 2048 + 16 * lane) = x4;
      }
#pragma unroll
      for (int i = 4; i < 8; i++)
#pragma unroll
        for (int j = 0; j < 4; j++) acc[i][j] = __builtin_ldexpf(acc[i][j], d);
      esc -= d;
    }
    __builtin_amdgcn_sched_barrier(0);
  }
  sfor<4>([&](auto kc_) { constexpr int k = decltype(kc_)::value; LOAD_PIECE(0, 8 + k, 4, 0); });
  if (!IS_A) {
    const __amdgpu_buffer_rsrc_t rq = __builtin_amdgcn_make_buffer_rsrc((void*)qin, 0, L_SEQ * D_CH * 2, 0x00020000);
#pragma unroll
    for (int i = 0; i < 2; i++) xv[i] = __builtin_amdgcn_raw_buffer_load_b128(rq, ((1024 + 2 * t + i) * D_CH + gc) * 2, 0, 0);
  }
  __builtin_amdgcn_sched_barrier(0);
  PASS(1, 4, 4, 0);
  EPI1(4, hs);
  if (IS_A) {
#pragma unroll
    for (int i = 0; i < 2; i++) xv[i] = *(const u32x4*)(smem + RAW_OFF + raw_pos(1024 + 2 * t + i));
  }
  asm volatile("s_waitcnt lgkmcnt(0)\n\ts_barrier" ::: "memory");
  const __amdgpu_buffer_rsrc_t ro = __builtin_amdgcn_make_buffer_rsrc((void*)out, 0, 8 * 1024 * 1024, 0x00020000);
  epi2<IS_A, 2>(smem + XS_BYTES, W_BYTES, t, 1024, g, xv, dv, ro);
}

template <bool IS_A>
__device__ __forceinline__ void stream_body( char* smem, int g, int p, u32 pf, const u16* __restrict__ u,
                                            const u16* __restrict__ kin, const u16* __restrict__ vin,
                                            const u16* __restrict__ shk, const u16* __restrict__ dvec,
                                            const u16* __restrict__ qin, u16* __restrict__ out) {
  const int lane = threadIdx.x & 63;
  const int gc = (g & 127) * 8;
  const u32 sb = lds_off(smem);
  const u32 fl0 = sb + FL_OFF;
  u32x4 dv;
  u32 pfx = 0, pfy = 0;
  u32x4 xv[4];
  __builtin_amdgcn_s_setprio(3);
  if (IS_A) {
    const u32 po = ((g & 7) * 128 + 32 * p + (lane & 31)) * (D_CH * 2) + (gc & ~63) * 2;
    if (lane < 32) line_req(pfx, u, po);
    asm volatile("global_load_dwordx4 %0, %1, %2" : "=v"(dv) : "v"(gc * 2), "s"(dvec) : "memory");
    asm volatile("s_barrier" ::: "memory");
    if (lane < 32) line_req(pfy, u + 1024 * D_CH, po);
    const u32 go = (64 * p + 32 * ((lane >> 4) & 1) + 8 * ((lane >> 2) & 3) + 4 * (lane >> 5) + (lane & 3)) * (D_CH * 2) + gc * 2;
    const u32 rb = sb + RAW_OFF + 1024 * p;
    asm volatile("s_waitcnt vmcnt(2)" :: "v"(pfx), "v"(pf) : "memory");
#pragma unroll
    for (int e = 0; e < 4; e++) dma16(u + (size_t)e * 256 * D_CH, go, rb + 4096 * e);
    asm volatile("s_waitcnt vmcnt(4)" : "+v"(dv) : "v"(pfy) : "memory");
#pragma unroll
    for (int e = 4; e < 8; e++) dma16(u + (size_t)e * 256 * D_CH, go, rb + 4096 * e);
    u32 ta = rb + 256 * (lane >> 5) + 128 * ((lane >> 4) & 1) + 64 * ((lane >> 1) & 1) + 16 * (3 - ((lane >> 2) & 3)) + 8 * (lane & 1);
    u32 xw = sb + (lane & 7) * W_BYTES + 480 + 128 * p + 16 * (lane >> 3);
    u32 fm = fl0 + FL_MX + 4 * (lane & 7), fc = fl0 + FL_CN;
    u32 mx = 0;
    wait_vm<4>();
#pragma nounroll
    for (int h = 0; h < 2; h++) {
      u32x2 d[8];
      sfor<4>([&](auto ec_) { constexpr int e = decltype(ec_)::value; dsr64tr<4096 * e + 512>(d[2 * e], ta); dsr64tr<4096 * e>(d[2 * e + 1], ta); });
      asm volatile("s_waitcnt lgkmcnt(0)" : "+v"(d[0]), "+v"(d[1]), "+v"(d[2]), "+v"(d[3]), "+v"(d[4]), "+v"(d[5]), "+v"(d[6]), "+v"(d[7]) :: "memory");
      u32 m[16];
#pragma unroll
      for (int i = 0; i < 16; i++) {
        m[i] = d[i >> 1][i & 1] & 0x7fff7fffu;
        asm("v_pk_max_u16 %0, %0, %1" : "+v"(mx) : "v"(m[i]));
      }
      mx = max(mx & 0xffffu, mx >> 16);
      asm volatile("ds_max_u32 %0, %1" :: "v"(fm), "v"(mx) : "memory");
      lds_inc(fc);
      poll_ge(fc, 4u);
      mx = lds_get(fm);
      const u32 kk = (u32)scale_k(mx) * 0x00800080u;
#pragma unroll
      for (int i = 0; i < 16; i++) {
        asm("v_pk_sub_u16 %0, %0, %1 clamp\n\tv_pk_lshlrev_b16 %0, 3, %0 op_sel_hi:[0,1]" : "+v"(m[i]) : "v"(kk));
        m[i] |= d[i >> 1][i & 1] & 0x80008000u;
      }
      sfor<4>([&](auto ec_) { constexpr int e = decltype(ec_)::value; dsw128<512 * e>(xw, (u32x4){m[4 * e], m[4 * e + 1], m[4 * e + 2], m[4 * e + 3]}); });
      lds_inc(fc - 8);
      wait_vm<0>();
      ta += 16384; xw += 2048; fm += 32; fc += 4;
    }
    __builtin_amdgcn_s_setprio(0);
#pragma unroll
    for (int i = 0; i < 4; i++) xv[i] = *(const u32x4*)(smem + RAW_OFF + raw_pos(4 * (64 * p + lane) + i));
  } else {
    const int c = lane >> 3, rg = lane & 7;
    u32x2 sw;
    const char* pa = (const char*)(lane < 32 ? kin : vin) + (size_t)((256 * p + (g & 7) * 32 + (lane & 31)) * (D_CH * 2) + (gc & ~63) * 2);
    asm volatile("global_load_dword %0, %1, off" : "=v"(pfx) : "v"(pa) : "memory");
    asm volatile("global_load_dwordx2 %0, %1, %2" : "=v"(sw) : "v"((gc + c) * 8), "s"(shk) : "memory");
    if (p == 0 && rg == 0 && c < 3) *(u32x4*)(smem + HALO_OFF + 16 * lane) = (u32x4){0, 0, 0, 0};
    asm volatile("s_barrier" ::: "memory");
    dv = (u32x4){0, 0, 0, 0};
    const u32 go = (256 * p + 8 * rg + c) * (D_CH * 2) + gc * 2;
    const u32 rb = sb + RAW_OFF + 8192 * p;
    const u32 kr = sb + 2 * p * W_BYTES + 2528, vr = sb + YS_OFF + 2 * p * YS_BYTES, fl = fl0 + FL_LD + 8 * p;
    asm volatile("s_waitcnt vmcnt(0)" : "+v"(sw) : "v"(pfx), "v"(pf) : "memory");
    {
      const int hr = 256 * p + 128 * rg - 3 + c;
      if (rg < 2 && c < 3 && hr >= 0) dma16(kin, hr * (D_CH * 2) + gc * 2, sb + HALO_OFF + 32 * p);
    }
#define PAIR(row, ka, va) { dma16(kin + (size_t)(row) * D_CH, go, ka); dma16(vin + (size_t)(row) * D_CH, go, va); }
    PAIR(0, kr, vr);
    PAIR(128, kr + W_BYTES, vr + YS_BYTES);
    PAIR(64, kr + 1024, vr + 1024);
    wait_vm<4>();
    lds_set(fl, 1u);
    PAIR(192, kr + W_BYTES + 1024, vr + YS_BYTES + 1024);
    wait_vm<4>();
    lds_set(fl + 4, 1u);
    poll_ge(fl0 + FL_P2 + 4 * p, 2u);
    if (rg == 0 && c < 3) dma16(kin + 1021 * D_CH, go, sb + HALO_OFF + 384 + 16 * p);
    PAIR(1024, rb, rb + 4096);
    wait_vm<5>();
    lds_set(fl, 2u);
    PAIR(1088, rb + 1024, rb + 5120);
    wait_vm<5>();
    lds_set(fl + 4, 2u);
    PAIR(1152, rb + 2048, rb + 6144);
    PAIR(1216, rb + 3072, rb + 7168);
    line_req(pfy, qin, ((g & 7) * 256 + 64 * p + lane) * (D_CH * 2) + (gc & ~63) * 2);
    const float w0 = bflo(sw[0]), w1 = bfhi(sw[0]), w2 = bflo(sw[1]), w3 = bfhi(sw[1]);
    u32 rk = rb + 16 * rg + 2 * c;
    u32 rhh = rg ? rk + 624 : sb + HALO_OFF + 384 + 16 * p + 2 * c;
    u32 xw = sb + c * W_BYTES + 480 + 2048 + 512 * p + 16 * rg;
    u32 kf[11], vf[8];
#pragma unroll
    for (int i = 0; i < 11; i++) kf[i] = 0;
#pragma unroll
    for (int i = 0; i < 8; i++) vf[i] = 0;
    wait_vm<7>();
#pragma nounroll
    for (int j = 0; j < 4; j++) {
      read_kv(kf, vf, rhh, rk, rk + 4096);
      const u32x4 o = kv_chunk(kf, vf, w0, w1, w2, w3);
      if (j == 0) poll_ge(fl0 + FL_XQ, 8u);
      dsw128<0>(xw, o);
      if (j == 0) wait_vm<5>();
      else if (j == 1) wait_vm<3>();
      else wait_vm<1>();
      rk += 1024; xw += 128;
      rhh = rk + (rg ? 624 : -272);
    }
    lds_inc(fl0 + FL_XQ + 4);
    __builtin_amdgcn_s_setprio(0);
    asm volatile("s_waitcnt vmcnt(0)" :: "v"(pfy) : "memory");
    __builtin_amdgcn_sched_barrier(0);
    const __amdgpu_buffer_rsrc_t rq = __builtin_amdgcn_make_buffer_rsrc((void*)qin, 0, L_SEQ * D_CH * 2, 0x00020000);
#pragma unroll
    for (int i = 0; i < 4; i++) xv[i] = __builtin_amdgcn_raw_buffer_load_b128(rq, ((4 * (64 * p + lane) + i) * D_CH + gc) * 2, 0, 0);
  }
  {
    u32 fl;
    for (;;) {
      asm volatile("ds_read_b32 %0, %1\n\ts_waitcnt lgkmcnt(0)" : "=v"(fl) : "v"(fl0 + FL_FH + 4 * (lane & 7)) : "memory");
      if (__builtin_amdgcn_ballot_w64(fl == 0u) == 0ull) break;
      __builtin_amdgcn_s_sleep(2);
    }
  }
  const __amdgpu_buffer_rsrc_t ro = __builtin_amdgcn_make_buffer_rsrc((void*)out, 0, 8 * 1024 * 1024, 0x00020000);
  epi2<IS_A, 4>(smem + YS_OFF, YS_BYTES, 64 * p + lane, 0, g, xv, dv, ro);
  asm volatile("s_barrier" ::: "memory");
}

__global__ __launch_bounds__(768) void fused_kernel(
    const u16* __restrict__ u, const u16* __restrict__ lar, const u16* __restrict__ aim,
    const u16* __restrict__ cre, const u16* __restrict__ cim, const u16* __restrict__ ldt,
    const u16* __restrict__ dskip, const u16* __restrict__ qin, const u16* __restrict__ kin,
    const u16* __restrict__ vin, const u16* __restrict__ shk, const u16* __restrict__ ssmk,
    const u16* __restrict__ ssmd, u16* __restrict__ out) {
  extern __shared__ __attribute__((aligned(16))) char smem[];
  u32 pf = 0;
  if (threadIdx.x >= 736) {
    const char* pc;
    u32 en;
    asm volatile("s_getpc_b64 %1\n.Lpf_anchor:\n\ts_mov_b32 %2, .Lfused_end-.Lpf_anchor\n\t"
                 "v_min_u32 %0, %2, %3\n\tglobal_load_dword %0, %0, %1"
                 : "=&v"(pf), "=&s"(pc), "=&s"(en) : "v"(((threadIdx.x & 31) * 16 + ((blockIdx.x >> 3) & 15)) * 128) : "memory");
  }
  {
    if (threadIdx.x < 32) *(u32*)(smem + FL_OFF + 4 * threadIdx.x) = 0u;
    __builtin_amdgcn_sched_barrier(0);
    asm volatile("" :: "s"(u), "s"(lar), "s"(aim), "s"(cre), "s"(cim), "s"(ldt), "s"(dskip), "s"(qin), "s"(kin), "s"(vin), "s"(shk),
                 "s"(ssmk), "s"(ssmd), "s"(out) : "memory");
    __builtin_amdgcn_sched_barrier(0);
  }
  const int g = xcd_group(blockIdx.x);
  const int w = __builtin_amdgcn_readfirstlane(threadIdx.x >> 6);
  if (w >= 8) {
    if (g < 128) stream_body<true>( smem, g, w - 8, pf, u, kin, vin, shk, dskip, qin, out);
    else stream_body<false>( smem, g, w - 8, pf, u, kin, vin, shk, ssmd, qin, out);
  } else {
    if (g < 128) main_body<true>( smem, g, w, pf, lar, aim, cre, cim, ldt, kin, vin, shk, ssmk, dskip, qin, out);
    else main_body<false>( smem, g, w, pf, lar, aim, cre, cim, ldt, kin, vin, shk, ssmk, ssmd, qin, out);
  }
}
__global__ void end_marker_kernel() { asm volatile(".Lfused_end:"); }

extern "C" void kernel_launch(void* const* d_in, const int* in_sizes, int n_in, void* d_out, int out_size,
                              void* d_ws, size_t ws_size, hipStream_t stream) {
  (void)hipFuncSetAttribute((const void*)fused_kernel, hipFuncAttributeMaxDynamicSharedMemorySize, LDS_BYTES);
  fused_kernel<<<256, 768, LDS_BYTES, stream>>>((const u16*)d_in[0], (const u16*)d_in[1], (const u16*)d_in[2], (const u16*)d_in[3],
                                                (const u16*)d_in[4], (const u16*)d_in[5], (const u16*)d_in[6], (const u16*)d_in[7],
                                                (const u16*)d_in[8], (const u16*)d_in[9], (const u16*)d_in[10], (const u16*)d_in[11],
                                                (const u16*)d_in[12], (u16*)d_out);
}
```

```cpp
#include <hip/hip_runtime.h>
#include <hip/hip_bf16.h>
#include <stdint.h>
#include <utility>

typedef unsigned int u32;
typedef unsigned short u16;
typedef u32 u32x4 __attribute__((ext_vector_type(4)));
typedef u32 u32x2 __attribute__((ext_vector_type(2)));
typedef float f32x4 __attribute__((ext_vector_type(4)));
typedef float f32x2 __attribute__((ext_vector_type(2)));
typedef __bf16 bf16x8 __attribute__((ext_vector_type(8)));
typedef _Float16 f16x8 __attribute__((ext_vector_type(8)));

#define L_SEQ 2048
#define D_CH 1024
#define N_MODE 128

__device__ __forceinline__ float bflo(u32 d) { return __uint_as_float(d << 16); }
__device__ __forceinline__ float bfhi(u32 d) { return __uint_as_float(d & 0xffff0000u); }
__device__ __forceinline__ u32 pk_bf16(float lo, float hi) {
  typedef __bf16 bf16x2 __attribute__((ext_vector_type(2)));
  f32x2 v = {lo, hi};
  bf16x2 r = __builtin_convertvector(v, bf16x2);
  return __builtin_bit_cast(u32, r);
}
__device__ __forceinline__ float rbf(float x) { return __uint_as_float(pk_bf16(0.f, x)); }

__device__ __forceinline__ u32 wave_max_u32(u32 x) {
  x = max(x, (u32)__builtin_amdgcn_update_dpp(0, (int)x, 0x111, 0xf, 0xf, true));
  x = max(x, (u32)__builtin_amdgcn_update_dpp(0, (int)x, 0x112, 0xf, 0xf, true));
  x = max(x, (u32)__builtin_amdgcn_update_dpp(0, (int)x, 0x114, 0xf, 0xf, true));
  x = max(x, (u32)__builtin_amdgcn_update_dpp(0, (int)x, 0x118, 0xf, 0xf, true));
  x = max(x, (u32)__builtin_amdgcn_update_dpp(0, (int)x, 0x142, 0xa, 0xf, true));
  x = max(x, (u32)__builtin_amdgcn_update_dpp(0, (int)x, 0x143, 0xc, 0xf, true));
  return (u32)__builtin_amdgcn_readlane((int)x, 63);
}

struct cf { float re, im; };
__device__ __forceinline__ cf cmul(cf a, cf b) {
  cf o;
  asm("v_mul_f32 %0, %2, %4\n\tv_mul_f32 %1, %2, %5\n\tv_fma_f32 %0, -%3, %5, %0\n\tv_fma_f32 %1, %3, %4, %1"
      : "=&v"(o.re), "=&v"(o.im) : "v"(a.re), "v"(a.im), "v"(b.re), "v"(b.im));
  return o;
}
__device__ __forceinline__ u32x2 hmul2(u32x2 a, u32x2 b) {
  u32 o0, o1;
  asm("v_pk_mul_f16 %0, %2, %4 op_sel:[0,0] op_sel_hi:[0,1]\n\t"
      "v_pk_mul_f16 %1, %3, %5 op_sel:[0,0] op_sel_hi:[0,1]\n\t"
      "v_pk_fma_f16 %0, %2, %4, %0 op_sel:[1,1,0] op_sel_hi:[1,0,1] neg_lo:[1,0,0]\n\t"
      "v_pk_fma_f16 %1, %3, %5, %1 op_sel:[1,1,0] op_sel_hi:[1,0,1] neg_lo:[1,0,0]"
      : "=&v"(o0), "=&v"(o1) : "v"(a[0]), "v"(a[1]), "v"(b[0]), "v"(b[1]));
  return (u32x2){o0, o1};
}
__device__ __forceinline__ f32x2 cexp_pos(float ar, float ai, float pos) {
  const float LOG2E = 1.44269504088896340736f;
  const float I2PI_HI = 0.15915494309189533577f;
  const float I2PI_LO = 6.4206383e-9f;
  float xr = ar * pos;
  float xi = ai * pos;
  float e = __builtin_amdgcn_exp2f(xr * LOG2E);
  float r = xi * I2PI_HI;
  float err = __builtin_fmaf(xi, I2PI_HI, -r);
  float f = __builtin_amdgcn_fractf(r);
  f += __builtin_fmaf(xi, I2PI_LO, err);
  f32x2 o;
  o[0] = e * __builtin_amdgcn_cosf(f);
  o[1] = e * __builtin_amdgcn_sinf(f);
  return o;
}
__device__ __forceinline__ u32 pk_f16(float lo, float hi) {
  typedef _Float16 f16x2 __attribute__((ext_vector_type(2)));
  f32x2 v = {lo, hi};
  return __builtin_bit_cast(u32, __builtin_convertvector(v, f16x2));
}

__device__ __forceinline__ f32x4 mma_f16(u32x4 a, u32x4 b, f32x4 c) {
  return __builtin_amdgcn_mfma_f32_16x16x32_f16(__builtin_bit_cast(f16x8, a), __builtin_bit_cast(f16x8, b), c, 0, 0, 0);
}
__device__ __forceinline__ f32x4 mma_bf16(u32x4 a, u32x4 b, f32x4 c) {
  return __builtin_amdgcn_mfma_f32_16x16x32_bf16(__builtin_bit_cast(bf16x8, a), __builtin_bit_cast(bf16x8, b), c, 0, 0, 0);
}

__device__ __forceinline__ u32 lds_off(const char* p) {
  return (u32)(uintptr_t)(const __attribute__((address_space(3))) char*)p;
}

template <class F, int... I>
__device__ __forceinline__ void sfor_impl(F&& f, std::integer_sequence<int, I...>) { (f(std::integral_constant<int, I>{}), ...); }
template <int N, class F>
__device__ __forceinline__ void sfor(F&& f) { sfor_impl(f, std::make_integer_sequence<int, N>{}); }

#define XP 2304
#define XW (2 * XP + 16)
#define HS_OFF (8 * XW)
#define HW 8768
#define C2 4228
#define RAW_OFF (HS_OFF + 8 * HW)
#define HALO_OFF (RAW_OFF + 49152)
#define FL_OFF (HALO_OFF + 768)
#define FL_XQ 0
#define FL_Y 32
#define LDS_BYTES (FL_OFF + 512)
#define GT_ROW 544

#define KR 7360
#define BAND_OFF (RAW_OFF + 32768)
__device__ __forceinline__ float exp_bf(float x) {
  x = __builtin_amdgcn_fmed3f(x, -120.0f, 88.9f);
  const float hi = x * 1.44269504088896340736f;
  float d = __builtin_fmaf(-hi, 0.693147182464599609375f, x);
  d = __builtin_fmaf(-hi, -1.90465429995776804525e-9f, d);
  const float e = __builtin_amdgcn_exp2f(hi);
  return rbf(__builtin_fmaf(e, d, e));
}
__device__ __forceinline__ u32 pk_h(cf z) { u32 o; asm("v_cvt_pk_f16_f32 %0, %1, %2" : "=v"(o) : "v"(z.re), "v"(z.im)); return o; }
__device__ __forceinline__ u32 pk_hc(cf z) { u32 o; asm("v_cvt_pk_f16_f32 %0, %1, -%2" : "=v"(o) : "v"(z.re), "v"(z.im)); return o; }
__device__ __forceinline__ float fmul(float a, float b) { float o; asm("v_mul_f32 %0, %1, %2" : "=v"(o) : "v"(a), "v"(b)); return o; }
__device__ __forceinline__ u32x2 hmul2c(u32x2 a, u32x2 b) {
  u32 o0, o1;
  asm("v_pk_mul_f16 %0, %2, %4 op_sel:[0,0] op_sel_hi:[0,1] neg_hi:[1,0]\n\t"
      "v_pk_mul_f16 %1, %3, %5 op_sel:[0,0] op_sel_hi:[0,1] neg_hi:[1,0]\n\t"
      "v_pk_fma_f16 %0, %2, %4, %0 op_sel:[1,1,0] op_sel_hi:[1,0,1]\n\t"
      "v_pk_fma_f16 %1, %3, %5, %1 op_sel:[1,1,0] op_sel_hi:[1,0,1]"
      : "=&v"(o0), "=&v"(o1) : "v"(a[0]), "v"(a[1]), "v"(b[0]), "v"(b[1]));
  return (u32x2){o0, o1};
}
template <int J> __device__ __forceinline__ void put2(u32 base, u32x2 a, u32x2 b) {
  asm volatile("ds_write2_b64 %0, %1, %2 offset0:%3 offset1:%4" :: "v"(base), "v"(a), "v"(b), "i"(J * (GT_ROW / 4)), "i"(J * (GT_ROW / 4) + GT_ROW / 8) : "memory");
}
__device__ __forceinline__ void get8(u32x4 (&d)[8], u32 rp) {
  asm volatile("ds_read_b128 %0, %8\n\tds_read_b128 %1, %8 offset:64\n\tds_read_b128 %2, %8 offset:128\n\tds_read_b128 %3, %8 offset:192\n\t"
               "ds_read_b128 %4, %8 offset:256\n\tds_read_b128 %5, %8 offset:320\n\tds_read_b128 %6, %8 offset:384\n\tds_read_b128 %7, %8 offset:448"
               : "=&v"(d[0]), "=&v"(d[1]), "=&v"(d[2]), "=&v"(d[3]), "=&v"(d[4]), "=&v"(d[5]), "=&v"(d[6]), "=&v"(d[7]) : "v"(rp) : "memory");
}
template <int N> __device__ __forceinline__ void wait8(u32x4 (&d)[8]) {
  asm volatile("s_waitcnt lgkmcnt(%8)" : "+v"(d[0]), "+v"(d[1]), "+v"(d[2]), "+v"(d[3]), "+v"(d[4]), "+v"(d[5]), "+v"(d[6]), "+v"(d[7]) : "i"(N) : "memory");
}
__device__ __forceinline__ f32x4 mma8(const u32x4 (&g)[8], const u32x4 (&f)[8]) {
  f32x4 a = {0.f, 0.f, 0.f, 0.f};
#pragma unroll
  for (int k = 0; k < 8; k++) a = mma_f16(g[k], f[k], a);
  return a;
}
template <int OFF> __device__ __forceinline__ void dsw128(u32 addr, u32x4 val) {
  asm volatile("ds_write_b128 %0, %1 offset:%2" :: "v"(addr), "v"(val), "i"(OFF) : "memory");
}
template <int OFF> __device__ __forceinline__ void dsw2(u32 addr, u32 a, u32 b) {
  asm volatile("ds_write2_b32 %0, %1, %2 offset0:%3 offset1:%3+1" :: "v"(addr), "v"(a), "v"(b), "i"(OFF) : "memory");
}

struct GenAddr { u32 wb[4], rp, h16, t0, t0c, t1c, pz, kr; };
__device__ __forceinline__ void gen_taps(const GenAddr& ad, bool odd, const float (&pf)[9]) {
  const float dt = exp_bf(pf[0]);
  u32 mc;
  asm("v_max3_f32 %0, |%1|, |%2|, |%3|\n\tv_max_f32 %0, %0, |%4|" : "=&v"(mc) : "v"(pf[5]), "v"(pf[6]), "v"(pf[7]), "v"(pf[8]));
  mc = max(wave_max_u32(mc), 0x0d800000u);
  const float dt2 = dt + dt;
  const int ec = __builtin_amdgcn_frexp_expf(__uint_as_float(mc) * dt2) - 14;
  const float cs = __builtin_ldexpf(dt2, -ec);
  u32x2 h1, h2, h4, f8, f16, f32, f64, h512, h1024, P[4];
#pragma unroll
  for (int m = 0; m < 2; m++) {
    const float I2PI_HI = 0.15915494309189533577f;
    const float I2PI_LO = 6.4206383e-9f;
    const float al = (-exp_bf(pf[1 + m]) * dt) * 1.44269504088896340736f;
    const float ai = -pf[3 + m] * dt;
    const float rh = ai * I2PI_HI;
    const float rl = __builtin_fmaf(ai, I2PI_LO, __builtin_fmaf(ai, I2PI_HI, -rh));
    const float fa = __builtin_amdgcn_fractf(rh) + rl, fb = __builtin_fmaf(rl, 128.0f, __builtin_amdgcn_fractf(rh * 128.0f));
    const float ea = __builtin_amdgcn_exp2f(al), eb = __builtin_amdgcn_exp2f(al * 128.0f);
    const cf z1 = {fmul(ea, __builtin_amdgcn_cosf(fa)), fmul(ea, __builtin_amdgcn_sinf(fa))};
    const cf z128 = {fmul(eb, __builtin_amdgcn_cosf(fb)), fmul(eb, __builtin_amdgcn_sinf(fb))};
    const cf z2 = cmul(z1, z1), z256 = cmul(z128, z128), z4 = cmul(z2, z2), z512 = cmul(z256, z256), z8 = cmul(z4, z4);
    const cf z1024 = cmul(z512, z512), z16 = cmul(z8, z8), z32 = cmul(z16, z16), z64 = cmul(z32, z32);
    const cf p0 = {fmul(pf[5 + m], cs), fmul(pf[7 + m], -cs)}, p1 = cmul(p0, z128), p2 = cmul(p0, z256), p3 = cmul(p1, z256);
    h1[m] = pk_h(z1); h2[m] = pk_h(z2); h4[m] = pk_h(z4); h512[m] = pk_h(z512); h1024[m] = pk_h(z1024);
    f8[m] = pk_hc(z8); f16[m] = pk_hc(z16); f32[m] = pk_hc(z32); f64[m] = pk_hc(z64);
    P[0][m] = pk_h(p0); P[1][m] = pk_h(p1); P[2][m] = pk_h(p2); P[3][m] = pk_h(p3);
  }
  u32x4 Fh[8], Ga[8], Gb[8];
  u32x2 r512, r1928;
  {
#pragma unroll
    for (int al = 0; al < 4; al++) {
      const u32x2 a = hmul2(P[al], h512), b = hmul2(P[al], h1024), c = hmul2(b, h512);
      put2<0>(ad.wb[al], a, hmul2(a, h4));
      put2<1>(ad.wb[al], b, c);
      if (al == 0) r512 = a;
      if (al == 3) r1928 = hmul2c(c, f8);
    }
    get8(Ga, ad.rp);
  }
  {
    const u32x2 f24 = hmul2(f8, f16), f96 = hmul2(f32, f64);
    put2<0>(ad.wb[0], (u32x2){0x3c00u, 0x3c00u}, f8);
    put2<1>(ad.wb[0], f16, f24);
#pragma unroll
    for (int a = 0; a < 3; a++) {
      const u32x2 b = a == 0 ? f32 : a == 1 ? f64 : f96;
      put2<0>(ad.wb[1 + a], b, hmul2(b, f8));
      put2<1>(ad.wb[1 + a], hmul2(b, f16), hmul2(b, f24));
    }
    get8(Fh, ad.rp);
  }
#pragma unroll
  for (int al = 0; al < 2; al++) {
    const u32x2 b = hmul2(P[2 + al], h4);
    put2<0>(ad.wb[al], P[2 + al], hmul2(P[2 + al], h2));
    put2<1>(ad.wb[al], b, hmul2(b, h2));
  }
  asm volatile("ds_write_b64 %0, %1\n\tds_write_b64 %2, %3" :: "v"(ad.wb[2]), "v"(r512), "v"(ad.wb[3]), "v"(r1928) : "memory");
  get8(Gb, ad.rp);
  wait8<14>(Ga);
  wait8<14>(Fh);
  f32x4 k2 = mma8(Ga, Fh);
  __builtin_amdgcn_sched_barrier(0);
#pragma unroll
  for (int i = 0; i < 4; i++) {
    const u32x2 b = (i & 1) ? hmul2(P[i >> 1], h4) : P[i >> 1], c = hmul2(b, h1);
    put2<0>(ad.wb[i], b, c);
    put2<1>(ad.wb[i], hmul2(b, h2), hmul2(c, h2));
  }
  asm volatile("" : "+v"(k2));
  get8(Ga, ad.rp);
  wait8<15>(Gb);
  f32x4 k1 = mma8(Gb, Fh);
  __builtin_amdgcn_sched_barrier(0);
#define DPP(old, x, ctl) __int_as_float(__builtin_amdgcn_update_dpp(__float_as_int(old), __float_as_int(x), ctl, 0xf, 0xf, false))
#pragma unroll
  for (int j = 0; j < 4; j++) k2[j] = __builtin_ldexpf(k2[j], ec);
  {
    const float f2 = DPP(DPP(0.f, k2[2], 0x134), k2[0], 0x130);
    const u32 d0 = pk_bf16(k2[0], k2[0]), d1 = pk_bf16(k2[0], k2[1]), d2 = pk_bf16(k2[1], k2[1]);
    dsw128<C2 + 1052>(ad.h16, (u32x4){d0, d0, d1, d2});
    dsw128<1056>(ad.h16, (u32x4){d0, d1, d2, pk_bf16(f2, f2)});
  }
  {
    float a;
    asm("v_cndmask_b32 %0, 0, %1, %2" : "=v"(a) : "v"(k2[2]), "s"(63ull));
    const float b = DPP(0.f, a, 0x130);
    const u32 aa = pk_bf16(a, a), ab = pk_bf16(odd ? b : a, b);
    dsw128<2080>(ad.h16, (u32x4){aa, aa, ab, pk_bf16(b, b)});
    dsw128<C2 + 2076>(ad.h16, (u32x4){aa, aa, aa, ab});
    asm volatile("ds_write_b16 %0, %1 offset:128\n\tds_write_b16_d16_hi %0, %1" :: "v"(ad.kr), "v"(pk_bf16(k2[2], k2[3])) : "memory");
  }
  __builtin_amdgcn_sched_barrier(0);
  wait8<6>(Ga);
  f32x4 k0 = mma8(Ga, Fh);
  __builtin_amdgcn_sched_barrier(0);
#pragma unroll
  for (int j = 0; j < 4; j++) k1[j] = __builtin_ldexpf(k1[j], ec);
  {
    const float e4 = DPP(0.f, k1[0], 0x134);
    const u32x4 o = {pk_bf16(k1[0], k1[0]), pk_bf16(k1[1], k1[2]), pk_bf16(k1[2], k1[2]), pk_bf16(k1[3], e4)};
    asm volatile("s_mov_b32 exec_hi, 0\n\tds_write_b128 %0, %2 offset:544\n\tds_write2_b32 %1, %3, %4 offset0:0 offset1:1\n\t"
                 "ds_write2_b32 %1, %5, %6 offset0:2 offset1:3\n\ts_mov_b32 exec_lo, 0xff\n\tds_write_b32 %7, %8\n\tds_write_b32 %7, %8 offset:%9\n\t"
                 "s_mov_b32 exec_lo, 0x1ffff\n\tds_write_b32 %7, %8 offset:%10+248\n\ts_lshl_b64 exec, 1, 63\n\tds_write_b32 %7, %3 offset:%10-252\n\t"
                 "s_mov_b64 exec, -1"
                 :: "v"(ad.h16), "v"(ad.t1c), "v"(o), "v"(o[0]), "v"(o[1]), "v"(o[2]), "v"(o[3]), "v"(ad.pz), "v"(0u), "i"(C2), "i"(KR)
                 : "memory", "scc");
  }
  __builtin_amdgcn_sched_barrier(0);
#pragma unroll
  for (int j = 0; j < 4; j++) k0[j] = __builtin_ldexpf(k0[j], ec);
  const u32 d0 = pk_bf16(k0[0], k0[1]), d1 = pk_bf16(k0[2], k0[3]);
  dsw2<0>(ad.t0, d0, d1);
  dsw2<0>(ad.t0c, d0, d1);
}

#define POLL_SLEEP 1
#define E_PRIO0 "s_setprio 2\n\t"
#define E_PRIO1 "s_setprio 1\n\t"
#define E_PRIO2 "s_setprio 1\n\t"
#define E_PRIO3 "s_setprio 0\n\t"
#define H_PRIO0 3
#define H_PRIO1 3
#define H_PRIO2 3
#define H_PRIO3 3
#define DIAG(k)
#define A_DELAY0
#define LINE_WAIT "s_waitcnt vmcnt(3)"
#define A_ORDER 3
#define A_R2
#define B_ORDER 6
#define AUX0 0
#define AUX1 0
#define WB_HOOK
#define BX 0
#define A_DELAY1
#define E_DECL
#define E_OUT
#define E_SAVE
#define E_STAMP0
#define E_STAMP1
#define E_STAMP2
#define E_STAMP3
#define E_STAMP4
#define E_STAMP5
#define E_CLOB "v8", "v9", "v10", "v11", "v12", "v13", "v14", "v15", "v16", "v17", "v18", "v19", "v20", "v21", "v22", "v23", "v24", \
  "v25", "v26", "v27", "v28", "v29", "v30", "v31", "v32", "v33", "v34", "v35", "v36", "v37", "v38", "v39", "v40", "v41", \
  "v42", "v43", "v44", "v45", "v46", "v47", "v48", "v49", "v50", "v51", "v52", "v53", "v54", "v55", "v56", "v57", "v58", \
  "v59", "v60", "v61", "v62", "v63", "v64", "v65", "v66", "v67", "v68", "v69", "v70", "v71", "v72", "v73", "v74", "v75", \
  "v76", "v77", "v78", "v79", "v80", "v81", "v82", "v83", "v84", "v85", "v86", "v87", "v88", "v89", "v90", "v91", "v92", \
  "v93", "v94", "v95", "v96", "v97", "v98", "v99", "v100", "v101", "v102", "v103", "v104", "v105", "v106", "v107", \
  "v108", "v109", "v110", "v111", "v112", "v113", "v114", "v115", "v116", "v117", "v118", "v119", "v120", "v121", \
  "v122", "v123", "v124", "v125", "v126", "v127", "v132", "v133", "v134", "v135", "v136", "v137", "v138", "v139", \
  "v140", "v141", "v142", "v143", "v144", "v145", "v146", "v147", "v148", "v149", "v150", "v151", "v152", "v153", \
  "v154", "v155", "v162", "v163", "v164", "v165", "v166"
#define G_PRIO1 2
#define G_PRIO2 1
#define G_PRIO3 0
#define EA_PRIO0
#define EA_PRIO1
#define EA_PRIO2 "s_setprio 0\n\t"
#define EA_PRIO3
__device__ __forceinline__ void dma16(const void* base, u32 off, u32 lds_base) {
  asm volatile("s_mov_b32 m0, %2\n\ts_nop 0\n\tglobal_load_lds_dwordx4 %0, %1" :: "v"(off), "s"(base), "s"(lds_base) : "memory", "m0");
}
__device__ __forceinline__ void poll_all(u32 addr, u32 val) {
  for (;;) {
    u32 fl;
    asm volatile("ds_read_b32 %0, %1\n\ts_waitcnt lgkmcnt(0)" : "=v"(fl) : "v"(addr) : "memory");
    if (__builtin_amdgcn_ballot_w64(fl < val) == 0ull) break;
    __builtin_amdgcn_s_sleep(POLL_SLEEP);
  }
}
__device__ __forceinline__ void lds_add(u32 addr, u32 val) {
  asm volatile("s_mov_b64 exec, 1\n\tds_add_u32 %0, %1\n\ts_mov_b64 exec, -1" :: "v"(addr), "v"(val) : "memory");
}

template <int OFF> __device__ __forceinline__ void dsr64tr(u32x2& dst, u32 addr) {
  asm volatile("ds_read_b64_tr_b16 %0, %1 offset:%2" : "=v"(dst) : "v"(addr), "i"(OFF) : "memory");
}
__device__ __forceinline__ void line_req(u32& dst, const void* p) {
  asm volatile("global_load_dword %0, %1, off" : "=v"(dst) : "v"(p) : "memory");
}
template <int N> __device__ __forceinline__ void wait_vm() { asm volatile("s_waitcnt vmcnt(%0)" :: "i"(N) : "memory"); }

#define P_CLOB1 "v100", "v101", "v102", "v103", "v104", "v105", "v106", "v107", "v108", "v109", "v110", "v111", "v112", "v113", "v114", \
  "v115", "v116", "v117", "v118", "v119", "v120", "v121", "v122", "v123", "v124", "v125", "v126", "v127", "v128", "v129", \
  "v130", "v131", "v132", "v133", "v134", "v135", "v136", "v137", "v138", "v139", "v140", "v141", "v142", "v143", "v144", \
  "v145", "v146", "v147"
#define P_CLOB P_CLOB1, "v148", "v149", "v150", "v151", "v152", "v153", "v154", "v155", "v156", "v157", "v158", "v159", "v160", "v161", "v162", \
  "v163"
template <int RO, int XO> __device__ __forceinline__ void kv_1(u32 ka0, u32 ka, u32 va, u32 xw, u32x4 w1, u32x4 w2) {
  asm volatile(
      "ds_read_b128 v[100:103], %0\n\tds_read_b128 v[104:107], %1 offset:%6+64\n\tds_read_b128 v[108:111], %1 offset:%6+512\n\t"
      "ds_read_b128 v[112:115], %1 offset:%6+576\n\tds_read_b64_tr_b16 v[132:133], %2 offset:%6+0\n\t"
      "ds_read_b64_tr_b16 v[134:135], %2 offset:%6+512\n\ts_waitcnt lgkmcnt(5)\n\t"
      "v_mfma_f32_16x16x32_bf16 v[140:143], v[100:103], %4, 0\n\ts_waitcnt lgkmcnt(4)\n\t"
      "v_mfma_f32_16x16x32_bf16 v[144:147], v[104:107], %4, 0\n\ts_waitcnt lgkmcnt(3)\n\t"
      "v_mfma_f32_16x16x32_bf16 v[140:143], v[108:111], %5, v[140:143]\n\ts_waitcnt lgkmcnt(2)\n\t"
      "v_mfma_f32_16x16x32_bf16 v[144:147], v[112:115], %5, v[144:147]\n\ts_waitcnt lgkmcnt(0)\n\tv_lshlrev_b32 v116, 16, v132\n\t"
      "v_and_b32 v117, 0xffff0000, v132\n\tv_lshlrev_b32 v118, 16, v133\n\tv_and_b32 v119, 0xffff0000, v133\n\t"
      "v_lshlrev_b32 v120, 16, v134\n\tv_and_b32 v121, 0xffff0000, v134\n\tv_lshlrev_b32 v122, 16, v135\n\t"
      "v_and_b32 v123, 0xffff0000, v135\n\tv_mul_f32 v140, v140, v116\n\tv_mul_f32 v141, v141, v117\n\t"
      "v_mul_f32 v142, v142, v118\n\tv_mul_f32 v143, v143, v119\n\tv_mul_f32 v144, v144, v120\n\tv_mul_f32 v145, v145, v121\n\t"
      "v_mul_f32 v146, v146, v122\n\tv_mul_f32 v147, v147, v123\n\tv_cvt_pk_bf16_f32 v116, v140, v142\n\t"
      "v_cvt_pk_bf16_f32 v117, v144, v146\n\tv_cvt_pk_bf16_f32 v118, v141, v143\n\tv_cvt_pk_bf16_f32 v119, v145, v147\n\t"
      "ds_write_b64 %3, v[116:117] offset:%7+0\n\tds_write_b64 %3, v[118:119] offset:%7+2304\n\t"
      :: "v"(ka0), "v"(ka), "v"(va), "v"(xw), "v"(w1), "v"(w2), "i"(RO), "i"(XO) : "memory", P_CLOB1);
}
template <int RO, int XO> __device__ __forceinline__ void kv_2(u32 ka0, u32 ka, u32 va, u32 xw, u32x4 w1, u32x4 w2) {
  asm volatile(
      "ds_read_b128 v[100:103], %0\n\tds_read_b128 v[104:107], %1 offset:%6+64\n\tds_read_b128 v[108:111], %1 offset:%6+512\n\t"
      "ds_read_b128 v[112:115], %1 offset:%6+576\n\tds_read_b128 v[116:119], %1 offset:%6+1024\n\t"
      "ds_read_b128 v[120:123], %1 offset:%6+1088\n\tds_read_b128 v[124:127], %1 offset:%6+1536\n\t"
      "ds_read_b128 v[128:131], %1 offset:%6+1600\n\tds_read_b64_tr_b16 v[132:133], %2 offset:%6+0\n\t"
      "ds_read_b64_tr_b16 v[134:135], %2 offset:%6+512\n\tds_read_b64_tr_b16 v[136:137], %2 offset:%6+1024\n\t"
      "ds_read_b64_tr_b16 v[138:139], %2 offset:%6+1536\n\ts_waitcnt lgkmcnt(11)\n\t"
      "v_mfma_f32_16x16x32_bf16 v[140:143], v[100:103], %4, 0\n\ts_waitcnt lgkmcnt(10)\n\t"
      "v_mfma_f32_16x16x32_bf16 v[144:147], v[104:107], %4, 0\n\ts_waitcnt lgkmcnt(9)\n\t"
      "v_mfma_f32_16x16x32_bf16 v[140:143], v[108:111], %5, v[140:143]\n\ts_waitcnt lgkmcnt(8)\n\t"
      "v_mfma_f32_16x16x32_bf16 v[144:147], v[112:115], %5, v[144:147]\n\ts_waitcnt lgkmcnt(7)\n\t"
      "v_mfma_f32_16x16x32_bf16 v[148:151], v[116:119], %4, 0\n\ts_waitcnt lgkmcnt(6)\n\t"
      "v_mfma_f32_16x16x32_bf16 v[152:155], v[120:123], %4, 0\n\ts_waitcnt lgkmcnt(5)\n\t"
      "v_mfma_f32_16x16x32_bf16 v[148:151], v[124:127], %5, v[148:151]\n\ts_waitcnt lgkmcnt(4)\n\t"
      "v_mfma_f32_16x16x32_bf16 v[152:155], v[128:131], %5, v[152:155]\n\ts_waitcnt lgkmcnt(2)\n\tv_lshlrev_b32 v156, 16, v132\n\t"
      "v_and_b32 v157, 0xffff0000, v132\n\tv_lshlrev_b32 v158, 16, v133\n\tv_and_b32 v159, 0xffff0000, v133\n\t"
      "v_lshlrev_b32 v160, 16, v134\n\tv_and_b32 v161, 0xffff0000, v134\n\tv_lshlrev_b32 v162, 16, v135\n\t"
      "v_and_b32 v163, 0xffff0000, v135\n\tv_mul_f32 v140, v140, v156\n\tv_mul_f32 v141, v141, v157\n\t"
      "v_mul_f32 v142, v142, v158\n\tv_mul_f32 v143, v143, v159\n\tv_mul_f32 v144, v144, v160\n\tv_mul_f32 v145, v145, v161\n\t"
      "v_mul_f32 v146, v146, v162\n\tv_mul_f32 v147, v147, v163\n\tv_cvt_pk_bf16_f32 v156, v140, v142\n\t"
      "v_cvt_pk_bf16_f32 v157, v144, v146\n\tv_cvt_pk_bf16_f32 v158, v141, v143\n\tv_cvt_pk_bf16_f32 v159, v145, v147\n\t"
      "ds_write_b64 %3, v[156:157] offset:%7+0\n\tds_write_b64 %3, v[158:159] offset:%7+2304\n\ts_waitcnt lgkmcnt(2)\n\t"
      "v_lshlrev_b32 v156, 16, v136\n\tv_and_b32 v157, 0xffff0000, v136\n\tv_lshlrev_b32 v158, 16, v137\n\t"
      "v_and_b32 v159, 0xffff0000, v137\n\tv_lshlrev_b32 v160, 16, v138\n\tv_and_b32 v161, 0xffff0000, v138\n\t"
      "v_lshlrev_b32 v162, 16, v139\n\tv_and_b32 v163, 0xffff0000, v139\n\tv_mul_f32 v148, v148, v156\n\t"
      "v_mul_f32 v149, v149, v157\n\tv_mul_f32 v150, v150, v158\n\tv_mul_f32 v151, v151, v159\n\tv_mul_f32 v152, v152, v160\n\t"
      "v_mul_f32 v153, v153, v161\n\tv_mul_f32 v154, v154, v162\n\tv_mul_f32 v155, v155, v163\n\t"
      "v_cvt_pk_bf16_f32 v156, v148, v150\n\tv_cvt_pk_bf16_f32 v157, v152, v154\n\tv_cvt_pk_bf16_f32 v158, v149, v151\n\t"
      "v_cvt_pk_bf16_f32 v159, v153, v155\n\tds_write_b64 %3, v[156:157] offset:%7+64\n\t"
      "ds_write_b64 %3, v[158:159] offset:%7+2368\n\t"
      :: "v"(ka0), "v"(ka), "v"(va), "v"(xw), "v"(w1), "v"(w2), "i"(RO), "i"(XO) : "memory", P_CLOB);
}
__device__ __forceinline__ void kv_weights(u32 sw, int lane, u32x4& w1, u32x4& w2) {
  const u32 s = sw << (16 * (lane & 1));
  const int d = (lane >> 1) & 7;
#pragma unroll
  for (int i = 0; i < 4; i++) { w1[i] = d == i ? s : 0u; w2[i] = d == 4 + i ? s : 0u; }
}

template <int AUX> __device__ __forceinline__ void out_a(u32 ya, u32 oo, __amdgpu_buffer_rsrc_t ro) {
  u32x2 t0, t1;
  dsr64tr<0>(t0, ya);
  dsr64tr<4 * HW>(t1, ya);
  asm volatile("s_waitcnt lgkmcnt(0)" : "+v"(t0), "+v"(t1) :: "memory");
  __builtin_amdgcn_raw_buffer_store_b128((u32x4){t0[0], t0[1], t1[0], t1[1]}, ro, oo, 0, AUX);
}
template <int AUX> __device__ __forceinline__ void out_b(u32 ya, u32 qa, u32 oo, __amdgpu_buffer_rsrc_t ro) {
  u32x2 t[4];
  u32x4 qv, o;
  asm volatile("ds_read_b128 %0, %1" : "=v"(qv) : "v"(qa) : "memory");
  sfor<4>([&](auto ic) { constexpr int i = decltype(ic)::value; dsr64tr<2 * i * HW>(t[i], ya); });
  asm volatile("s_waitcnt lgkmcnt(4)" : "+v"(qv) :: "memory");
  float ql[4], qh[4];
#pragma unroll
  for (int p = 0; p < 4; p++) { ql[p] = bflo(qv[p]); qh[p] = bfhi(qv[p]); }
  asm volatile("s_waitcnt lgkmcnt(0)" : "+v"(t[0]), "+v"(t[1]), "+v"(t[2]), "+v"(t[3]), "+v"(ql[0]), "+v"(ql[1]), "+v"(ql[2]), "+v"(ql[3]),
               "+v"(qh[0]), "+v"(qh[1]), "+v"(qh[2]), "+v"(qh[3]) :: "memory");
#pragma unroll
  for (int p = 0; p < 4; p++) {
    asm("v_mul_f32 %0, %0, %2\n\tv_mul_f32 %1, %1, %3" : "+v"(ql[p]), "+v"(qh[p]) : "v"(t[p][0]), "v"(t[p][1]));
    o[p] = pk_bf16(ql[p], qh[p]);
  }
  __builtin_amdgcn_raw_buffer_store_b128(o, ro, oo, 0, AUX);
}

__device__ __forceinline__ void main_body( char* smem, int g, int w, u32x4 l4, u32 la, u32 lb, u32x2 pfx, u32 so,
                                          const u16* __restrict__ kin, const u16* __restrict__ vin, const u16* __restrict__ shk,
                                          const u16* __restrict__ ssmk) {
  int lane = threadIdx.x;
  asm volatile("v_and_b32 %0, 63, %0" : "+v"(lane));
  __builtin_assume(lane >= 0 && lane < 64);
  const bool isa = g < 128;
  const int gc = (g & 127) * 8;
  const u32 sb = lds_off(smem);
  const u32 fl0 = sb + FL_OFF;
  const u32 hsa = sb + HS_OFF + w * HW;
  const int r = lane & 15, q = lane >> 4;
  u32 xb = sb + w * XW + 16 + 16 * (r + q);
  u32 tb = hsa + 112 - 32 * q - (r & 12) + 8 * (r & 3) + ((r & 4) ? C2 : 0);
  u32 yw = hsa + 32 * r + 8 * q;
  u32 nb = 4 * (q ? lane - 16 : r ? lane + 47 : 63);
  u32 ds = 8 * (r & 2);
  u32 p_d;
  u32 fa = fl0, fy = fl0 + FL_Y + 4 * w;
#define PIN asm volatile("" : "+{v156}"(xb), "+{v157}"(tb), "+{v158}"(yw), "+{v159}"(nb), "+{v160}"(fa), "+{v161}"(fy), "+v"(ds));
  if (isa) {
    if (w < 4) __builtin_amdgcn_s_setprio(G_PRIO1);
    else __builtin_amdgcn_s_setprio(G_PRIO2);
    asm volatile("s_barrier" ::: "memory");
    GenAddr ad;
#pragma unroll
    for (int i = 0; i < 4; i++) ad.wb[i] = hsa + 8 * lane + 4 * GT_ROW * i;
    ad.rp = hsa + r * GT_ROW + 16 * q;
    ad.h16 = hsa + 16 * lane;
    ad.t0 = hsa + 32 + 256 * (q >> 1) + 8 * (q & 1) + 16 * r;
    ad.t0c = ad.t0 + C2;
    ad.t1c = ad.h16 + 544 + C2;
    ad.pz = hsa + 4 * lane;
    ad.kr = hsa + KR + 130 - 2 * lane;
    PIN
    asm volatile("" : "+v"(ad.wb[0]), "+v"(ad.wb[1]), "+v"(ad.wb[2]), "+v"(ad.wb[3]), "+v"(ad.rp), "+v"(ad.h16), "+v"(ad.t0), "+v"(ad.t0c), "+v"(ad.t1c), "+v"(ad.pz),
                 "+v"(ad.kr));
    float pf[9];
    asm volatile("s_waitcnt vmcnt(0)\n\tv_lshlrev_b32 %0, 16, %10\n\tv_lshlrev_b32 %1, 16, %11\n\tv_and_b32 %2, 0xffff0000, %11\n\t"
                 "v_lshlrev_b32 %3, 16, %12\n\tv_and_b32 %4, 0xffff0000, %12\n\tv_lshlrev_b32 %5, 16, %13\n\tv_and_b32 %6, 0xffff0000, %13\n\t"
                 "v_lshlrev_b32 %7, 16, %14\n\tv_and_b32 %8, 0xffff0000, %14\n\tv_lshlrev_b32 %9, %17, %15"
                 : "=&v"(pf[0]), "=&v"(pf[1]), "=&v"(pf[2]), "=&v"(pf[3]), "=&v"(pf[4]), "=&v"(pf[5]), "=&v"(pf[6]), "=&v"(pf[7]), "=&v"(pf[8]), "=&v"(p_d)
                 : "v"(la), "v"(l4[0]), "v"(l4[1]), "v"(l4[2]), "v"(l4[3]), "v"(lb), "v"(pfx), "v"(ds) : "memory");
    __builtin_amdgcn_sched_barrier(0);
    gen_taps(ad, lane & 1, pf);
    if (w < 4) __builtin_amdgcn_s_setprio(G_PRIO3);
  } else {
    const int rg = lane & 7;
    u32 sw;
    asm volatile("global_load_ushort %0, %1, %2" : "=v"(sw) : "v"((gc + rg) * 8 + 6 - 2 * q), "s"(shk) : "memory");
    const u32 gk = (64 * w + lane) * (D_CH * 2) + gc * 2;
    const u32 gv = (64 * w + (lane & 27) + 8 * (lane & 4) + ((lane >> 3) & 4)) * (D_CH * 2) + gc * 2;
    const u32 rb = sb + RAW_OFF + 1024 * w;
    __builtin_amdgcn_sched_barrier(0);
    asm volatile(LINE_WAIT :: "v"(pfx) : "memory");
    asm volatile("s_mov_b32 m0, %2\n\ts_mov_b64 exec, %3\n\tglobal_load_lds_dwordx4 %0, %1\n\ts_mov_b64 exec, -1"
                 :: "v"(gk - 3 * (D_CH * 2)), "s"(kin), "s"(rb - 48), "s"(w ? 7ull : 0ull) : "memory", "m0");
    dma16(kin, gk, rb);
    dma16(vin, gv, rb + 8192);
    dma16(ssmk + 512, so, hsa + 1056);
    dma16(ssmk + 512, so, hsa + 1056 + C2);
    asm volatile("s_barrier" ::: "memory");
    __builtin_amdgcn_sched_barrier(0);
    const u32 ka = rb + 16 * (2 * (r & 12) + (r & 3) + q - 3);
    const u32 va = rb + 8192 + 128 * q + 4 * (r & 12) + 32 * (r & 2) + 8 * (r & 1);
    const u32 xw = sb + rg * XW + 256 + 64 * w + 4 * (r & 8) + 8 * q;
    PIN
    asm volatile("s_waitcnt vmcnt(4)\n\tv_lshlrev_b32 %1, %4, %2" : "+v"(sw), "=&v"(p_d) : "v"(lb), "v"(pfx), "v"(ds) : "memory");
    dsw128<32>(hsa + 16 * lane, l4);
    dsw2<0>(hsa + 16 * lane + 32 + C2, l4[0], l4[1]);
    dsw2<2>(hsa + 16 * lane + 32 + C2, l4[2], l4[3]);
    u32x4 w1, w2;
    kv_weights(sw, lane, w1, w2);
    asm volatile("s_waitcnt vmcnt(2) lgkmcnt(0)" : "+v"(w1), "+v"(w2) :: "memory");
    kv_1<0, 0>(ka, ka, va, xw, w1, w2);
    lds_add(fa, 1u);
  }
  __builtin_amdgcn_sched_barrier(0);
  {
    u32x4 dfr;
    asm("v_cndmask_b32 %0, 0, %4, %5\n\tv_cndmask_b32 %1, 0, %4, %6\n\tv_cndmask_b32 %2, 0, %4, %7\n\tv_cndmask_b32 %3, 0, %4, %8"
        : "=&v"(dfr[0]), "=&v"(dfr[1]), "=&v"(dfr[2]), "=&v"(dfr[3]) : "v"(p_d), "s"(5ull << 48), "s"(5ull << 52), "s"(5ull << 56), "s"(5ull << 60));
    E_DECL
    if (isa) {
      int l2 = threadIdx.x;
      asm volatile("v_and_b32 %0, 63, %0" : "+v"(l2));
      const u32 r2 = l2 & 15, q2 = (u32)l2 >> 4;
      const u32 vr = sb + w * XW + 224 + (q2 >> 1) * XP + 16 * (r2 + (q2 & 1)), vt = hsa + KR + 60 - 4 * r2 + 16 * q2, vb = sb + BAND_OFF + 16 * l2;
      asm volatile(
      "ds_read_b32 v164, v160\n\t"
      "ds_read_b64_tr_b16 v[8:9], v157 offset:400\n\t"
      "ds_read_b64_tr_b16 v[10:11], v157 offset:384\n\t"
      "ds_read_b64_tr_b16 v[12:13], v157 offset:912\n\t"
      "ds_read_b64_tr_b16 v[14:15], v157 offset:896\n\t"
      "ds_read_b64_tr_b16 v[16:17], v157 offset:1424\n\t"
      "ds_read_b64_tr_b16 v[18:19], v157 offset:1408\n\t"
      "ds_read_b64_tr_b16 v[20:21], v157 offset:1936\n\t"
      "ds_read_b64_tr_b16 v[22:23], v157 offset:1920\n\t"
      "ds_read_b64_tr_b16 v[24:25], v157 offset:272\n\t"
      "ds_read_b64_tr_b16 v[26:27], v157 offset:256\n\t"
      "ds_read_b64_tr_b16 v[28:29], v157 offset:784\n\t"
      "ds_read_b64_tr_b16 v[30:31], v157 offset:768\n\t"
      "ds_read_b64_tr_b16 v[32:33], v157 offset:1296\n\t"
      "ds_read_b64_tr_b16 v[34:35], v157 offset:1280\n\t"
      "ds_read_b64_tr_b16 v[36:37], v157 offset:1808\n\t"
      "ds_read_b64_tr_b16 v[38:39], v157 offset:1792\n\t"
      E_STAMP0
      "s_waitcnt lgkmcnt(15)\n\t"
      "v_readfirstlane_b32 s40, v164\n\t"
      "s_cmp_ge_u32 s40, 8\n\t"
      "s_cbranch_scc1 .Lqe0_%=\n\t"
      ".Lql0_%=:\n\t"
      "s_sleep 1\n\t"
      "ds_read_b32 v164, v160 offset:0\n\t"
      "s_waitcnt lgkmcnt(0)\n\t"
      "v_readfirstlane_b32 s40, v164\n\t"
      "s_cmp_lt_u32 s40, 8\n\t"
      "s_cbranch_scc1 .Lql0_%=\n\t"
      ".Lqe0_%=:\n\t"
      E_STAMP1
      "ds_read_b128 v[40:43], v156 offset:2304\n\t"
      "ds_read_b128 v[44:47], v156 offset:0\n\t"
      "ds_read_b128 v[48:51], v156 offset:2368\n\t"
      EA_PRIO0
      "s_waitcnt lgkmcnt(2)\n\t"
      "v_mfma_f32_16x16x32_bf16 v[52:55], v[8:11], v[40:43], 0\n\t"
      "ds_read_b64_tr_b16 v[56:57], v157 offset:144\n\t"
      "v_mfma_f32_16x16x32_bf16 v[60:63], v[12:15], v[40:43], 0\n\t"
      "ds_read_b64_tr_b16 v[58:59], v157 offset:128\n\t"
      "v_mfma_f32_16x16x32_bf16 v[64:67], v[16:19], v[40:43], 0\n\t"
      "ds_read_b128 v[68:71], v156 offset:64\n\t"
      "ds_read_b64_tr_b16 v[72:73], v157 offset:656\n\t"
      "v_mfma_f32_16x16x32_bf16 v[76:79], v[20:23], v[40:43], 0\n\t"
      "ds_read_b64_tr_b16 v[74:75], v157 offset:640\n\t"
      "s_waitcnt lgkmcnt(6)\n\t"
      "v_mfma_f32_16x16x32_bf16 v[80:83], v[8:11], v[44:47], 0\n\t"
      "ds_read_b64_tr_b16 v[84:85], v157 offset:1168\n\t"
      "v_mfma_f32_16x16x32_bf16 v[88:91], v[12:15], v[44:47], 0\n\t"
      "ds_read_b64_tr_b16 v[86:87], v157 offset:1152\n\t"
      "v_mfma_f32_16x16x32_bf16 v[92:95], v[16:19], v[44:47], 0\n\t"
      "ds_read_b128 v[96:99], v156 offset:2432\n\t"
      "ds_read_b64_tr_b16 v[100:101], v157 offset:1680\n\t"
      "v_mfma_f32_16x16x32_bf16 v[104:107], v[20:23], v[44:47], 0\n\t"
      "ds_read_b64_tr_b16 v[102:103], v157 offset:1664\n\t"
      "s_waitcnt lgkmcnt(10)\n\t"
      "v_mfma_f32_16x16x32_bf16 v[52:55], v[24:27], v[48:51], v[52:55]\n\t"
      "ds_read_b64_tr_b16 v[108:109], v157 offset:16\n\t"
      "v_mfma_f32_16x16x32_bf16 v[60:63], v[28:31], v[48:51], v[60:63]\n\t"
      "ds_read_b64_tr_b16 v[110:111], v157 offset:0\n\t"
      "v_mfma_f32_16x16x32_bf16 v[64:67], v[32:35], v[48:51], v[64:67]\n\t"
      "ds_read_b128 v[112:115], v156 offset:128\n\t"
      "ds_read_b64_tr_b16 v[116:117], v157 offset:528\n\t"
      "v_mfma_f32_16x16x32_bf16 v[76:79], v[36:39], v[48:51], v[76:79]\n\t"
      "ds_read_b64_tr_b16 v[118:119], v157 offset:512\n\t"
      "s_waitcnt lgkmcnt(12)\n\t"
      "v_mfma_f32_16x16x32_bf16 v[80:83], v[24:27], v[68:71], v[80:83]\n\t"
      "ds_read_b64_tr_b16 v[120:121], v157 offset:1040\n\t"
      "v_mfma_f32_16x16x32_bf16 v[88:91], v[28:31], v[68:71], v[88:91]\n\t"
      "ds_read_b64_tr_b16 v[122:123], v157 offset:1024\n\t"
      "v_mfma_f32_16x16x32_bf16 v[92:95], v[32:35], v[68:71], v[92:95]\n\t"
      "ds_read_b128 v[124:127], v156 offset:2496\n\t"
      "ds_read_b64_tr_b16 v[132:133], v157 offset:1552\n\t"
      "v_mfma_f32_16x16x32_bf16 v[104:107], v[36:39], v[68:71], v[104:107]\n\t"
      "ds_read_b64_tr_b16 v[134:135], v157 offset:1536\n\t"
      "s_waitcnt lgkmcnt(12)\n\t"
      "v_mfma_f32_16x16x32_bf16 v[52:55], v[56:59], v[96:99], v[52:55]\n\t"
      "ds_read_b64_tr_b16 v[136:137], v157 offset:2064\n\t"
      "v_mfma_f32_16x16x32_bf16 v[60:63], v[72:75], v[96:99], v[60:63]\n\t"
      "ds_read_b64_tr_b16 v[138:139], v157 offset:2048\n\t"
      "v_mfma_f32_16x16x32_bf16 v[64:67], v[84:87], v[96:99], v[64:67]\n\t"
      "s_waitcnt lgkmcnt(12)\n\t"
      "v_mfma_f32_16x16x32_bf16 v[76:79], v[100:103], v[96:99], v[76:79]\n\t"
      "s_waitcnt lgkmcnt(9)\n\t"
      "v_mfma_f32_16x16x32_bf16 v[80:83], v[56:59], v[112:115], v[80:83]\n\t"
      "ds_read_b128 v[140:143], v156 offset:192\n\t"
      "v_mfma_f32_16x16x32_bf16 v[88:91], v[72:75], v[112:115], v[88:91]\n\t"
      "v_mfma_f32_16x16x32_bf16 v[92:95], v[84:87], v[112:115], v[92:95]\n\t"
      "v_mfma_f32_16x16x32_bf16 v[104:107], v[100:103], v[112:115], v[104:107]\n\t"
      "s_waitcnt lgkmcnt(5)\n\t"
      "v_mfma_f32_16x16x32_bf16 v[52:55], v[108:111], v[124:127], v[52:55]\n\t"
      "v_mfma_f32_16x16x32_bf16 v[60:63], v[116:119], v[124:127], v[60:63]\n\t"
      "v_mfma_f32_16x16x32_bf16 v[52:55], v[128:131], v[124:127], v[52:55]\n\t"
      "ds_read_b128 v[144:147], v156 offset:2560\n\t"
      "v_mfma_f32_16x16x32_bf16 v[64:67], v[120:123], v[124:127], v[64:67]\n\t"
      "s_waitcnt lgkmcnt(4)\n\t"
      "v_mfma_f32_16x16x32_bf16 v[76:79], v[132:135], v[124:127], v[76:79]\n\t"
      "s_waitcnt lgkmcnt(2)\n\t"
      "v_mfma_f32_16x16x32_bf16 v[148:151], v[136:139], v[124:127], 0\n\t"
      "s_waitcnt lgkmcnt(1)\n\t"
      "v_mfma_f32_16x16x32_bf16 v[80:83], v[108:111], v[140:143], v[80:83]\n\t"
      "ds_read_b128 v[152:155], v156 offset:256\n\t"
      "v_mfma_f32_16x16x32_bf16 v[88:91], v[116:119], v[140:143], v[88:91]\n\t"
      "ds_bpermute_b32 v162, v159, v55\n\t"
      "v_mfma_f32_16x16x32_bf16 v[80:83], v[128:131], v[140:143], v[80:83]\n\t"
      "v_mfma_f32_16x16x32_bf16 v[92:95], v[120:123], v[140:143], v[92:95]\n\t"
      "v_mfma_f32_16x16x32_bf16 v[104:107], v[132:135], v[140:143], v[104:107]\n\t"
      "ds_read_b128 v[40:43], v156 offset:2624\n\t"
      "v_mfma_f32_16x16x32_bf16 v[44:47], v[136:139], v[140:143], 0\n\t"
      "s_waitcnt lgkmcnt(3)\n\t"
      "v_mfma_f32_16x16x32_bf16 v[60:63], v[8:11], v[144:147], v[60:63]\n\t"
      "v_mfma_f32_16x16x32_bf16 v[64:67], v[12:15], v[144:147], v[64:67]\n\t"
      "v_mfma_f32_16x16x32_bf16 v[76:79], v[16:19], v[144:147], v[76:79]\n\t"
      "ds_read_b128 v[48:51], v156 offset:320\n\t"
      "s_waitcnt lgkmcnt(2)\n\t"
      "v_cndmask_b32_e64 v165, v162, 0, s[42:43]\n\t"
      "v_mfma_f32_16x16x32_bf16 v[148:151], v[20:23], v[144:147], v[148:151]\n\t"
      "v_add_f32_e32 v81, v81, v52\n\t"
      "v_add_f32_e32 v82, v82, v53\n\t"
      "v_mfma_f32_16x16x32_bf16 v[88:91], v[8:11], v[152:155], v[88:91]\n\t"
      "v_add_f32_e32 v83, v83, v54\n\t"
      "v_cvt_pk_bf16_f32 v53, v82, v83\n\t"
      "v_mfma_f32_16x16x32_bf16 v[92:95], v[12:15], v[152:155], v[92:95]\n\t"
      "v_add_f32_e32 v80, v80, v165\n\t"
      "v_cvt_pk_bf16_f32 v52, v80, v81\n\t"
      "v_mfma_f32_16x16x32_bf16 v[104:107], v[16:19], v[152:155], v[104:107]\n\t"
      "ds_read_b128 v[68:71], v156 offset:2688\n\t"
      "v_mfma_f32_16x16x32_bf16 v[44:47], v[20:23], v[152:155], v[44:47]\n\t"
      "ds_write_b64 v158, v[52:53] offset:0\n\t"
      "s_waitcnt lgkmcnt(3)\n\t"
      "v_mfma_f32_16x16x32_bf16 v[60:63], v[24:27], v[40:43], v[60:63]\n\t"
      "ds_write_b32 v161, v161 offset:0\n\t"
      "v_mfma_f32_16x16x32_bf16 v[64:67], v[28:31], v[40:43], v[64:67]\n\t"
      "v_mfma_f32_16x16x32_bf16 v[76:79], v[32:35], v[40:43], v[76:79]\n\t"
      "ds_read_b128 v[96:99], v156 offset:384\n\t"
      "v_mfma_f32_16x16x32_bf16 v[148:151], v[36:39], v[40:43], v[148:151]\n\t"
      "s_waitcnt lgkmcnt(4)\n\t"
      "v_mfma_f32_16x16x32_bf16 v[88:91], v[24:27], v[48:51], v[88:91]\n\t"
      "v_mfma_f32_16x16x32_bf16 v[92:95], v[28:31], v[48:51], v[92:95]\n\t"
      "v_mfma_f32_16x16x32_bf16 v[104:107], v[32:35], v[48:51], v[104:107]\n\t"
      "ds_read_b128 v[112:115], v156 offset:2752\n\t"
      "v_mfma_f32_16x16x32_bf16 v[44:47], v[36:39], v[48:51], v[44:47]\n\t"
      "s_waitcnt lgkmcnt(4)\n\t"
      "v_mfma_f32_16x16x32_bf16 v[60:63], v[56:59], v[68:71], v[60:63]\n\t"
      "v_mfma_f32_16x16x32_bf16 v[64:67], v[72:75], v[68:71], v[64:67]\n\t"
      "v_mfma_f32_16x16x32_bf16 v[76:79], v[84:87], v[68:71], v[76:79]\n\t"
      "v_mfma_f32_16x16x32_bf16 v[148:151], v[100:103], v[68:71], v[148:151]\n\t"
      "s_waitcnt lgkmcnt(1)\n\t"
      "v_mfma_f32_16x16x32_bf16 v[88:91], v[56:59], v[96:99], v[88:91]\n\t"
      "ds_read_b128 v[124:127], v156 offset:448\n\t"
      "v_mfma_f32_16x16x32_bf16 v[92:95], v[72:75], v[96:99], v[92:95]\n\t"
      "ds_read_b32 v164, v160 offset:4\n\t"
      "v_mfma_f32_16x16x32_bf16 v[104:107], v[84:87], v[96:99], v[104:107]\n\t"
      "v_mfma_f32_16x16x32_bf16 v[44:47], v[100:103], v[96:99], v[44:47]\n\t"
      "s_waitcnt lgkmcnt(2)\n\t"
      "v_mfma_f32_16x16x32_bf16 v[60:63], v[108:111], v[112:115], v[60:63]\n\t"
      "v_mfma_f32_16x16x32_bf16 v[64:67], v[116:119], v[112:115], v[64:67]\n\t"
      "v_mfma_f32_16x16x32_bf16 v[60:63], v[128:131], v[112:115], v[60:63]\n\t"
      "s_waitcnt lgkmcnt(0)\n\t"
      "v_readfirstlane_b32 s40, v164\n\t"
      "s_cmp_ge_u32 s40, 8\n\t"
      "s_cbranch_scc1 .Lqe1_%=\n\t"
      ".Lql1_%=:\n\t"
      "s_sleep 1\n\t"
      "ds_read_b32 v164, v160 offset:4\n\t"
      "s_waitcnt lgkmcnt(0)\n\t"
      "v_readfirstlane_b32 s40, v164\n\t"
      "s_cmp_lt_u32 s40, 8\n\t"
      "s_cbranch_scc1 .Lql1_%=\n\t"
      ".Lqe1_%=:\n\t"
      "ds_read_b128 v[140:143], v156 offset:2816\n\t"
      "v_mfma_f32_16x16x32_bf16 v[76:79], v[120:123], v[112:115], v[76:79]\n\t"
      "v_mfma_f32_16x16x32_bf16 v[148:151], v[132:135], v[112:115], v[148:151]\n\t"
      "v_mfma_f32_16x16x32_bf16 v[144:147], v[136:139], v[112:115], 0\n\t"
      "ds_bpermute_b32 v163, v159, v63\n\t"
      "v_mfma_f32_16x16x32_bf16 v[88:91], v[108:111], v[124:127], v[88:91]\n\t"
      "ds_read_b128 v[152:155], v156 offset:512\n\t"
      "v_mfma_f32_16x16x32_bf16 v[92:95], v[116:119], v[124:127], v[92:95]\n\t"
      "v_mfma_f32_16x16x32_bf16 v[88:91], v[128:131], v[124:127], v[88:91]\n\t"
      "v_mfma_f32_16x16x32_bf16 v[104:107], v[120:123], v[124:127], v[104:107]\n\t"
      "v_mfma_f32_16x16x32_bf16 v[44:47], v[132:135], v[124:127], v[44:47]\n\t"
      "ds_read_b128 v[80:83], v156 offset:2880\n\t"
      "v_mfma_f32_16x16x32_bf16 v[40:43], v[136:139], v[124:127], 0\n\t"
      E_STAMP2
      EA_PRIO1
      "s_waitcnt lgkmcnt(3)\n\t"
      "v_mfma_f32_16x16x32_bf16 v[64:67], v[8:11], v[140:143], v[64:67]\n\t"
      "v_mfma_f32_16x16x32_bf16 v[76:79], v[12:15], v[140:143], v[76:79]\n\t"
      "v_mfma_f32_16x16x32_bf16 v[148:151], v[16:19], v[140:143], v[148:151]\n\t"
      "ds_read_b128 v[48:51], v156 offset:576\n\t"
      "s_waitcnt lgkmcnt(3)\n\t"
      "v_cndmask_b32_e64 v165, v163, v162, s[42:43]\n\t"
      "v_mfma_f32_16x16x32_bf16 v[144:147], v[20:23], v[140:143], v[144:147]\n\t"
      "v_add_f32_e32 v89, v89, v60\n\t"
      "v_add_f32_e32 v90, v90, v61\n\t"
      "s_waitcnt lgkmcnt(2)\n\t"
      "v_mfma_f32_16x16x32_bf16 v[92:95], v[8:11], v[152:155], v[92:95]\n\t"
      "v_add_f32_e32 v91, v91, v62\n\t"
      "v_cvt_pk_bf16_f32 v61, v90, v91\n\t"
      "v_mfma_f32_16x16x32_bf16 v[104:107], v[12:15], v[152:155], v[104:107]\n\t"
      "v_add_f32_e32 v88, v88, v165\n\t"
      "v_cvt_pk_bf16_f32 v60, v88, v89\n\t"
      "v_mfma_f32_16x16x32_bf16 v[44:47], v[16:19], v[152:155], v[44:47]\n\t"
      "ds_read_b128 v[68:71], v156 offset:2944\n\t"
      "v_mfma_f32_16x16x32_bf16 v[40:43], v[20:23], v[152:155], v[40:43]\n\t"
      "ds_write_b64 v158, v[60:61] offset:512\n\t"
      "s_waitcnt lgkmcnt(3)\n\t"
      "v_mfma_f32_16x16x32_bf16 v[64:67], v[24:27], v[80:83], v[64:67]\n\t"
      "ds_write_b32 v161, v161 offset:32\n\t"
      "v_mfma_f32_16x16x32_bf16 v[76:79], v[28:31], v[80:83], v[76:79]\n\t"
      "v_mfma_f32_16x16x32_bf16 v[148:151], v[32:35], v[80:83], v[148:151]\n\t"
      "ds_read_b128 v[52:55], v156 offset:640\n\t"
      "v_mfma_f32_16x16x32_bf16 v[144:147], v[36:39], v[80:83], v[144:147]\n\t"
      "s_waitcnt lgkmcnt(4)\n\t"
      "v_mfma_f32_16x16x32_bf16 v[92:95], v[24:27], v[48:51], v[92:95]\n\t"
      "v_mfma_f32_16x16x32_bf16 v[104:107], v[28:31], v[48:51], v[104:107]\n\t"
      "v_mfma_f32_16x16x32_bf16 v[44:47], v[32:35], v[48:51], v[44:47]\n\t"
      "ds_read_b128 v[96:99], v156 offset:3008\n\t"
      "v_mfma_f32_16x16x32_bf16 v[40:43], v[36:39], v[48:51], v[40:43]\n\t"
      "s_waitcnt lgkmcnt(4)\n\t"
      "v_mfma_f32_16x16x32_bf16 v[64:67], v[56:59], v[68:71], v[64:67]\n\t"
      "v_mfma_f32_16x16x32_bf16 v[76:79], v[72:75], v[68:71], v[76:79]\n\t"
      "v_mfma_f32_16x16x32_bf16 v[148:151], v[84:87], v[68:71], v[148:151]\n\t"
      "v_mfma_f32_16x16x32_bf16 v[144:147], v[100:103], v[68:71], v[144:147]\n\t"
      "s_waitcnt lgkmcnt(1)\n\t"
      "v_mfma_f32_16x16x32_bf16 v[92:95], v[56:59], v[52:55], v[92:95]\n\t"
      "ds_read_b128 v[112:115], v156 offset:704\n\t"
      "v_mfma_f32_16x16x32_bf16 v[104:107], v[72:75], v[52:55], v[104:107]\n\t"
      "v_mfma_f32_16x16x32_bf16 v[44:47], v[84:87], v[52:55], v[44:47]\n\t"
      "v_mfma_f32_16x16x32_bf16 v[40:43], v[100:103], v[52:55], v[40:43]\n\t"
      "s_waitcnt lgkmcnt(1)\n\t"
      "v_mfma_f32_16x16x32_bf16 v[64:67], v[108:111], v[96:99], v[64:67]\n\t"
      "v_mfma_f32_16x16x32_bf16 v[76:79], v[116:119], v[96:99], v[76:79]\n\t"
      "v_mfma_f32_16x16x32_bf16 v[64:67], v[128:131], v[96:99], v[64:67]\n\t"
      "ds_read_b128 v[124:127], v156 offset:3072\n\t"
      "v_mfma_f32_16x16x32_bf16 v[148:151], v[120:123], v[96:99], v[148:151]\n\t"
      "v_mfma_f32_16x16x32_bf16 v[144:147], v[132:135], v[96:99], v[144:147]\n\t"
      "v_mfma_f32_16x16x32_bf16 v[140:143], v[136:139], v[96:99], 0\n\t"
      "s_waitcnt lgkmcnt(1)\n\t"
      "v_mfma_f32_16x16x32_bf16 v[92:95], v[108:111], v[112:115], v[92:95]\n\t"
      "ds_read_b128 v[152:155], v156 offset:768\n\t"
      "v_mfma_f32_16x16x32_bf16 v[104:107], v[116:119], v[112:115], v[104:107]\n\t"
      "ds_bpermute_b32 v162, v159, v67\n\t"
      "v_mfma_f32_16x16x32_bf16 v[92:95], v[128:131], v[112:115], v[92:95]\n\t"
      "v_mfma_f32_16x16x32_bf16 v[44:47], v[120:123], v[112:115], v[44:47]\n\t"
      "v_mfma_f32_16x16x32_bf16 v[40:43], v[132:135], v[112:115], v[40:43]\n\t"
      "ds_read_b128 v[88:91], v156 offset:3136\n\t"
      "v_mfma_f32_16x16x32_bf16 v[80:83], v[136:139], v[112:115], 0\n\t"
      "s_waitcnt lgkmcnt(3)\n\t"
      "v_mfma_f32_16x16x32_bf16 v[76:79], v[8:11], v[124:127], v[76:79]\n\t"
      "v_mfma_f32_16x16x32_bf16 v[148:151], v[12:15], v[124:127], v[148:151]\n\t"
      "v_mfma_f32_16x16x32_bf16 v[144:147], v[16:19], v[124:127], v[144:147]\n\t"
      "ds_read_b128 v[48:51], v156 offset:832\n\t"
      "s_waitcnt lgkmcnt(2)\n\t"
      "v_cndmask_b32_e64 v165, v162, v163, s[42:43]\n\t"
      "v_mfma_f32_16x16x32_bf16 v[140:143], v[20:23], v[124:127], v[140:143]\n\t"
      "v_add_f32_e32 v93, v93, v64\n\t"
      "v_add_f32_e32 v94, v94, v65\n\t"
      "v_mfma_f32_16x16x32_bf16 v[104:107], v[8:11], v[152:155], v[104:107]\n\t"
      "v_add_f32_e32 v95, v95, v66\n\t"
      "v_cvt_pk_bf16_f32 v65, v94, v95\n\t"
      "v_mfma_f32_16x16x32_bf16 v[44:47], v[12:15], v[152:155], v[44:47]\n\t"
      "v_add_f32_e32 v92, v92, v165\n\t"
      "v_cvt_pk_bf16_f32 v64, v92, v93\n\t"
      "v_mfma_f32_16x16x32_bf16 v[40:43], v[16:19], v[152:155], v[40:43]\n\t"
      "ds_read_b128 v[68:71], v156 offset:3200\n\t"
      "v_mfma_f32_16x16x32_bf16 v[80:83], v[20:23], v[152:155], v[80:83]\n\t"
      "ds_write_b64 v158, v[64:65] offset:1024\n\t"
      "s_waitcnt lgkmcnt(3)\n\t"
      "v_mfma_f32_16x16x32_bf16 v[76:79], v[24:27], v[88:91], v[76:79]\n\t"
      "ds_write_b32 v161, v161 offset:64\n\t"
      "v_mfma_f32_16x16x32_bf16 v[148:151], v[28:31], v[88:91], v[148:151]\n\t"
      "v_mfma_f32_16x16x32_bf16 v[144:147], v[32:35], v[88:91], v[144:147]\n\t"
      "ds_read_b128 v[60:63], v156 offset:896\n\t"
      "v_mfma_f32_16x16x32_bf16 v[140:143], v[36:39], v[88:91], v[140:143]\n\t"
      "s_waitcnt lgkmcnt(4)\n\t"
      "v_mfma_f32_16x16x32_bf16 v[104:107], v[24:27], v[48:51], v[104:107]\n\t"
      "v_mfma_f32_16x16x32_bf16 v[44:47], v[28:31], v[48:51], v[44:47]\n\t"
      "v_mfma_f32_16x16x32_bf16 v[40:43], v[32:35], v[48:51], v[40:43]\n\t"
      "ds_read_b128 v[52:55], v156 offset:3264\n\t"
      "v_mfma_f32_16x16x32_bf16 v[80:83], v[36:39], v[48:51], v[80:83]\n\t"
      "s_waitcnt lgkmcnt(4)\n\t"
      "v_mfma_f32_16x16x32_bf16 v[76:79], v[56:59], v[68:71], v[76:79]\n\t"
      "v_mfma_f32_16x16x32_bf16 v[148:151], v[72:75], v[68:71], v[148:151]\n\t"
      "v_mfma_f32_16x16x32_bf16 v[144:147], v[84:87], v[68:71], v[144:147]\n\t"
      "v_mfma_f32_16x16x32_bf16 v[140:143], v[100:103], v[68:71], v[140:143]\n\t"
      "s_waitcnt lgkmcnt(1)\n\t"
      "v_mfma_f32_16x16x32_bf16 v[104:107], v[56:59], v[60:63], v[104:107]\n\t"
      "ds_read_b128 v[96:99], v156 offset:960\n\t"
      "v_mfma_f32_16x16x32_bf16 v[44:47], v[72:75], v[60:63], v[44:47]\n\t"
      "ds_read_b32 v164, v160 offset:8\n\t"
      "v_mfma_f32_16x16x32_bf16 v[40:43], v[84:87], v[60:63], v[40:43]\n\t"
      "v_mfma_f32_16x16x32_bf16 v[80:83], v[100:103], v[60:63], v[80:83]\n\t"
      "s_waitcnt lgkmcnt(2)\n\t"
      "v_mfma_f32_16x16x32_bf16 v[76:79], v[108:111], v[52:55], v[76:79]\n\t"
      "v_mfma_f32_16x16x32_bf16 v[148:151], v[116:119], v[52:55], v[148:151]\n\t"
      "v_mfma_f32_16x16x32_bf16 v[76:79], v[128:131], v[52:55], v[76:79]\n\t"
      "s_waitcnt lgkmcnt(0)\n\t"
      "v_readfirstlane_b32 s40, v164\n\t"
      "s_cmp_ge_u32 s40, 8\n\t"
      "s_cbranch_scc1 .Lqe2_%=\n\t"
      ".Lql2_%=:\n\t"
      "s_sleep 1\n\t"
      "ds_read_b32 v164, v160 offset:8\n\t"
      "s_waitcnt lgkmcnt(0)\n\t"
      "v_readfirstlane_b32 s40, v164\n\t"
      "s_cmp_lt_u32 s40, 8\n\t"
      "s_cbranch_scc1 .Lql2_%=\n\t"
      ".Lqe2_%=:\n\t"
      "ds_read_b128 v[112:115], v156 offset:3328\n\t"
      "v_mfma_f32_16x16x32_bf16 v[144:147], v[120:123], v[52:55], v[144:147]\n\t"
      "v_mfma_f32_16x16x32_bf16 v[140:143], v[132:135], v[52:55], v[140:143]\n\t"
      "v_mfma_f32_16x16x32_bf16 v[124:127], v[136:139], v[52:55], 0\n\t"
      "ds_bpermute_b32 v163, v159, v79\n\t"
      "v_mfma_f32_16x16x32_bf16 v[104:107], v[108:111], v[96:99], v[104:107]\n\t"
      "ds_read_b128 v[152:155], v156 offset:1024\n\t"
      "v_mfma_f32_16x16x32_bf16 v[44:47], v[116:119], v[96:99], v[44:47]\n\t"
      "v_mfma_f32_16x16x32_bf16 v[104:107], v[128:131], v[96:99], v[104:107]\n\t"
      "v_mfma_f32_16x16x32_bf16 v[40:43], v[120:123], v[96:99], v[40:43]\n\t"
      "v_mfma_f32_16x16x32_bf16 v[80:83], v[132:135], v[96:99], v[80:83]\n\t"
      "ds_read2_b64 v[92:95], %[vr] offset0:1 offset1:2\n\t"
      "v_mfma_f32_16x16x32_bf16 v[88:91], v[136:139], v[96:99], 0\n\t"
      "ds_read_b128 v[48:51], %[vb] offset:0\n\t"
      E_STAMP3
      EA_PRIO2
      "s_waitcnt lgkmcnt(4)\n\t"
      "v_mfma_f32_16x16x32_bf16 v[148:151], v[8:11], v[112:115], v[148:151]\n\t"
      "ds_read_b128 v[68:71], %[vr] offset:0\n\t"
      "s_waitcnt lgkmcnt(4)\n\t"
      "v_cndmask_b32_e64 v165, v163, v162, s[42:43]\n\t"
      "v_mfma_f32_16x16x32_bf16 v[144:147], v[12:15], v[112:115], v[144:147]\n\t"
      "ds_read_b128 v[64:67], %[vb] offset:1024\n\t"
      "v_add_f32_e32 v105, v105, v76\n\t"
      "v_mfma_f32_16x16x32_bf16 v[140:143], v[16:19], v[112:115], v[140:143]\n\t"
      "ds_read_b128 v[60:63], v156 offset:3392\n\t"
      "v_add_f32_e32 v106, v106, v77\n\t"
      "v_mfma_f32_16x16x32_bf16 v[124:127], v[20:23], v[112:115], v[124:127]\n\t"
      "v_add_f32_e32 v107, v107, v78\n\t"
      "v_cvt_pk_bf16_f32 v77, v106, v107\n\t"
      "s_waitcnt lgkmcnt(5)\n\t"
      "v_mfma_f32_16x16x32_bf16 v[44:47], v[8:11], v[152:155], v[44:47]\n\t"
      "ds_read_b128 v[52:55], v156 offset:1088\n\t"
      "v_add_f32_e32 v104, v104, v165\n\t"
      "v_mfma_f32_16x16x32_bf16 v[40:43], v[12:15], v[152:155], v[40:43]\n\t"
      "v_cvt_pk_bf16_f32 v76, v104, v105\n\t"
      "ds_write_b64 v158, v[76:77] offset:1536\n\t"
      "v_mfma_f32_16x16x32_bf16 v[80:83], v[16:19], v[152:155], v[80:83]\n\t"
      "ds_write_b32 v161, v161 offset:96\n\t"
      "v_mfma_f32_16x16x32_bf16 v[88:91], v[20:23], v[152:155], v[88:91]\n\t"
      "s_waitcnt lgkmcnt(6)\n\t"
      "v_mfma_f32_16x16x32_bf16 v[136:139], v[92:95], v[48:51], 0\n\t"
      "ds_read_b128 v[96:99], v156 offset:3456\n\t"
      "s_waitcnt lgkmcnt(5)\n\t"
      "v_mfma_f32_16x16x32_bf16 v[112:115], v[68:71], v[64:67], 0\n\t"
      "s_waitcnt lgkmcnt(4)\n\t"
      "v_mfma_f32_16x16x32_bf16 v[148:151], v[24:27], v[60:63], v[148:151]\n\t"
      "v_mfma_f32_16x16x32_bf16 v[144:147], v[28:31], v[60:63], v[144:147]\n\t"
      "v_mfma_f32_16x16x32_bf16 v[140:143], v[32:35], v[60:63], v[140:143]\n\t"
      "ds_read2_b32 v[104:105], %[vt] offset0:48 offset1:49\n\t"
      "v_mfma_f32_16x16x32_bf16 v[124:127], v[36:39], v[60:63], v[124:127]\n\t"
      "ds_read2_b32 v[106:107], %[vt] offset0:50 offset1:51\n\t"
      "s_waitcnt lgkmcnt(5)\n\t"
      "v_mfma_f32_16x16x32_bf16 v[44:47], v[24:27], v[52:55], v[44:47]\n\t"
      "ds_read2_b32 v[20:21], %[vt] offset0:32 offset1:33\n\t"
      "v_cvt_pk_bf16_f32 v112, v112, v136\n\t"
      "v_mfma_f32_16x16x32_bf16 v[40:43], v[28:31], v[52:55], v[40:43]\n\t"
      "ds_read2_b32 v[22:23], %[vt] offset0:34 offset1:35\n\t"
      "v_cvt_pk_bf16_f32 v113, v113, v137\n\t"
      "v_mfma_f32_16x16x32_bf16 v[80:83], v[32:35], v[52:55], v[80:83]\n\t"
      "ds_read2_b32 v[152:153], %[vt] offset0:16 offset1:17\n\t"
      "v_cvt_pk_bf16_f32 v114, v114, v138\n\t"
      "v_mfma_f32_16x16x32_bf16 v[88:91], v[36:39], v[52:55], v[88:91]\n\t"
      "ds_read2_b32 v[154:155], %[vt] offset0:18 offset1:19\n\t"
      "v_cvt_pk_bf16_f32 v115, v115, v139\n\t"
      "s_waitcnt lgkmcnt(6)\n\t"
      "v_mfma_f32_16x16x32_bf16 v[148:151], v[56:59], v[96:99], v[148:151]\n\t"
      "ds_read2_b32 v[92:93], %[vt] offset0:0 offset1:1\n\t"
      "v_mfma_f32_16x16x32_bf16 v[144:147], v[72:75], v[96:99], v[144:147]\n\t"
      "ds_read2_b32 v[94:95], %[vt] offset0:2 offset1:3\n\t"
      "v_mfma_f32_16x16x32_bf16 v[140:143], v[84:87], v[96:99], v[140:143]\n\t"
      "ds_read_b128 v[68:71], v156 offset:1152\n\t"
      "v_mfma_f32_16x16x32_bf16 v[124:127], v[100:103], v[96:99], v[124:127]\n\t"
      "ds_read_b128 v[60:63], v156 offset:3520\n\t"
      "s_waitcnt lgkmcnt(8)\n\t"
      "v_mfma_f32_16x16x32_bf16 v[44:47], v[112:115], v[104:107], v[44:47]\n\t"
      "s_waitcnt lgkmcnt(6)\n\t"
      "v_mfma_f32_16x16x32_bf16 v[40:43], v[112:115], v[20:23], v[40:43]\n\t"
      "s_waitcnt lgkmcnt(4)\n\t"
      "v_mfma_f32_16x16x32_bf16 v[80:83], v[112:115], v[152:155], v[80:83]\n\t"
      "s_waitcnt lgkmcnt(2)\n\t"
      "v_mfma_f32_16x16x32_bf16 v[88:91], v[112:115], v[92:95], v[88:91]\n\t"
      "ds_read_b128 v[36:39], v156 offset:1216\n\t"
      "s_waitcnt lgkmcnt(2)\n\t"
      "v_mfma_f32_16x16x32_bf16 v[44:47], v[56:59], v[68:71], v[44:47]\n\t"
      "v_mfma_f32_16x16x32_bf16 v[40:43], v[72:75], v[68:71], v[40:43]\n\t"
      "v_mfma_f32_16x16x32_bf16 v[80:83], v[84:87], v[68:71], v[80:83]\n\t"
      "v_mfma_f32_16x16x32_bf16 v[88:91], v[100:103], v[68:71], v[88:91]\n\t"
      "s_waitcnt lgkmcnt(1)\n\t"
      "v_mfma_f32_16x16x32_bf16 v[148:151], v[108:111], v[60:63], v[148:151]\n\t"
      "ds_read2_b64 v[52:55], %[vr] offset0:33 offset1:34\n\t"
      "v_mfma_f32_16x16x32_bf16 v[144:147], v[116:119], v[60:63], v[144:147]\n\t"
      "ds_read_b128 v[136:139], %[vr] offset:256\n\t"
      "v_mfma_f32_16x16x32_bf16 v[148:151], v[128:131], v[60:63], v[148:151]\n\t"
      "ds_read_b128 v[76:79], v156 offset:3584\n\t"
      "v_mfma_f32_16x16x32_bf16 v[140:143], v[120:123], v[60:63], v[140:143]\n\t"
      "v_mfma_f32_16x16x32_bf16 v[124:127], v[132:135], v[60:63], v[124:127]\n\t"
      "s_waitcnt lgkmcnt(3)\n\t"
      "v_mfma_f32_16x16x32_bf16 v[44:47], v[108:111], v[36:39], v[44:47]\n\t"
      "ds_read_b128 v[96:99], v156 offset:1280\n\t"
      "v_mfma_f32_16x16x32_bf16 v[40:43], v[116:119], v[36:39], v[40:43]\n\t"
      "v_mfma_f32_16x16x32_bf16 v[44:47], v[128:131], v[36:39], v[44:47]\n\t"
      "ds_bpermute_b32 v162, v159, v151\n\t"
      "v_mfma_f32_16x16x32_bf16 v[80:83], v[120:123], v[36:39], v[80:83]\n\t"
      "ds_read_b128 v[112:115], v156 offset:3648\n\t"
      "v_mfma_f32_16x16x32_bf16 v[88:91], v[132:135], v[36:39], v[88:91]\n\t"
      "s_waitcnt lgkmcnt(5)\n\t"
      "v_mfma_f32_16x16x32_bf16 v[92:95], v[52:55], v[48:51], 0\n\t"
      "s_waitcnt lgkmcnt(4)\n\t"
      "v_mfma_f32_16x16x32_bf16 v[100:103], v[136:139], v[64:67], 0\n\t"
      "v_add_f32_e32 v45, v45, v148\n\t"
      "v_add_f32_e32 v46, v46, v149\n\t"
      "s_waitcnt lgkmcnt(3)\n\t"
      "v_mfma_f32_16x16x32_bf16 v[144:147], v[8:11], v[76:79], v[144:147]\n\t"
      "v_add_f32_e32 v47, v47, v150\n\t"
      "v_cvt_pk_bf16_f32 v149, v46, v47\n\t"
      "v_mfma_f32_16x16x32_bf16 v[140:143], v[12:15], v[76:79], v[140:143]\n\t"
      "s_waitcnt lgkmcnt(1)\n\t"
      "v_cndmask_b32_e64 v165, v162, v163, s[42:43]\n\t"
      "v_add_f32_e32 v44, v44, v165\n\t"
      "v_mfma_f32_16x16x32_bf16 v[124:127], v[16:19], v[76:79], v[124:127]\n\t"
      "ds_read_b128 v[68:71], v156 offset:1344\n\t"
      "v_cvt_pk_bf16_f32 v100, v100, v92\n\t"
      "v_mfma_f32_16x16x32_bf16 v[40:43], v[8:11], v[96:99], v[40:43]\n\t"
      "v_cvt_pk_bf16_f32 v101, v101, v93\n\t"
      "v_cvt_pk_bf16_f32 v102, v102, v94\n\t"
      "v_mfma_f32_16x16x32_bf16 v[80:83], v[12:15], v[96:99], v[80:83]\n\t"
      "v_cvt_pk_bf16_f32 v103, v103, v95\n\t"
      "v_cvt_pk_bf16_f32 v148, v44, v45\n\t"
      "v_mfma_f32_16x16x32_bf16 v[88:91], v[16:19], v[96:99], v[88:91]\n\t"
      "ds_read_b128 v[60:63], v156 offset:3712\n\t"
      "ds_write_b64 v158, v[148:149] offset:2048\n\t"
      "s_waitcnt lgkmcnt(3)\n\t"
      "v_mfma_f32_16x16x32_bf16 v[144:147], v[24:27], v[112:115], v[144:147]\n\t"
      "ds_write_b32 v161, v161 offset:128\n\t"
      "v_mfma_f32_16x16x32_bf16 v[140:143], v[28:31], v[112:115], v[140:143]\n\t"
      "v_mfma_f32_16x16x32_bf16 v[124:127], v[32:35], v[112:115], v[124:127]\n\t"
      "ds_read_b128 v[132:135], v156 offset:1408\n\t"
      "v_mfma_f32_16x16x32_bf16 v[40:43], v[100:103], v[104:107], v[40:43]\n\t"
      "v_mfma_f32_16x16x32_bf16 v[80:83], v[100:103], v[20:23], v[80:83]\n\t"
      "v_mfma_f32_16x16x32_bf16 v[88:91], v[100:103], v[152:155], v[88:91]\n\t"
      "ds_read2_b64 v[36:39], %[vr] offset0:65 offset1:66\n\t"
      "s_waitcnt lgkmcnt(5)\n\t"
      "v_mfma_f32_16x16x32_bf16 v[40:43], v[24:27], v[68:71], v[40:43]\n\t"
      "ds_read_b128 v[52:55], %[vr] offset:512\n\t"
      "v_mfma_f32_16x16x32_bf16 v[80:83], v[28:31], v[68:71], v[80:83]\n\t"
      "ds_read_b128 v[136:139], v156 offset:3776\n\t"
      "v_mfma_f32_16x16x32_bf16 v[88:91], v[32:35], v[68:71], v[88:91]\n\t"
      "s_waitcnt lgkmcnt(6)\n\t"
      "v_mfma_f32_16x16x32_bf16 v[144:147], v[56:59], v[60:63], v[144:147]\n\t"
      "ds_read_b32 v164, v160 offset:12\n\t"
      "v_mfma_f32_16x16x32_bf16 v[140:143], v[72:75], v[60:63], v[140:143]\n\t"
      "v_mfma_f32_16x16x32_bf16 v[124:127], v[84:87], v[60:63], v[124:127]\n\t"
      "ds_read_b128 v[76:79], v156 offset:1472\n\t"
      "s_waitcnt lgkmcnt(5)\n\t"
      "v_mfma_f32_16x16x32_bf16 v[40:43], v[56:59], v[132:135], v[40:43]\n\t"
      "v_mfma_f32_16x16x32_bf16 v[80:83], v[72:75], v[132:135], v[80:83]\n\t"
      "v_mfma_f32_16x16x32_bf16 v[88:91], v[84:87], v[132:135], v[88:91]\n\t"
      "s_waitcnt lgkmcnt(4)\n\t"
      "v_mfma_f32_16x16x32_bf16 v[92:95], v[36:39], v[48:51], 0\n\t"
      "s_waitcnt lgkmcnt(1)\n\t"
      "v_readfirstlane_b32 s40, v164\n\t"
      "s_cmp_ge_u32 s40, 8\n\t"
      "s_cbranch_scc1 .Lqe3_%=\n\t"
      ".Lql3_%=:\n\t"
      "s_sleep 1\n\t"
      "ds_read_b32 v164, v160 offset:12\n\t"
      "s_waitcnt lgkmcnt(0)\n\t"
      "v_readfirstlane_b32 s40, v164\n\t"
      "s_cmp_lt_u32 s40, 8\n\t"
      "s_cbranch_scc1 .Lql3_%=\n\t"
      ".Lqe3_%=:\n\t"
      "ds_read_b128 v[16:19], v156 offset:3840\n\t"
      "v_mfma_f32_16x16x32_bf16 v[96:99], v[52:55], v[64:67], 0\n\t"
      "v_mfma_f32_16x16x32_bf16 v[144:147], v[108:111], v[136:139], v[144:147]\n\t"
      "ds_read_b128 v[44:47], v156 offset:1536\n\t"
      "v_mfma_f32_16x16x32_bf16 v[140:143], v[116:119], v[136:139], v[140:143]\n\t"
      "v_mfma_f32_16x16x32_bf16 v[144:147], v[128:131], v[136:139], v[144:147]\n\t"
      "v_mfma_f32_16x16x32_bf16 v[124:127], v[120:123], v[136:139], v[124:127]\n\t"
      "s_waitcnt lgkmcnt(2)\n\t"
      "v_mfma_f32_16x16x32_bf16 v[40:43], v[108:111], v[76:79], v[40:43]\n\t"
      "ds_read_b128 v[112:115], v156 offset:3904\n\t"
      "v_cvt_pk_bf16_f32 v96, v96, v92\n\t"
      "v_mfma_f32_16x16x32_bf16 v[80:83], v[116:119], v[76:79], v[80:83]\n\t"
      "v_cvt_pk_bf16_f32 v97, v97, v93\n\t"
      "v_cvt_pk_bf16_f32 v98, v98, v94\n\t"
      "v_mfma_f32_16x16x32_bf16 v[40:43], v[128:131], v[76:79], v[40:43]\n\t"
      "ds_read_b128 v[100:103], v156 offset:1600\n\t"
      "v_cvt_pk_bf16_f32 v99, v99, v95\n\t"
      "v_mfma_f32_16x16x32_bf16 v[88:91], v[120:123], v[76:79], v[88:91]\n\t"
      "ds_bpermute_b32 v163, v159, v147\n\t"
      E_STAMP4
      EA_PRIO3
      "s_waitcnt lgkmcnt(4)\n\t"
      "v_mfma_f32_16x16x32_bf16 v[140:143], v[8:11], v[16:19], v[140:143]\n\t"
      "ds_read2_b64 v[152:155], %[vr] offset0:97 offset1:98\n\t"
      "v_mfma_f32_16x16x32_bf16 v[124:127], v[12:15], v[16:19], v[124:127]\n\t"
      "ds_read_b128 v[32:35], %[vr] offset:768\n\t"
      "v_add_f32_e32 v41, v41, v144\n\t"
      "s_waitcnt lgkmcnt(5)\n\t"
      "v_mfma_f32_16x16x32_bf16 v[80:83], v[8:11], v[44:47], v[80:83]\n\t"
      "ds_read_b128 v[68:71], v156 offset:3968\n\t"
      "v_add_f32_e32 v42, v42, v145\n\t"
      "v_mfma_f32_16x16x32_bf16 v[88:91], v[12:15], v[44:47], v[88:91]\n\t"
      "v_add_f32_e32 v43, v43, v146\n\t"
      "v_cvt_pk_bf16_f32 v145, v42, v43\n\t"
      "v_mfma_f32_16x16x32_bf16 v[80:83], v[96:99], v[104:107], v[80:83]\n\t"
      "ds_read_b128 v[60:63], v156 offset:1664\n\t"
      "s_waitcnt lgkmcnt(4)\n\t"
      "v_cndmask_b32_e64 v165, v163, v162, s[42:43]\n\t"
      "v_mfma_f32_16x16x32_bf16 v[88:91], v[96:99], v[20:23], v[88:91]\n\t"
      "v_add_f32_e32 v40, v40, v165\n\t"
      "v_cvt_pk_bf16_f32 v144, v40, v41\n\t"
      "v_mfma_f32_16x16x32_bf16 v[140:143], v[24:27], v[112:115], v[140:143]\n\t"
      "ds_read_b128 v[148:151], v156 offset:4032\n\t"
      "ds_write_b64 v158, v[144:145] offset:2560\n\t"
      "v_mfma_f32_16x16x32_bf16 v[124:127], v[28:31], v[112:115], v[124:127]\n\t"
      "ds_write_b32 v161, v161 offset:160\n\t"
      "v_mfma_f32_16x16x32_bf16 v[80:83], v[24:27], v[100:103], v[80:83]\n\t"
      "v_mfma_f32_16x16x32_bf16 v[88:91], v[28:31], v[100:103], v[88:91]\n\t"
      "ds_read_b128 v[84:87], v156 offset:1728\n\t"
      "s_waitcnt lgkmcnt(7)\n\t"
      "v_mfma_f32_16x16x32_bf16 v[132:135], v[152:155], v[48:51], 0\n\t"
      "s_waitcnt lgkmcnt(6)\n\t"
      "v_mfma_f32_16x16x32_bf16 v[36:39], v[32:35], v[64:67], 0\n\t"
      "s_waitcnt lgkmcnt(5)\n\t"
      "v_mfma_f32_16x16x32_bf16 v[140:143], v[56:59], v[68:71], v[140:143]\n\t"
      "ds_read_b128 v[52:55], v156 offset:4096\n\t"
      "v_mfma_f32_16x16x32_bf16 v[124:127], v[72:75], v[68:71], v[124:127]\n\t"
      "s_waitcnt lgkmcnt(5)\n\t"
      "v_mfma_f32_16x16x32_bf16 v[80:83], v[56:59], v[60:63], v[80:83]\n\t"
      "ds_read_b128 v[136:139], v156 offset:4160\n\t"
      "v_mfma_f32_16x16x32_bf16 v[88:91], v[72:75], v[60:63], v[88:91]\n\t"
      "ds_read_b128 v[92:95], v156 offset:4224\n\t"
      "v_cvt_pk_bf16_f32 v36, v36, v132\n\t"
      "s_waitcnt lgkmcnt(6)\n\t"
      "v_mfma_f32_16x16x32_bf16 v[140:143], v[108:111], v[148:151], v[140:143]\n\t"
      "ds_read_b128 v[120:123], v156 offset:4288\n\t"
      "v_cvt_pk_bf16_f32 v37, v37, v133\n\t"
      "v_mfma_f32_16x16x32_bf16 v[124:127], v[116:119], v[148:151], v[124:127]\n\t"
      "v_cvt_pk_bf16_f32 v38, v38, v134\n\t"
      "v_cvt_pk_bf16_f32 v39, v39, v135\n\t"
      "v_mfma_f32_16x16x32_bf16 v[140:143], v[128:131], v[148:151], v[140:143]\n\t"
      "ds_read_b128 v[76:79], v156 offset:1792\n\t"
      "s_waitcnt lgkmcnt(5)\n\t"
      "v_mfma_f32_16x16x32_bf16 v[80:83], v[108:111], v[84:87], v[80:83]\n\t"
      "ds_read_b128 v[16:19], v156 offset:1856\n\t"
      "v_mfma_f32_16x16x32_bf16 v[88:91], v[116:119], v[84:87], v[88:91]\n\t"
      "ds_read_b128 v[12:15], v156 offset:1920\n\t"
      "v_mfma_f32_16x16x32_bf16 v[80:83], v[128:131], v[84:87], v[80:83]\n\t"
      "ds_read_b128 v[44:47], v156 offset:1984\n\t"
      "ds_bpermute_b32 v162, v159, v143\n\t"
      "s_waitcnt lgkmcnt(8)\n\t"
      "v_mfma_f32_16x16x32_bf16 v[124:127], v[8:11], v[52:55], v[124:127]\n\t"
      "v_mfma_f32_16x16x32_bf16 v[88:91], v[36:39], v[104:107], v[88:91]\n\t"
      "s_waitcnt lgkmcnt(7)\n\t"
      "v_mfma_f32_16x16x32_bf16 v[124:127], v[24:27], v[136:139], v[124:127]\n\t"
      "s_waitcnt lgkmcnt(6)\n\t"
      "v_mfma_f32_16x16x32_bf16 v[124:127], v[56:59], v[92:95], v[124:127]\n\t"
      "v_add_f32_e32 v81, v81, v140\n\t"
      "v_add_f32_e32 v82, v82, v141\n\t"
      "s_waitcnt lgkmcnt(5)\n\t"
      "v_mfma_f32_16x16x32_bf16 v[124:127], v[108:111], v[120:123], v[124:127]\n\t"
      "v_add_f32_e32 v83, v83, v142\n\t"
      "v_cvt_pk_bf16_f32 v141, v82, v83\n\t"
      "v_mfma_f32_16x16x32_bf16 v[124:127], v[128:131], v[120:123], v[124:127]\n\t"
      "s_waitcnt lgkmcnt(0)\n\t"
      "v_cndmask_b32_e64 v165, v162, v163, s[42:43]\n\t"
      "v_add_f32_e32 v80, v80, v165\n\t"
      "v_mfma_f32_16x16x32_bf16 v[88:91], v[8:11], v[76:79], v[88:91]\n\t"
      "v_cvt_pk_bf16_f32 v140, v80, v81\n\t"
      "ds_write_b64 v158, v[140:141] offset:3072\n\t"
      "v_mfma_f32_16x16x32_bf16 v[88:91], v[24:27], v[16:19], v[88:91]\n\t"
      "ds_write_b32 v161, v161 offset:192\n\t"
      "v_mfma_f32_16x16x32_bf16 v[88:91], v[56:59], v[12:15], v[88:91]\n\t"
      "ds_bpermute_b32 v163, v159, v127\n\t"
      "v_mfma_f32_16x16x32_bf16 v[88:91], v[108:111], v[44:47], v[88:91]\n\t"
      "v_mfma_f32_16x16x32_bf16 v[88:91], v[128:131], v[44:47], v[88:91]\n\t"
      E_STAMP5
      "s_nop 7\n\t"
      "v_add_f32_e32 v89, v89, v124\n\t"
      "v_add_f32_e32 v90, v90, v125\n\t"
      "v_add_f32_e32 v91, v91, v126\n\t"
      "v_cvt_pk_bf16_f32 v125, v90, v91\n\t"
      "s_waitcnt lgkmcnt(0)\n\t"
      "v_cndmask_b32_e64 v165, v163, v162, s[42:43]\n\t"
      "v_add_f32_e32 v88, v88, v165\n\t"
      "v_cvt_pk_bf16_f32 v124, v88, v89\n\t"
      "ds_write_b64 v158, v[124:125] offset:3584\n\t"
      "ds_write_b32 v161, v161 offset:224\n\t"
        : E_OUT
        : "{v[128:131]}"(dfr), "{v156}"(xb), "{v157}"(tb), "{v158}"(yw), "{v159}"(nb), "{v160}"(fa), "{v161}"(fy), "{s[42:43]}"(1ull),
          [vr] "v"(vr), [vt] "v"(vt), [vb] "v"(vb)
        : "memory", "scc", "s40", E_CLOB);
    } else {
      asm volatile(
      "ds_read_b32 v164, v160\n\t"
      "ds_read_b64_tr_b16 v[8:9], v157 offset:400\n\t"
      "ds_read_b64_tr_b16 v[10:11], v157 offset:384\n\t"
      "ds_read_b64_tr_b16 v[12:13], v157 offset:912\n\t"
      "ds_read_b64_tr_b16 v[14:15], v157 offset:896\n\t"
      "s_waitcnt vmcnt(0)\n\t"
      "ds_read_b64_tr_b16 v[16:17], v157 offset:1424\n\t"
      "ds_read_b64_tr_b16 v[18:19], v157 offset:1408\n\t"
      "ds_read_b64_tr_b16 v[20:21], v157 offset:1936\n\t"
      "ds_read_b64_tr_b16 v[22:23], v157 offset:1920\n\t"
      "ds_read_b64_tr_b16 v[24:25], v157 offset:272\n\t"
      "ds_read_b64_tr_b16 v[26:27], v157 offset:256\n\t"
      "ds_read_b64_tr_b16 v[28:29], v157 offset:784\n\t"
      "ds_read_b64_tr_b16 v[30:31], v157 offset:768\n\t"
      "ds_read_b64_tr_b16 v[32:33], v157 offset:1296\n\t"
      "ds_read_b64_tr_b16 v[34:35], v157 offset:1280\n\t"
      "ds_read_b64_tr_b16 v[36:37], v157 offset:1808\n\t"
      "ds_read_b64_tr_b16 v[38:39], v157 offset:1792\n\t"
      E_STAMP0
      "s_waitcnt lgkmcnt(15)\n\t"
      "v_readfirstlane_b32 s40, v164\n\t"
      "s_cmp_ge_u32 s40, 8\n\t"
      "s_cbranch_scc1 .Lpe0_%=\n\t"
      ".Lpl0_%=:\n\t"
      "s_sleep 1\n\t"
      "ds_read_b32 v164, v160 offset:0\n\t"
      "s_waitcnt lgkmcnt(0)\n\t"
      "v_readfirstlane_b32 s40, v164\n\t"
      "s_cmp_lt_u32 s40, 8\n\t"
      "s_cbranch_scc1 .Lpl0_%=\n\t"
      ".Lpe0_%=:\n\t"
      E_STAMP1
      "ds_read_b128 v[40:43], v156 offset:2304\n\t"
      "ds_read_b128 v[44:47], v156 offset:0\n\t"
      "ds_read_b128 v[48:51], v156 offset:2368\n\t"
      E_PRIO0
      "s_waitcnt lgkmcnt(2)\n\t"
      "v_mfma_f32_16x16x32_bf16 v[52:55], v[8:11], v[40:43], 0\n\t"
      "ds_read_b64_tr_b16 v[56:57], v157 offset:144\n\t"
      "v_mfma_f32_16x16x32_bf16 v[60:63], v[12:15], v[40:43], 0\n\t"
      "ds_read_b64_tr_b16 v[58:59], v157 offset:128\n\t"
      "v_mfma_f32_16x16x32_bf16 v[64:67], v[16:19], v[40:43], 0\n\t"
      "ds_read_b128 v[68:71], v156 offset:64\n\t"
      "ds_read_b64_tr_b16 v[72:73], v157 offset:656\n\t"
      "v_mfma_f32_16x16x32_bf16 v[76:79], v[20:23], v[40:43], 0\n\t"
      "ds_read_b64_tr_b16 v[74:75], v157 offset:640\n\t"
      "s_waitcnt lgkmcnt(6)\n\t"
      "v_mfma_f32_16x16x32_bf16 v[80:83], v[8:11], v[44:47], 0\n\t"
      "ds_read_b64_tr_b16 v[84:85], v157 offset:1168\n\t"
      "v_mfma_f32_16x16x32_bf16 v[88:91], v[12:15], v[44:47], 0\n\t"
      "ds_read_b64_tr_b16 v[86:87], v157 offset:1152\n\t"
      "v_mfma_f32_16x16x32_bf16 v[92:95], v[16:19], v[44:47], 0\n\t"
      "ds_read_b128 v[96:99], v156 offset:2432\n\t"
      "ds_read_b64_tr_b16 v[100:101], v157 offset:1680\n\t"
      "v_mfma_f32_16x16x32_bf16 v[104:107], v[20:23], v[44:47], 0\n\t"
      "ds_read_b64_tr_b16 v[102:103], v157 offset:1664\n\t"
      "s_waitcnt lgkmcnt(10)\n\t"
      "v_mfma_f32_16x16x32_bf16 v[52:55], v[24:27], v[48:51], v[52:55]\n\t"
      "ds_read_b64_tr_b16 v[108:109], v157 offset:16\n\t"
      "v_mfma_f32_16x16x32_bf16 v[60:63], v[28:31], v[48:51], v[60:63]\n\t"
      "ds_read_b64_tr_b16 v[110:111], v157 offset:0\n\t"
      "v_mfma_f32_16x16x32_bf16 v[64:67], v[32:35], v[48:51], v[64:67]\n\t"
      "ds_read_b128 v[112:115], v156 offset:128\n\t"
      "ds_read_b64_tr_b16 v[116:117], v157 offset:528\n\t"
      "v_mfma_f32_16x16x32_bf16 v[76:79], v[36:39], v[48:51], v[76:79]\n\t"
      "ds_read_b64_tr_b16 v[118:119], v157 offset:512\n\t"
      "s_waitcnt lgkmcnt(12)\n\t"
      "v_mfma_f32_16x16x32_bf16 v[80:83], v[24:27], v[68:71], v[80:83]\n\t"
      "ds_read_b64_tr_b16 v[120:121], v157 offset:1040\n\t"
      "v_mfma_f32_16x16x32_bf16 v[88:91], v[28:31], v[68:71], v[88:91]\n\t"
      "ds_read_b64_tr_b16 v[122:123], v157 offset:1024\n\t"
      "v_mfma_f32_16x16x32_bf16 v[92:95], v[32:35], v[68:71], v[92:95]\n\t"
      "ds_read_b128 v[124:127], v156 offset:2496\n\t"
      "v_mfma_f32_16x16x32_bf16 v[104:107], v[36:39], v[68:71], v[104:107]\n\t"
      "ds_read_b64_tr_b16 v[132:133], v157 offset:1552\n\t"
      "s_waitcnt lgkmcnt(11)\n\t"
      "v_mfma_f32_16x16x32_bf16 v[52:55], v[56:59], v[96:99], v[52:55]\n\t"
      "ds_read_b64_tr_b16 v[134:135], v157 offset:1536\n\t"
      "v_mfma_f32_16x16x32_bf16 v[60:63], v[72:75], v[96:99], v[60:63]\n\t"
      "v_mfma_f32_16x16x32_bf16 v[64:67], v[84:87], v[96:99], v[64:67]\n\t"
      "s_waitcnt lgkmcnt(10)\n\t"
      "v_mfma_f32_16x16x32_bf16 v[76:79], v[100:103], v[96:99], v[76:79]\n\t"
      "ds_read_b128 v[136:139], v156 offset:192\n\t"
      "s_waitcnt lgkmcnt(8)\n\t"
      "v_mfma_f32_16x16x32_bf16 v[80:83], v[56:59], v[112:115], v[80:83]\n\t"
      "v_mfma_f32_16x16x32_bf16 v[88:91], v[72:75], v[112:115], v[88:91]\n\t"
      "v_mfma_f32_16x16x32_bf16 v[92:95], v[84:87], v[112:115], v[92:95]\n\t"
      "v_mfma_f32_16x16x32_bf16 v[104:107], v[100:103], v[112:115], v[104:107]\n\t"
      "s_waitcnt lgkmcnt(3)\n\t"
      "v_mfma_f32_16x16x32_bf16 v[52:55], v[108:111], v[124:127], v[52:55]\n\t"
      "ds_read_b128 v[140:143], v156 offset:2560\n\t"
      "v_mfma_f32_16x16x32_bf16 v[60:63], v[116:119], v[124:127], v[60:63]\n\t"
      "v_mfma_f32_16x16x32_bf16 v[52:55], v[128:131], v[124:127], v[52:55]\n\t"
      "v_mfma_f32_16x16x32_bf16 v[64:67], v[120:123], v[124:127], v[64:67]\n\t"
      "s_waitcnt lgkmcnt(2)\n\t"
      "v_mfma_f32_16x16x32_bf16 v[76:79], v[132:135], v[124:127], v[76:79]\n\t"
      "ds_read_b128 v[144:147], v156 offset:256\n\t"
      "s_waitcnt lgkmcnt(2)\n\t"
      "v_mfma_f32_16x16x32_bf16 v[80:83], v[108:111], v[136:139], v[80:83]\n\t"
      "v_mfma_f32_16x16x32_bf16 v[88:91], v[116:119], v[136:139], v[88:91]\n\t"
      "v_mfma_f32_16x16x32_bf16 v[80:83], v[128:131], v[136:139], v[80:83]\n\t"
      "ds_bpermute_b32 v162, v159, v55\n\t"
      "v_mfma_f32_16x16x32_bf16 v[92:95], v[120:123], v[136:139], v[92:95]\n\t"
      "ds_read_b128 v[148:151], v156 offset:2624\n\t"
      "v_mfma_f32_16x16x32_bf16 v[104:107], v[132:135], v[136:139], v[104:107]\n\t"
      "s_waitcnt lgkmcnt(3)\n\t"
      "v_mfma_f32_16x16x32_bf16 v[60:63], v[8:11], v[140:143], v[60:63]\n\t"
      "v_mfma_f32_16x16x32_bf16 v[64:67], v[12:15], v[140:143], v[64:67]\n\t"
      "v_mfma_f32_16x16x32_bf16 v[76:79], v[16:19], v[140:143], v[76:79]\n\t"
      "ds_read_b128 v[152:155], v156 offset:320\n\t"
      "v_add_f32_e32 v81, v81, v52\n\t"
      "v_mfma_f32_16x16x32_bf16 v[40:43], v[20:23], v[140:143], 0\n\t"
      "v_add_f32_e32 v82, v82, v53\n\t"
      "v_add_f32_e32 v83, v83, v54\n\t"
      "s_waitcnt lgkmcnt(3)\n\t"
      "v_mfma_f32_16x16x32_bf16 v[88:91], v[8:11], v[144:147], v[88:91]\n\t"
      "v_cvt_pk_bf16_f32 v53, v82, v83\n\t"
      "s_waitcnt lgkmcnt(2)\n\t"
      "v_cndmask_b32_e64 v165, v162, 0, s[42:43]\n\t"
      "v_mfma_f32_16x16x32_bf16 v[92:95], v[12:15], v[144:147], v[92:95]\n\t"
      "v_add_f32_e32 v80, v80, v165\n\t"
      "v_cvt_pk_bf16_f32 v52, v80, v81\n\t"
      "v_mfma_f32_16x16x32_bf16 v[104:107], v[16:19], v[144:147], v[104:107]\n\t"
      "ds_read_b128 v[44:47], v156 offset:2688\n\t"
      "v_mfma_f32_16x16x32_bf16 v[48:51], v[20:23], v[144:147], 0\n\t"
      "ds_write_b64 v158, v[52:53] offset:0\n\t"
      "s_waitcnt lgkmcnt(3)\n\t"
      "v_mfma_f32_16x16x32_bf16 v[60:63], v[24:27], v[148:151], v[60:63]\n\t"
      "ds_write_b32 v161, v161 offset:0\n\t"
      "v_mfma_f32_16x16x32_bf16 v[64:67], v[28:31], v[148:151], v[64:67]\n\t"
      "v_mfma_f32_16x16x32_bf16 v[76:79], v[32:35], v[148:151], v[76:79]\n\t"
      "ds_read_b128 v[68:71], v156 offset:384\n\t"
      "v_mfma_f32_16x16x32_bf16 v[40:43], v[36:39], v[148:151], v[40:43]\n\t"
      "s_waitcnt lgkmcnt(4)\n\t"
      "v_mfma_f32_16x16x32_bf16 v[88:91], v[24:27], v[152:155], v[88:91]\n\t"
      "v_mfma_f32_16x16x32_bf16 v[92:95], v[28:31], v[152:155], v[92:95]\n\t"
      "v_mfma_f32_16x16x32_bf16 v[104:107], v[32:35], v[152:155], v[104:107]\n\t"
      "ds_read_b128 v[96:99], v156 offset:2752\n\t"
      "v_mfma_f32_16x16x32_bf16 v[48:51], v[36:39], v[152:155], v[48:51]\n\t"
      "s_waitcnt lgkmcnt(4)\n\t"
      "v_mfma_f32_16x16x32_bf16 v[60:63], v[56:59], v[44:47], v[60:63]\n\t"
      "v_mfma_f32_16x16x32_bf16 v[64:67], v[72:75], v[44:47], v[64:67]\n\t"
      "v_mfma_f32_16x16x32_bf16 v[76:79], v[84:87], v[44:47], v[76:79]\n\t"
      "ds_read_b32 v164, v160 offset:4\n\t"
      "v_mfma_f32_16x16x32_bf16 v[40:43], v[100:103], v[44:47], v[40:43]\n\t"
      "ds_read_b128 v[112:115], v156 offset:448\n\t"
      "s_waitcnt lgkmcnt(3)\n\t"
      "v_mfma_f32_16x16x32_bf16 v[88:91], v[56:59], v[68:71], v[88:91]\n\t"
      "v_mfma_f32_16x16x32_bf16 v[92:95], v[72:75], v[68:71], v[92:95]\n\t"
      "v_mfma_f32_16x16x32_bf16 v[104:107], v[84:87], v[68:71], v[104:107]\n\t"
      "v_mfma_f32_16x16x32_bf16 v[48:51], v[100:103], v[68:71], v[48:51]\n\t"
      "s_waitcnt lgkmcnt(2)\n\t"
      "v_mfma_f32_16x16x32_bf16 v[60:63], v[108:111], v[96:99], v[60:63]\n\t"
      "s_waitcnt lgkmcnt(1)\n\t"
      "v_readfirstlane_b32 s40, v164\n\t"
      "s_cmp_ge_u32 s40, 8\n\t"
      "s_cbranch_scc1 .Lpe1_%=\n\t"
      ".Lpl1_%=:\n\t"
      "s_sleep 1\n\t"
      "ds_read_b32 v164, v160 offset:4\n\t"
      "s_waitcnt lgkmcnt(0)\n\t"
      "v_readfirstlane_b32 s40, v164\n\t"
      "s_cmp_lt_u32 s40, 8\n\t"
      "s_cbranch_scc1 .Lpl1_%=\n\t"
      ".Lpe1_%=:\n\t"
      "ds_read_b128 v[124:127], v156 offset:2816\n\t"
      "v_mfma_f32_16x16x32_bf16 v[64:67], v[116:119], v[96:99], v[64:67]\n\t"
      "v_mfma_f32_16x16x32_bf16 v[60:63], v[128:131], v[96:99], v[60:63]\n\t"
      "v_mfma_f32_16x16x32_bf16 v[76:79], v[120:123], v[96:99], v[76:79]\n\t"
      "v_mfma_f32_16x16x32_bf16 v[40:43], v[132:135], v[96:99], v[40:43]\n\t"
      "ds_read_b128 v[136:139], v156 offset:512\n\t"
      "s_waitcnt lgkmcnt(2)\n\t"
      "v_mfma_f32_16x16x32_bf16 v[88:91], v[108:111], v[112:115], v[88:91]\n\t"
      "v_mfma_f32_16x16x32_bf16 v[92:95], v[116:119], v[112:115], v[92:95]\n\t"
      "v_mfma_f32_16x16x32_bf16 v[88:91], v[128:131], v[112:115], v[88:91]\n\t"
      "v_mfma_f32_16x16x32_bf16 v[104:107], v[120:123], v[112:115], v[104:107]\n\t"
      "ds_read_b128 v[140:143], v156 offset:2880\n\t"
      "v_mfma_f32_16x16x32_bf16 v[48:51], v[132:135], v[112:115], v[48:51]\n\t"
      "ds_bpermute_b32 v163, v159, v63\n\t"
      E_STAMP2
      E_PRIO1
      "s_waitcnt lgkmcnt(3)\n\t"
      "v_mfma_f32_16x16x32_bf16 v[64:67], v[8:11], v[124:127], v[64:67]\n\t"
      "v_mfma_f32_16x16x32_bf16 v[76:79], v[12:15], v[124:127], v[76:79]\n\t"
      "v_mfma_f32_16x16x32_bf16 v[40:43], v[16:19], v[124:127], v[40:43]\n\t"
      "ds_read_b128 v[144:147], v156 offset:576\n\t"
      "v_add_f32_e32 v89, v89, v60\n\t"
      "v_mfma_f32_16x16x32_bf16 v[80:83], v[20:23], v[124:127], 0\n\t"
      "v_add_f32_e32 v90, v90, v61\n\t"
      "v_add_f32_e32 v91, v91, v62\n\t"
      "s_waitcnt lgkmcnt(3)\n\t"
      "v_mfma_f32_16x16x32_bf16 v[92:95], v[8:11], v[136:139], v[92:95]\n\t"
      "v_cvt_pk_bf16_f32 v61, v90, v91\n\t"
      "s_waitcnt lgkmcnt(1)\n\t"
      "v_cndmask_b32_e64 v165, v163, v162, s[42:43]\n\t"
      "v_mfma_f32_16x16x32_bf16 v[104:107], v[12:15], v[136:139], v[104:107]\n\t"
      "v_add_f32_e32 v88, v88, v165\n\t"
      "v_cvt_pk_bf16_f32 v60, v88, v89\n\t"
      "v_mfma_f32_16x16x32_bf16 v[48:51], v[16:19], v[136:139], v[48:51]\n\t"
      "ds_read_b128 v[148:151], v156 offset:2944\n\t"
      "v_mfma_f32_16x16x32_bf16 v[152:155], v[20:23], v[136:139], 0\n\t"
      "ds_write_b64 v158, v[60:61] offset:512\n\t"
      "v_mfma_f32_16x16x32_bf16 v[64:67], v[24:27], v[140:143], v[64:67]\n\t"
      "ds_write_b32 v161, v161 offset:32\n\t"
      "v_mfma_f32_16x16x32_bf16 v[76:79], v[28:31], v[140:143], v[76:79]\n\t"
      "v_mfma_f32_16x16x32_bf16 v[40:43], v[32:35], v[140:143], v[40:43]\n\t"
      "ds_read_b128 v[44:47], v156 offset:640\n\t"
      "v_mfma_f32_16x16x32_bf16 v[80:83], v[36:39], v[140:143], v[80:83]\n\t"
      "s_waitcnt lgkmcnt(4)\n\t"
      "v_mfma_f32_16x16x32_bf16 v[92:95], v[24:27], v[144:147], v[92:95]\n\t"
      "v_mfma_f32_16x16x32_bf16 v[104:107], v[28:31], v[144:147], v[104:107]\n\t"
      "v_mfma_f32_16x16x32_bf16 v[48:51], v[32:35], v[144:147], v[48:51]\n\t"
      "ds_read_b128 v[52:55], v156 offset:3008\n\t"
      "v_mfma_f32_16x16x32_bf16 v[152:155], v[36:39], v[144:147], v[152:155]\n\t"
      "s_waitcnt lgkmcnt(4)\n\t"
      "v_mfma_f32_16x16x32_bf16 v[64:67], v[56:59], v[148:151], v[64:67]\n\t"
      "v_mfma_f32_16x16x32_bf16 v[76:79], v[72:75], v[148:151], v[76:79]\n\t"
      "v_mfma_f32_16x16x32_bf16 v[40:43], v[84:87], v[148:151], v[40:43]\n\t"
      "v_mfma_f32_16x16x32_bf16 v[80:83], v[100:103], v[148:151], v[80:83]\n\t"
      "ds_read_b128 v[68:71], v156 offset:704\n\t"
      "s_waitcnt lgkmcnt(2)\n\t"
      "v_mfma_f32_16x16x32_bf16 v[92:95], v[56:59], v[44:47], v[92:95]\n\t"
      "v_mfma_f32_16x16x32_bf16 v[104:107], v[72:75], v[44:47], v[104:107]\n\t"
      "v_mfma_f32_16x16x32_bf16 v[48:51], v[84:87], v[44:47], v[48:51]\n\t"
      "v_mfma_f32_16x16x32_bf16 v[152:155], v[100:103], v[44:47], v[152:155]\n\t"
      "s_waitcnt lgkmcnt(1)\n\t"
      "v_mfma_f32_16x16x32_bf16 v[64:67], v[108:111], v[52:55], v[64:67]\n\t"
      "ds_read_b128 v[96:99], v156 offset:3072\n\t"
      "v_mfma_f32_16x16x32_bf16 v[76:79], v[116:119], v[52:55], v[76:79]\n\t"
      "v_mfma_f32_16x16x32_bf16 v[64:67], v[128:131], v[52:55], v[64:67]\n\t"
      "v_mfma_f32_16x16x32_bf16 v[40:43], v[120:123], v[52:55], v[40:43]\n\t"
      "v_mfma_f32_16x16x32_bf16 v[80:83], v[132:135], v[52:55], v[80:83]\n\t"
      "ds_read_b128 v[112:115], v156 offset:768\n\t"
      "s_waitcnt lgkmcnt(2)\n\t"
      "v_mfma_f32_16x16x32_bf16 v[92:95], v[108:111], v[68:71], v[92:95]\n\t"
      "v_mfma_f32_16x16x32_bf16 v[104:107], v[116:119], v[68:71], v[104:107]\n\t"
      "v_mfma_f32_16x16x32_bf16 v[92:95], v[128:131], v[68:71], v[92:95]\n\t"
      "v_mfma_f32_16x16x32_bf16 v[48:51], v[120:123], v[68:71], v[48:51]\n\t"
      "ds_read_b128 v[124:127], v156 offset:3136\n\t"
      "v_mfma_f32_16x16x32_bf16 v[152:155], v[132:135], v[68:71], v[152:155]\n\t"
      "ds_bpermute_b32 v162, v159, v67\n\t"
      "s_waitcnt lgkmcnt(3)\n\t"
      "v_mfma_f32_16x16x32_bf16 v[76:79], v[8:11], v[96:99], v[76:79]\n\t"
      "v_mfma_f32_16x16x32_bf16 v[40:43], v[12:15], v[96:99], v[40:43]\n\t"
      "v_mfma_f32_16x16x32_bf16 v[80:83], v[16:19], v[96:99], v[80:83]\n\t"
      "ds_read_b128 v[136:139], v156 offset:832\n\t"
      "v_add_f32_e32 v93, v93, v64\n\t"
      "v_mfma_f32_16x16x32_bf16 v[88:91], v[20:23], v[96:99], 0\n\t"
      "v_add_f32_e32 v94, v94, v65\n\t"
      "v_add_f32_e32 v95, v95, v66\n\t"
      "s_waitcnt lgkmcnt(3)\n\t"
      "v_mfma_f32_16x16x32_bf16 v[104:107], v[8:11], v[112:115], v[104:107]\n\t"
      "v_cvt_pk_bf16_f32 v65, v94, v95\n\t"
      "s_waitcnt lgkmcnt(1)\n\t"
      "v_cndmask_b32_e64 v165, v162, v163, s[42:43]\n\t"
      "v_mfma_f32_16x16x32_bf16 v[48:51], v[12:15], v[112:115], v[48:51]\n\t"
      "v_add_f32_e32 v92, v92, v165\n\t"
      "v_cvt_pk_bf16_f32 v64, v92, v93\n\t"
      "v_mfma_f32_16x16x32_bf16 v[152:155], v[16:19], v[112:115], v[152:155]\n\t"
      "ds_read_b128 v[140:143], v156 offset:3200\n\t"
      "v_mfma_f32_16x16x32_bf16 v[144:147], v[20:23], v[112:115], 0\n\t"
      "ds_write_b64 v158, v[64:65] offset:1024\n\t"
      "v_mfma_f32_16x16x32_bf16 v[76:79], v[24:27], v[124:127], v[76:79]\n\t"
      "ds_write_b32 v161, v161 offset:64\n\t"
      "v_mfma_f32_16x16x32_bf16 v[40:43], v[28:31], v[124:127], v[40:43]\n\t"
      "v_mfma_f32_16x16x32_bf16 v[80:83], v[32:35], v[124:127], v[80:83]\n\t"
      "ds_read_b128 v[148:151], v156 offset:896\n\t"
      "v_mfma_f32_16x16x32_bf16 v[88:91], v[36:39], v[124:127], v[88:91]\n\t"
      "s_waitcnt lgkmcnt(4)\n\t"
      "v_mfma_f32_16x16x32_bf16 v[104:107], v[24:27], v[136:139], v[104:107]\n\t"
      "v_mfma_f32_16x16x32_bf16 v[48:51], v[28:31], v[136:139], v[48:51]\n\t"
      "v_mfma_f32_16x16x32_bf16 v[152:155], v[32:35], v[136:139], v[152:155]\n\t"
      "ds_read_b128 v[60:63], v156 offset:3264\n\t"
      "v_mfma_f32_16x16x32_bf16 v[144:147], v[36:39], v[136:139], v[144:147]\n\t"
      "s_waitcnt lgkmcnt(4)\n\t"
      "v_mfma_f32_16x16x32_bf16 v[76:79], v[56:59], v[140:143], v[76:79]\n\t"
      "v_mfma_f32_16x16x32_bf16 v[40:43], v[72:75], v[140:143], v[40:43]\n\t"
      "v_mfma_f32_16x16x32_bf16 v[80:83], v[84:87], v[140:143], v[80:83]\n\t"
      "ds_read_b32 v164, v160 offset:8\n\t"
      "v_mfma_f32_16x16x32_bf16 v[88:91], v[100:103], v[140:143], v[88:91]\n\t"
      "ds_read_b128 v[44:47], v156 offset:960\n\t"
      "s_waitcnt lgkmcnt(3)\n\t"
      "v_mfma_f32_16x16x32_bf16 v[104:107], v[56:59], v[148:151], v[104:107]\n\t"
      "v_mfma_f32_16x16x32_bf16 v[48:51], v[72:75], v[148:151], v[48:51]\n\t"
      "v_mfma_f32_16x16x32_bf16 v[152:155], v[84:87], v[148:151], v[152:155]\n\t"
      "v_mfma_f32_16x16x32_bf16 v[144:147], v[100:103], v[148:151], v[144:147]\n\t"
      "s_waitcnt lgkmcnt(2)\n\t"
      "v_mfma_f32_16x16x32_bf16 v[76:79], v[108:111], v[60:63], v[76:79]\n\t"
      "s_waitcnt lgkmcnt(1)\n\t"
      "v_readfirstlane_b32 s40, v164\n\t"
      "s_cmp_ge_u32 s40, 8\n\t"
      "s_cbranch_scc1 .Lpe2_%=\n\t"
      ".Lpl2_%=:\n\t"
      "s_sleep 1\n\t"
      "ds_read_b32 v164, v160 offset:8\n\t"
      "s_waitcnt lgkmcnt(0)\n\t"
      "v_readfirstlane_b32 s40, v164\n\t"
      "s_cmp_lt_u32 s40, 8\n\t"
      "s_cbranch_scc1 .Lpl2_%=\n\t"
      ".Lpe2_%=:\n\t"
      "ds_read_b128 v[52:55], v156 offset:3328\n\t"
      "v_mfma_f32_16x16x32_bf16 v[40:43], v[116:119], v[60:63], v[40:43]\n\t"
      "v_mfma_f32_16x16x32_bf16 v[76:79], v[128:131], v[60:63], v[76:79]\n\t"
      "v_mfma_f32_16x16x32_bf16 v[80:83], v[120:123], v[60:63], v[80:83]\n\t"
      "v_mfma_f32_16x16x32_bf16 v[88:91], v[132:135], v[60:63], v[88:91]\n\t"
      "ds_read_b128 v[68:71], v156 offset:1024\n\t"
      "s_waitcnt lgkmcnt(2)\n\t"
      "v_mfma_f32_16x16x32_bf16 v[104:107], v[108:111], v[44:47], v[104:107]\n\t"
      "v_mfma_f32_16x16x32_bf16 v[48:51], v[116:119], v[44:47], v[48:51]\n\t"
      "v_mfma_f32_16x16x32_bf16 v[104:107], v[128:131], v[44:47], v[104:107]\n\t"
      "v_mfma_f32_16x16x32_bf16 v[152:155], v[120:123], v[44:47], v[152:155]\n\t"
      "ds_read_b128 v[96:99], v156 offset:3392\n\t"
      "v_mfma_f32_16x16x32_bf16 v[144:147], v[132:135], v[44:47], v[144:147]\n\t"
      "ds_bpermute_b32 v163, v159, v79\n\t"
      E_STAMP3
      E_PRIO2
      "s_waitcnt lgkmcnt(3)\n\t"
      "v_mfma_f32_16x16x32_bf16 v[40:43], v[8:11], v[52:55], v[40:43]\n\t"
      "v_mfma_f32_16x16x32_bf16 v[80:83], v[12:15], v[52:55], v[80:83]\n\t"
      "v_mfma_f32_16x16x32_bf16 v[88:91], v[16:19], v[52:55], v[88:91]\n\t"
      "ds_read_b128 v[112:115], v156 offset:1088\n\t"
      "v_add_f32_e32 v105, v105, v76\n\t"
      "v_mfma_f32_16x16x32_bf16 v[92:95], v[20:23], v[52:55], 0\n\t"
      "v_add_f32_e32 v106, v106, v77\n\t"
      "v_add_f32_e32 v107, v107, v78\n\t"
      "s_waitcnt lgkmcnt(3)\n\t"
      "v_mfma_f32_16x16x32_bf16 v[48:51], v[8:11], v[68:71], v[48:51]\n\t"
      "v_cvt_pk_bf16_f32 v77, v106, v107\n\t"
      "s_waitcnt lgkmcnt(1)\n\t"
      "v_cndmask_b32_e64 v165, v163, v162, s[42:43]\n\t"
      "v_mfma_f32_16x16x32_bf16 v[152:155], v[12:15], v[68:71], v[152:155]\n\t"
      "v_add_f32_e32 v104, v104, v165\n\t"
      "v_cvt_pk_bf16_f32 v76, v104, v105\n\t"
      "v_mfma_f32_16x16x32_bf16 v[144:147], v[16:19], v[68:71], v[144:147]\n\t"
      "ds_read_b128 v[124:127], v156 offset:3456\n\t"
      "v_mfma_f32_16x16x32_bf16 v[136:139], v[20:23], v[68:71], 0\n\t"
      "ds_write_b64 v158, v[76:77] offset:1536\n\t"
      "v_mfma_f32_16x16x32_bf16 v[40:43], v[24:27], v[96:99], v[40:43]\n\t"
      "ds_write_b32 v161, v161 offset:96\n\t"
      "v_mfma_f32_16x16x32_bf16 v[80:83], v[28:31], v[96:99], v[80:83]\n\t"
      "v_mfma_f32_16x16x32_bf16 v[88:91], v[32:35], v[96:99], v[88:91]\n\t"
      "ds_read_b128 v[140:143], v156 offset:1152\n\t"
      "v_mfma_f32_16x16x32_bf16 v[92:95], v[36:39], v[96:99], v[92:95]\n\t"
      "s_waitcnt lgkmcnt(4)\n\t"
      "v_mfma_f32_16x16x32_bf16 v[48:51], v[24:27], v[112:115], v[48:51]\n\t"
      "v_mfma_f32_16x16x32_bf16 v[152:155], v[28:31], v[112:115], v[152:155]\n\t"
      "v_mfma_f32_16x16x32_bf16 v[144:147], v[32:35], v[112:115], v[144:147]\n\t"
      "ds_read_b128 v[64:67], v156 offset:3520\n\t"
      "v_mfma_f32_16x16x32_bf16 v[136:139], v[36:39], v[112:115], v[136:139]\n\t"
      "s_waitcnt lgkmcnt(4)\n\t"
      "v_mfma_f32_16x16x32_bf16 v[40:43], v[56:59], v[124:127], v[40:43]\n\t"
      "v_mfma_f32_16x16x32_bf16 v[80:83], v[72:75], v[124:127], v[80:83]\n\t"
      "v_mfma_f32_16x16x32_bf16 v[88:91], v[84:87], v[124:127], v[88:91]\n\t"
      "v_mfma_f32_16x16x32_bf16 v[92:95], v[100:103], v[124:127], v[92:95]\n\t"
      "ds_read_b128 v[148:151], v156 offset:1216\n\t"
      "s_waitcnt lgkmcnt(2)\n\t"
      "v_mfma_f32_16x16x32_bf16 v[48:51], v[56:59], v[140:143], v[48:51]\n\t"
      "v_mfma_f32_16x16x32_bf16 v[152:155], v[72:75], v[140:143], v[152:155]\n\t"
      "v_mfma_f32_16x16x32_bf16 v[144:147], v[84:87], v[140:143], v[144:147]\n\t"
      "v_mfma_f32_16x16x32_bf16 v[136:139], v[100:103], v[140:143], v[136:139]\n\t"
      "s_waitcnt lgkmcnt(1)\n\t"
      "v_mfma_f32_16x16x32_bf16 v[40:43], v[108:111], v[64:67], v[40:43]\n\t"
      "ds_read_b128 v[60:63], v156 offset:3584\n\t"
      "v_mfma_f32_16x16x32_bf16 v[80:83], v[116:119], v[64:67], v[80:83]\n\t"
      "v_mfma_f32_16x16x32_bf16 v[40:43], v[128:131], v[64:67], v[40:43]\n\t"
      "v_mfma_f32_16x16x32_bf16 v[88:91], v[120:123], v[64:67], v[88:91]\n\t"
      "ds_read_b128 v[44:47], v156 offset:1280\n\t"
      "v_mfma_f32_16x16x32_bf16 v[92:95], v[132:135], v[64:67], v[92:95]\n\t"
      "s_waitcnt lgkmcnt(2)\n\t"
      "v_mfma_f32_16x16x32_bf16 v[48:51], v[108:111], v[148:151], v[48:51]\n\t"
      "v_mfma_f32_16x16x32_bf16 v[152:155], v[116:119], v[148:151], v[152:155]\n\t"
      "ds_read_b128 v[52:55], v156 offset:3648\n\t"
      "v_mfma_f32_16x16x32_bf16 v[48:51], v[128:131], v[148:151], v[48:51]\n\t"
      "v_mfma_f32_16x16x32_bf16 v[144:147], v[120:123], v[148:151], v[144:147]\n\t"
      "v_mfma_f32_16x16x32_bf16 v[136:139], v[132:135], v[148:151], v[136:139]\n\t"
      "ds_read_b128 v[68:71], v156 offset:1344\n\t"
      "s_waitcnt lgkmcnt(3)\n\t"
      "v_mfma_f32_16x16x32_bf16 v[80:83], v[8:11], v[60:63], v[80:83]\n\t"
      "v_mfma_f32_16x16x32_bf16 v[88:91], v[12:15], v[60:63], v[88:91]\n\t"
      "ds_read_b32 v166, v160 offset:16\n\t"
      "v_mfma_f32_16x16x32_bf16 v[92:95], v[16:19], v[60:63], v[92:95]\n\t"
      "ds_read_b128 v[20:23], v156 offset:3712\n\t"
      "s_waitcnt lgkmcnt(4)\n\t"
      "v_mfma_f32_16x16x32_bf16 v[152:155], v[8:11], v[44:47], v[152:155]\n\t"
      "v_mfma_f32_16x16x32_bf16 v[144:147], v[12:15], v[44:47], v[144:147]\n\t"
      "v_mfma_f32_16x16x32_bf16 v[136:139], v[16:19], v[44:47], v[136:139]\n\t"
      "ds_read_b128 v[104:107], v156 offset:1408\n\t"
      "s_waitcnt lgkmcnt(4)\n\t"
      "v_mfma_f32_16x16x32_bf16 v[80:83], v[24:27], v[52:55], v[80:83]\n\t"
      "s_waitcnt lgkmcnt(2)\n\t"
      "v_readfirstlane_b32 s40, v166\n\t"
      "s_cmp_ge_u32 s40, 8\n\t"
      "s_cbranch_scc1 .Lpe3_%=\n\t"
      ".Lpl3_%=:\n\t"
      "s_sleep 1\n\t"
      "ds_read_b32 v166, v160 offset:16\n\t"
      "s_waitcnt lgkmcnt(0)\n\t"
      "v_readfirstlane_b32 s40, v166\n\t"
      "s_cmp_lt_u32 s40, 8\n\t"
      "s_cbranch_scc1 .Lpl3_%=\n\t"
      ".Lpe3_%=:\n\t"
      "ds_read_b64_tr_b16 v[96:97], v157 offset:2448\n\t"
      "v_mfma_f32_16x16x32_bf16 v[88:91], v[28:31], v[52:55], v[88:91]\n\t"
      "ds_read_b64_tr_b16 v[98:99], v157 offset:2432\n\t"
      "v_mfma_f32_16x16x32_bf16 v[92:95], v[32:35], v[52:55], v[92:95]\n\t"
      "ds_read_b128 v[112:115], v156 offset:3776\n\t"
      "ds_read_b64_tr_b16 v[36:37], v157 offset:2960\n\t"
      "v_mfma_f32_16x16x32_bf16 v[152:155], v[24:27], v[68:71], v[152:155]\n\t"
      "ds_read_b64_tr_b16 v[38:39], v157 offset:2944\n\t"
      "v_mfma_f32_16x16x32_bf16 v[144:147], v[28:31], v[68:71], v[144:147]\n\t"
      "ds_read_b32 v166, v160 offset:20\n\t"
      "v_mfma_f32_16x16x32_bf16 v[136:139], v[32:35], v[68:71], v[136:139]\n\t"
      "s_waitcnt lgkmcnt(7)\n\t"
      "v_mfma_f32_16x16x32_bf16 v[80:83], v[56:59], v[20:23], v[80:83]\n\t"
      "ds_read_b128 v[124:127], v156 offset:1472\n\t"
      "v_mfma_f32_16x16x32_bf16 v[88:91], v[72:75], v[20:23], v[88:91]\n\t"
      "v_mfma_f32_16x16x32_bf16 v[92:95], v[84:87], v[20:23], v[92:95]\n\t"
      "s_waitcnt lgkmcnt(7)\n\t"
      "v_mfma_f32_16x16x32_bf16 v[152:155], v[56:59], v[104:107], v[152:155]\n\t"
      "s_waitcnt lgkmcnt(1)\n\t"
      "v_readfirstlane_b32 s40, v166\n\t"
      "s_cmp_ge_u32 s40, 8\n\t"
      "s_cbranch_scc1 .Lpe4_%=\n\t"
      ".Lpl4_%=:\n\t"
      "s_sleep 1\n\t"
      "ds_read_b32 v166, v160 offset:20\n\t"
      "s_waitcnt lgkmcnt(0)\n\t"
      "v_readfirstlane_b32 s40, v166\n\t"
      "s_cmp_lt_u32 s40, 8\n\t"
      "s_cbranch_scc1 .Lpl4_%=\n\t"
      ".Lpe4_%=:\n\t"
      "ds_read_b64_tr_b16 v[76:77], v157 offset:3472\n\t"
      "v_mfma_f32_16x16x32_bf16 v[144:147], v[72:75], v[104:107], v[144:147]\n\t"
      "ds_read_b128 v[140:143], v156 offset:2304\n\t"
      "ds_read_b64_tr_b16 v[78:79], v157 offset:3456\n\t"
      "v_mfma_f32_16x16x32_bf16 v[136:139], v[84:87], v[104:107], v[136:139]\n\t"
      "ds_read_b64_tr_b16 v[100:101], v157 offset:3984\n\t"
      "v_mfma_f32_16x16x32_bf16 v[80:83], v[108:111], v[112:115], v[80:83]\n\t"
      "ds_read_b64_tr_b16 v[102:103], v157 offset:3968\n\t"
      "v_mfma_f32_16x16x32_bf16 v[88:91], v[116:119], v[112:115], v[88:91]\n\t"
      "ds_read_b64_tr_b16 v[64:65], v157 offset:2320\n\t"
      "v_mfma_f32_16x16x32_bf16 v[80:83], v[128:131], v[112:115], v[80:83]\n\t"
      "ds_read_b128 v[148:151], v156 offset:0\n\t"
      "ds_read_b64_tr_b16 v[66:67], v157 offset:2304\n\t"
      "v_mfma_f32_16x16x32_bf16 v[92:95], v[120:123], v[112:115], v[92:95]\n\t"
      "ds_read_b64_tr_b16 v[132:133], v157 offset:2832\n\t"
      "s_waitcnt lgkmcnt(9)\n\t"
      "v_mfma_f32_16x16x32_bf16 v[152:155], v[108:111], v[124:127], v[152:155]\n\t"
      "ds_read_b64_tr_b16 v[134:135], v157 offset:2816\n\t"
      "v_mfma_f32_16x16x32_bf16 v[144:147], v[116:119], v[124:127], v[144:147]\n\t"
      "ds_read_b64_tr_b16 v[60:61], v157 offset:3344\n\t"
      "v_mfma_f32_16x16x32_bf16 v[152:155], v[128:131], v[124:127], v[152:155]\n\t"
      "ds_read_b128 v[44:47], v156 offset:2368\n\t"
      "ds_read_b64_tr_b16 v[62:63], v157 offset:3328\n\t"
      "v_mfma_f32_16x16x32_bf16 v[136:139], v[120:123], v[124:127], v[136:139]\n\t"
      "ds_read_b64_tr_b16 v[16:17], v157 offset:3856\n\t"
      "s_waitcnt lgkmcnt(12)\n\t"
      "v_mfma_f32_16x16x32_bf16 v[40:43], v[96:99], v[140:143], v[40:43]\n\t"
      "ds_read_b64_tr_b16 v[18:19], v157 offset:3840\n\t"
      "v_mfma_f32_16x16x32_bf16 v[80:83], v[36:39], v[140:143], v[80:83]\n\t"
      "ds_read_b64_tr_b16 v[52:53], v157 offset:2192\n\t"
      "s_waitcnt lgkmcnt(13)\n\t"
      "v_mfma_f32_16x16x32_bf16 v[88:91], v[76:79], v[140:143], v[88:91]\n\t"
      "ds_read_b128 v[68:71], v156 offset:64\n\t"
      "ds_read_b64_tr_b16 v[54:55], v157 offset:2176\n\t"
      "s_waitcnt lgkmcnt(13)\n\t"
      "v_mfma_f32_16x16x32_bf16 v[92:95], v[100:103], v[140:143], v[92:95]\n\t"
      "ds_read_b64_tr_b16 v[32:33], v157 offset:2704\n\t"
      "s_waitcnt lgkmcnt(12)\n\t"
      "v_mfma_f32_16x16x32_bf16 v[48:51], v[96:99], v[148:151], v[48:51]\n\t"
      "ds_read_b64_tr_b16 v[34:35], v157 offset:2688\n\t"
      "v_mfma_f32_16x16x32_bf16 v[152:155], v[36:39], v[148:151], v[152:155]\n\t"
      "ds_read_b64_tr_b16 v[20:21], v157 offset:3216\n\t"
      "v_mfma_f32_16x16x32_bf16 v[144:147], v[76:79], v[148:151], v[144:147]\n\t"
      "ds_read_b128 v[104:107], v156 offset:2432\n\t"
      "ds_read_b64_tr_b16 v[22:23], v157 offset:3200\n\t"
      "v_mfma_f32_16x16x32_bf16 v[136:139], v[100:103], v[148:151], v[136:139]\n\t"
      "ds_read_b64_tr_b16 v[84:85], v157 offset:3728\n\t"
      "s_waitcnt lgkmcnt(12)\n\t"
      "v_mfma_f32_16x16x32_bf16 v[40:43], v[64:67], v[44:47], v[40:43]\n\t"
      "ds_read_b64_tr_b16 v[86:87], v157 offset:3712\n\t"
      "v_mfma_f32_16x16x32_bf16 v[80:83], v[132:135], v[44:47], v[80:83]\n\t"
      "ds_read_b64_tr_b16 v[112:113], v157 offset:2064\n\t"
      "s_waitcnt lgkmcnt(13)\n\t"
      "v_mfma_f32_16x16x32_bf16 v[88:91], v[60:63], v[44:47], v[88:91]\n\t"
      "ds_read_b128 v[124:127], v156 offset:128\n\t"
      "ds_read_b64_tr_b16 v[114:115], v157 offset:2048\n\t"
      "s_waitcnt lgkmcnt(13)\n\t"
      "v_mfma_f32_16x16x32_bf16 v[92:95], v[16:19], v[44:47], v[92:95]\n\t"
      "ds_read_b64_tr_b16 v[120:121], v157 offset:2576\n\t"
      "s_waitcnt lgkmcnt(12)\n\t"
      "v_mfma_f32_16x16x32_bf16 v[48:51], v[64:67], v[68:71], v[48:51]\n\t"
      "ds_read_b64_tr_b16 v[122:123], v157 offset:2560\n\t"
      "v_mfma_f32_16x16x32_bf16 v[152:155], v[132:135], v[68:71], v[152:155]\n\t"
      "ds_read_b64_tr_b16 v[140:141], v157 offset:3088\n\t"
      "v_mfma_f32_16x16x32_bf16 v[144:147], v[60:63], v[68:71], v[144:147]\n\t"
      "ds_read_b128 v[148:151], v156 offset:2496\n\t"
      "ds_read_b64_tr_b16 v[142:143], v157 offset:3072\n\t"
      "v_mfma_f32_16x16x32_bf16 v[136:139], v[16:19], v[68:71], v[136:139]\n\t"
      "ds_read_b64_tr_b16 v[100:101], v157 offset:3600\n\t"
      "s_waitcnt lgkmcnt(12)\n\t"
      "v_mfma_f32_16x16x32_bf16 v[40:43], v[52:55], v[104:107], v[40:43]\n\t"
      "ds_read_b64_tr_b16 v[102:103], v157 offset:3584\n\t"
      "v_mfma_f32_16x16x32_bf16 v[80:83], v[32:35], v[104:107], v[80:83]\n\t"
      "s_waitcnt lgkmcnt(12)\n\t"
      "v_mfma_f32_16x16x32_bf16 v[88:91], v[20:23], v[104:107], v[88:91]\n\t"
      "ds_read_b128 v[44:47], v156 offset:192\n\t"
      "s_waitcnt lgkmcnt(11)\n\t"
      "v_mfma_f32_16x16x32_bf16 v[92:95], v[84:87], v[104:107], v[92:95]\n\t"
      "s_waitcnt lgkmcnt(9)\n\t"
      "v_mfma_f32_16x16x32_bf16 v[48:51], v[52:55], v[124:127], v[48:51]\n\t"
      "v_mfma_f32_16x16x32_bf16 v[152:155], v[32:35], v[124:127], v[152:155]\n\t"
      "v_mfma_f32_16x16x32_bf16 v[144:147], v[20:23], v[124:127], v[144:147]\n\t"
      "ds_read_b128 v[68:71], v156 offset:2560\n\t"
      "v_mfma_f32_16x16x32_bf16 v[136:139], v[84:87], v[124:127], v[136:139]\n\t"
      "s_waitcnt lgkmcnt(5)\n\t"
      "v_mfma_f32_16x16x32_bf16 v[40:43], v[112:115], v[148:151], v[40:43]\n\t"
      "v_mfma_f32_16x16x32_bf16 v[80:83], v[120:123], v[148:151], v[80:83]\n\t"
      "ds_read_b128 v[16:19], v156 offset:256\n\t"
      "s_waitcnt lgkmcnt(5)\n\t"
      "v_mfma_f32_16x16x32_bf16 v[88:91], v[140:143], v[148:151], v[88:91]\n\t"
      "s_waitcnt lgkmcnt(3)\n\t"
      "v_mfma_f32_16x16x32_bf16 v[92:95], v[100:103], v[148:151], v[92:95]\n\t"
      "s_waitcnt lgkmcnt(2)\n\t"
      "v_mfma_f32_16x16x32_bf16 v[48:51], v[112:115], v[44:47], v[48:51]\n\t"
      "ds_read_b128 v[104:107], v156 offset:2624\n\t"
      "v_mfma_f32_16x16x32_bf16 v[152:155], v[120:123], v[44:47], v[152:155]\n\t"
      "ds_bpermute_b32 v162, v159, v43\n\t"
      "v_mfma_f32_16x16x32_bf16 v[144:147], v[140:143], v[44:47], v[144:147]\n\t"
      "v_mfma_f32_16x16x32_bf16 v[136:139], v[100:103], v[44:47], v[136:139]\n\t"
      "ds_read_b128 v[124:127], v156 offset:320\n\t"
      E_STAMP4
      E_PRIO3
      "s_waitcnt lgkmcnt(4)\n\t"
      "v_mfma_f32_16x16x32_bf16 v[80:83], v[96:99], v[68:71], v[80:83]\n\t"
      "v_add_f32_e32 v49, v49, v40\n\t"
      "v_add_f32_e32 v50, v50, v41\n\t"
      "v_mfma_f32_16x16x32_bf16 v[88:91], v[36:39], v[68:71], v[88:91]\n\t"
      "v_add_f32_e32 v51, v51, v42\n\t"
      "v_cvt_pk_bf16_f32 v41, v50, v51\n\t"
      "v_mfma_f32_16x16x32_bf16 v[92:95], v[76:79], v[68:71], v[92:95]\n\t"
      "ds_read_b128 v[84:87], v156 offset:2688\n\t"
      "s_waitcnt lgkmcnt(2)\n\t"
      "v_cndmask_b32_e64 v165, v162, v163, s[42:43]\n\t"
      "v_mfma_f32_16x16x32_bf16 v[152:155], v[96:99], v[16:19], v[152:155]\n\t"
      "v_add_f32_e32 v48, v48, v165\n\t"
      "v_cvt_pk_bf16_f32 v40, v48, v49\n\t"
      "v_mfma_f32_16x16x32_bf16 v[144:147], v[36:39], v[16:19], v[144:147]\n\t"
      "ds_write_b64 v158, v[40:41] offset:2048\n\t"
      "v_mfma_f32_16x16x32_bf16 v[136:139], v[76:79], v[16:19], v[136:139]\n\t"
      "ds_read_b128 v[148:151], v156 offset:384\n\t"
      "v_mfma_f32_16x16x32_bf16 v[80:83], v[64:67], v[104:107], v[80:83]\n\t"
      "ds_write_b32 v161, v161 offset:128\n\t"
      "v_mfma_f32_16x16x32_bf16 v[88:91], v[132:135], v[104:107], v[88:91]\n\t"
      "v_mfma_f32_16x16x32_bf16 v[92:95], v[60:63], v[104:107], v[92:95]\n\t"
      "ds_read_b128 v[44:47], v156 offset:2752\n\t"
      "s_waitcnt lgkmcnt(5)\n\t"
      "v_mfma_f32_16x16x32_bf16 v[152:155], v[64:67], v[124:127], v[152:155]\n\t"
      "v_mfma_f32_16x16x32_bf16 v[144:147], v[132:135], v[124:127], v[144:147]\n\t"
      "v_mfma_f32_16x16x32_bf16 v[136:139], v[60:63], v[124:127], v[136:139]\n\t"
      "ds_read_b128 v[100:103], v156 offset:448\n\t"
      "s_waitcnt lgkmcnt(5)\n\t"
      "v_mfma_f32_16x16x32_bf16 v[80:83], v[52:55], v[84:87], v[80:83]\n\t"
      "v_mfma_f32_16x16x32_bf16 v[88:91], v[32:35], v[84:87], v[88:91]\n\t"
      "v_mfma_f32_16x16x32_bf16 v[92:95], v[20:23], v[84:87], v[92:95]\n\t"
      "ds_read_b128 v[68:71], v156 offset:2816\n\t"
      "s_waitcnt lgkmcnt(4)\n\t"
      "v_mfma_f32_16x16x32_bf16 v[152:155], v[52:55], v[148:151], v[152:155]\n\t"
      "v_mfma_f32_16x16x32_bf16 v[144:147], v[32:35], v[148:151], v[144:147]\n\t"
      "ds_read_b128 v[48:51], v156 offset:512\n\t"
      "v_mfma_f32_16x16x32_bf16 v[136:139], v[20:23], v[148:151], v[136:139]\n\t"
      "s_waitcnt lgkmcnt(3)\n\t"
      "v_mfma_f32_16x16x32_bf16 v[80:83], v[112:115], v[44:47], v[80:83]\n\t"
      "ds_read_b128 v[16:19], v156 offset:2880\n\t"
      "v_mfma_f32_16x16x32_bf16 v[88:91], v[120:123], v[44:47], v[88:91]\n\t"
      "v_mfma_f32_16x16x32_bf16 v[92:95], v[140:143], v[44:47], v[92:95]\n\t"
      "ds_read_b128 v[76:79], v156 offset:576\n\t"
      "s_waitcnt lgkmcnt(4)\n\t"
      "v_mfma_f32_16x16x32_bf16 v[152:155], v[112:115], v[100:103], v[152:155]\n\t"
      "v_mfma_f32_16x16x32_bf16 v[144:147], v[120:123], v[100:103], v[144:147]\n\t"
      "ds_read_b128 v[104:107], v156 offset:2944\n\t"
      "ds_bpermute_b32 v163, v159, v83\n\t"
      "v_mfma_f32_16x16x32_bf16 v[136:139], v[140:143], v[100:103], v[136:139]\n\t"
      "s_waitcnt lgkmcnt(5)\n\t"
      "v_mfma_f32_16x16x32_bf16 v[88:91], v[96:99], v[68:71], v[88:91]\n\t"
      "ds_read_b128 v[124:127], v156 offset:640\n\t"
      "v_mfma_f32_16x16x32_bf16 v[92:95], v[36:39], v[68:71], v[92:95]\n\t"
      "ds_read_b32 v164, v160 offset:12\n\t"
      "v_add_f32_e32 v153, v153, v80\n\t"
      "s_waitcnt lgkmcnt(6)\n\t"
      "v_mfma_f32_16x16x32_bf16 v[144:147], v[96:99], v[48:51], v[144:147]\n\t"
      "ds_read_b128 v[60:63], v156 offset:3008\n\t"
      "v_add_f32_e32 v154, v154, v81\n\t"
      "v_mfma_f32_16x16x32_bf16 v[136:139], v[36:39], v[48:51], v[136:139]\n\t"
      "v_add_f32_e32 v155, v155, v82\n\t"
      "v_cvt_pk_bf16_f32 v81, v154, v155\n\t"
      "s_waitcnt lgkmcnt(6)\n\t"
      "v_mfma_f32_16x16x32_bf16 v[88:91], v[64:67], v[16:19], v[88:91]\n\t"
      "ds_read_b128 v[84:87], v156 offset:704\n\t"
      "s_waitcnt lgkmcnt(4)\n\t"
      "v_cndmask_b32_e64 v165, v163, v162, s[42:43]\n\t"
      "v_mfma_f32_16x16x32_bf16 v[92:95], v[132:135], v[16:19], v[92:95]\n\t"
      "v_add_f32_e32 v152, v152, v165\n\t"
      "v_cvt_pk_bf16_f32 v80, v152, v153\n\t"
      "v_mfma_f32_16x16x32_bf16 v[144:147], v[64:67], v[76:79], v[144:147]\n\t"
      "s_waitcnt lgkmcnt(2)\n\t"
      "v_readfirstlane_b32 s40, v164\n\t"
      "s_cmp_ge_u32 s40, 8\n\t"
      "s_cbranch_scc1 .Lpe5_%=\n\t"
      ".Lpl5_%=:\n\t"
      "s_sleep 1\n\t"
      "ds_read_b32 v164, v160 offset:12\n\t"
      "s_waitcnt lgkmcnt(0)\n\t"
      "v_readfirstlane_b32 s40, v164\n\t"
      "s_cmp_lt_u32 s40, 8\n\t"
      "s_cbranch_scc1 .Lpl5_%=\n\t"
      ".Lpe5_%=:\n\t"
      "ds_read_b128 v[40:43], v156 offset:3840\n\t"
      "v_mfma_f32_16x16x32_bf16 v[136:139], v[132:135], v[76:79], v[136:139]\n\t"
      "ds_write_b64 v158, v[80:81] offset:2560\n\t"
      "ds_write_b32 v161, v161 offset:160\n\t"
      "v_mfma_f32_16x16x32_bf16 v[88:91], v[52:55], v[104:107], v[88:91]\n\t"
      "ds_read_b128 v[148:151], v156 offset:1536\n\t"
      "v_mfma_f32_16x16x32_bf16 v[92:95], v[32:35], v[104:107], v[92:95]\n\t"
      "v_mfma_f32_16x16x32_bf16 v[144:147], v[52:55], v[124:127], v[144:147]\n\t"
      "ds_read_b128 v[20:23], v156 offset:3904\n\t"
      "v_mfma_f32_16x16x32_bf16 v[136:139], v[32:35], v[124:127], v[136:139]\n\t"
      "s_waitcnt lgkmcnt(6)\n\t"
      "v_mfma_f32_16x16x32_bf16 v[88:91], v[112:115], v[60:63], v[88:91]\n\t"
      "ds_read_b128 v[44:47], v156 offset:1600\n\t"
      "v_mfma_f32_16x16x32_bf16 v[92:95], v[120:123], v[60:63], v[92:95]\n\t"
      "s_waitcnt lgkmcnt(6)\n\t"
      "v_mfma_f32_16x16x32_bf16 v[144:147], v[112:115], v[84:87], v[144:147]\n\t"
      "ds_read_b128 v[100:103], v156 offset:3968\n\t"
      "v_mfma_f32_16x16x32_bf16 v[136:139], v[120:123], v[84:87], v[136:139]\n\t"
      "s_waitcnt lgkmcnt(6)\n\t"
      "v_mfma_f32_16x16x32_bf16 v[88:91], v[8:11], v[40:43], v[88:91]\n\t"
      "ds_read_b128 v[140:143], v156 offset:1664\n\t"
      "v_mfma_f32_16x16x32_bf16 v[92:95], v[12:15], v[40:43], v[92:95]\n\t"
      "s_waitcnt lgkmcnt(4)\n\t"
      "v_mfma_f32_16x16x32_bf16 v[144:147], v[8:11], v[148:151], v[144:147]\n\t"
      "ds_read_b128 v[68:71], v156 offset:4032\n\t"
      "v_mfma_f32_16x16x32_bf16 v[136:139], v[12:15], v[148:151], v[136:139]\n\t"
      "s_waitcnt lgkmcnt(4)\n\t"
      "v_mfma_f32_16x16x32_bf16 v[88:91], v[24:27], v[20:23], v[88:91]\n\t"
      "v_mfma_f32_16x16x32_bf16 v[92:95], v[28:31], v[20:23], v[92:95]\n\t"
      "ds_read_b128 v[48:51], v156 offset:1728\n\t"
      "s_waitcnt lgkmcnt(4)\n\t"
      "v_mfma_f32_16x16x32_bf16 v[144:147], v[24:27], v[44:47], v[144:147]\n\t"
      "v_mfma_f32_16x16x32_bf16 v[136:139], v[28:31], v[44:47], v[136:139]\n\t"
      "s_waitcnt lgkmcnt(3)\n\t"
      "v_mfma_f32_16x16x32_bf16 v[88:91], v[56:59], v[100:103], v[88:91]\n\t"
      "ds_read_b128 v[36:39], v156 offset:3072\n\t"
      "v_mfma_f32_16x16x32_bf16 v[92:95], v[72:75], v[100:103], v[92:95]\n\t"
      "ds_read_b128 v[16:19], v156 offset:3136\n\t"
      "s_waitcnt lgkmcnt(4)\n\t"
      "v_mfma_f32_16x16x32_bf16 v[144:147], v[56:59], v[140:143], v[144:147]\n\t"
      "ds_read_b128 v[76:79], v156 offset:3200\n\t"
      "v_mfma_f32_16x16x32_bf16 v[136:139], v[72:75], v[140:143], v[136:139]\n\t"
      "ds_read_b128 v[132:135], v156 offset:3264\n\t"
      "s_waitcnt lgkmcnt(5)\n\t"
      "v_mfma_f32_16x16x32_bf16 v[88:91], v[108:111], v[68:71], v[88:91]\n\t"
      "ds_read_b128 v[152:155], v156 offset:4096\n\t"
      "v_mfma_f32_16x16x32_bf16 v[92:95], v[116:119], v[68:71], v[92:95]\n\t"
      "ds_read_b128 v[104:107], v156 offset:4160\n\t"
      "v_mfma_f32_16x16x32_bf16 v[88:91], v[128:131], v[68:71], v[88:91]\n\t"
      "ds_read_b128 v[124:127], v156 offset:4224\n\t"
      "s_waitcnt lgkmcnt(7)\n\t"
      "v_mfma_f32_16x16x32_bf16 v[144:147], v[108:111], v[48:51], v[144:147]\n\t"
      "ds_read_b128 v[32:35], v156 offset:4288\n\t"
      "v_mfma_f32_16x16x32_bf16 v[136:139], v[116:119], v[48:51], v[136:139]\n\t"
      "v_mfma_f32_16x16x32_bf16 v[144:147], v[128:131], v[48:51], v[144:147]\n\t"
      "ds_read_b128 v[60:63], v156 offset:768\n\t"
      "s_waitcnt lgkmcnt(8)\n\t"
      "v_mfma_f32_16x16x32_bf16 v[92:95], v[96:99], v[36:39], v[92:95]\n\t"
      "ds_read_b128 v[84:87], v156 offset:832\n\t"
      "ds_bpermute_b32 v162, v159, v91\n\t"
      "s_waitcnt lgkmcnt(9)\n\t"
      "v_mfma_f32_16x16x32_bf16 v[92:95], v[64:67], v[16:19], v[92:95]\n\t"
      "ds_read_b128 v[120:123], v156 offset:896\n\t"
      "s_waitcnt lgkmcnt(9)\n\t"
      "v_mfma_f32_16x16x32_bf16 v[92:95], v[52:55], v[76:79], v[92:95]\n\t"
      "ds_read_b128 v[40:43], v156 offset:960\n\t"
      "v_add_f32_e32 v145, v145, v88\n\t"
      "s_waitcnt lgkmcnt(9)\n\t"
      "v_mfma_f32_16x16x32_bf16 v[92:95], v[112:115], v[132:135], v[92:95]\n\t"
      "ds_read_b128 v[80:83], v156 offset:1792\n\t"
      "v_add_f32_e32 v146, v146, v89\n\t"
      "s_waitcnt lgkmcnt(9)\n\t"
      "v_mfma_f32_16x16x32_bf16 v[92:95], v[8:11], v[152:155], v[92:95]\n\t"
      "ds_read_b128 v[148:151], v156 offset:1856\n\t"
      "v_add_f32_e32 v147, v147, v90\n\t"
      "s_waitcnt lgkmcnt(9)\n\t"
      "v_mfma_f32_16x16x32_bf16 v[92:95], v[24:27], v[104:107], v[92:95]\n\t"
      "ds_read_b128 v[12:15], v156 offset:1920\n\t"
      "v_cvt_pk_bf16_f32 v89, v146, v147\n\t"
      "s_waitcnt lgkmcnt(9)\n\t"
      "v_mfma_f32_16x16x32_bf16 v[92:95], v[56:59], v[124:127], v[92:95]\n\t"
      "ds_read_b128 v[20:23], v156 offset:1984\n\t"
      "s_waitcnt lgkmcnt(6)\n\t"
      "v_cndmask_b32_e64 v165, v162, v163, s[42:43]\n\t"
      "v_mfma_f32_16x16x32_bf16 v[92:95], v[108:111], v[32:35], v[92:95]\n\t"
      "v_add_f32_e32 v144, v144, v165\n\t"
      "v_cvt_pk_bf16_f32 v88, v144, v145\n\t"
      "v_mfma_f32_16x16x32_bf16 v[92:95], v[128:131], v[32:35], v[92:95]\n\t"
      "ds_write_b64 v158, v[88:89] offset:3072\n\t"
      "ds_write_b32 v161, v161 offset:192\n\t"
      "v_mfma_f32_16x16x32_bf16 v[136:139], v[96:99], v[60:63], v[136:139]\n\t"
      "v_mfma_f32_16x16x32_bf16 v[136:139], v[64:67], v[84:87], v[136:139]\n\t"
      "s_waitcnt lgkmcnt(7)\n\t"
      "v_mfma_f32_16x16x32_bf16 v[136:139], v[52:55], v[120:123], v[136:139]\n\t"
      "s_waitcnt lgkmcnt(6)\n\t"
      "v_mfma_f32_16x16x32_bf16 v[136:139], v[112:115], v[40:43], v[136:139]\n\t"
      "ds_bpermute_b32 v163, v159, v95\n\t"
      "s_waitcnt lgkmcnt(6)\n\t"
      "v_mfma_f32_16x16x32_bf16 v[136:139], v[8:11], v[80:83], v[136:139]\n\t"
      "s_waitcnt lgkmcnt(5)\n\t"
      "v_mfma_f32_16x16x32_bf16 v[136:139], v[24:27], v[148:151], v[136:139]\n\t"
      "s_waitcnt lgkmcnt(4)\n\t"
      "v_mfma_f32_16x16x32_bf16 v[136:139], v[56:59], v[12:15], v[136:139]\n\t"
      "s_waitcnt lgkmcnt(3)\n\t"
      "v_mfma_f32_16x16x32_bf16 v[136:139], v[108:111], v[20:23], v[136:139]\n\t"
      "v_mfma_f32_16x16x32_bf16 v[136:139], v[128:131], v[20:23], v[136:139]\n\t"
      E_STAMP5
      "s_waitcnt lgkmcnt(0)\n\t"
      "v_cndmask_b32_e64 v165, v163, v162, s[42:43]\n\t"
      "s_nop 5\n\t"
      "v_add_f32_e32 v137, v137, v92\n\t"
      "v_add_f32_e32 v138, v138, v93\n\t"
      "v_add_f32_e32 v139, v139, v94\n\t"
      "v_cvt_pk_bf16_f32 v93, v138, v139\n\t"
      "v_add_f32_e32 v136, v136, v165\n\t"
      "v_cvt_pk_bf16_f32 v92, v136, v137\n\t"
      "ds_write_b64 v158, v[92:93] offset:3584\n\t"
      "ds_write_b32 v161, v161 offset:224\n\t"
        : E_OUT
        : "{v[128:131]}"(dfr), "{v156}"(xb), "{v157}"(tb), "{v158}"(yw), "{v159}"(nb), "{v160}"(fa), "{v161}"(fy), "{s[42:43]}"(1ull)
        : "memory", "scc", "s40", E_CLOB);
    }
    E_SAVE
  }
  __builtin_amdgcn_endpgm();
}

__device__ __forceinline__ void line_req_s(u32& dst, const void* p, u32 off) {
  asm volatile("global_load_dword %0, %1, %2" : "=v"(dst) : "v"(off), "s"(p) : "memory");
}
__device__ __forceinline__ void stream_body( char* smem, int g, int p, u32 la, u32 lb, u32 po, u32 ps, const u16* pb,
                                            const u16* __restrict__ u, const u16* __restrict__ kin, const u16* __restrict__ vin,
                                            const u16* __restrict__ shk, const u16* __restrict__ ssmk,
                                            const u16* __restrict__ qin, u16* __restrict__ out) {
  int lane = threadIdx.x;
  asm volatile("v_and_b32 %0, 63, %0" : "+v"(lane));
  __builtin_assume(lane >= 0 && lane < 64);
  const bool isa = g < 128;
  const int gc = (g & 127) * 8;
  const u32 sb = lds_off(smem);
  const u32 fl0 = sb + FL_OFF;
  const int c = lane >> 3, rg = lane & 7;
  const u32 rb = sb + RAW_OFF + 2048 * p;
  __builtin_amdgcn_s_setprio(H_PRIO0);
  asm volatile("ds_write_b64 %0, %1" :: "v"(fl0 + 8 * lane), "v"((u32x2){0u, 0u}) : "memory");
  const __amdgpu_buffer_rsrc_t ro = __builtin_amdgcn_make_buffer_rsrc((void*)out, 0, 8 * 1024 * 1024, 0x00020000);
#define XPAD dsw128<0>(sb + (2 * p + (lane >> 5)) * XW + (lane & 16) * (XP / 16) + 16 * (lane & 15), (u32x4){0, 0, 0, 0});
  if (isa) {
    asm volatile("s_waitcnt lgkmcnt(0)\n\ts_barrier" ::: "memory");
    __builtin_amdgcn_sched_barrier(0);
    XPAD
    if (p < 2) {
      const u32 z = (lane & 16) + (lane >> 5) - (lane & 15) - 4 - p, h = 8 - 2 * p;
      u32x4 bd;
#pragma unroll
      for (int j = 0; j < 4; j++) bd[j] = (z + 4 * j <= h ? 0x3f80u : 0u) | (z + 4 * j + 2 <= h ? 0x3f800000u : 0u);
      dsw128<0>(sb + BAND_OFF + 1024 * p + 16 * lane, bd);
    }
    u32 pr[5];
    const u32 rq = (16 * (lane >> 1) + (g & 15)) * (D_CH * 2) + 128 * (lane & 1) + ((gc * 2 + 512) & 0x700);
    const u32 go = (128 * p + 16 * (c & 3) + ((lane >> 2) & 1) + 2 * (lane & 3) + 8 * (lane >> 5)) * (D_CH * 2) + gc * 2;
#define UQ(j) { dma16(u + (size_t)(512 * (j)) * D_CH, go, rb + 8192 * (j)); dma16(u + (size_t)(512 * (j) + 64) * D_CH, go, rb + 8192 * (j) + 1024); }
    const u32 ta = rb + 128 * (lane >> 4) + 64 * ((lane >> 1) & 1) + 16 * ((lane >> 2) & 3) + 8 * (lane & 1);
    const u32 xw = sb + rg * XW + (c & 1) * XP + 256 + 128 * p + 16 * (c >> 1);
    auto tr = [&](auto jc) {
      constexpr int j = decltype(jc)::value;
      u32x2 d[4];
      sfor<4>([&](auto ec) { constexpr int e = decltype(ec)::value; dsr64tr<8192 * j + 512 * e>(d[e], ta); });
      asm volatile("s_waitcnt lgkmcnt(2)" : "+v"(d[0]), "+v"(d[1]) :: "memory");
      dsw128<512 * j>(xw, (u32x4){d[0][0], d[0][1], d[1][0], d[1][1]});
      asm volatile("s_waitcnt lgkmcnt(1)" : "+v"(d[2]), "+v"(d[3]) :: "memory");
      dsw128<512 * j + 64>(xw, (u32x4){d[2][0], d[2][1], d[3][0], d[3][1]});
      lds_add(fl0 + FL_XQ + 4 * j, 2u);
    };
#define TR(j) tr(std::integral_constant<int, j>{});
#define R4 { line_req_s(pr[3], p == 3 ? u : qin + 512 * p * D_CH, p == 3 ? po + 2 * ps : rq); pr[4] = 0; if (lane < 16) line_req_s(pr[4], qin + (1536 + 128 * p) * D_CH, rq); }
    asm volatile("s_waitcnt vmcnt(0)" :: "v"(la) : "memory");
    A_DELAY1
    UQ(0)
    line_req_s(pr[1], pb, po);
    wait_vm<1>(); TR(0)
    wait_vm<0>(); UQ(1)
    line_req_s(pr[2], pb, po + ps);
    R4
    wait_vm<3>(); TR(1)
    wait_vm<2>(); UQ(2)
    wait_vm<3>(); UQ(3)
    wait_vm<2>(); TR(2)
    wait_vm<0>(); TR(3)
    asm volatile("" :: "v"(pr[1]));
    asm volatile("" :: "v"(pr[2]), "v"(pr[3]), "v"(pr[4]));
    const u32 ya = sb + HS_OFF + ((lane >> 2) & 3) * HW + 32 * (lane >> 4) + 8 * (lane & 3) + 128 * p;
    const u32 oo = ((64 * p + lane) * (2 * D_CH) + g * 8) * 2;
#pragma nounroll
    for (int st = 0; st < 8; st++) {
      poll_all(fl0 + FL_Y + 32 * st + 4 * rg, 1u);
      if (st == 5) {
      }
      if (st == 7) {
      }
      out_a<AUX0>(ya + 512 * st, oo + st * (256 * 2 * D_CH * 2), ro);
      if (st == 3) {
      }
    }
  } else {
    const int r = lane & 15, q = lane >> 4;
    const u32 ho = sb + HALO_OFF + 384 * (p >> 1) + 64 * (p & 1);
    u32 sw;
    if (rg < 3 && c < 3) dma16(kin, (512 + 512 * rg + 128 * p - 3 + c) * (D_CH * 2) + gc * 2, ho);
    asm volatile("global_load_ushort %0, %1, %2" : "=v"(sw) : "v"((gc + rg) * 8 + 6 - 2 * q), "s"(shk) : "memory");
    __builtin_amdgcn_sched_barrier(0);
    XPAD
    {
      const u32 a1 = HS_OFF + (2 * p + q) * HW + (lane & 8) * ((C2 - 4) / 8) + (lane & 8) / 2 + 4 * rg;
      if (lane < 44) asm volatile("ds_write_b32 %0, %1" :: "v"(sb + (lane < 32 ? a1 : RAW_OFF - 176 + 4 * lane)), "v"(0u) : "memory");
    }
    const u32 gk = (128 * p + lane) * (D_CH * 2) + gc * 2;
    const u32 gv = (128 * p + (lane & 27) + 8 * (lane & 4) + ((lane >> 3) & 4)) * (D_CH * 2) + gc * 2;
    const u32 so = (gc + 2 * p) * (L_SEQ * 2) + 16 * lane, hb = sb + HS_OFF + 2 * p * HW + 2080;
    const u32 ka = rb + 16 * (2 * (r & 12) + (r & 3) + q - 3);
    u32 ka0[3];
#pragma unroll
    for (int j = 0; j < 3; j++) ka0[j] = r + q < 3 ? ho + 16 * j + 128 * (r + q) : ka + 16384 * ((j + 1) % 3);
    const u32 va = rb + 8192 + 128 * q + 4 * (r & 12) + 32 * (r & 2) + 8 * (r & 1);
    const u32 xw = sb + rg * XW + 256 + 128 * p + 4 * (r & 8) + 8 * q;
    asm volatile("s_waitcnt lgkmcnt(0)\n\ts_barrier" ::: "memory");
#define KV(j) { _Pragma("unroll") for (int i = 0; i < 4; i++)                                                                       \
      dma16(((i & 1) ? vin : kin) + (size_t)(512 * (j) + 64 * (i >> 1)) * D_CH, (i & 1) ? gv : gk, rb + 16384 * ((j) % 3) + 8192 * (i & 1) + 1024 * (i >> 1)); }
#define TAPS(j) { _Pragma("unroll") for (int i = 0; i < 4; i++) dma16(ssmk + 1024 + 512 * (j) + L_SEQ * (i >> 1), so, hb + 1024 * (j) + HW * (i >> 1) + C2 * (i & 1)); }
#define QL(i) dma16(qin + (size_t)(256 * (i)) * D_CH, gk - 64 * p * (D_CH * 2), rb + 16384 + 8192 * ((i) >> 1) + 1024 * ((i) & 1));
    KV(1)
    asm volatile("s_waitcnt vmcnt(4)" : "+v"(sw) :: "memory");
    u32x4 w1, w2;
    kv_weights(sw, lane, w1, w2);
    asm volatile("" : "+v"(w1), "+v"(w2));
    wait_vm<0>();
    KV(2)
    kv_2<16384, 512>(ka0[0], ka, va, xw, w1, w2);
    lds_add(fl0 + FL_XQ + 4, 2u);
    TAPS(0) TAPS(1) QL(0) QL(1)
    poll_all(fl0 + FL_XQ, 8u);
    wait_vm<10>();
    KV(3)
    kv_2<32768, 1024>(ka0[1], ka, va, xw, w1, w2);
    lds_add(fl0 + FL_XQ + 8, 2u);
    wait_vm<6>();
    lds_add(fl0 + FL_XQ + 16, 2u);
    lds_add(fl0 + FL_XQ + 20, 2u);
    QL(2) QL(3)
    const bool zl = !(lane & 4);
    const u32 yi = zl ? 0u : 512u;
    const u32 ya = sb + HS_OFF + (zl ? C2 + 4 : ((lane >> 3) & 1) * HW + 32 * (lane >> 4) + 8 * (lane & 3) + 128 * p);
    const u32 oo = ((64 * p + lane) * (2 * D_CH) + g * 8) * 2;
    const u32 qa = rb + 16384 + 16 * lane;
#pragma nounroll
    for (int st = 0; st < 8; st++) {
      poll_all(fl0 + FL_Y + 32 * st + 4 * rg, 1u);
      if (st == 5) {
      }
      if (st == 7) {
      }
      wait_vm<7>();
      if (st >= 4) wait_vm<5>();
      out_b<AUX0>(ya + yi * st, qa + 8192 * (st >> 1) + 1024 * (st & 1), oo + st * (256 * 2 * D_CH * 2), ro);
      if (st == 1) {
        wait_vm<4>();
        kv_2<0, 1536>(ka0[2], ka, va, xw, w1, w2);
        lds_add(fl0 + FL_XQ + 12, 2u);
        QL(4) QL(5) QL(6) QL(7)
      }
      if (st == 3) {
      }
    }
  }
}

__global__ __launch_bounds__(768) void fused_kernel(
    const u16* __restrict__ u, const u16* __restrict__ lar, const u16* __restrict__ aim,
    const u16* __restrict__ cre, const u16* __restrict__ cim, const u16* __restrict__ ldt,
    const u16* __restrict__ dskip, const u16* __restrict__ qin, const u16* __restrict__ kin,
    const u16* __restrict__ vin, const u16* __restrict__ shk, const u16* __restrict__ ssmk,
    const u16* __restrict__ ssmd, u16* __restrict__ out) {
  extern __shared__ __attribute__((aligned(16))) char smem[];
  typedef u32 u32x16 __attribute__((ext_vector_type(16)));
  typedef u32 u32x8 __attribute__((ext_vector_type(8)));
  u32x16 a0;
  u32x8 a1;
  u32x4 a2, l4;
  u32x2 lc;
  u32 la, lb, vo, t0, t1, t2, t3, st, sg, ss;
  asm volatile(
      "s_load_dwordx16 s[44:59], %[ka], 0\n\ts_load_dwordx8 s[60:67], %[ka], 64\n\ts_load_dwordx4 s[68:71], %[ka], 96\n\t"
      "s_getpc_b64 s[72:73]\n.Lpf_anchor:\n\ts_and_b32 s82, %[b], 0x78\n\ts_lshl_b32 s74, s82, 4\n\t"
      "v_lshl_add_u32 %[t3], %[tid], 11, s74\n\tv_cmp_gt_u32 vcc, .Lfused_end-.Lpf_anchor, %[t3]\n\ts_mov_b64 exec, vcc\n\t"
      "global_load_dword v2, %[t3], s[72:73]\n\ts_mov_b64 exec, -1\n\t"
      "v_readfirstlane_b32 %[st], %[tid]\n\ts_lshr_b32 s74, %[b], 6\n\ts_and_b32 s74, s74, 2\n\ts_add_u32 s74, s74, %[b]\n\t"
      "s_and_b32 s74, s74, 7\n\ts_lshl_b32 s83, s74, 8\n\ts_lshl1_add_u32 %[sg], s82, s83\n\ts_lshl_b32 s74, s82, 10\n\ts_or_b32 s74, s74, s83\n\t"
      "v_lshl_add_u32 %[t0], %[tid], 11, s74\n\t"
      "s_lshl_b32 s74, %[sg], 7\n\tv_lshl_add_u32 %[t1], %[tid], 2, s74\n\tv_lshrrev_b32 %[t2], 6, %[tid]\n\t"
      "v_lshl_add_u32 %[t2], %[t2], 1, %[sg]\n\ts_mul_i32 s74, %[st], 48\n\ts_lshl_b32 s75, %[sg], 11\n\ts_add_u32 s74, s74, s75\n\t"
      "v_lshl_add_u32 %[vo], %[tid], 4, s74\n\ts_cmpk_lt_u32 %[st], 0x200\n\ts_cselect_b64 s[76:77], -1, 0\n\ts_cmpk_lt_u32 %[b], 0x80\n\t"
      "s_cselect_b64 s[78:79], s[76:77], 0\n\ts_cselect_b64 s[80:81], 0, s[76:77]\n\ts_cselect_b32 s76, s76, -1\n\t"
      "s_and_b64 exec, s[80:81], 15\n\ts_waitcnt lgkmcnt(0)\n\t"
      "global_load_dwordx2 v[2:3], %[t0], s[60:61] offset:124\n\tglobal_load_dwordx2 v[2:3], %[t0], s[62:63] offset:124\n\ts_mov_b64 exec, s[78:79]\n\t"
      "global_load_ushort %[la], %[t2], s[54:55]\n\tglobal_load_dword v4, %[t1], s[46:47] nt\n\tglobal_load_dword v5, %[t1], s[48:49] nt\n\t"
      "global_load_dword v6, %[t1], s[50:51] nt\n\tglobal_load_dword v7, %[t1], s[52:53] nt\n\tglobal_load_ushort %[lb], %[t2], s[56:57]\n\t"
      "s_mov_b64 exec, s[80:81]\n\tglobal_load_dwordx4 v[4:7], %[vo], s[66:67]\n\tglobal_load_ushort %[lb], %[t2], s[68:69]\n\t"
      "s_mov_b64 exec, -1\n\ts_cmp_lg_u32 s76, 0\n\ts_cbranch_scc1 .Lfe\n\t"
      "s_add_u32 s75, s75, 0x100000\n\ts_and_b32 s75, s75, 0x3fffff\n\ts_lshr_b32 s74, s75, 11\n\ts_and_b32 s74, s74, 0x700\n\t"
      "s_bitset1_b32 s74, 20\n\ts_cmpk_eq_u32 %[st], 0x2c0\n\ts_cselect_b32 s74, s83, s74\n\ts_lshl_b32 s82, s82, 8\n\ts_or_b32 s74, s74, s82\n\t"
      "s_bitset1_b32 s75, 10\n\t"
      "s_cmpk_eq_u32 %[st], 0x280\n\ts_cselect_b32 s76, 12, 15\n\ts_cselect_b32 s77, 3, 1\n\ts_cselect_b32 s78, 7, 1\n\ts_cselect_b32 s75, s75, s74\n\t"
      "s_cselect_b32 s74, 10, 20\n\ts_cselect_b64 s[46:47], s[66:67], s[44:45]\n\ts_lshl_b32 %[ss], 1, s74\n\t"
      "s_cmpk_eq_u32 %[st], 0x200\n\ts_cselect_b64 s[46:47], s[60:61], s[46:47]\n\ts_cmpk_eq_u32 %[st], 0x240\n\t"
      "s_cselect_b64 s[46:47], s[62:63], s[46:47]\n\t"
      "v_and_b32 %[t1], 63, %[tid]\n\tv_lshrrev_b32 %[t1], s77, %[t1]\n\tv_and_b32 %[t2], s78, %[tid]\n\tv_lshlrev_b32 %[t1], s76, %[t1]\n\t"
      "v_lshl_add_u32 %[t1], %[t2], 7, %[t1]\n\tv_add_u32 %[t1], s75, %[t1]\n\tv_add_u32 %[vo], %[ss], %[t1]\n\t" A_DELAY0
      "global_load_dword %[la], %[t1], s[46:47]\n" A_R2
      ".Lfe:"
      : "=&{v[4:7]}"(l4), [la] "=&v"(la), [lb] "=&v"(lb), "=&{v[2:3]}"(lc), [vo] "=&v"(vo), [t0] "=&v"(t0), [t1] "=&v"(t1), [t2] "=&v"(t2),
        [t3] "=&v"(t3), "=&{s[44:59]}"(a0), "=&{s[60:67]}"(a1), "=&{s[68:71]}"(a2), [st] "=&s"(st), [sg] "=&s"(sg), [ss] "=&s"(ss)
      : [ka] "s"(__builtin_amdgcn_kernarg_segment_ptr()), [b] "s"(blockIdx.x), [tid] "v"(threadIdx.x)
      : "memory", "scc", "vcc", "s72", "s73", "s74", "s75", "s76", "s77", "s78", "s79", "s80", "s81", "s82", "s83");
#define UNI(x) (u32)__builtin_amdgcn_readfirstlane(x)
#define PTR(a, i) ((const u16*)(((uint64_t)UNI(a[2 * (i) + 1]) << 32) | UNI(a[2 * (i)])))
  const int w = UNI(st) >> 6, g = (blockIdx.x & 128) | (UNI(sg) >> 4);
  if (w >= 8) stream_body( smem, g, w - 8, la, lb, vo, UNI(ss), PTR(a0, 1), PTR(a0, 0), PTR(a1, 0), PTR(a1, 1), PTR(a1, 2), PTR(a1, 3),
                          PTR(a0, 7), (u16*)PTR(a2, 1));
  else main_body( smem, g, w, l4, la, lb, lc, vo, PTR(a1, 0), PTR(a1, 1), PTR(a1, 2), PTR(a1, 3));
}
__global__ void end_marker_kernel() { asm volatile(".Lfused_end:"); }

extern "C" void kernel_launch(void* const* d_in, const int* in_sizes, int n_in, void* d_out, int out_size,
                              void* d_ws, size_t ws_size, hipStream_t stream) {
  (void)hipFuncSetAttribute((const void*)fused_kernel, hipFuncAttributeMaxDynamicSharedMemorySize, LDS_BYTES);
  fused_kernel<<<256, 768, LDS_BYTES, stream>>>((const u16*)d_in[0], (const u16*)d_in[1], (const u16*)d_in[2], (const u16*)d_in[3],
                                                (const u16*)d_in[4], (const u16*)d_in[5], (const u16*)d_in[6], (const u16*)d_in[7],
                                                (const u16*)d_in[8], (const u16*)d_in[9], (const u16*)d_in[10], (const u16*)d_in[11],
                                                (const u16*)d_in[12], (u16*)d_out);
}
```
